```python
import torch

def _softmax(x, axis=-1):
    return torch.softmax(x, dim=axis)

def _gelu(x):
    return torch.nn.functional.gelu(x, approximate='tanh')

def _silu(x):
    return torch.nn.functional.silu(x)

def _sigmoid(x):
    return torch.sigmoid(x)

def _layernorm(x, axis=-1, eps=1e-05):
    m = x.mean(dim=axis, keepdim=True)
    v = x.var(dim=axis, keepdim=True, unbiased=False)
    return (x - m) / torch.sqrt(v + eps)

def _scatter_add(tgt, idx, src, axis=0):
    return tgt.clone().index_add_(axis, idx, src)

def _segment_sum(src, seg, n):
    out = torch.zeros((n,) + src.shape[1:], dtype=src.dtype)
    return out.index_add_(0, seg, src)

def _pad(x, pw):
    flat = tuple((v for pair in reversed(pw) for v in pair))
    return torch.nn.functional.pad(x, flat)
kernel_name = 'adaLN_cond__grouped_kv_mqa__geglu_ffn__prompt'

def setup_inputs():

    _w = lambda a: (lambda t: t.to(torch.bfloat16) if t.is_floating_point() else t)(torch.as_tensor(a))

    def _A_setup():
        B, S, D, H = (1, 2048, 512, 32)
        Dh = D // H
        return {'x': _w((lambda *s: torch.randn(*s, dtype=torch.bfloat16) * 0.05)(B, S, D)), 'wq': _w((lambda *s: torch.randn(*s, dtype=torch.bfloat16) * 0.1)(D, H * Dh)), 'wk': _w((lambda *s: torch.randn(*s, dtype=torch.bfloat16) * 0.1)(D, Dh)), 'wv': _w((lambda *s: torch.randn(*s, dtype=torch.bfloat16) * 0.05)(D, Dh)), 'wo': _w((lambda *s: torch.randn(*s, dtype=torch.bfloat16) * 0.05)(H * Dh, D))}
    _A_in = _A_setup()

    def _B_setup():
        B, S, D, F = (1, 2048, 512, 1792)
        return {'x': _w((lambda *s: torch.randn(*s, dtype=torch.bfloat16) * 0.05)(B, S, D)), 'w_gate': _w((lambda *s: torch.randn(*s, dtype=torch.bfloat16) * 0.05)(D, F)), 'w_up': _w((lambda *s: torch.randn(*s, dtype=torch.bfloat16) * 0.05)(D, F)), 'w_down': _w((lambda *s: torch.randn(*s, dtype=torch.bfloat16) * 0.05)(F, D))}
    _B_in = _B_setup()
    return {'A_x': _A_in['x'], 'A_wq': _A_in['wq'], 'A_wk': _A_in['wk'], 'A_wv': _A_in['wv'], 'A_wo': _A_in['wo'], 'B_x': _B_in['x'], 'B_w_gate': _B_in['w_gate'], 'B_w_up': _B_in['w_up'], 'B_w_down': _B_in['w_down'], 'c': _w((lambda *s: torch.randn(*s, dtype=torch.bfloat16) * 0.1)(1, 512) * 8.0), 'w_mod': _w((lambda *s: torch.randn(*s, dtype=torch.bfloat16) * 0.1)(512, 6 * 512) * 512 ** (-0.5) * 8.0)}

def reference(A_x, A_wq, A_wk, A_wv, A_wo, B_x, B_w_gate, B_w_up, B_w_down, c, w_mod):

    def _A_ref(x, wq, wk, wv, wo):
        B, S, D = x.shape
        H = 32
        Dh = D // H
        q = (lambda x, *d: x.permute(*d))((x @ wq).reshape(B, S, H, Dh), 0, 2, 1, 3)
        k = x @ wk
        v = x @ wv
        scores = torch.einsum('bhqd,bkd->bhqk', q, k) * Dh ** (-0.5)
        p = _softmax(scores, axis=-1)
        o = torch.einsum('bhqk,bkd->bhqd', p, v)
        return (lambda x, *d: x.permute(*d))(o, 0, 2, 1, 3).reshape(B, S, H * Dh) @ wo

    def _B_ref(x, w_gate, w_up, w_down):
        return _gelu(x @ w_gate) * (x @ w_up) @ w_down
    x = A_x
    mod = (_silu(c) @ w_mod).reshape(c.shape[0], 6, 1, -1)
    sa, ca, ga, sb, cb, gb = (mod[:, 0].clone(), mod[:, 1].clone(), mod[:, 2].clone(), mod[:, 3].clone(), mod[:, 4].clone(), mod[:, 5].clone())
    h = _layernorm(x, axis=-1) * (1 + ca) + sa
    x = x + ga * _A_ref(h, A_wq, A_wk, A_wv, A_wo)
    h = _layernorm(x, axis=-1) * (1 + cb) + sb
    x = x + gb * _B_ref(h, B_w_gate, B_w_up, B_w_down)
    return x
```

```cpp
#include <hip/hip_runtime.h>
#include <hip/hip_bf16.h>
typedef unsigned short u16;
__device__ inline float b2f(u16 b){ return __uint_as_float((unsigned)b<<16); }
__device__ inline u16 f2b(float f){ unsigned u=__float_as_uint(f); u+=0x7fff+((u>>16)&1); return u>>16; }
__device__ inline float rb(float f){ return b2f(f2b(f)); }
constexpr int S=2048,D=512,H=32,DH=16,F=1792;

__global__ void k_mod(const u16* c,const u16* wm,float* mod){
  int n=blockIdx.x*blockDim.x+threadIdx.x; float s=0;
  for(int k=0;k<D;++k){ float cv=b2f(c[k]); float sv=rb(cv/(1.f+__expf(-cv))); s+=sv*b2f(wm[k*3072+n]); }
  mod[n]=rb(s);
}
__global__ void k_ln(const u16* x,const float* sh,const float* sc,u16* h){
  __shared__ float red[256];
  int r=blockIdx.x,t=threadIdx.x; float a=b2f(x[r*D+t]),b=b2f(x[r*D+t+256]);
  red[t]=a+b; __syncthreads(); for(int s=128;s;s>>=1){ if(t<s) red[t]+=red[t+s]; __syncthreads(); }
  float m=red[0]/D; __syncthreads();
  red[t]=(a-m)*(a-m)+(b-m)*(b-m); __syncthreads(); for(int s=128;s;s>>=1){ if(t<s) red[t]+=red[t+s]; __syncthreads(); }
  float rs=rsqrtf(red[0]/D+1e-5f);
  h[r*D+t]=f2b((a-m)*rs*(1.f+sc[t])+sh[t]); h[r*D+t+256]=f2b((b-m)*rs*(1.f+sc[t+256])+sh[t+256]);
}
template<int MODE>
__global__ void k_gemm(const u16* A,const u16* B,const u16* B2,u16* C,int N,int K,const u16* res,const float* g,float scale){
  int n=blockIdx.x*blockDim.x+threadIdx.x, m=blockIdx.y; if(n>=N) return;
  float s=0,s2=0;
  for(int k=0;k<K;++k){ float a=b2f(A[m*K+k]); s+=a*b2f(B[k*N+n]); if(MODE==2) s2+=a*b2f(B2[k*N+n]); }
  if(MODE==0) C[m*N+n]=f2b(s*scale);
  if(MODE==1) C[m*N+n]=f2b(b2f(res[m*N+n])+g[n]*s);
  if(MODE==2){ float u=0.7978845608f*(s+0.044715f*s*s*s); float ge=0.5f*s*(1.f+tanhf(u)); C[m*N+n]=f2b(ge*s2); }
}
__global__ void k_attn(const u16* q,const u16* k,const u16* v,u16* o){
  int row=blockIdx.x*blockDim.x+threadIdx.x;
  float qq[16]; for(int d=0;d<16;++d) qq[d]=b2f(q[row*16+d]);
  float m=-1e30f,l=0,acc[16]; for(int d=0;d<16;++d) acc[d]=0;
  for(int j=0;j<S;++j){ float s=0; for(int d=0;d<16;++d) s+=qq[d]*b2f(k[j*16+d]);
    float mn=fmaxf(m,s); float al=exp2f(m-mn); float p=rb(exp2f(s-mn)); l=l*al+p; for(int d=0;d<16;++d) acc[d]=acc[d]*al+p*b2f(v[j*16+d]); m=mn; }
  for(int d=0;d<16;++d) o[row*16+d]=f2b(acc[d]/l);
}
extern "C" void kernel_launch(void* const* d_in, const int* in_sizes, int n_in, void* d_out, int out_size, void* d_ws, size_t ws_size, hipStream_t stream) {
  const u16* x=(const u16*)d_in[0]; const u16* wq=(const u16*)d_in[1]; const u16* wk=(const u16*)d_in[2]; const u16* wv=(const u16*)d_in[3]; const u16* wo=(const u16*)d_in[4];
  const u16* wg=(const u16*)d_in[6]; const u16* wu=(const u16*)d_in[7]; const u16* wd=(const u16*)d_in[8]; const u16* c=(const u16*)d_in[9]; const u16* wm=(const u16*)d_in[10];
  char* ws=(char*)d_ws; float* mod=(float*)ws; u16* h=(u16*)(ws+(1<<20)); u16* q=(u16*)(ws+(4<<20)); u16* kk=(u16*)(ws+(8<<20)); u16* vv=(u16*)(ws+(9<<20));
  u16* o=(u16*)(ws+(12<<20)); u16* x1=(u16*)(ws+(16<<20)); u16* act=(u16*)(ws+(20<<20));
  k_mod<<<12,256,0,stream>>>(c,wm,mod);
  k_ln<<<S,256,0,stream>>>(x,mod,mod+512,h);
  k_gemm<0><<<dim3(2,S),256,0,stream>>>(h,wq,nullptr,q,512,512,nullptr,nullptr,0.25f*1.4426950408889634f);
  k_gemm<0><<<dim3(1,S),64,0,stream>>>(h,wk,nullptr,kk,16,512,nullptr,nullptr,1.f);
  k_gemm<0><<<dim3(1,S),64,0,stream>>>(h,wv,nullptr,vv,16,512,nullptr,nullptr,1.f);
  k_attn<<<S*H/256,256,0,stream>>>(q,kk,vv,o);
  k_gemm<1><<<dim3(2,S),256,0,stream>>>(o,wo,nullptr,x1,512,512,x,mod+1024,1.f);
  k_ln<<<S,256,0,stream>>>(x1,mod+1536,mod+2048,h);
  k_gemm<2><<<dim3(7,S),256,0,stream>>>(h,wg,wu,act,F,512,nullptr,nullptr,1.f);
  k_gemm<1><<<dim3(2,S),256,0,stream>>>(act,wd,nullptr,(u16*)d_out,512,F,x1,mod+2560,1.f);
}
```

```cpp
#include <hip/hip_runtime.h>
#include <stdint.h>

typedef unsigned short u16;
typedef unsigned int u32;
typedef __attribute__((ext_vector_type(8))) __bf16 bf16x8;
typedef __attribute__((ext_vector_type(2))) __bf16 bf16x2;
typedef __attribute__((ext_vector_type(2))) float f32x2;
typedef __attribute__((ext_vector_type(4))) float f32x4;
typedef __attribute__((ext_vector_type(16))) float f32x16;
typedef __attribute__((ext_vector_type(4))) u32 u32x4;
typedef __attribute__((ext_vector_type(2))) u32 u32x2;
typedef __attribute__((ext_vector_type(4))) short s16x4;
#define LAS __attribute__((address_space(3)))
#define GAS __attribute__((address_space(1)))
#define DI __device__ __forceinline__

constexpr int SEQ = 2048, DM = 512, FF = 1792;
constexpr float EPS = 1e-5f;
constexpr float C2 = 0.25f * 1.4426950408889634f;

constexpr size_t WS_MOD = 0;
constexpr size_t WS_UW = 16384;
constexpr size_t WS_KV = 131072;
constexpr size_t WS_STAT = WS_KV + 139264 + 8192;
constexpr size_t WS_WO = 1u << 20;
constexpr size_t WS_WD = WS_WO + (512u << 10);
constexpr size_t WS_WGU = WS_WD + (1792u << 10);
constexpr size_t WS_Q = WS_WGU + (3584u << 10);
constexpr size_t WS_O = WS_Q + (2u << 20);
constexpr size_t WS_X1 = WS_O + (2u << 20);
constexpr size_t WS_ACT = WS_X1 + (2u << 20);

struct Params {
  const u16 *x, *wq, *wk, *wv, *wo, *wg, *wu, *wd, *c, *wm;
  u16* out;
  char* ws;
};

DI f32x16 mfma(u32x4 a, u32x4 b, f32x16 c) {
  return __builtin_amdgcn_mfma_f32_32x32x16_bf16(__builtin_bit_cast(bf16x8, a), __builtin_bit_cast(bf16x8, b), c, 0, 0, 0);
}
DI u32 pk(float lo, float hi) {
  f32x2 v = {lo, hi};
  return __builtin_bit_cast(u32, __builtin_convertvector(v, bf16x2));
}
DI float bflo(u32 u) { return __uint_as_float(u << 16); }
DI float bfhi(u32 u) { return __uint_as_float(u & 0xffff0000u); }
DI float rbf(float f) { return bflo(pk(f, 0.f)); }
DI void glds16(const void* g, u32 lds) {
  u32 sv;
  asm volatile("s_mov_b32 %0, m0\n\ts_mov_b32 m0, %2\n\ts_nop 0\n\tglobal_load_lds_dwordx4 %1, off\n\ts_mov_b32 m0, %0"
               : "=&s"(sv) : "v"(g), "s"(lds) : "memory");
}
DI u32x4 gload16(const void* g) {
  u32x4 r;
  asm volatile("global_load_dwordx4 %0, %1, off" : "=v"(r) : "v"(g) : "memory");
  return r;
}
#define WAITV(N) asm volatile("s_waitcnt vmcnt(" #N ")" ::: "memory")
#define WAITV_BAR(N) asm volatile("s_waitcnt vmcnt(" #N ") lgkmcnt(0)\n\ts_barrier" ::: "memory")
#define BAR() asm volatile("s_waitcnt lgkmcnt(0)\n\ts_barrier" ::: "memory")
template <int N> DI void waitv() { asm volatile("s_waitcnt vmcnt(%0)" ::"n"(N) : "memory"); }
template <int N> DI void waitv_pin(u32x4& v) { asm volatile("s_waitcnt vmcnt(%1)" : "+v"(v) : "n"(N) : "memory"); }
DI u32x4 lds128(u32 a) { return *(const LAS u32x4*)(uintptr_t)a; }
DI f32x4 ldsf4(u32 a) { return *(const LAS f32x4*)(uintptr_t)a; }
DI void sts128(u32 a, u32x4 v) { *(LAS u32x4*)(uintptr_t)a = v; }
DI void stsf4(u32 a, f32x4 v) { *(LAS f32x4*)(uintptr_t)a = v; }
DI u32x2 ldstr(u32 a) { return __builtin_bit_cast(u32x2, __builtin_amdgcn_ds_read_tr16_b64_v4i16((LAS s16x4*)(uintptr_t)a)); }
DI u32x4 ldstr2(u32 a0, u32 a1) { u32x2 p = ldstr(a0), q = ldstr(a1); return u32x4{p[0], p[1], q[0], q[1]}; }
template <int CTRL> DI float dpp(float v) { return __int_as_float(__builtin_amdgcn_mov_dpp(__float_as_int(v), CTRL, 0xf, 0xf, true)); }
DI float swap32_sum(float v) { auto r = __builtin_amdgcn_permlane32_swap(__float_as_uint(v), __float_as_uint(v), false, false); return __uint_as_float(r[0]) + __uint_as_float(r[1]); }
DI float swap32_max(float v) { auto r = __builtin_amdgcn_permlane32_swap(__float_as_uint(v), __float_as_uint(v), false, false); return fmaxf(__uint_as_float(r[0]), __uint_as_float(r[1])); }
DI float swap16_sum(float v) { auto r = __builtin_amdgcn_permlane16_swap(__float_as_uint(v), __float_as_uint(v), false, false); return __uint_as_float(r[0]) + __uint_as_float(r[1]); }
DI float wave_sum(float v) {
  v += dpp<0xB1>(v); v += dpp<0x4E>(v); v += dpp<0x141>(v); v += dpp<0x140>(v);
  v = swap16_sum(v); return swap32_sum(v);
}
DI int rfl(int v) { return __builtin_amdgcn_readfirstlane(v); }

struct Ids { int G, c, g, j; };
DI Ids ids() { const int b = blockIdx.x, x = b & 7, i = b >> 3; Ids r; r.G = x * 2 + (i >> 4); r.c = i & 15; r.g = 2 * r.G + (r.c >> 3); r.j = r.c & 7; return r; }

__global__ __launch_bounds__(512, 2) void k0_prep(Params p) {
  extern __shared__ __attribute__((aligned(16))) char smem[];
  const u32 L = (u32)(uintptr_t)smem;
  const int tid = threadIdx.x, l = tid & 63, w = rfl(tid >> 6), b = blockIdx.x;
  const int h = l >> 5, nh = (l >> 4) & 1, q = (l & 15) >> 2, pp = l & 3;
  const int dcb = b & 7, drb = b >> 3;
  if (b < 224) {
    const int row = 8 * w + (l >> 3), ch = (l & 7) ^ (((row >> 1) & 1) << 2);
    glds16(p.wd + (size_t)(64 * drb + row) * DM + 64 * dcb + ch * 8, L + w * 1024);
  }
  if (w < 2) {
    const int row = 8 * w + (l >> 3), ch = (l & 7) ^ (((row >> 1) & 1) << 2);
    glds16(p.wo + (size_t)(16 * drb + row) * DM + 64 * dcb + ch * 8, L + 8192 + w * 1024);
  }
  if (b < 192) {
    const int half = tid & 1, r0 = tid >> 1;
    const u32x4 w0 = *(const u32x4*)(p.wm + (size_t)r0 * 3072 + 16 * b + 8 * half);
    const u32x4 w1 = *(const u32x4*)(p.wm + (size_t)(r0 + 256) * 3072 + 16 * b + 8 * half);
    const float c0 = bflo(p.c[r0]), c1 = bflo(p.c[r0 + 256]);
    const float s0 = rbf(c0 / (1.f + __expf(-c0))), s1 = rbf(c1 / (1.f + __expf(-c1)));
    float a[8];
#pragma unroll
    for (int i = 0; i < 4; ++i) { a[2 * i] = s0 * bflo(w0[i]) + s1 * bflo(w1[i]); a[2 * i + 1] = s0 * bfhi(w0[i]) + s1 * bfhi(w1[i]); }
#pragma unroll
    for (int i = 0; i < 8; ++i) {
      float v = a[i];
      v += dpp<0x4E>(v);
      v += dpp<0x124>(v);
      v += dpp<0x128>(v);
      v = swap16_sum(v); v = swap32_sum(v); a[i] = v;
    }
    if (l < 2) { stsf4(L + 12288 + w * 64 + l * 32, f32x4{a[0], a[1], a[2], a[3]}); stsf4(L + 12288 + w * 64 + l * 32 + 16, f32x4{a[4], a[5], a[6], a[7]}); }
  }
  WAITV_BAR(0);
  if (b < 192 && tid < 16) {
    float s = 0.f;
#pragma unroll
    for (int i = 0; i < 8; ++i) s += *(const LAS float*)(uintptr_t)(L + 12288 + i * 64 + tid * 4);
    ((float*)(p.ws + WS_MOD))[16 * b + tid] = rbf(s);
  }
  const int sw = ((q >> 1) & 1) << 2;
  if (b < 224) {
    const int nt = w & 1, ks = w >> 1;
    const u32 a0 = L + (16 * ks + 4 * h + q) * 128 + (((nt * 4 + nh * 2 + (pp >> 1)) ^ sw) * 16) + (pp & 1) * 8;
    const u32x4 f = ldstr2(a0, a0 + 1024);
    *(u32x4*)(p.ws + WS_WD + ((size_t)(2 * dcb + nt) * 112 + 4 * drb + ks) * 1024 + l * 16) = f;
  }
  if (w < 2) {
    const int nt = w;
    const u32 a0 = L + 8192 + (4 * h + q) * 128 + (((nt * 4 + nh * 2 + (pp >> 1)) ^ sw) * 16) + (pp & 1) * 8;
    const u32x4 f = ldstr2(a0, a0 + 1024);
    *(u32x4*)(p.ws + WS_WO + ((size_t)(2 * dcb + nt) * 32 + drb) * 1024 + l * 16) = f;
  }
}

constexpr u32 K1_A = 0, K1_B = 65536, K1_X = K1_B + 33024, K1_M = K1_X + 16384, K1_R = K1_M + 4096, K1_LDS = K1_R + 2048;
__global__ __launch_bounds__(512, 2) void k1_qkv(Params p) {
  extern __shared__ __attribute__((aligned(16))) char smem[];
  const u32 L = (u32)(uintptr_t)smem;
  const int tid = threadIdx.x, l = tid & 63, w = rfl(tid >> 6);
  const Ids id = ids();
  const int g = id.g, j = id.j;
  const int h = l >> 5, nh = (l >> 4) & 1, q = (l & 15) >> 2, pp = l & 3;
  const int tt = w & 1, nt = (w >> 1) & 1, kh = w >> 2;
  const float* mod = (const float*)(p.ws + WS_MOD);
#pragma unroll
  for (int i = 0; i < 8; ++i) {
    const int row = 64 * w + 8 * i + (l >> 3), ch = (l & 7) ^ (((row >> 1) & 1) << 2);
    glds16(p.wq + (size_t)row * DM + 64 * j + ch * 8, L + K1_A + (8 * w + i) * 1024);
  }
  const int uid = g * 7 + j - 1, uT = uid >> 1, ukh = uid & 1;
  if (j == 0) {
#pragma unroll
    for (int i = 0; i < 2; ++i) {
      glds16(p.wv + (2 * w + i) * 512 + l * 8, L + K1_B + (2 * w + i) * 1024);
      glds16(p.wk + (2 * w + i) * 512 + l * 8, L + K1_B + 16384 + 128 + (2 * w + i) * 1024);
    }
  } else {
    const int row = 256 * ukh + 32 * w + (l >> 1);
    glds16(p.wg + (size_t)row * FF + 16 * uT + (l & 1) * 8, L + K1_B + w * 1024);
    glds16(p.wu + (size_t)row * FF + 16 * uT + (l & 1) * 8, L + K1_B + 8192 + 128 + w * 1024);
  }
  float xh[8][8];
#pragma unroll
  for (int i = 0; i < 8; ++i) {
    const u32x4 v = *(const u32x4*)(p.x + (size_t)(64 * g + 8 * w + i) * DM + 8 * l);
#pragma unroll
    for (int e = 0; e < 4; ++e) { xh[i][2 * e] = bflo(v[e]); xh[i][2 * e + 1] = bfhi(v[e]); }
  }
#pragma unroll
  for (int i = 0; i < 8; ++i) {
    float s = 0.f;
#pragma unroll
    for (int e = 0; e < 8; ++e) s += xh[i][e];
    const float m = wave_sum(s) * (1.f / DM);
    float s2 = 0.f;
#pragma unroll
    for (int e = 0; e < 8; ++e) { xh[i][e] -= m; s2 += xh[i][e] * xh[i][e]; }
    const float r = __builtin_amdgcn_rsqf(wave_sum(s2) * (1.f / DM) + EPS);
#pragma unroll
    for (int e = 0; e < 8; ++e) xh[i][e] *= r;
  }
  if (tid < 256) {
    const int k = 256 * ukh + tid;
    if (j != 0) { *(LAS float*)(uintptr_t)(L + K1_M + tid * 4) = rbf(1.f + mod[2048 + k]); *(LAS float*)(uintptr_t)(L + K1_M + 1024 + tid * 4) = mod[1536 + k]; }
  }
  WAITV_BAR(0);
  u32x4 wqf[16], wkvf[16];
  {
    const u32 a0 = L + K1_A + (256 * kh + 8 * h + q) * 128 + (((nt * 4 + nh * 2 + (pp >> 1)) ^ (((q >> 1) & 1) << 2)) * 16) + (pp & 1) * 8;
#pragma unroll
    for (int s = 0; s < 16; ++s) wqf[s] = ldstr2(a0 + s * 2048, a0 + s * 2048 + 512);
  }
  if (j == 0) {
    const u32 a0 = L + K1_B + nh * (16384 + 128) + (256 * kh + 8 * h + q) * 32 + pp * 8;
#pragma unroll
    for (int s = 0; s < 16; ++s) wkvf[s] = ldstr2(a0 + s * 512, a0 + s * 512 + 128);
  }
  BAR();
  {
    const f32x4 a0 = *(const f32x4*)(mod + 512 + 8 * l), a1 = *(const f32x4*)(mod + 512 + 8 * l + 4);
    const f32x4 b0 = *(const f32x4*)(mod + 8 * l), b1 = *(const f32x4*)(mod + 8 * l + 4);
    float A[8], B[8];
#pragma unroll
    for (int e = 0; e < 4; ++e) { A[e] = rbf(1.f + a0[e]); A[4 + e] = rbf(1.f + a1[e]); B[e] = b0[e]; B[4 + e] = b1[e]; }
#pragma unroll
    for (int i = 0; i < 8; ++i) {
      u32x4 o;
#pragma unroll
      for (int e = 0; e < 4; ++e) o[e] = pk(fmaf(xh[i][2 * e], A[2 * e], B[2 * e]), fmaf(xh[i][2 * e + 1], A[2 * e + 1], B[2 * e + 1]));
      const int tk = 8 * w + i;
      sts128(L + K1_A + ((tk >> 5) * 32 + (l >> 1)) * 1024 + (l & 1) * 512 + (((tk & 31) ^ (l & 7)) * 16), o);
    }
  }
  BAR();
  u32x4 xf[16];
#pragma unroll
  for (int s = 0; s < 16; ++s) xf[s] = lds128(L + K1_A + (tt * 32 + 16 * kh + s) * 1024 + h * 512 + (((l & 31) ^ ((2 * s + h) & 7)) * 16));
  const u32 xs = L + K1_X + w * 2048 + l * 16, xr = L + K1_X + (w ^ 4) * 2048 + l * 16;
  if (j == 0) {
    f32x16 acc = {};
    if (nt == 0) {
#pragma unroll
      for (int s = 0; s < 16; ++s) acc = mfma(wkvf[s], xf[s], acc);
    } else {
#pragma unroll
      for (int s = 0; s < 16; ++s) acc = mfma(xf[s], wkvf[s], acc);
    }
    float keep[8];
    if (kh == 0) { stsf4(xs, f32x4{acc[8], acc[9], acc[10], acc[11]}); stsf4(xs + 1024, f32x4{acc[12], acc[13], acc[14], acc[15]});
#pragma unroll
      for (int e = 0; e < 8; ++e) keep[e] = acc[e];
    } else { stsf4(xs, f32x4{acc[0], acc[1], acc[2], acc[3]}); stsf4(xs + 1024, f32x4{acc[4], acc[5], acc[6], acc[7]});
#pragma unroll
      for (int e = 0; e < 8; ++e) keep[e] = acc[8 + e];
    }
    BAR();
    const f32x4 r0 = ldsf4(xr), r1 = ldsf4(xr + 1024);
    u32x4 o = {pk(keep[0] + r0[0], keep[1] + r0[1]), pk(keep[2] + r0[2], keep[3] + r0[3]), pk(keep[4] + r1[0], keep[5] + r1[1]), pk(keep[6] + r1[2], keep[7] + r1[3])};
    char* img = p.ws + WS_KV + (size_t)(2 * g + tt) * 2112;
    if (nt == 0) { if (kh == 1) *(u32x4*)(img + l * 16) = o; }
    else if ((l & 31) <= 16) {
      if ((l & 31) == 16) o = u32x4{0x3f803f80u, 0x3f803f80u, 0x3f803f80u, 0x3f803f80u};
      *(u32x4*)(img + 1024 + kh * 544 + h * 272 + (l & 31) * 16) = o;
    }
    BAR();
  }
  {
    f32x16 acc = {};
#pragma unroll
    for (int s = 0; s < 16; ++s) acc = mfma(wqf[s], xf[s], acc);
    float keep[8];
    if (kh == 0) { stsf4(xs, f32x4{acc[8], acc[9], acc[10], acc[11]}); stsf4(xs + 1024, f32x4{acc[12], acc[13], acc[14], acc[15]});
#pragma unroll
      for (int e = 0; e < 8; ++e) keep[e] = acc[e];
    } else { stsf4(xs, f32x4{acc[0], acc[1], acc[2], acc[3]}); stsf4(xs + 1024, f32x4{acc[4], acc[5], acc[6], acc[7]});
#pragma unroll
      for (int e = 0; e < 8; ++e) keep[e] = acc[8 + e];
    }
    BAR();
    const f32x4 r0 = ldsf4(xr), r1 = ldsf4(xr + 1024);
    const u32x4 o = {pk((keep[0] + r0[0]) * C2, (keep[1] + r0[1]) * C2), pk((keep[2] + r0[2]) * C2, (keep[3] + r0[3]) * C2),
                     pk((keep[4] + r1[0]) * C2, (keep[5] + r1[1]) * C2), pk((keep[6] + r1[2]) * C2, (keep[7] + r1[3]) * C2)};
    *(u32x4*)(p.ws + WS_Q + ((size_t)(2 * g + tt) * 32 + 4 * j + 2 * nt + kh) * 1024 + l * 16) = o;
  }
  if (j != 0) {
    float us = 0.f, wsum = 0.f;
#pragma unroll
    for (int i = 0; i < 2; ++i) {
      const int s = 2 * w + i;
      const u32 a0 = L + K1_B + (pp >> 1) * (8192 + 128) + (16 * s + 4 * h + q) * 32 + nh * 16 + (pp & 1) * 8;
      const u32x4 f = ldstr2(a0, a0 + 256);
      const f32x4 c0 = ldsf4(L + K1_M + (16 * s + 4 * h) * 4), c1 = ldsf4(L + K1_M + (16 * s + 8 + 4 * h) * 4);
      const f32x4 s0 = ldsf4(L + K1_M + 1024 + (16 * s + 4 * h) * 4), s1 = ldsf4(L + K1_M + 1024 + (16 * s + 8 + 4 * h) * 4);
      u32x4 o;
      o[0] = pk(bflo(f[0]) * c0[0], bfhi(f[0]) * c0[1]); o[1] = pk(bflo(f[1]) * c0[2], bfhi(f[1]) * c0[3]);
      o[2] = pk(bflo(f[2]) * c1[0], bfhi(f[2]) * c1[1]); o[3] = pk(bflo(f[3]) * c1[2], bfhi(f[3]) * c1[3]);
#pragma unroll
      for (int e = 0; e < 4; ++e) us += bflo(o[e]) + bfhi(o[e]);
      wsum += bflo(f[0]) * s0[0] + bfhi(f[0]) * s0[1] + bflo(f[1]) * s0[2] + bfhi(f[1]) * s0[3] + bflo(f[2]) * s1[0] + bfhi(f[2]) * s1[1] + bflo(f[3]) * s1[2] + bfhi(f[3]) * s1[3];
      *(u32x4*)(p.ws + WS_WGU + ((size_t)uT * 32 + 16 * ukh + s) * 1024 + l * 16) = o;
    }
    us = swap32_sum(us); wsum = swap32_sum(wsum);
    if (l < 32) *(LAS f32x2*)(uintptr_t)(L + K1_R + w * 256 + l * 8) = f32x2{us, wsum};
    BAR();
    if (tid < 64) {
      float s = 0.f;
#pragma unroll
      for (int i = 0; i < 8; ++i) s += *(const LAS float*)(uintptr_t)(L + K1_R + i * 256 + tid * 4);
      ((float*)(p.ws + WS_UW))[(ukh * 112 + uT) * 64 + tid] = s;
    }
  }
}

constexpr u32 K2_LDS = 139264;
#define EXP2(v) __builtin_amdgcn_exp2f(v)
__global__ __launch_bounds__(512, 2) void k2_attn(Params p) {
  extern __shared__ __attribute__((aligned(16))) char smem[];
  const u32 L = (u32)(uintptr_t)smem;
  const int tid = threadIdx.x, l = tid & 63, w = rfl(tid >> 6);
  const Ids id = ids();
  const int h = l >> 5;
  const size_t fo = ((size_t)(2 * id.g + (w & 1)) * 32 + 4 * id.j + (w & 2) + (w >> 2)) * 1024 + l * 16;
  u32x4 qf = gload16(p.ws + WS_Q + fo);
#pragma unroll
  for (int i = 0; i < 17; ++i) glds16(p.ws + WS_KV + (w + 8 * i) * 1024 + l * 16, L + (w + 8 * i) * 1024);
  asm volatile("s_waitcnt vmcnt(14)\n\ts_barrier" : "+v"(qf)::"memory");
  const u32 ka = L + l * 16, va = L + 1024 + h * 272 + min(l & 31, 16) * 16;
  f32x16 negm;
  {
    const f32x16 s0 = mfma(lds128(ka), qf, f32x16{});
    float m = fmaxf(fmaxf(s0[0], s0[1]), s0[2]);
#pragma unroll
    for (int r = 3; r < 15; r += 2) m = fmaxf(fmaxf(m, s0[r]), s0[r + 1]);
    m = swap32_max(fmaxf(m, s0[15]));
#pragma unroll
    for (int r = 0; r < 16; ++r) negm[r] = -m;
    asm volatile("" : "+v"(negm));
  }
  f32x16 o = {};
  f32x16 sA = mfma(lds128(ka), qf, negm), sB;
  u32x4 kn = lds128(ka + 2112), v0 = lds128(va), v1 = lds128(va + 544);
#define STEP(SC, SN, T)                                                                                   \
  {                                                                                                       \
    SN = mfma(kn, qf, negm);                                                                              \
    kn = lds128(ka + ((T) + 2) * 2112);                                                                   \
    u32x4 pa, pb;                                                                                         \
    pa[0] = pk(EXP2(SC[0]), EXP2(SC[1])); pa[1] = pk(EXP2(SC[2]), EXP2(SC[3]));                           \
    pa[2] = pk(EXP2(SC[4]), EXP2(SC[5])); pa[3] = pk(EXP2(SC[6]), EXP2(SC[7]));                           \
    o = mfma(v0, pa, o);                                                                                  \
    v0 = lds128(va + ((T) + 1) * 2112);                                                                   \
    pb[0] = pk(EXP2(SC[8]), EXP2(SC[9])); pb[1] = pk(EXP2(SC[10]), EXP2(SC[11]));                         \
    pb[2] = pk(EXP2(SC[12]), EXP2(SC[13])); pb[3] = pk(EXP2(SC[14]), EXP2(SC[15]));                       \
    o = mfma(v1, pb, o);                                                                                  \
    v1 = lds128(va + ((T) + 1) * 2112 + 544);                                                             \
  }
  for (int t = 0; t < 8; t += 2) { STEP(sA, sB, t) STEP(sB, sA, t + 1) }
  WAITV_BAR(9);
  for (int t = 8; t < 28; t += 2) { STEP(sA, sB, t) STEP(sB, sA, t + 1) }
  WAITV_BAR(0);
  for (int t = 28; t < 64; t += 2) { STEP(sA, sB, t) STEP(sB, sA, t + 1) }
#undef STEP
  bool bad = !(o[8] < 1.2676506e30f);
#pragma unroll
  for (int r = 0; r < 8; ++r) bad |= !(fabsf(o[r]) < 3.0e38f);
  if (__builtin_expect(__any(bad), 0)) {
    float m = -INFINITY;
    o = f32x16{};
    for (int t = 0; t < 64; ++t) {
      f32x16 s = mfma(lds128(ka + t * 2112), qf, f32x16{});
      float mx = s[0];
#pragma unroll
      for (int r = 1; r < 16; ++r) mx = fmaxf(mx, s[r]);
      const float mn = fmaxf(m, swap32_max(mx));
      const float al = EXP2(m - mn);
      m = mn;
      u32x4 pa, pb;
#pragma unroll
      for (int r = 0; r < 4; ++r) { pa[r] = pk(EXP2(s[2 * r] - mn), EXP2(s[2 * r + 1] - mn)); pb[r] = pk(EXP2(s[8 + 2 * r] - mn), EXP2(s[9 + 2 * r] - mn)); }
#pragma unroll
      for (int r = 0; r < 16; ++r) o[r] *= al;
      o = mfma(lds128(va + t * 2112), pa, o);
      o = mfma(lds128(va + t * 2112 + 544), pb, o);
    }
  }
  const float rl = 1.f / o[8];
  const u32x4 of = {pk(o[0] * rl, o[1] * rl), pk(o[2] * rl, o[3] * rl), pk(o[4] * rl, o[5] * rl), pk(o[6] * rl, o[7] * rl)};
  *(u32x4*)(p.ws + WS_O + fo) = of;
}

constexpr u32 K3_X = 65536, K3_S = K3_X + 16384, K3_LDS = K3_S + 2048;
__global__ __launch_bounds__(512, 2) void k3_wo(Params p) {
  extern __shared__ __attribute__((aligned(16))) char smem[];
  const u32 L = (u32)(uintptr_t)smem;
  const int tid = threadIdx.x, l = tid & 63, w = rfl(tid >> 6);
  const Ids id = ids();
  const int g = id.g, j = id.j, h = l >> 5, tt = w & 1, nt = (w >> 1) & 1, kh = w >> 2;
  const float* mod = (const float*)(p.ws + WS_MOD);
  u32x4 wf[16];
#pragma unroll
  for (int s = 0; s < 16; ++s) wf[s] = gload16(p.ws + WS_WO + ((size_t)(2 * j + nt) * 32 + 16 * kh + s) * 1024 + l * 16);
#pragma unroll
  for (int i = 0; i < 8; ++i) glds16(p.ws + WS_O + ((size_t)g * 64 + 8 * w + i) * 1024 + l * 16, L + (8 * w + i) * 1024);
  const int tok = 64 * g + 32 * tt + (l & 31), n0 = 64 * j + 32 * nt + 16 * kh + 4 * h;
  const u32x2 xa = *(const u32x2*)(p.x + (size_t)tok * DM + n0), xb = *(const u32x2*)(p.x + (size_t)tok * DM + n0 + 8);
  const f32x4 ga = *(const f32x4*)(mod + 1024 + n0), gb = *(const f32x4*)(mod + 1024 + n0 + 8);
  asm volatile("s_waitcnt vmcnt(0)\n\ts_barrier"
               : "+v"(wf[0]), "+v"(wf[1]), "+v"(wf[2]), "+v"(wf[3]), "+v"(wf[4]), "+v"(wf[5]), "+v"(wf[6]), "+v"(wf[7]), "+v"(wf[8]), "+v"(wf[9]),
                 "+v"(wf[10]), "+v"(wf[11]), "+v"(wf[12]), "+v"(wf[13]), "+v"(wf[14]), "+v"(wf[15])::"memory");
  f32x16 acc = {};
#pragma unroll
  for (int s = 0; s < 16; ++s) acc = mfma(wf[s], lds128(L + (tt * 32 + 16 * kh + s) * 1024 + l * 16), acc);
  const u32 xs = L + K3_X + w * 2048 + l * 16, xr = L + K3_X + (w ^ 4) * 2048 + l * 16;
  float keep[8];
  if (kh == 0) { stsf4(xs, f32x4{acc[8], acc[9], acc[10], acc[11]}); stsf4(xs + 1024, f32x4{acc[12], acc[13], acc[14], acc[15]});
#pragma unroll
    for (int e = 0; e < 8; ++e) keep[e] = acc[e];
  } else { stsf4(xs, f32x4{acc[0], acc[1], acc[2], acc[3]}); stsf4(xs + 1024, f32x4{acc[4], acc[5], acc[6], acc[7]});
#pragma unroll
    for (int e = 0; e < 8; ++e) keep[e] = acc[8 + e];
  }
  BAR();
  const f32x4 r0 = ldsf4(xr), r1 = ldsf4(xr + 1024);
  const u32x4 o = {pk(fmaf(ga[0], keep[0] + r0[0], bflo(xa[0])), fmaf(ga[1], keep[1] + r0[1], bfhi(xa[0]))),
                   pk(fmaf(ga[2], keep[2] + r0[2], bflo(xa[1])), fmaf(ga[3], keep[3] + r0[3], bfhi(xa[1]))),
                   pk(fmaf(gb[0], keep[4] + r1[0], bflo(xb[0])), fmaf(gb[1], keep[5] + r1[1], bfhi(xb[0]))),
                   pk(fmaf(gb[2], keep[6] + r1[2], bflo(xb[1])), fmaf(gb[3], keep[7] + r1[3], bfhi(xb[1])))};
  *(u32x4*)(p.ws + WS_X1 + ((size_t)(2 * g + tt) * 32 + 4 * j + 2 * nt + kh) * 1024 + l * 16) = o;
  float s1 = 0.f, s2 = 0.f;
#pragma unroll
  for (int e = 0; e < 4; ++e) { const float a = bflo(o[e]), b = bfhi(o[e]); s1 += a + b; s2 += a * a + b * b; }
  s1 = swap32_sum(s1); s2 = swap32_sum(s2);
  if (l < 32) *(LAS f32x2*)(uintptr_t)(L + K3_S + (w >> 1) * 512 + tt * 256 + l * 8) = f32x2{s1, s2};
  BAR();
  if (tid < 64) {
    f32x2 s = {0.f, 0.f};
#pragma unroll
    for (int i = 0; i < 4; ++i) s += *(const LAS f32x2*)(uintptr_t)(L + K3_S + i * 512 + tid * 8);
    *(f32x2*)(p.ws + WS_STAT + ((size_t)(64 * g + tid) * 8 + j) * 8) = s;
  }
}

constexpr u32 K4_SLOT = 28672, K4_UW = 5 * K4_SLOT, K4_LDS = K4_UW + 1792;
template <int NT, int T0> DI void k4_body(const Params& p, u32 L, int l, int w, int G, int c) {
  const int tt = w & 3, h = l >> 5;
  const char* wsrc = p.ws + WS_WGU + (size_t)c * 7 * 32768 + l * 16;
#define K4_STAGE(S)                                                                                                  \
  _Pragma("unroll") for (int i = 0; i < NT; ++i) {                                                                   \
    const int idx = w + 8 * i, ks = idx / 7, t = idx - 7 * ks;                                                       \
    glds16(wsrc + t * 32768 + (4 * (S) + ks) * 1024, L + ((S) % 5) * K4_SLOT + idx * 1024);                          \
  }
  K4_STAGE(0) K4_STAGE(1) K4_STAGE(2) K4_STAGE(3) K4_STAGE(4)
  u32x4 xf[32];
#pragma unroll
  for (int s = 0; s < 32; ++s) xf[s] = gload16(p.ws + WS_X1 + ((size_t)(4 * G + tt) * 32 + s) * 1024 + l * 16);
  f32x16 acc[NT];
#pragma unroll
  for (int t = 0; t < NT; ++t) acc[t] = f32x16{};
#define K4_PIN(S, N) asm volatile("s_waitcnt vmcnt(%4)" : "+v"(xf[4 * (S)]), "+v"(xf[4 * (S) + 1]), "+v"(xf[4 * (S) + 2]), "+v"(xf[4 * (S) + 3]) : "n"(N) : "memory");
#define K4_COMP(S)                                                                                                   \
  _Pragma("unroll") for (int ks = 0; ks < 4; ++ks) _Pragma("unroll") for (int t = 0; t < NT; ++t)                     \
      acc[t] = mfma(lds128(L + ((S) % 5) * K4_SLOT + (ks * 7 + T0 + t) * 1024 + l * 16), xf[4 * (S) + ks], acc[t]);
  K4_PIN(0, 28) BAR(); K4_COMP(0)
  K4_PIN(1, 24) K4_COMP(1)
  K4_PIN(2, 20) K4_COMP(2)
  BAR();
  K4_STAGE(5) K4_STAGE(6) K4_STAGE(7)
  K4_PIN(3, 16 + 3 * NT) K4_COMP(3)
  K4_PIN(4, 12 + 3 * NT) K4_COMP(4)
  K4_PIN(5, 0) K4_PIN(6, 0) K4_PIN(7, 0) BAR();
  K4_COMP(5) K4_COMP(6) K4_COMP(7)
#undef K4_STAGE
#undef K4_PIN
#undef K4_COMP
  const int tok = 128 * G + 32 * tt + (l & 31);
  float s1 = 0.f, s2 = 0.f;
#pragma unroll
  for (int i = 0; i < 4; ++i) { const f32x4 v = *(const f32x4*)(p.ws + WS_STAT + (size_t)tok * 64 + i * 16); s1 += v[0] + v[2]; s2 += v[1] + v[3]; }
  const float mu = s1 * (1.f / DM), rs = __builtin_amdgcn_rsqf(fmaxf(s2 * (1.f / DM) - mu * mu, 0.f) + EPS), nmr = -mu * rs;
#pragma unroll
  for (int t = 0; t < NT; ++t) {
    float a[8];
#pragma unroll
    for (int q2 = 0; q2 < 2; ++q2) {
      const u32 ub = L + K4_UW + (T0 + t) * 256 + (16 * q2 + 4 * h) * 8;
      const f32x4 g0 = ldsf4(ub), g1 = ldsf4(ub + 16), u0 = ldsf4(ub + 64), u1 = ldsf4(ub + 80);
      const float gu[4] = {g0[0], g0[2], g1[0], g1[2]}, gw[4] = {g0[1], g0[3], g1[1], g1[3]};
      const float uu[4] = {u0[0], u0[2], u1[0], u1[2]}, uw[4] = {u0[1], u0[3], u1[1], u1[3]};
#pragma unroll
      for (int e = 0; e < 4; ++e) {
        const float gv = fmaf(rs, acc[t][8 * q2 + e], fmaf(nmr, gu[e], gw[e]));
        const float uv = fmaf(rs, acc[t][8 * q2 + 4 + e], fmaf(nmr, uu[e], uw[e]));
        const float z = gv * fmaf(gv * gv, -2.f * 0.7978845608028654f * 0.044715f * 1.4426950408889634f, -2.f * 0.7978845608028654f * 1.4426950408889634f);
        a[4 * q2 + e] = gv * uv * __builtin_amdgcn_rcpf(1.f + EXP2(z));
      }
    }
    const u32x4 o = {pk(a[0], a[1]), pk(a[2], a[3]), pk(a[4], a[5]), pk(a[6], a[7])};
    *(u32x4*)(p.ws + WS_ACT + ((size_t)(4 * G + tt) * 112 + 7 * c + T0 + t) * 1024 + l * 16) = o;
  }
}
__global__ __launch_bounds__(512, 2) void k4_gu(Params p) {
  extern __shared__ __attribute__((aligned(16))) char smem[];
  const u32 L = (u32)(uintptr_t)smem;
  const int tid = threadIdx.x, l = tid & 63, w = rfl(tid >> 6);
  const Ids id = ids();
  if (tid < 448) {
    const float* uw = (const float*)(p.ws + WS_UW);
    *(LAS float*)(uintptr_t)(L + K4_UW + tid * 4) = uw[id.c * 448 + tid] + uw[112 * 64 + id.c * 448 + tid];
  }
  if (w < 4) k4_body<4, 0>(p, L, l, w, id.G, id.c);
  else k4_body<3, 4>(p, L, l, w, id.G, id.c);
}

constexpr u32 K5_LDS = 152 * 1024;
__global__ __launch_bounds__(512, 2) void k5_down(Params p) {
  extern __shared__ __attribute__((aligned(16))) char smem[];
  const u32 L = (u32)(uintptr_t)smem;
  const int tid = threadIdx.x, l = tid & 63, w = rfl(tid >> 6);
  const Ids id = ids();
  const int g = id.g, j = id.j, h = l >> 5, tt = w & 1, kq = w >> 1;
  const float* mod = (const float*)(p.ws + WS_MOD);
  const char* wsrc = p.ws + WS_WD + ((size_t)(2 * j) * 112 + 28 * kq) * 1024 + l * 16;
#pragma unroll
  for (int s = 0; s < 19; ++s) glds16(wsrc + (tt * 112 + s) * 1024, L + (kq * 38 + 2 * s + tt) * 1024);
  u32x4 wr[9][2];
#pragma unroll
  for (int s = 0; s < 9; ++s) { wr[s][0] = gload16(wsrc + (19 + s) * 1024); wr[s][1] = gload16(wsrc + (112 + 19 + s) * 1024); }
  const char* xsrc = p.ws + WS_ACT + ((size_t)(2 * g + tt) * 112 + 28 * kq) * 1024 + l * 16;
  constexpr int XW = 14;
  u32x4 xf[XW];
#pragma unroll
  for (int s = 0; s < XW; ++s) xf[s] = gload16(xsrc + s * 1024);
  f32x16 acc0 = {}, acc1 = {};
  asm volatile("s_waitcnt vmcnt(13)\n\ts_barrier"
               : "+v"(wr[0][0]), "+v"(wr[0][1]), "+v"(wr[1][0]), "+v"(wr[1][1]), "+v"(wr[2][0]), "+v"(wr[2][1]), "+v"(wr[3][0]), "+v"(wr[3][1]), "+v"(wr[4][0]),
                 "+v"(wr[4][1]), "+v"(wr[5][0]), "+v"(wr[5][1]), "+v"(wr[6][0]), "+v"(wr[6][1]), "+v"(wr[7][0]), "+v"(wr[7][1]), "+v"(wr[8][0]), "+v"(wr[8][1])::"memory");
#define K5_STEP(S)                                                                                   \
  {                                                                                                  \
    waitv_pin<((S) < 14 ? 13 : 27 - (S))>(xf[(S) % XW]);                                              \
    const u32x4 a0 = (S) < 19 ? lds128(L + (kq * 38 + 2 * (S)) * 1024 + l * 16) : wr[(S) < 19 ? 0 : (S) - 19][0];     \
    const u32x4 a1 = (S) < 19 ? lds128(L + (kq * 38 + 2 * (S) + 1) * 1024 + l * 16) : wr[(S) < 19 ? 0 : (S) - 19][1]; \
    acc0 = mfma(a0, xf[(S) % XW], acc0);                                                             \
    acc1 = mfma(a1, xf[(S) % XW], acc1);                                                             \
    if ((S) + XW < 28) xf[(S) % XW] = gload16(xsrc + ((S) + XW) * 1024);                             \
  }
  K5_STEP(0) K5_STEP(1) K5_STEP(2) K5_STEP(3) K5_STEP(4) K5_STEP(5) K5_STEP(6) K5_STEP(7) K5_STEP(8) K5_STEP(9)
  K5_STEP(10) K5_STEP(11) K5_STEP(12) K5_STEP(13) K5_STEP(14) K5_STEP(15) K5_STEP(16) K5_STEP(17) K5_STEP(18) K5_STEP(19)
  K5_STEP(20) K5_STEP(21) K5_STEP(22) K5_STEP(23) K5_STEP(24) K5_STEP(25) K5_STEP(26) K5_STEP(27)
#undef K5_STEP
  const int nt = kq & 1, hf = kq >> 1;
  const int tok = 64 * g + 32 * tt + (l & 31), n0 = 64 * j + 32 * nt + 16 * hf + 4 * h;
  const u32x4 x1 = *(const u32x4*)(p.ws + WS_X1 + ((size_t)(2 * g + tt) * 32 + 4 * j + 2 * nt + hf) * 1024 + l * 16);
  const f32x4 ga = *(const f32x4*)(mod + 2560 + n0), gb = *(const f32x4*)(mod + 2560 + n0 + 8);
  BAR();
#pragma unroll
  for (int pc = 0; pc < 4; ++pc) {
    const f32x16& a = (pc & 1) ? acc1 : acc0;
    const int o8 = (pc >> 1) * 8;
    const u32 d = L + ((tt * 4 + pc) * 4 + kq) * 2048 + l * 16;
    stsf4(d, f32x4{a[o8], a[o8 + 1], a[o8 + 2], a[o8 + 3]}); stsf4(d + 1024, f32x4{a[o8 + 4], a[o8 + 5], a[o8 + 6], a[o8 + 7]});
  }
  BAR();
  f32x4 r0 = {}, r1 = {};
#pragma unroll
  for (int i = 0; i < 4; ++i) { const u32 d = L + ((tt * 4 + kq) * 4 + i) * 2048 + l * 16; r0 += ldsf4(d); r1 += ldsf4(d + 1024); }
  u32 a0 = pk(fmaf(ga[0], r0[0], bflo(x1[0])), fmaf(ga[1], r0[1], bfhi(x1[0]))), a1 = pk(fmaf(ga[2], r0[2], bflo(x1[1])), fmaf(ga[3], r0[3], bfhi(x1[1])));
  u32 b0 = pk(fmaf(gb[0], r1[0], bflo(x1[2])), fmaf(gb[1], r1[1], bfhi(x1[2]))), b1 = pk(fmaf(gb[2], r1[2], bflo(x1[3])), fmaf(gb[3], r1[3], bfhi(x1[3])));
  const auto s0 = __builtin_amdgcn_permlane32_swap(a0, b0, false, false);
  const auto s1 = __builtin_amdgcn_permlane32_swap(a1, b1, false, false);
  *(u32x4*)(p.out + (size_t)tok * DM + 64 * j + 32 * nt + 16 * hf + 8 * h) = u32x4{s0[0], s1[0], s0[1], s1[1]};
}

extern "C" void kernel_launch(void* const* d_in, const int* in_sizes, int n_in, void* d_out, int out_size, void* d_ws, size_t ws_size, hipStream_t stream) {
  Params p;
  p.x = (const u16*)d_in[0]; p.wq = (const u16*)d_in[1]; p.wk = (const u16*)d_in[2]; p.wv = (const u16*)d_in[3]; p.wo = (const u16*)d_in[4];
  p.wg = (const u16*)d_in[6]; p.wu = (const u16*)d_in[7]; p.wd = (const u16*)d_in[8]; p.c = (const u16*)d_in[9]; p.wm = (const u16*)d_in[10];
  p.out = (u16*)d_out; p.ws = (char*)d_ws;
  (void)hipFuncSetAttribute((const void*)k1_qkv, hipFuncAttributeMaxDynamicSharedMemorySize, K1_LDS);
  (void)hipFuncSetAttribute((const void*)k2_attn, hipFuncAttributeMaxDynamicSharedMemorySize, K2_LDS);
  (void)hipFuncSetAttribute((const void*)k3_wo, hipFuncAttributeMaxDynamicSharedMemorySize, K3_LDS);
  (void)hipFuncSetAttribute((const void*)k4_gu, hipFuncAttributeMaxDynamicSharedMemorySize, K4_LDS);
  (void)hipFuncSetAttribute((const void*)k5_down, hipFuncAttributeMaxDynamicSharedMemorySize, K5_LDS);
  k0_prep<<<256, 512, 16384, stream>>>(p);
  k1_qkv<<<256, 512, K1_LDS, stream>>>(p);
  k2_attn<<<256, 512, K2_LDS, stream>>>(p);
  k3_wo<<<256, 512, K3_LDS, stream>>>(p);
  k4_gu<<<256, 512, K4_LDS, stream>>>(p);
  k5_down<<<256, 512, K5_LDS, stream>>>(p);
}
```

```cpp
#include <hip/hip_runtime.h>
#include <stdint.h>

typedef unsigned short u16;
typedef unsigned int u32;
typedef __attribute__((ext_vector_type(8))) __bf16 bf16x8;
typedef __attribute__((ext_vector_type(2))) __bf16 bf16x2;
typedef __attribute__((ext_vector_type(2))) float f32x2;
typedef __attribute__((ext_vector_type(4))) float f32x4;
typedef __attribute__((ext_vector_type(16))) float f32x16;
typedef __attribute__((ext_vector_type(4))) u32 u32x4;
typedef __attribute__((ext_vector_type(2))) u32 u32x2;
typedef __attribute__((ext_vector_type(4))) short s16x4;
#define LAS __attribute__((address_space(3)))
#define GAS __attribute__((address_space(1)))
#define DI __device__ __forceinline__

constexpr int DM = 512, FF = 1792;
constexpr float EPS = 1e-5f;
constexpr float C2 = 0.25f * 1.4426950408889634f;

constexpr size_t WS_EPOCH = 0;
constexpr size_t WS_FO = 4096;
constexpr size_t WS_FX = WS_FO + 8192;
constexpr size_t WS_FA = WS_FX + 4096;
constexpr size_t WS_FKV = WS_FA + 4096;
constexpr size_t WS_FP = WS_FKV + 4096;
constexpr size_t WS_MOD = 65536;
constexpr size_t WS_KV = WS_MOD + 8 * 12288;
constexpr size_t WS_STAT = WS_KV + 139264;
constexpr size_t WS_AUG = WS_STAT + 131072;
constexpr size_t WS_WO = 1u << 20;
constexpr size_t WS_WD = WS_WO + (512u << 10);
constexpr size_t WS_WGU = WS_WD + (1792u << 10);
constexpr size_t WS_O = WS_WGU + (3584u << 10);
constexpr size_t WS_X1 = WS_O + (2u << 20);
constexpr size_t WS_ACT = WS_X1 + (2u << 20);
static_assert(WS_AUG + 229376 <= WS_WO, "workspace map");
constexpr int TS = 2176;

struct Params {
  const u16 *x, *wq, *wk, *wv, *wo, *wg, *wu, *wd, *c, *wm;
  u16* out;
  char* ws;
};

DI f32x16 mfma(u32x4 a, u32x4 b, f32x16 c) {
  return __builtin_amdgcn_mfma_f32_32x32x16_bf16(__builtin_bit_cast(bf16x8, a), __builtin_bit_cast(bf16x8, b), c, 0, 0, 0);
}
DI u32 pk(float lo, float hi) {
  f32x2 v = {lo, hi};
  return __builtin_bit_cast(u32, __builtin_convertvector(v, bf16x2));
}
DI float bflo(u32 u) { return __uint_as_float(u << 16); }
DI float bfhi(u32 u) { return __uint_as_float(u & 0xffff0000u); }
DI float rbf(float f) { return bflo(pk(f, 0.f)); }
DI float dot2(u32 a, u32 b, float c) { return __builtin_amdgcn_fdot2_f32_bf16(__builtin_bit_cast(bf16x2, a), __builtin_bit_cast(bf16x2, b), c, false); }
DI void set_m0(u32 lds) { asm volatile("s_mov_b32 m0, %0\n\ts_nop 0" ::"s"(lds) : "memory"); }
template <int OFF> DI void dma(const void* base, u32 vo) { asm volatile("global_load_lds_dwordx4 %0, %1 offset:%2" ::"v"(vo), "s"(base), "n"(OFF) : "memory"); }
template <int OFF> DI void dma1(const void* base, u32 vo) { asm volatile("global_load_lds_dwordx4 %0, %1 offset:%2 sc1" ::"v"(vo), "s"(base), "n"(OFF) : "memory"); }
DI void dma4(const void* base, u32 vo, u32 lds) { set_m0(lds); dma<0>(base, vo); dma<1024>(base, vo); dma<2048>(base, vo); dma<3072>(base, vo); }
DI void dma4s(const void* base, u32 vo, u32 lds) { set_m0(lds); dma1<0>(base, vo); dma1<1024>(base, vo); dma1<2048>(base, vo); dma1<3072>(base, vo); }
template <int SC, int OFF> DI u32x4 gld_s(const void* base, u32 vo) {
  u32x4 r;
  if (SC) asm volatile("global_load_dwordx4 %0, %1, %2 offset:%3 sc1" : "=v"(r) : "v"(vo), "s"(base), "n"(OFF) : "memory");
  else asm volatile("global_load_dwordx4 %0, %1, %2 offset:%3" : "=v"(r) : "v"(vo), "s"(base), "n"(OFF) : "memory");
  return r;
}
template <int OFF> DI u32x2 gld8_s(const void* base, u32 vo) { u32x2 r; asm volatile("global_load_dwordx2 %0, %1, %2 offset:%3" : "=v"(r) : "v"(vo), "s"(base), "n"(OFF) : "memory"); return r; }
template <int OFF> DI u32 gld2_s(const void* base, u32 vo) { u32 r; asm volatile("global_load_ushort %0, %1, %2 offset:%3" : "=v"(r) : "v"(vo), "s"(base), "n"(OFF) : "memory"); return r; }
DI void touch8k(const char* base, u32 l128) { asm volatile("global_load_lds_dword %0, %1 sc1" ::"v"(l128), "s"(base) : "memory"); }
DI void gst16_sc1(char* base, u32 vo, u32x4 v) { asm volatile("global_store_dwordx4 %0, %1, %2 sc1\n\ts_nop 1" ::"v"(vo), "v"(v), "s"(base) : "memory"); }
DI void gst8_sc1(char* base, u32 vo, u32x2 v) { asm volatile("global_store_dwordx2 %0, %1, %2 sc1\n\ts_nop 1" ::"v"(vo), "v"(v), "s"(base) : "memory"); }
#define RLX_AGENT __ATOMIC_RELAXED, __HIP_MEMORY_SCOPE_AGENT
DI void put_flag(char* f, u32 tag) { __hip_atomic_store((GAS u32*)f, tag, RLX_AGENT); }
constexpr int SPIN_CAP = 1 << 20;
template <int N> DI void poll_flags(const char* f, u32 tag, int l) {
#pragma unroll 1
  for (int it = 0; it < SPIN_CAP; ++it) {
    const u32 v = __hip_atomic_load((const GAS u32*)f + (l < N ? l : 0), RLX_AGENT);
    if (__all(v == tag)) break;
    __builtin_amdgcn_s_sleep(1);
  }
  __builtin_amdgcn_fence(__ATOMIC_ACQUIRE, "wavefront");
}
#define SB() __builtin_amdgcn_sched_barrier(0)
#define PIN(x) asm volatile("" : "+v"(x))
#define WAITV(N) asm volatile("s_waitcnt vmcnt(" #N ")" ::: "memory")
#define WAITV_BAR(N) asm volatile("s_waitcnt vmcnt(" #N ") lgkmcnt(0)\n\ts_barrier" ::: "memory")
#define BAR() asm volatile("s_waitcnt lgkmcnt(0)\n\ts_barrier" ::: "memory")
template <int N> DI void waitv_pin(u32x4& v) { asm volatile("s_waitcnt vmcnt(%1)" : "+v"(v) : "n"(N) : "memory"); }
DI u32x4 lds128(u32 a) { return *(const LAS u32x4*)(uintptr_t)a; }
DI f32x4 ldsf4(u32 a) { return *(const LAS f32x4*)(uintptr_t)a; }
DI f32x2 ldsf2(u32 a) { return *(const LAS f32x2*)(uintptr_t)a; }
DI void sts128(u32 a, u32x4 v) { *(LAS u32x4*)(uintptr_t)a = v; }
DI void stsf4(u32 a, f32x4 v) { *(LAS f32x4*)(uintptr_t)a = v; }
DI u32x2 ldstr(u32 a) { return __builtin_bit_cast(u32x2, __builtin_amdgcn_ds_read_tr16_b64_v4i16((LAS s16x4*)(uintptr_t)a)); }
DI u32x4 ldstr2(u32 a0, u32 a1) { u32x2 p = ldstr(a0), q = ldstr(a1); return u32x4{p[0], p[1], q[0], q[1]}; }
template <int CTRL> DI float dpp(float v) { return __int_as_float(__builtin_amdgcn_mov_dpp(__float_as_int(v), CTRL, 0xf, 0xf, true)); }
DI float swap32_sum(float v) { auto r = __builtin_amdgcn_permlane32_swap(__float_as_uint(v), __float_as_uint(v), false, false); return __uint_as_float(r[0]) + __uint_as_float(r[1]); }
DI float swap32_max(float v) { auto r = __builtin_amdgcn_permlane32_swap(__float_as_uint(v), __float_as_uint(v), false, false); return fmaxf(__uint_as_float(r[0]), __uint_as_float(r[1])); }
DI float swap16_sum(float v) { auto r = __builtin_amdgcn_permlane16_swap(__float_as_uint(v), __float_as_uint(v), false, false); return __uint_as_float(r[0]) + __uint_as_float(r[1]); }
DI float lowhalf(float v) { auto r = __builtin_amdgcn_permlane32_swap(__float_as_uint(v), __float_as_uint(v), false, false); return __uint_as_float(r[0]); }
template <int CTRL> DI float halve(bool bit, float a, float b) { return (bit ? b : a) + dpp<CTRL>(bit ? a : b); }
DI int rfl(int v) { return __builtin_amdgcn_readfirstlane(v); }
#define EXP2(v) __builtin_amdgcn_exp2f(v)

struct Ids { int G, c, g, j; };
DI Ids ids() { const int b = blockIdx.x, x = b & 7, i = b >> 3; Ids r; r.G = (x >> 1) * 4 + (i >> 3); r.c = (x & 1) * 8 + (i & 7); r.g = 4 * x + (i >> 3); r.j = i & 7; return r; }

#define STAMP_DECL
#define STAMP(I)
#define STAMP_FLUSH(K)
#define STAMP_ARG
#define STAMP_PASS

constexpr u32 M_LDS = 163840;
constexpr u32 F_A = 0, F_B = 65536, F_D = F_B + 33024, F_X = F_D + 10240, F_M = F_X + 32768, F_R = F_M + 4096;
static_assert(F_R + 2048 <= M_LDS, "front-end LDS map");
constexpr u32 SL0 = 0, SL1 = 69632, SL2 = 32768, SL3 = 102400, TOPA = 135168, GAPA = 65536, OEX = 143616, JUNK = M_LDS - 256;

DI float rowmax16(const f32x16& s) {
  float m = fmaxf(fmaxf(s[0], s[1]), s[2]);
#pragma unroll
  for (int r = 3; r < 15; r += 2) m = fmaxf(fmaxf(m, s[r]), s[r + 1]);
  return swap32_max(fmaxf(m, s[15]));
}
constexpr float GC0 = -2.f * 0.7978845608028654f * 1.4426950408889634f, GC1 = GC0 * 0.044715f;
DI u32 exbuf(int i) { return i < 7 ? TOPA + i * 4096 : GAPA; }
template <u32 A> DI u32x4 ldsr(u32 lo, u32 mid) { return A < 65536 ? lds128(lo + A) : lds128(mid + (A - 65536)); }

DI void reduce4(const f32x16& acc0, const f32x16& acc1, int tt, int kq, u32 buf, u32 vo, float (&r)[8]) {
#pragma unroll
  for (int pc = 0; pc < 4; ++pc) {
    const f32x16& a = (pc & 1) ? acc1 : acc0;
    const int o8 = (pc >> 1) * 8;
    if (pc != kq) {
      const u32 d = buf + ((tt * 4 + pc) * 3 + ((kq - pc - 1) & 3)) * 2048 + vo;
      stsf4(d, f32x4{a[o8], a[o8 + 1], a[o8 + 2], a[o8 + 3]}); stsf4(d + 1024, f32x4{a[o8 + 4], a[o8 + 5], a[o8 + 6], a[o8 + 7]});
    } else {
#pragma unroll
      for (int e = 0; e < 8; ++e) r[e] = a[o8 + e];
    }
  }
  BAR();
#pragma unroll
  for (int i = 0; i < 3; ++i) {
    const u32 d = buf + ((tt * 4 + kq) * 3 + i) * 2048 + vo;
    const f32x4 u = ldsf4(d), v = ldsf4(d + 1024);
#pragma unroll
    for (int e = 0; e < 4; ++e) { r[e] += u[e]; r[4 + e] += v[e]; }
  }
}

template <int KH> DI void p4_body(const Params& p, const Ids& id, int l, int w, u32 tag, u32x4 (&wr)[9][2] STAMP_ARG) {
  const int tt = w & 3;
  const u32 vo = l * 16;
  const char* wsrc = p.ws + WS_WGU + (size_t)id.c * 7 * 32768 + w * 4096;
  const char* dsrc = p.ws + WS_WD + ((size_t)(2 * id.j + (w & 1)) * 112 + 28 * (w >> 1)) * 1024;
  const char* dsrc0 = p.ws + WS_WD + ((size_t)(2 * id.j) * 112 + 28 * (w >> 1) + 19) * 1024;
  char* adst = p.ws + WS_ACT + ((size_t)(4 * id.G + tt) * 112 + 7 * id.c) * 1024;
  const char* asrc = p.ws + WS_AUG + (size_t)(id.c * 14 + KH) * 1024;
  u32x4 aug[7];
  aug[0] = gld_s<1, 0>(asrc, vo); aug[1] = gld_s<1, 2048>(asrc, vo); aug[2] = gld_s<1, 0>(asrc + 4096, vo); aug[3] = gld_s<1, 2048>(asrc + 4096, vo);
  if (w == 0) {
    poll_flags<16>(p.ws + WS_FX + id.G * 256, tag, l);
    const char* t = p.ws + WS_X1 + (size_t)id.G * 131072 + (id.c & 7) * 16384;
    set_m0(TOPA); touch8k(t, l * 128); touch8k(t + 8192, l * 128);
    WAITV(2);
  } else {
    WAITV(0);
  }
  asm volatile("s_barrier" : "+v"(aug[0]), "+v"(aug[1]), "+v"(aug[2]), "+v"(aug[3])::"memory");
  STAMP(9)
  const char* xsrc = p.ws + WS_X1 + ((size_t)(4 * id.G + tt) * 32 + 16 * KH) * 1024;
  u32x4 xf[16];
#pragma unroll
  for (int s4 = 0; s4 < 4; ++s4) {
    xf[4 * s4] = gld_s<1, 0>(xsrc + s4 * 4096, vo); xf[4 * s4 + 1] = gld_s<1, 1024>(xsrc + s4 * 4096, vo);
    xf[4 * s4 + 2] = gld_s<1, 2048>(xsrc + s4 * 4096, vo); xf[4 * s4 + 3] = gld_s<1, 3072>(xsrc + s4 * 4096, vo);
  }
  const char* ssrc = p.ws + WS_STAT + (size_t)(128 * id.G + 32 * tt) * 64;
  u32x4 st0 = gld_s<1, 0>(ssrc, (l & 31) * 64), st1 = gld_s<1, 16>(ssrc, (l & 31) * 64), st2 = gld_s<1, 32>(ssrc, (l & 31) * 64), st3 = gld_s<1, 48>(ssrc, (l & 31) * 64);
  float rs; u32x4 xaug;
  u32 wlo = KH * 16384 + vo, wmid = wlo + 65536;
  PIN(wlo); PIN(wmid);
  f32x16 kp;
#define P4_SEND(A, WB) { stsf4(WB, f32x4{A[0], A[1], A[2], A[3]}); stsf4(WB + 1024, f32x4{A[4], A[5], A[6], A[7]}); stsf4(WB + 2048, f32x4{A[8], A[9], A[10], A[11]}); stsf4(WB + 3072, f32x4{A[12], A[13], A[14], A[15]}); }
#define GG_STAGE(ST, GO, RG, RU, OUT)                                                                              \
    { if (ST == 0) { _Pragma("unroll") for (int e = 0; e < 4; ++e) gv[e] = (kp[GO + e] + RG[e]) * rs; PIN(gv); }   \
      if (ST == 1) { _Pragma("unroll") for (int e = 0; e < 4; ++e) uv[e] = (kp[GO + 4 + e] + RU[e]) * rs; PIN(uv); } \
      if (ST == 2) { _Pragma("unroll") for (int e = 0; e < 4; ++e) zz[e] = fmaf(gv[e] * gv[e], GC1, GC0); PIN(zz); } \
      if (ST == 3) { _Pragma("unroll") for (int e = 0; e < 4; ++e) { zz[e] *= gv[e]; uv[e] *= gv[e]; } PIN(zz); PIN(uv); } \
      if (ST == 4) { _Pragma("unroll") for (int e = 0; e < 4; ++e) zz[e] = EXP2(zz[e]); PIN(zz); }                 \
      if (ST == 5) { _Pragma("unroll") for (int e = 0; e < 4; ++e) zz[e] += 1.f; PIN(zz); }                        \
      if (ST == 6) { _Pragma("unroll") for (int e = 0; e < 4; ++e) zz[e] = __builtin_amdgcn_rcpf(zz[e]); PIN(zz); } \
      if (ST == 7) { OUT[0] = pk(uv[0] * zz[0], uv[1] * zz[1]); OUT[1] = pk(uv[2] * zz[2], uv[3] * zz[3]); PIN(OUT); } }
  constexpr int LG = 4;
#define P4_ROUND(SS, SK, AUGS, AUGK, T, RB, WB, PFM)                                                               \
    {                                                                                                              \
      f32x16 a, b; u32x4 ra[16], rb[16]; f32x4 r0, r1, r2, r3, gv, uv, zz; u32x2 q0, q1;                           \
      _Pragma("unroll") for (int s = 0; s < 4; ++s) ra[s] = ldsr<SS>(wlo + s * 1024, wmid + s * 1024);             \
      r0 = ldsf4(RB); r1 = ldsf4(RB + 1024); r2 = ldsf4(RB + 2048); r3 = ldsf4(RB + 3072);                         \
      a = mfma(AUGS, xaug, f32x16{}); b = mfma(AUGK, xaug, f32x16{});                                              \
      SB();                                                                                                        \
      _Pragma("unroll") for (int s = 0; s < 16 + LG; ++s) {                                                        \
        if (s < 16) a = mfma(ra[s], xf[s], a);                                                                     \
        if (s + 4 < 16) ra[s + 4] = ldsr<SS>(wlo + (s + 4) * 1024, wmid + (s + 4) * 1024);                         \
        if (s + 3 - LG >= 0 && s + 3 - LG < 16) rb[s + 3 - LG < 0 ? 0 : (s + 3 - LG) & 15] = ldsr<SK>(wlo + ((s + 3 - LG) & 15) * 1024, wmid + ((s + 3 - LG) & 15) * 1024); \
        if (s < 16) PFM(s);                                                                                        \
        SB();                                                                                                      \
        if (s >= 1 && s < 9) GG_STAGE(s - 1, 0, r0, r1, q0) else if (s >= 9 && s < 17) GG_STAGE(s - 9, 8, r2, r3, q1) \
        if (s == 17) gst16_sc1(adst + (T) * 1024, vo, u32x4{q0[0], q0[1], q1[0], q1[1]});                          \
        if (s == 18) P4_SEND(a, WB)                                                                                \
        SB();                                                                                                      \
        if (s >= LG) { b = mfma(rb[(s - LG) & 15], xf[(s - LG) & 15], b); SB(); }                                  \
      }                                                                                                            \
      kp = b; PIN(kp);                                                                                             \
    }
#define WRL(I) (wr[(I) >> 1][(I) & 1] = gld_s<1, 0>(dsrc0 + (((I) >> 1) + ((I) & 1) * 112) * 1024, vo))
#define DM4(s, S0, SRC, DST) { if (s == S0) { set_m0(DST); dma1<0>(SRC, vo); } if (s == S0 + 1) dma1<1024>(SRC, vo); if (s == S0 + 2) dma1<2048>(SRC, vo); if (s == S0 + 3) dma1<3072>(SRC, vo); }
#define PF1(s) do { DM4(s, 0, wsrc + 4 * 32768, SL0 + w * 4096) DM4(s, 4, wsrc + 5 * 32768, SL1 + w * 4096)        \
                    if (s == 8) aug[4] = gld_s<1, 0>(asrc + 8192, vo); if (s == 9) aug[5] = gld_s<1, 2048>(asrc + 8192, vo); if (s >= 10 && s < 14) WRL((s >= 10 && s < 14 ? s - 10 : 0)); } while (0)
#define PF2(s) do { DM4(s, 0, wsrc + 6 * 32768, SL2 + w * 4096) if (s == 4) aug[6] = gld_s<1, 0>(asrc + 12288, vo);  \
                    DM4(s, 5, dsrc, SL3 + w * 4096) if (s >= 9 && s < 15) WRL((s >= 9 && s < 15 ? s - 5 : 0)); } while (0)
#define PF3(s) do { DM4(s, 0, dsrc + 4096, SL0 + w * 4096) DM4(s, 4, dsrc + 8192, SL1 + w * 4096) if (s >= 8 && !(s & 1)) WRL((s >= 8 ? 10 + ((s - 8) >> 1) : 0)); } while (0)
  const u32 eA = exbuf(w) + vo, eB = exbuf(w ^ 4) + vo;
  {
    constexpr u32 SS = KH ? SL0 : SL1, SK = KH ? SL1 : SL0;
    f32x16 a = {}, b = {}; u32x4 ra[16], rb[16];
#pragma unroll
    for (int s = 0; s < 3; ++s) { ra[s] = ldsr<SS>(wlo + s * 1024, wmid + s * 1024); rb[s] = ldsr<SK>(wlo + s * 1024, wmid + s * 1024); }
#pragma unroll
    for (int s = 0; s < 16; ++s) {
      if (s == 0) asm volatile("s_waitcnt vmcnt(18)" : "+v"(xf[0]), "+v"(xf[1])::"memory");
      if (s == 2) asm volatile("s_waitcnt vmcnt(16)" : "+v"(xf[2]), "+v"(xf[3])::"memory");
      if (s == 4) asm volatile("s_waitcnt vmcnt(12)" : "+v"(xf[4]), "+v"(xf[5]), "+v"(xf[6]), "+v"(xf[7])::"memory");
      if (s == 8) asm volatile("s_waitcnt vmcnt(8)" : "+v"(xf[8]), "+v"(xf[9]), "+v"(xf[10]), "+v"(xf[11])::"memory");
      if (s == 12) asm volatile("s_waitcnt vmcnt(4)" : "+v"(xf[12]), "+v"(xf[13]), "+v"(xf[14]), "+v"(xf[15])::"memory");
      a = mfma(ra[s], xf[s], a);
      if (s + 3 < 16) { ra[s + 3] = ldsr<SS>(wlo + (s + 3) * 1024, wmid + (s + 3) * 1024); rb[s + 3] = ldsr<SK>(wlo + (s + 3) * 1024, wmid + (s + 3) * 1024); }
      if (s < 13) b = mfma(rb[s], xf[s], b);
      SB();
    }
    asm volatile("s_waitcnt vmcnt(0)" : "+v"(st0), "+v"(st1), "+v"(st2), "+v"(st3)::"memory");
    const float s1 = (__uint_as_float(st0[0]) + __uint_as_float(st0[2])) + (__uint_as_float(st1[0]) + __uint_as_float(st1[2])) + (__uint_as_float(st2[0]) + __uint_as_float(st2[2])) + (__uint_as_float(st3[0]) + __uint_as_float(st3[2]));
    const float s2 = (__uint_as_float(st0[1]) + __uint_as_float(st0[3])) + (__uint_as_float(st1[1]) + __uint_as_float(st1[3])) + (__uint_as_float(st2[1]) + __uint_as_float(st2[3])) + (__uint_as_float(st3[1]) + __uint_as_float(st3[3]));
    const float nmu = -s1 * (1.f / DM), var = fmaxf(s2 * (1.f / DM) - nmu * nmu, 0.f) + EPS;
    rs = __builtin_amdgcn_rsqf(var);
    const float sg = var * rs, mh = rbf(nmu), sh = rbf(sg);
    xaug = u32x4{pk(mh, mh), pk(nmu - mh, sh), pk(sh, sg - sh), 0u};
    if (l >= 32) xaug = u32x4{0u, 0u, 0u, 0u};
    a = mfma(aug[KH ? 0 : 1], xaug, a);
    SB();
    b = mfma(rb[13], xf[13], b); b = mfma(rb[14], xf[14], b); b = mfma(rb[15], xf[15], b);
    SB();
    P4_SEND(a, eA)
    SB();
    b = mfma(aug[KH ? 1 : 0], xaug, b);
    kp = b; PIN(kp);
  }
  STAMP(11)
  BAR();
  STAMP(12)
  P4_ROUND((KH ? SL2 : SL3), (KH ? SL3 : SL2), aug[KH ? 2 : 3], aug[KH ? 3 : 2], KH, eB, eB, PF1)
  STAMP(13)
  asm volatile("s_waitcnt vmcnt(5) lgkmcnt(0)\n\ts_barrier" : "+v"(aug[4]), "+v"(aug[5])::"memory");
  STAMP(14)
  P4_ROUND((KH ? SL0 : SL1), (KH ? SL1 : SL0), aug[KH ? 4 : 5], aug[KH ? 5 : 4], 2 + KH, eA, eA, PF2)
  STAMP(15)
  asm volatile("s_waitcnt vmcnt(11) lgkmcnt(0)\n\ts_barrier" : "+v"(aug[6])::"memory");
  STAMP(16)
  {
    f32x16 a; u32x4 ra[16]; f32x4 r0, r1, r2, r3, gv, uv, zz; u32x2 q0, q1;
#pragma unroll
    for (int s = 0; s < 4; ++s) ra[s] = ldsr<SL2>(wlo + s * 1024, wmid);
    r0 = ldsf4(eB); r1 = ldsf4(eB + 1024); r2 = ldsf4(eB + 2048); r3 = ldsf4(eB + 3072);
    a = mfma(aug[6], xaug, f32x16{});
    SB();
#pragma unroll
    for (int s = 0; s < 16; ++s) {
      a = mfma(ra[s], xf[s], a);
      if (s + 4 < 16) ra[s + 4] = ldsr<SL2>(wlo + (s + 4) * 1024, wmid);
      PF3(s);
      SB();
      if (s < 8) GG_STAGE(s, 0, r0, r1, q0)
      SB();
    }
    constexpr int K8 = KH * 8, S8 = 8 - K8;
    GG_STAGE(0, 8, r2, r3, q1) GG_STAGE(1, 8, r2, r3, q1) GG_STAGE(2, 8, r2, r3, q1) GG_STAGE(3, 8, r2, r3, q1)
    SB();
    stsf4(eB, f32x4{a[S8], a[S8 + 1], a[S8 + 2], a[S8 + 3]}); stsf4(eB + 1024, f32x4{a[S8 + 4], a[S8 + 5], a[S8 + 6], a[S8 + 7]});
    SB();
    GG_STAGE(4, 8, r2, r3, q1) GG_STAGE(5, 8, r2, r3, q1) GG_STAGE(6, 8, r2, r3, q1) GG_STAGE(7, 8, r2, r3, q1)
    gst16_sc1(adst + (4 + KH) * 1024, vo, u32x4{q0[0], q0[1], q1[0], q1[1]});
    kp = a;
    BAR();
    r0 = ldsf4(eA); r1 = ldsf4(eA + 1024);
#pragma unroll
    for (int s = 0; s < 8; ++s) GG_STAGE(s, K8, r0, r1, q0)
    gst8_sc1(adst + 6 * 1024 + KH * 8, vo, q0);
  }
  STAMP(17)
  BAR();
  dma4s(dsrc + 12288, vo, SL2 + w * 4096);
  set_m0(TOPA + w * 3072); dma1<0>(dsrc + 16384, vo); dma1<1024>(dsrc + 16384, vo); dma1<2048>(dsrc + 16384, vo);
  WRL(14); WRL(15); WRL(16); WRL(17);
  WAITV_BAR(11);
  STAMP(18)
  if (w == 0 && l == 0) put_flag(p.ws + WS_FA + id.G * 256 + id.c * 4, tag);
#undef P4_ROUND
#undef P4_SEND
#undef GG_STAGE
}

__global__ __launch_bounds__(512, 2) void k_main(Params p) {
  extern __shared__ __attribute__((aligned(16))) char smem[];
  const int tid = threadIdx.x, l = tid & 63, w = rfl(tid >> 6);
  const Ids id = ids();
  const int g = id.g, j = id.j, h = l >> 5;
  const u32 vo = l * 16;
  const u32 epoch = *(const u32*)(p.ws + WS_EPOCH);
  const u32 tag = epoch * 0x9E3779B1u + 0x7F4A7C15u;
  const int tt = w & 1, nt = (w >> 1) & 1, kh = w >> 2, kq = w >> 1;
  const char* modr = p.ws + WS_MOD + (blockIdx.x & 7) * 12288;
  STAMP_DECL STAMP(0)
  u32x4 q0, q1;
  {
    const int nh = (l >> 4) & 1, q = (l & 15) >> 2, pp = l & 3;
    const int uid = g * 7 + j - 1, uT = uid >> 1, ukh = uid & 1;
    const int rk = (blockIdx.x >> 6) * 7 + j - 1;
    const bool gemv = j != 0 && rk < 24;
    const int mu = 4 * ((blockIdx.x & 7) + 8 * (rk >> 2)) + (rk & 3);
    const u32 rawo = (l >> 3) * 1024 + (((l & 7) ^ (((l >> 4) & 1) << 2)) * 16);
    if (w < 2) { set_m0(F_D + 8192 + w * 1024); dma<0>(p.wo + (size_t)(16 * g + 8 * w) * DM + 64 * j, rawo); }
    u32x4 w0, w1; u32 c0, c1;
    if (gemv) {
      const u32 mo = (tid >> 1) * 6144 + (tid & 1) * 16;
      w0 = gld_s<0, 0>(p.wm + 16 * mu, mo); w1 = gld_s<0, 0>(p.wm + 16 * mu + 256 * 3072, mo);
      c0 = gld2_s<0>(p.c, (tid >> 1) * 2); c1 = gld2_s<512>(p.c, (tid >> 1) * 2);
    }
    u32x4 xv[8];
    {
      const char* xs = (const char*)(p.x + (size_t)(64 * g + 8 * w) * DM);
      xv[0] = gld_s<0, 0>(xs, vo); xv[1] = gld_s<0, 1024>(xs, vo); xv[2] = gld_s<0, 2048>(xs, vo); xv[3] = gld_s<0, 3072>(xs, vo);
      xv[4] = gld_s<0, 0>(xs + 4096, vo); xv[5] = gld_s<0, 1024>(xs + 4096, vo); xv[6] = gld_s<0, 2048>(xs + 4096, vo); xv[7] = gld_s<0, 3072>(xs + 4096, vo);
    }
#pragma unroll
    for (int i = 0; i < 8; ++i) { set_m0(F_A + (8 * w + i) * 1024); dma<0>(p.wq + (size_t)(64 * w + 8 * i) * DM + 64 * j, rawo); }
    if (j == 0) {
      set_m0(F_B + w * 2048); dma<0>(p.wv + w * 1024, vo); dma<1024>(p.wv + w * 1024, vo);
      set_m0(F_B + 16384 + 128 + w * 2048); dma<0>(p.wk + w * 1024, vo); dma<1024>(p.wk + w * 1024, vo);
    } else {
      const u32 go = (l >> 1) * (FF * 2) + (l & 1) * 16;
      set_m0(F_B + w * 1024); dma<0>(p.wg + (size_t)(256 * ukh + 32 * w) * FF + 16 * uT, go);
      set_m0(F_B + 8192 + 128 + w * 1024); dma<0>(p.wu + (size_t)(256 * ukh + 32 * w) * FF + 16 * uT, go);
      set_m0(F_D + w * 1024); dma<0>(p.wd + (size_t)(64 * (uid >> 3) + 8 * w) * DM + 64 * (uid & 7), rawo);
    }
    if (gemv) {
      asm volatile("s_waitcnt vmcnt(19)" : "+v"(w0), "+v"(w1), "+v"(c0), "+v"(c1)::"memory");
      const float f0 = bflo(c0), f1 = bflo(c1);
      const float s0 = rbf(f0 * __builtin_amdgcn_rcpf(1.f + EXP2(-1.4426950408889634f * f0))), s1 = rbf(f1 * __builtin_amdgcn_rcpf(1.f + EXP2(-1.4426950408889634f * f1)));
      float a[8];
#pragma unroll
      for (int i = 0; i < 4; ++i) { a[2 * i] = s0 * bflo(w0[i]) + s1 * bflo(w1[i]); a[2 * i + 1] = s0 * bfhi(w0[i]) + s1 * bfhi(w1[i]); }
#pragma unroll
      for (int i = 0; i < 4; ++i) a[i] = halve<0x4E>(l & 2, a[i], a[i + 4]);
#pragma unroll
      for (int i = 0; i < 2; ++i) a[i] = halve<0x124>(l & 4, a[i], a[i + 2]);
      a[0] = halve<0x128>(l & 8, a[0], a[1]);
      a[0] = swap32_sum(swap16_sum(a[0]));
      if (l < 16) *(LAS float*)(uintptr_t)(F_R + w * 64 + ((l & 1) * 8 + (l & 2) * 2 + ((l >> 1) & 2) + (l >> 3)) * 4) = a[0];
      BAR();
      if (w == 0) {
        f32x2 s = {0.f, 0.f};
#pragma unroll
        for (int i = 0; i < 8; ++i) s += ldsf2(F_R + i * 64 + (l & 7) * 8);
        gst8_sc1(p.ws + WS_MOD + mu * 64, (l >> 3) * 12288 + (l & 7) * 8, u32x2{pk(s[0], s[1]), tag});
      }
    }
    STAMP(22)
    float xh[8][8];
    {
      if (gemv && w == 0) WAITV(12); else WAITV(11);
      asm volatile("" : "+v"(xv[0]), "+v"(xv[1]), "+v"(xv[2]), "+v"(xv[3]), "+v"(xv[4]), "+v"(xv[5]), "+v"(xv[6]), "+v"(xv[7]));
      float s1[8], s2[8];
#pragma unroll
      for (int i = 0; i < 8; ++i) {
        s1[i] = 0.f; s2[i] = 0.f;
#pragma unroll
        for (int e = 0; e < 4; ++e) { const u32 v = xv[i][e]; s1[i] = dot2(v, 0x3f803f80u, s1[i]); s2[i] = dot2(v, v, s2[i]); }
      }
#pragma unroll
      for (int i = 0; i < 4; ++i) { s1[i] = halve<0xB1>(l & 1, s1[i], s1[i + 4]); s2[i] = halve<0xB1>(l & 1, s2[i], s2[i + 4]); }
#pragma unroll
      for (int i = 0; i < 2; ++i) { s1[i] = halve<0x4E>(l & 2, s1[i], s1[i + 2]); s2[i] = halve<0x4E>(l & 2, s2[i], s2[i + 2]); }
      float t1 = halve<0x124>(l & 4, s1[0], s1[1]), t2 = halve<0x124>(l & 4, s2[0], s2[1]);
      t1 += dpp<0x128>(t1); t2 += dpp<0x128>(t2);
      t1 = swap32_sum(swap16_sum(t1)); t2 = swap32_sum(swap16_sum(t2));
      const float m = t1 * (1.f / DM), r = __builtin_amdgcn_rsqf(fmaxf(t2 * (1.f / DM) - m * m, 0.f) + EPS), nm = -m * r;
#define LNROW(I, LN) { const float ri = dpp<0x150 + (LN)>(r), ni = dpp<0x150 + (LN)>(nm); _Pragma("unroll") for (int e = 0; e < 4; ++e) { xh[I][2 * e] = fmaf(bflo(xv[I][e]), ri, ni); xh[I][2 * e + 1] = fmaf(bfhi(xv[I][e]), ri, ni); } }
      LNROW(0, 0) LNROW(1, 4) LNROW(2, 2) LNROW(3, 6) LNROW(4, 1) LNROW(5, 5) LNROW(6, 3) LNROW(7, 7)
#undef LNROW
    }
    STAMP(23)
    WAITV_BAR(0);
    u32x4 wqf[16], wkvf[16];
    const u32 rawr = (((nh * 2 + (pp >> 1)) ^ (((q >> 1) & 1) << 2)) * 16) + (pp & 1) * 8;
    {
      const u32 a0 = F_A + (256 * kh + 8 * h + q) * 128 + (rawr ^ (nt * 64));
#pragma unroll
      for (int s = 0; s < 16; ++s) wqf[s] = ldstr2(a0 + s * 2048, a0 + s * 2048 + 512);
    }
    if (j == 0) {
      const u32 a0 = F_B + nh * (16384 + 128) + (256 * kh + 8 * h + q) * 32 + pp * 8;
#pragma unroll
      for (int s = 0; s < 16; ++s) wkvf[s] = ldstr2(a0 + s * 512, a0 + s * 512 + 128);
    }
    STAMP(24)
    if (w == 0) {
      u32x4 a0, a1, b0, b1;
#pragma unroll 1
      for (int it = 0; it < SPIN_CAP; ++it) {
        asm volatile("global_load_dwordx4 %0, %4, %5 sc1\n\tglobal_load_dwordx4 %1, %4, %5 offset:16 sc1\n\tglobal_load_dwordx4 %2, %4, %5 offset:2048 sc1\n\t"
                     "global_load_dwordx4 %3, %4, %5 offset:2064 sc1\n\ts_waitcnt vmcnt(0)" : "=&v"(b0), "=&v"(b1), "=&v"(a0), "=&v"(a1) : "v"(l * 32), "s"(modr) : "memory");
        if (__all(a0[1] == tag && a0[3] == tag && a1[1] == tag && a1[3] == tag && b0[1] == tag && b0[3] == tag && b1[1] == tag && b1[3] == tag)) break;
        __builtin_amdgcn_s_sleep(1);
      }
      sts128(F_M + vo, u32x4{a0[0], a0[2], a1[0], a1[2]}); sts128(F_M + 1024 + vo, u32x4{b0[0], b0[2], b1[0], b1[2]});
    } else if (w == 1) {
      if (j != 0) {
        u32x4 a0, b0;
#pragma unroll 1
        for (int it = 0; it < SPIN_CAP; ++it) {
          asm volatile("global_load_dwordx4 %0, %2, %3 sc1\n\tglobal_load_dwordx4 %1, %2, %3 offset:2048 sc1\n\ts_waitcnt vmcnt(0)" : "=&v"(b0), "=&v"(a0) : "v"(vo), "s"(modr + 6144 + ukh * 1024) : "memory");
          if (__all(a0[1] == tag && a0[3] == tag && b0[1] == tag && b0[3] == tag)) break;
          __builtin_amdgcn_s_sleep(1);
        }
        stsf4(F_M + 2048 + vo, f32x4{rbf(1.f + bflo(a0[0])), rbf(1.f + bfhi(a0[0])), rbf(1.f + bflo(a0[2])), rbf(1.f + bfhi(a0[2]))});
        stsf4(F_M + 3072 + vo, f32x4{bflo(b0[0]), bfhi(b0[0]), bflo(b0[2]), bfhi(b0[2])});
      }
    } else if (w == 2) {
#pragma unroll 1
      for (int it = 0; it < SPIN_CAP; ++it) {
        const u32 v = __hip_atomic_load((const GAS u32*)(modr + 4096 + j * 256 + h * 6144 + (l & 31) * 8 + 4), RLX_AGENT);
        if (__all(v == tag)) break;
        __builtin_amdgcn_s_sleep(1);
      }
    }
    BAR();
    STAMP(25)
    {
      const u32x4 ca = lds128(F_M + vo), sa = lds128(F_M + 1024 + vo);
      float A[8], B[8];
#pragma unroll
      for (int e = 0; e < 4; ++e) { A[2 * e] = rbf(1.f + bflo(ca[e])); A[2 * e + 1] = rbf(1.f + bfhi(ca[e])); B[2 * e] = bflo(sa[e]); B[2 * e + 1] = bfhi(sa[e]); }
#pragma unroll
      for (int i = 0; i < 8; ++i) {
        u32x4 o;
#pragma unroll
        for (int e = 0; e < 4; ++e) o[e] = pk(fmaf(xh[i][2 * e], A[2 * e], B[2 * e]), fmaf(xh[i][2 * e + 1], A[2 * e + 1], B[2 * e + 1]));
        const int tk = 8 * w + i;
        sts128(F_A + ((tk >> 5) * 32 + (l >> 1)) * 1024 + (l & 1) * 512 + (((tk & 31) ^ (l & 7)) * 16), o);
      }
    }
    BAR();
    STAMP(26)
    u32x4 xf[16];
#pragma unroll
    for (int s = 0; s < 16; ++s) xf[s] = lds128(F_A + (tt * 32 + 16 * kh + s) * 1024 + h * 512 + (((l & 31) ^ ((2 * s + h) & 7)) * 16));
    const u32 xs = F_X + w * 4096 + vo, xr = F_X + (w ^ 4) * 4096 + vo;
    if (j == 0) {
      f32x16 acc = {};
      if (nt == 0) {
#pragma unroll
        for (int s = 0; s < 16; ++s) acc = mfma(wkvf[s], xf[s], acc);
      } else {
#pragma unroll
        for (int s = 0; s < 16; ++s) acc = mfma(xf[s], wkvf[s], acc);
      }
      float keep[8];
      if (kh == 0) { stsf4(xs, f32x4{acc[8], acc[9], acc[10], acc[11]}); stsf4(xs + 1024, f32x4{acc[12], acc[13], acc[14], acc[15]});
#pragma unroll
        for (int e = 0; e < 8; ++e) keep[e] = acc[e];
      } else { stsf4(xs, f32x4{acc[0], acc[1], acc[2], acc[3]}); stsf4(xs + 1024, f32x4{acc[4], acc[5], acc[6], acc[7]});
#pragma unroll
        for (int e = 0; e < 8; ++e) keep[e] = acc[8 + e];
      }
      BAR();
      const f32x4 r0 = ldsf4(xr), r1 = ldsf4(xr + 1024);
      u32x4 o = {pk(keep[0] + r0[0], keep[1] + r0[1]), pk(keep[2] + r0[2], keep[3] + r0[3]), pk(keep[4] + r1[0], keep[5] + r1[1]), pk(keep[6] + r1[2], keep[7] + r1[3])};
      char* img = p.ws + WS_KV + (size_t)(2 * g + tt) * TS;
      if (nt == 0) { if (kh == 1) gst16_sc1(img, vo, o); }
      else if ((l & 31) <= 17) {
        if ((l & 31) == 16) o = u32x4{0x3f803f80u, 0x3f803f80u, 0x3f803f80u, 0x3f803f80u};
        if ((l & 31) == 17) o = u32x4{0u, 0u, 0u, 0u};
        gst16_sc1(img + 1024 + kh * 576, h * 288 + (l & 31) * 16, o);
      }
    }
    f32x16 acc = {};
#pragma unroll
    for (int s = 0; s < 16; ++s) acc = mfma(wqf[s], xf[s], acc);
    if (j == 0) {
      WAITV_BAR(0);
      if (w == 0 && l < 32) put_flag(p.ws + WS_FKV + l * 128 + g * 4, tag);
    }
    stsf4(xs, f32x4{acc[0], acc[1], acc[2], acc[3]}); stsf4(xs + 1024, f32x4{acc[4], acc[5], acc[6], acc[7]});
    stsf4(xs + 2048, f32x4{acc[8], acc[9], acc[10], acc[11]}); stsf4(xs + 3072, f32x4{acc[12], acc[13], acc[14], acc[15]});
    STAMP(27)
    if (w < 2) {
      const u32 a0 = F_D + 8192 + (4 * h + q) * 128 + (rawr ^ (w * 64));
      gst16_sc1(p.ws + WS_WO + ((size_t)(2 * j + w) * 32 + g) * 1024, vo, ldstr2(a0, a0 + 1024));
    }
    if (j != 0) {
      {
        const u32 a0 = F_D + (16 * kq + 4 * h + q) * 128 + (rawr ^ (tt * 64));
        gst16_sc1(p.ws + WS_WD + ((size_t)(2 * (uid & 7) + tt) * 112 + 4 * (uid >> 3) + kq) * 1024, vo, ldstr2(a0, a0 + 1024));
      }
      float us = 0.f, wsum = 0.f;
#pragma unroll
      for (int i = 0; i < 2; ++i) {
        const int s = 2 * w + i;
        const u32 a0 = F_B + (pp >> 1) * (8192 + 128) + (16 * s + 4 * h + q) * 32 + nh * 16 + (pp & 1) * 8;
        const u32x4 f = ldstr2(a0, a0 + 256);
        const f32x4 c0 = ldsf4(F_M + 2048 + (16 * s + 4 * h) * 4), c1 = ldsf4(F_M + 2048 + (16 * s + 8 + 4 * h) * 4);
        const f32x4 s0 = ldsf4(F_M + 3072 + (16 * s + 4 * h) * 4), s1 = ldsf4(F_M + 3072 + (16 * s + 8 + 4 * h) * 4);
        u32x4 o;
        o[0] = pk(bflo(f[0]) * c0[0], bfhi(f[0]) * c0[1]); o[1] = pk(bflo(f[1]) * c0[2], bfhi(f[1]) * c0[3]);
        o[2] = pk(bflo(f[2]) * c1[0], bfhi(f[2]) * c1[1]); o[3] = pk(bflo(f[3]) * c1[2], bfhi(f[3]) * c1[3]);
#pragma unroll
        for (int e = 0; e < 4; ++e) { const u32 v = o[e]; us = dot2(v, 0x3f803f80u, us); }
        wsum += bflo(f[0]) * s0[0] + bfhi(f[0]) * s0[1] + bflo(f[1]) * s0[2] + bfhi(f[1]) * s0[3] + bflo(f[2]) * s1[0] + bfhi(f[2]) * s1[1] + bflo(f[3]) * s1[2] + bfhi(f[3]) * s1[3];
        gst16_sc1(p.ws + WS_WGU + ((size_t)uT * 32 + 16 * ukh + s) * 1024, vo, o);
      }
      us = swap32_sum(us); wsum = swap32_sum(wsum);
      if (l < 32) *(LAS f32x2*)(uintptr_t)(F_R + w * 256 + l * 8) = f32x2{us, wsum};
    }
    BAR();
    {
      const f32x4 r0 = ldsf4(xr), r1 = ldsf4(xr + 1024), r2 = ldsf4(xr + 2048), r3 = ldsf4(xr + 3072);
      q0 = u32x4{pk((acc[0] + r0[0]) * C2, (acc[1] + r0[1]) * C2), pk((acc[2] + r0[2]) * C2, (acc[3] + r0[3]) * C2), pk((acc[4] + r1[0]) * C2, (acc[5] + r1[1]) * C2), pk((acc[6] + r1[2]) * C2, (acc[7] + r1[3]) * C2)};
      q1 = u32x4{pk((acc[8] + r2[0]) * C2, (acc[9] + r2[1]) * C2), pk((acc[10] + r2[2]) * C2, (acc[11] + r2[3]) * C2), pk((acc[12] + r3[0]) * C2, (acc[13] + r3[1]) * C2), pk((acc[14] + r3[2]) * C2, (acc[15] + r3[3]) * C2)};
    }
    if (w == 7 && j != 0) {
      f32x2 s = {0.f, 0.f};
#pragma unroll
      for (int i = 0; i < 8; ++i) s += ldsf2(F_R + i * 256 + (l & 31) * 8);
      const float uh = rbf(s[0]), wh = rbf(s[1]);
      u32x4 o = {pk(uh, s[0] - uh), pk(uh, wh), pk(s[1] - wh, wh), 0u};
      if (l >= 32) o = u32x4{0u, 0u, 0u, 0u};
      gst16_sc1(p.ws + WS_AUG + (size_t)(uT * 2 + ukh) * 1024, vo, o);
    }
    STAMP(28)
    if (w == 2) poll_flags<32>(p.ws + WS_FKV + (blockIdx.x & 31) * 128, tag, l);
    BAR();
    STAMP(29)
  }
  const size_t fo = ((size_t)(2 * g + tt) * 32 + 4 * j + 2 * nt) * 1024;
#pragma unroll
  for (int i = 0; i < 17; ++i) { const int pc = (w & 1) * 68 + (w >> 1) + 4 * i; set_m0(pc * 1024); dma1<0>(p.ws + WS_KV + pc * 1024, vo); }
  WAITV_BAR(13);
  if (w == 0 && l < 8) put_flag(p.ws + WS_FP + l * 1024 + blockIdx.x * 4, tag);
  STAMP(1)
  f32x16 nm0, nm1;
  {
    const u32x4 k0 = lds128(vo);
    const float m0 = rowmax16(mfma(k0, q0, f32x16{})), m1 = rowmax16(mfma(k0, q1, f32x16{}));
#pragma unroll
    for (int r = 0; r < 16; ++r) { nm0[r] = -m0; nm1[r] = -m1; }
    PIN(nm0); PIN(nm1);
  }
  const u32 vrow = 1024 + h * 288 + min(l & 31, 17) * 16;
  u32 ka = kh * 32 * TS + vo, va = kh * 32 * TS + vrow;
  f32x16 o0 = {}, o1 = {};
  u32x4 kA = lds128(ka), kB = lds128(ka + TS);
  f32x16 s0 = mfma(kA, q0, nm0), s1;
  u32x4 vA0 = lds128(va), vA1 = lds128(va + 576), vB0 = {}, vB1 = {};
  u32x4 pa0, pa1, pb0 = {}, pb1 = {};
  u32x4 wf[16];
#define HALF(SC, N0, N1, MF, OP, VP0, P0, VP1, P1, LD1, LD2)                                              \
  {                                                                                                       \
    MF; LD1; SB();                                                                                        \
    float e0 = EXP2(SC[0]), e1 = EXP2(SC[1]), e2 = EXP2(SC[2]), e3 = EXP2(SC[3]), e4 = EXP2(SC[4]), e5 = EXP2(SC[5]), e6 = EXP2(SC[6]), e7 = EXP2(SC[7]); SB(); \
    OP = mfma(VP0, P0, OP); SB();                                                                         \
    float e8 = EXP2(SC[8]), e9 = EXP2(SC[9]), e10 = EXP2(SC[10]), e11 = EXP2(SC[11]), e12 = EXP2(SC[12]), e13 = EXP2(SC[13]), e14 = EXP2(SC[14]), e15 = EXP2(SC[15]); SB(); \
    OP = mfma(VP1, P1, OP); LD2; SB();                                                                    \
    N0[0] = pk(e0, e1); N0[1] = pk(e2, e3); N0[2] = pk(e4, e5); N0[3] = pk(e6, e7); N1[0] = pk(e8, e9);   \
    N1[1] = pk(e10, e11); N1[2] = pk(e12, e13); N1[3] = pk(e14, e15); SB();                               \
  }
#define TILE(T, KC, KN, VC0, VC1, VP0, VP1)                                                               \
    HALF(s0, pa0, pa1, s1 = mfma(KC, q1, nm1), o1, VP0, pb0, VP1, pb1, KC = lds128(ka + ((T) + 2) * TS),  \
         (VP0 = lds128(va + ((T) + 1) * TS), VP1 = lds128(va + ((T) + 1) * TS + 576)))                    \
    HALF(s1, pb0, pb1, s0 = mfma(KN, q0, nm0), o0, VC0, pa0, VC1, pa1, , )
  const char* wgu = p.ws + WS_WGU + (size_t)id.c * 7 * 32768;
  const char* wob = p.ws + WS_WO + ((size_t)(2 * j) * 32 + 8 * kq) * 1024;
#define LDWF(SEL)                                                                                                     \
  asm volatile("s_cmp_lg_u32 %21, 0\n\ts_cbranch_scc1 .Lwf%=\n\t"                                                     \
               "global_load_dwordx4 %0, %16, %17 sc1\n\tglobal_load_dwordx4 %1, %16, %17 offset:1024 sc1\n\tglobal_load_dwordx4 %2, %16, %17 offset:2048 sc1\n\tglobal_load_dwordx4 %3, %16, %17 offset:3072 sc1\n\t"     \
               "global_load_dwordx4 %4, %16, %18 sc1\n\tglobal_load_dwordx4 %5, %16, %18 offset:1024 sc1\n\tglobal_load_dwordx4 %6, %16, %18 offset:2048 sc1\n\tglobal_load_dwordx4 %7, %16, %18 offset:3072 sc1\n\t"     \
               "global_load_dwordx4 %8, %16, %19 sc1\n\tglobal_load_dwordx4 %9, %16, %19 offset:1024 sc1\n\tglobal_load_dwordx4 %10, %16, %19 offset:2048 sc1\n\tglobal_load_dwordx4 %11, %16, %19 offset:3072 sc1\n\t"   \
               "global_load_dwordx4 %12, %16, %20 sc1\n\tglobal_load_dwordx4 %13, %16, %20 offset:1024 sc1\n\tglobal_load_dwordx4 %14, %16, %20 offset:2048 sc1\n\tglobal_load_dwordx4 %15, %16, %20 offset:3072 sc1\n"   \
               ".Lwf%=:"                                                                                              \
               : "+v"(wf[0]), "+v"(wf[1]), "+v"(wf[2]), "+v"(wf[3]), "+v"(wf[4]), "+v"(wf[5]), "+v"(wf[6]), "+v"(wf[7]), "+v"(wf[8]), "+v"(wf[9]), "+v"(wf[10]), \
                 "+v"(wf[11]), "+v"(wf[12]), "+v"(wf[13]), "+v"(wf[14]), "+v"(wf[15])                                  \
               : "v"(vo), "s"(wob), "s"(wob + 4096), "s"(wob + 32768), "s"(wob + 36864), "s"(SEL) : "memory", "scc")
#pragma unroll
  for (int s4 = 0; s4 < 16; ++s4) asm volatile("" : "=v"(wf[s4]));
  const int qt = blockIdx.x >> 6;
#pragma unroll 1
  for (int seg = 0; seg < 5; ++seg) {
    if (seg == 1) {
      if (w == 7) {
        u32x4 f;
#pragma unroll 1
        for (int it = 0; it < SPIN_CAP; ++it) {
          asm volatile("global_load_dwordx4 %0, %1, %2 sc1\n\ts_waitcnt vmcnt(0)" : "=&v"(f) : "v"(vo), "s"(p.ws + WS_FP + (blockIdx.x & 7) * 1024) : "memory");
          if (__all(f[0] == tag && f[1] == tag && f[2] == tag && f[3] == tag)) break;
          __builtin_amdgcn_s_sleep(1);
        }
        if (blockIdx.x == 0 && l == 0) put_flag(p.ws + WS_EPOCH, epoch + 1u);
      }
      WAITV_BAR(0);
      set_m0(JUNK); touch8k(w < 7 ? wgu + qt * 57344 + w * 8192 : p.ws + WS_WO + (size_t)j * 65536 + qt * 16384, l * 128);
    } else if (seg == 2) {
      set_m0(JUNK); touch8k(w < 7 ? p.ws + WS_WD + (size_t)(2 * j) * 114688 + qt * 57344 + w * 8192 : p.ws + WS_WO + (size_t)j * 65536 + qt * 16384 + 8192, l * 128);
    } else if (seg == 3) {
      BAR();
      dma4s(wgu + w * 4096, vo, SL0 + w * 4096);
    } else if (seg == 4) {
      dma4s(wgu + 32768 + w * 4096, vo, SL1 + w * 4096);
      if (w == 7) { set_m0(JUNK); touch8k(p.ws + WS_AUG + (size_t)id.c * 14336 + (qt & 1) * 6144, l * 128); }
    }
    LDWF((seg ^ 3) | (kh ^ 1));
    const int n = (seg == 0 || seg == 2) ? 1 : 2;
#pragma unroll 1
    for (int t = 0; t < n; ++t) {
      TILE(0, kA, kB, vA0, vA1, vB0, vB1) TILE(1, kB, kA, vB0, vB1, vA0, vA1) TILE(2, kA, kB, vA0, vA1, vB0, vB1) TILE(3, kB, kA, vB0, vB1, vA0, vA1)
      ka += 4 * TS; va += 4 * TS; PIN(ka); PIN(va);
    }
  }
  o1 = mfma(vB0, pb0, o1); o1 = mfma(vB1, pb1, o1);
  LDWF(kh);
#undef HALF
#undef TILE
  STAMP(2)
  const u32 xo = (u32)((l & 31) * DM + 4 * h) * 2;
  const char* xres = (const char*)(p.x + (size_t)(64 * g + 32 * tt) * DM + 64 * j + 32 * nt + 16 * kh);
  u32x2 xa = gld8_s<0>(xres, xo), xb = gld8_s<16>(xres, xo);
  u32x4 ga = gld_s<1, 0>(modr + 4096 + (64 * j + 32 * nt + 16 * kh) * 4, h * 16), gb = gld_s<1, 32>(modr + 4096 + (64 * j + 32 * nt + 16 * kh) * 4, h * 16);
  {
    const u32 xs = OEX + w * 2304, xr = OEX + (w ^ 4) * 2304;
    float o[9];
    if (kh) { stsf4(xs + vo, f32x4{o0[0], o0[1], o0[2], o0[3]}); stsf4(xs + 1024 + vo, f32x4{o0[4], o0[5], o0[6], o0[7]}); *(LAS float*)(uintptr_t)(xs + 2048 + l * 4) = o0[8];
#pragma unroll
      for (int e = 0; e < 9; ++e) o[e] = o1[e];
    } else { stsf4(xs + vo, f32x4{o1[0], o1[1], o1[2], o1[3]}); stsf4(xs + 1024 + vo, f32x4{o1[4], o1[5], o1[6], o1[7]}); *(LAS float*)(uintptr_t)(xs + 2048 + l * 4) = o1[8];
#pragma unroll
      for (int e = 0; e < 9; ++e) o[e] = o0[e];
    }
    BAR();
    const f32x4 r0 = ldsf4(xr + vo), r1 = ldsf4(xr + 1024 + vo);
#pragma unroll
    for (int e = 0; e < 4; ++e) { o[e] += r0[e]; o[4 + e] += r1[e]; }
    float ls = lowhalf(o[8] + *(const LAS float*)(uintptr_t)(xr + 2048 + l * 4));
    bool bad = !(ls < 1.2676506e30f);
#pragma unroll
    for (int e = 0; e < 8; ++e) bad |= !(fabsf(o[e]) < 3.0e38f);
    if (__builtin_expect(__any(bad), 0)) {
      const u32x4 qf = kh ? q1 : q0;
      float m = -INFINITY;
      f32x16 oo = {};
#pragma unroll 1
      for (int t = 0; t < 64; ++t) {
        u32x4 kf, v0, v1;
        asm volatile("global_load_dwordx4 %0, %3, %5 sc1\n\tglobal_load_dwordx4 %1, %4, %5 sc1\n\tglobal_load_dwordx4 %2, %4, %5 offset:576 sc1\n\ts_waitcnt vmcnt(0)"
                     : "=&v"(kf), "=&v"(v0), "=&v"(v1) : "v"(vo), "v"(vrow), "s"(p.ws + WS_KV + t * TS) : "memory");
        const f32x16 s = mfma(kf, qf, f32x16{});
        const float mn = fmaxf(m, rowmax16(s)), al = EXP2(m - mn);
        m = mn;
        u32x4 pa, pb;
#pragma unroll
        for (int r = 0; r < 4; ++r) { pa[r] = pk(EXP2(s[2 * r] - mn), EXP2(s[2 * r + 1] - mn)); pb[r] = pk(EXP2(s[8 + 2 * r] - mn), EXP2(s[9 + 2 * r] - mn)); }
#pragma unroll
        for (int r = 0; r < 16; ++r) oo[r] *= al;
        oo = mfma(v0, pa, oo); oo = mfma(v1, pb, oo);
      }
#pragma unroll
      for (int e = 0; e < 8; ++e) o[e] = oo[e];
      ls = lowhalf(oo[8]);
    }
    const float rl = __builtin_amdgcn_rcpf(ls);
    gst16_sc1(p.ws + WS_O + fo + kh * 1024, vo, u32x4{pk(o[0] * rl, o[1] * rl), pk(o[2] * rl, o[3] * rl), pk(o[4] * rl, o[5] * rl), pk(o[6] * rl, o[7] * rl)});
  }
  asm volatile("s_waitcnt vmcnt(0)\n\ts_barrier"
               : "+v"(wf[0]), "+v"(wf[1]), "+v"(wf[2]), "+v"(wf[3]), "+v"(wf[4]), "+v"(wf[5]), "+v"(wf[6]), "+v"(wf[7]), "+v"(wf[8]), "+v"(wf[9]),
                 "+v"(wf[10]), "+v"(wf[11]), "+v"(wf[12]), "+v"(wf[13]), "+v"(wf[14]), "+v"(wf[15]), "+v"(xa), "+v"(xb), "+v"(ga), "+v"(gb)::"memory");
  STAMP(3)
  if (w == 0) {
    if (l == 0) put_flag(p.ws + WS_FO + g * 256 + j * 4, tag);
    poll_flags<8>(p.ws + WS_FO + g * 256, tag, l);
    set_m0(JUNK); touch8k(p.ws + WS_O + (size_t)g * 65536 + j * 8192, l * 128);
  }
  BAR();
  STAMP(4)
  u32x4 x1;
  {
    const char* osrc = p.ws + WS_O + ((size_t)(2 * g + tt) * 32 + 8 * kq) * 1024;
    u32x4 xf[8];
    xf[0] = gld_s<1, 0>(osrc, vo); xf[1] = gld_s<1, 1024>(osrc, vo); xf[2] = gld_s<1, 2048>(osrc, vo); xf[3] = gld_s<1, 3072>(osrc, vo);
    xf[4] = gld_s<1, 0>(osrc + 4096, vo); xf[5] = gld_s<1, 1024>(osrc + 4096, vo); xf[6] = gld_s<1, 2048>(osrc + 4096, vo); xf[7] = gld_s<1, 3072>(osrc + 4096, vo);
    f32x16 acc0 = {}, acc1 = {};
#define P3_STEP(S) { waitv_pin<7 - (S)>(xf[S]); acc0 = mfma(wf[S], xf[S], acc0); acc1 = mfma(wf[8 + (S)], xf[S], acc1); }
    P3_STEP(0) P3_STEP(1) P3_STEP(2) P3_STEP(3) P3_STEP(4) P3_STEP(5) P3_STEP(6) P3_STEP(7)
#undef P3_STEP
    STAMP(5)
    float r[8];
    reduce4(acc0, acc1, tt, kq, SL3, vo, r);
    STAMP(6)
    x1 = u32x4{pk(fmaf(bflo(ga[0]), r[0], bflo(xa[0])), fmaf(bfhi(ga[0]), r[1], bfhi(xa[0]))), pk(fmaf(bflo(ga[2]), r[2], bflo(xa[1])), fmaf(bfhi(ga[2]), r[3], bfhi(xa[1]))),
               pk(fmaf(bflo(gb[0]), r[4], bflo(xb[0])), fmaf(bfhi(gb[0]), r[5], bfhi(xb[0]))), pk(fmaf(bflo(gb[2]), r[6], bflo(xb[1])), fmaf(bfhi(gb[2]), r[7], bfhi(xb[1])))};
    gst16_sc1(p.ws + WS_X1 + fo + kh * 1024, vo, x1);
    if (w != 0) dma4s(wgu + 2 * 32768 + w * 4096, vo, SL2 + w * 4096);
    float s1 = 0.f, s2 = 0.f;
#pragma unroll
    for (int e = 0; e < 4; ++e) { const u32 v = x1[e]; s1 = dot2(v, 0x3f803f80u, s1); s2 = dot2(v, v, s2); }
    s1 = swap32_sum(s1); s2 = swap32_sum(s2);
    if (l < 32) *(LAS f32x2*)(uintptr_t)(GAPA + kq * 512 + tt * 256 + l * 8) = f32x2{s1, s2};
    BAR();
    STAMP(7)
    if (w == 0) {
      f32x2 s = {0.f, 0.f};
#pragma unroll
      for (int i = 0; i < 4; ++i) s += ldsf2(GAPA + i * 512 + l * 8);
      gst8_sc1(p.ws + WS_STAT + ((size_t)64 * g * 8 + j) * 8, l * 64, u32x2{__float_as_uint(s[0]), __float_as_uint(s[1])});
      dma4s(wgu + 2 * 32768, vo, SL2);
    }
    dma4s(wgu + 3 * 32768 + w * 4096, vo, SL3 + w * 4096);
    WAITV_BAR(8);
    STAMP(8)
    if (w == 0 && l == 0) put_flag(p.ws + WS_FX + (g >> 1) * 256 + ((g & 1) * 8 + j) * 4, tag);
  }
  u32x4 wr[9][2];
  if (kh == 0) p4_body<0>(p, id, l, w, tag, wr STAMP_PASS); else p4_body<1>(p, id, l, w, tag, wr STAMP_PASS);
  {
    int ly = l; PIN(ly);
    u32x4 g0 = gld_s<1, 0>(modr + 10240 + (64 * j + 32 * nt + 16 * kh) * 4, (ly >> 5) * 16), g1 = gld_s<1, 32>(modr + 10240 + (64 * j + 32 * nt + 16 * kh) * 4, (ly >> 5) * 16);
    if (w == 0) {
      poll_flags<16>(p.ws + WS_FA + (g >> 1) * 256, tag, l);
      const char* t = p.ws + WS_ACT + (size_t)g * 229376 + j * 28672;
      set_m0(JUNK); touch8k(t, ly * 128); touch8k(t + 8192, ly * 128); touch8k(t + 16384, ly * 128); touch8k(t + 20480, ly * 128);
      WAITV(4);
    } else {
      WAITV(0);
    }
    asm volatile("s_waitcnt lgkmcnt(0)\n\ts_barrier"
                 : "+v"(wr[0][0]), "+v"(wr[0][1]), "+v"(wr[1][0]), "+v"(wr[1][1]), "+v"(wr[2][0]), "+v"(wr[2][1]), "+v"(wr[3][0]), "+v"(wr[3][1]), "+v"(wr[4][0]),
                   "+v"(wr[4][1]), "+v"(wr[5][0]), "+v"(wr[5][1]), "+v"(wr[6][0]), "+v"(wr[6][1]), "+v"(wr[7][0]), "+v"(wr[7][1]), "+v"(wr[8][0]), "+v"(wr[8][1]), "+v"(g0), "+v"(g1)::"memory");
    STAMP(19)
    const char* xsrc = p.ws + WS_ACT + ((size_t)(2 * g + tt) * 112 + 28 * kq) * 1024;
    constexpr int XW = 20;
    u32x4 xf[XW];
#pragma unroll
    for (int s4 = 0; s4 < XW / 4; ++s4) {
      xf[4 * s4] = gld_s<1, 0>(xsrc + s4 * 4096, vo); xf[4 * s4 + 1] = gld_s<1, 1024>(xsrc + s4 * 4096, vo);
      xf[4 * s4 + 2] = gld_s<1, 2048>(xsrc + s4 * 4096, vo); xf[4 * s4 + 3] = gld_s<1, 3072>(xsrc + s4 * 4096, vo);
    }
    f32x16 acc0 = {}, acc1 = {};
    u32 blo = kq * 8192 + vo, bmid = blo + 65536, bhi = TOPA + kq * 6144 + vo;
    PIN(blo); PIN(bmid); PIN(bhi);
#define K5_W(S, NT) ((S) < 16 ? ldsr<((S) < 4 ? SL3 : (S) < 8 ? SL0 : (S) < 12 ? SL1 : SL2) + ((S) & 3) * 1024 + (NT) * 4096>(blo, bmid) : lds128(bhi + ((S) & 3) * 1024 + (NT) * 3072))
#define K5_STEP(S)                                                                                   \
  {                                                                                                  \
    const u32x4 a0 = (S) < 19 ? K5_W(S, 0) : wr[(S) < 19 ? 0 : (S) - 19][0];                          \
    const u32x4 a1 = (S) < 19 ? K5_W(S, 1) : wr[(S) < 19 ? 0 : (S) - 19][1];                          \
    waitv_pin<((S) < 28 - XW ? XW - 1 : 27 - (S))>(xf[(S) % XW]);                                     \
    acc0 = mfma(a0, xf[(S) % XW], acc0);                                                             \
    acc1 = mfma(a1, xf[(S) % XW], acc1);                                                             \
    if ((S) + XW < 28) xf[(S) % XW] = gld_s<1, 0>(xsrc + ((S) + XW) * 1024, vo);                      \
  }
    K5_STEP(0) K5_STEP(1) K5_STEP(2) K5_STEP(3) K5_STEP(4) K5_STEP(5) K5_STEP(6) K5_STEP(7) K5_STEP(8) K5_STEP(9)
    K5_STEP(10) K5_STEP(11) K5_STEP(12) K5_STEP(13) K5_STEP(14) K5_STEP(15) K5_STEP(16) K5_STEP(17) K5_STEP(18)
    BAR();
    K5_STEP(19) K5_STEP(20) K5_STEP(21) K5_STEP(22) K5_STEP(23) K5_STEP(24) K5_STEP(25) K5_STEP(26) K5_STEP(27)
#undef K5_STEP
#undef K5_W
    STAMP(20)
    float r[8];
    reduce4(acc0, acc1, tt, kq, 0, vo, r);
    STAMP(21)
    const u32 a0 = pk(fmaf(bflo(g0[0]), r[0], bflo(x1[0])), fmaf(bfhi(g0[0]), r[1], bfhi(x1[0]))), a1 = pk(fmaf(bflo(g0[2]), r[2], bflo(x1[1])), fmaf(bfhi(g0[2]), r[3], bfhi(x1[1])));
    const u32 b0 = pk(fmaf(bflo(g1[0]), r[4], bflo(x1[2])), fmaf(bfhi(g1[0]), r[5], bfhi(x1[2]))), b1 = pk(fmaf(bflo(g1[2]), r[6], bflo(x1[3])), fmaf(bfhi(g1[2]), r[7], bfhi(x1[3])));
    const auto s0 = __builtin_amdgcn_permlane32_swap(a0, b0, false, false);
    const auto s1 = __builtin_amdgcn_permlane32_swap(a1, b1, false, false);
    int lz = l; PIN(lz);
    *(u32x4*)(p.out + (size_t)(64 * g + 32 * tt + (lz & 31)) * DM + 64 * j + 32 * nt + 16 * kh + 8 * (lz >> 5)) = u32x4{s0[0], s1[0], s0[1], s1[1]};
  }
  STAMP_FLUSH(0)
}

extern "C" void kernel_launch(void* const* d_in, const int* in_sizes, int n_in, void* d_out, int out_size, void* d_ws, size_t ws_size, hipStream_t stream) {
  Params p;
  p.x = (const u16*)d_in[0]; p.wq = (const u16*)d_in[1]; p.wk = (const u16*)d_in[2]; p.wv = (const u16*)d_in[3]; p.wo = (const u16*)d_in[4];
  p.wg = (const u16*)d_in[6]; p.wu = (const u16*)d_in[7]; p.wd = (const u16*)d_in[8]; p.c = (const u16*)d_in[9]; p.wm = (const u16*)d_in[10];
  p.out = (u16*)d_out; p.ws = (char*)d_ws;
  (void)hipFuncSetAttribute((const void*)k_main, hipFuncAttributeMaxDynamicSharedMemorySize, M_LDS);
  k_main<<<256, 512, M_LDS, stream>>>(p);
}
```

```cpp
#include <hip/hip_runtime.h>
#include <stdint.h>

typedef unsigned short u16;
typedef unsigned int u32;
typedef __attribute__((ext_vector_type(8))) __bf16 bf16x8;
typedef __attribute__((ext_vector_type(2))) __bf16 bf16x2;
typedef __attribute__((ext_vector_type(2))) float f32x2;
typedef __attribute__((ext_vector_type(4))) float f32x4;
typedef __attribute__((ext_vector_type(16))) float f32x16;
typedef __attribute__((ext_vector_type(4))) u32 u32x4;
typedef __attribute__((ext_vector_type(2))) u32 u32x2;
typedef __attribute__((ext_vector_type(4))) short s16x4;
#define LAS __attribute__((address_space(3)))
#define GAS __attribute__((address_space(1)))
#define DI __device__ __forceinline__

constexpr int DM = 512, FF = 1792;
constexpr float EPS = 1e-5f;
constexpr float C2 = 0.25f * 1.4426950408889634f;

constexpr size_t WS_EPOCH = 0;
constexpr size_t WS_FO = 4096;
constexpr size_t WS_FX = WS_FO + 8192;
constexpr size_t WS_FA = WS_FX + 4096;
constexpr size_t WS_FE = WS_FA + 128;
constexpr size_t WS_FKV = WS_FA + 4096;
constexpr size_t WS_FP = WS_FKV + 4096;
constexpr size_t WS_MOD = 65536;
constexpr size_t WS_KV = WS_MOD + 8 * 12288;
constexpr size_t WS_STAT = WS_KV + 139264;
constexpr size_t WS_AUG = WS_STAT + 131072;
constexpr size_t WS_WO = 1u << 20;
constexpr size_t WS_WD = WS_WO + (512u << 10);
constexpr size_t WS_WGU = WS_WD + (1792u << 10);
constexpr size_t WS_O = WS_WGU + (3584u << 10);
constexpr size_t WS_X1 = WS_O + (2u << 20);
constexpr size_t WS_ACT = WS_X1 + (2u << 20);
static_assert(WS_AUG + 229376 <= WS_WO, "workspace map");
constexpr int TS = 2176;

struct Params {
  const u16 *x, *wq, *wk, *wv, *wo, *wg, *wu, *wd, *c, *wm;
  u16* out;
  char* ws;
};

DI f32x16 mfma(u32x4 a, u32x4 b, f32x16 c) {
  return __builtin_amdgcn_mfma_f32_32x32x16_bf16(__builtin_bit_cast(bf16x8, a), __builtin_bit_cast(bf16x8, b), c, 0, 0, 0);
}
DI u32 pk(float lo, float hi) {
  f32x2 v = {lo, hi};
  return __builtin_bit_cast(u32, __builtin_convertvector(v, bf16x2));
}
DI float bflo(u32 u) { return __uint_as_float(u << 16); }
DI float bfhi(u32 u) { return __uint_as_float(u & 0xffff0000u); }
DI float rbf(float f) { return bflo(pk(f, 0.f)); }
DI float dot2(u32 a, u32 b, float c) { return __builtin_amdgcn_fdot2_f32_bf16(__builtin_bit_cast(bf16x2, a), __builtin_bit_cast(bf16x2, b), c, false); }
DI void set_m0(u32 lds) { asm volatile("s_mov_b32 m0, %0\n\ts_nop 0" ::"s"(lds) : "memory"); }
template <int OFF> DI void dma(const void* base, u32 vo) { asm volatile("global_load_lds_dwordx4 %0, %1 offset:%2" ::"v"(vo), "s"(base), "n"(OFF) : "memory"); }
template <int OFF> DI void dma1(const void* base, u32 vo) { asm volatile("global_load_lds_dwordx4 %0, %1 offset:%2 sc1" ::"v"(vo), "s"(base), "n"(OFF) : "memory"); }
DI void dma4(const void* base, u32 vo, u32 lds) { set_m0(lds); dma<0>(base, vo); dma<1024>(base, vo); dma<2048>(base, vo); dma<3072>(base, vo); }
DI void dma4s(const void* base, u32 vo, u32 lds) { set_m0(lds); dma1<0>(base, vo); dma1<1024>(base, vo); dma1<2048>(base, vo); dma1<3072>(base, vo); }
template <int SC, int OFF> DI u32x4 gld_s(const void* base, u32 vo) {
  u32x4 r;
  if (SC) asm volatile("global_load_dwordx4 %0, %1, %2 offset:%3 sc1" : "=v"(r) : "v"(vo), "s"(base), "n"(OFF) : "memory");
  else asm volatile("global_load_dwordx4 %0, %1, %2 offset:%3" : "=v"(r) : "v"(vo), "s"(base), "n"(OFF) : "memory");
  return r;
}
template <int OFF> DI u32x2 gld8_s(const void* base, u32 vo) { u32x2 r; asm volatile("global_load_dwordx2 %0, %1, %2 offset:%3" : "=v"(r) : "v"(vo), "s"(base), "n"(OFF) : "memory"); return r; }
template <int OFF> DI u32 gld2_s(const void* base, u32 vo) { u32 r; asm volatile("global_load_ushort %0, %1, %2 offset:%3" : "=v"(r) : "v"(vo), "s"(base), "n"(OFF) : "memory"); return r; }
DI void touch8k(const char* base, u32 l128) { asm volatile("global_load_lds_dword %0, %1 sc1" ::"v"(l128), "s"(base) : "memory"); }
DI void gst16_sc1(char* base, u32 vo, u32x4 v) { asm volatile("global_store_dwordx4 %0, %1, %2 sc1\n\ts_nop 1" ::"v"(vo), "v"(v), "s"(base) : "memory"); }
DI void gst8_sc1(char* base, u32 vo, u32x2 v) { asm volatile("global_store_dwordx2 %0, %1, %2 sc1\n\ts_nop 1" ::"v"(vo), "v"(v), "s"(base) : "memory"); }
#define RLX_AGENT __ATOMIC_RELAXED, __HIP_MEMORY_SCOPE_AGENT
DI void put_flag(char* f, u32 tag) { __hip_atomic_store((GAS u32*)f, tag, RLX_AGENT); }
constexpr int SPIN_CAP = 1 << 20;
template <int N> DI void poll_flags(const char* f, u32 tag, int l) {
#pragma unroll 1
  for (int it = 0; it < SPIN_CAP; ++it) {
    const u32 v = __hip_atomic_load((const GAS u32*)f + (l < N ? l : 0), RLX_AGENT);
    if (__all(v == tag)) break;
    __builtin_amdgcn_s_sleep(1);
  }
  __builtin_amdgcn_fence(__ATOMIC_ACQUIRE, "wavefront");
}
#define SB() __builtin_amdgcn_sched_barrier(0)
#define PIN(x) asm volatile("" : "+v"(x))
#define WAITV(N) asm volatile("s_waitcnt vmcnt(" #N ")" ::: "memory")
#define WAITV_BAR(N) asm volatile("s_waitcnt vmcnt(" #N ") lgkmcnt(0)\n\ts_barrier" ::: "memory")
#define BAR() asm volatile("s_waitcnt lgkmcnt(0)\n\ts_barrier" ::: "memory")
template <int N> DI void waitv_pin(u32x4& v) { asm volatile("s_waitcnt vmcnt(%1)" : "+v"(v) : "n"(N) : "memory"); }
DI u32x4 lds128(u32 a) { return *(const LAS u32x4*)(uintptr_t)a; }
DI f32x4 ldsf4(u32 a) { return *(const LAS f32x4*)(uintptr_t)a; }
DI f32x2 ldsf2(u32 a) { return *(const LAS f32x2*)(uintptr_t)a; }
DI void sts128(u32 a, u32x4 v) { *(LAS u32x4*)(uintptr_t)a = v; }
DI void stsf4(u32 a, f32x4 v) { *(LAS f32x4*)(uintptr_t)a = v; }
DI u32x2 ldstr(u32 a) { return __builtin_bit_cast(u32x2, __builtin_amdgcn_ds_read_tr16_b64_v4i16((LAS s16x4*)(uintptr_t)a)); }
DI u32x4 ldstr2(u32 a0, u32 a1) { u32x2 p = ldstr(a0), q = ldstr(a1); return u32x4{p[0], p[1], q[0], q[1]}; }
template <int CTRL> DI float dpp(float v) { return __int_as_float(__builtin_amdgcn_mov_dpp(__float_as_int(v), CTRL, 0xf, 0xf, true)); }
DI float swap32_sum(float v) { auto r = __builtin_amdgcn_permlane32_swap(__float_as_uint(v), __float_as_uint(v), false, false); return __uint_as_float(r[0]) + __uint_as_float(r[1]); }
DI float swap32_max(float v) { auto r = __builtin_amdgcn_permlane32_swap(__float_as_uint(v), __float_as_uint(v), false, false); return fmaxf(__uint_as_float(r[0]), __uint_as_float(r[1])); }
DI float swap16_sum(float v) { auto r = __builtin_amdgcn_permlane16_swap(__float_as_uint(v), __float_as_uint(v), false, false); return __uint_as_float(r[0]) + __uint_as_float(r[1]); }
DI float lowhalf(float v) { auto r = __builtin_amdgcn_permlane32_swap(__float_as_uint(v), __float_as_uint(v), false, false); return __uint_as_float(r[0]); }
template <int CTRL> DI float halve(bool bit, float a, float b) { return (bit ? b : a) + dpp<CTRL>(bit ? a : b); }
DI int rfl(int v) { return __builtin_amdgcn_readfirstlane(v); }
#define EXP2(v) __builtin_amdgcn_exp2f(v)

struct Ids { int G, c, g, j; };
DI Ids ids() { const int b = blockIdx.x, x = b & 7, i = b >> 3; Ids r; r.G = (x >> 1) * 4 + (i >> 3); r.c = (x & 1) * 8 + (i & 7); r.g = 4 * x + (i >> 3); r.j = i & 7; return r; }

#define STAMP_DECL
#define STAMP(I)
#define STAMP_FLUSH(K)
#define STAMP_ARG
#define STAMP_PASS

#define ASTAMP(I)
constexpr u32 M_LDS = 163840;
constexpr u32 F_A = 0;
constexpr u32 F_H = 65536;
constexpr u32 F_B = 131072;
constexpr u32 F_D = F_B + 16640;
constexpr u32 F_M = F_D + 10240;
static_assert(F_M + 4608 <= M_LDS, "front-end LDS map");
constexpr u32 SL0 = 0, SL1 = 69632, SL2 = 32768, SL3 = 102400, TOPA = 135168, GAPA = 65536, OEX = 143616, JUNK = M_LDS - 256;

DI void mfa(f32x16& c, u32x4 a, u32x4 b) { asm volatile("v_mfma_f32_32x32x16_bf16 %0, %1, %2, %0" : "+v"(c) : "v"(a), "v"(b) : "memory"); }
DI void mf0(f32x16& c, u32x4 a, u32x4 b) { asm volatile("v_mfma_f32_32x32x16_bf16 %0, %1, %2, 0" : "=&v"(c) : "v"(a), "v"(b) : "memory"); }
DI float vexp(float a) { float r; asm volatile("v_exp_f32 %0, %1" : "=v"(r) : "v"(a)); return r; }
DI float vrcp(float a) { float r; asm volatile("v_rcp_f32 %0, %1" : "=v"(r) : "v"(a)); return r; }
DI u32 vcvt(float a, float b) { u32 r; asm volatile("v_cvt_pk_bf16_f32 %0, %1, %2" : "=v"(r) : "v"(a), "v"(b)); return r; }
DI float vadd(float a, float b) { float r; asm volatile("v_add_f32 %0, %1, %2" : "=v"(r) : "v"(a), "v"(b)); return r; }
DI float vmul(float a, float b) { float r; asm volatile("v_mul_f32 %0, %1, %2" : "=v"(r) : "v"(a), "v"(b)); return r; }
DI float vfma_k(float a, float k, float c) { float r; asm volatile("v_fma_f32 %0, %1, %2, %3" : "=v"(r) : "v"(a), "s"(k), "v"(c)); return r; }
DI float vadd1(float a) { float r; asm volatile("v_add_f32 %0, 1.0, %1" : "=v"(r) : "v"(a)); return r; }
#define MFMA_SETTLE() asm volatile("s_nop 15\n\ts_nop 3" ::: "memory")
DI float rowmax16(const f32x16& s) {
  float m = fmaxf(fmaxf(s[0], s[1]), s[2]);
#pragma unroll
  for (int r = 3; r < 15; r += 2) m = fmaxf(fmaxf(m, s[r]), s[r + 1]);
  return swap32_max(fmaxf(m, s[15]));
}
constexpr float GC0 = -2.f * 0.7978845608028654f * 1.4426950408889634f, GC1 = GC0 * 0.044715f;
DI u32 exbuf(int i) { return i < 7 ? TOPA + i * 4096 : GAPA; }
template <u32 A> DI u32x4 ldsr(u32 lo, u32 mid) { return A < 65536 ? lds128(lo + A) : lds128(mid + (A - 65536)); }

DI void reduce4(const f32x16& acc0, const f32x16& acc1, int tt, int kq, u32 buf, u32 vo, float (&r)[8]) {
#pragma unroll
  for (int pc = 0; pc < 4; ++pc) {
    const f32x16& a = (pc & 1) ? acc1 : acc0;
    const int o8 = (pc >> 1) * 8;
    if (pc != kq) {
      const u32 d = buf + ((tt * 4 + pc) * 3 + ((kq - pc - 1) & 3)) * 2048 + vo;
      stsf4(d, f32x4{a[o8], a[o8 + 1], a[o8 + 2], a[o8 + 3]}); stsf4(d + 1024, f32x4{a[o8 + 4], a[o8 + 5], a[o8 + 6], a[o8 + 7]});
    } else {
#pragma unroll
      for (int e = 0; e < 8; ++e) r[e] = a[o8 + e];
    }
  }
  BAR();
#pragma unroll
  for (int i = 0; i < 3; ++i) {
    const u32 d = buf + ((tt * 4 + kq) * 3 + i) * 2048 + vo;
    const f32x4 u = ldsf4(d), v = ldsf4(d + 1024);
#pragma unroll
    for (int e = 0; e < 4; ++e) { r[e] += u[e]; r[4 + e] += v[e]; }
  }
}

DI constexpr int dks(int i) { return i < 16 ? 7 * (i >> 2) + (i & 3) : 7 * ((i - 16) / 3) + 4 + (i - 16) % 3; }
template <int KH> DI void p4_body(const Params& p, const Ids& id, int l, int w, u32 tag, u32x4 (&wr)[9][2], u32& fe STAMP_ARG) {
  const int tt = w & 3;
  const u32 vo = l * 16;
  const char* wsrc = p.ws + WS_WGU + (size_t)id.c * 7 * 32768 + w * 4096;
  const char* dsrc = p.ws + WS_WD + ((size_t)(2 * id.j + (w & 1)) * 112 + 28 * (w >> 1)) * 1024;
  const char* dsrc0 = p.ws + WS_WD + ((size_t)(2 * id.j) * 112 + 28 * (w >> 1)) * 1024;
  char* adst = p.ws + WS_ACT + ((size_t)(4 * id.G + tt) * 112 + 7 * id.c) * 1024;
  const char* asrc = p.ws + WS_AUG + (size_t)(id.c * 14 + KH) * 1024;
#define WRL(I) (wr[(I) >> 1][(I) & 1] = gld_s<1, 0>(dsrc0 + (dks(19 + ((I) >> 1)) + ((I) & 1) * 112) * 1024, vo))
  u32x4 aug[7];
  aug[0] = gld_s<1, 0>(asrc, vo); aug[1] = gld_s<1, 2048>(asrc, vo); aug[2] = gld_s<1, 0>(asrc + 4096, vo); aug[3] = gld_s<1, 2048>(asrc + 4096, vo);
  WRL(0); WRL(1); WRL(2); WRL(3); WRL(4); WRL(5); WRL(6); WRL(7); WRL(8); WRL(9);
  if (w == 0) poll_flags<16>(p.ws + WS_FX + id.G * 256, tag, l);
  WAITV(10);
  asm volatile("s_barrier" : "+v"(aug[0]), "+v"(aug[1]), "+v"(aug[2]), "+v"(aug[3])::"memory");
  STAMP(9)
  const char* xsrc = p.ws + WS_X1 + ((size_t)(4 * id.G + tt) * 32 + 16 * KH) * 1024;
  u32x4 xf[16];
#pragma unroll
  for (int s4 = 0; s4 < 4; ++s4) {
    xf[4 * s4] = gld_s<1, 0>(xsrc + s4 * 4096, vo); xf[4 * s4 + 1] = gld_s<1, 1024>(xsrc + s4 * 4096, vo);
    xf[4 * s4 + 2] = gld_s<1, 2048>(xsrc + s4 * 4096, vo); xf[4 * s4 + 3] = gld_s<1, 3072>(xsrc + s4 * 4096, vo);
  }
  const char* ssrc = p.ws + WS_STAT + (size_t)(128 * id.G + 32 * tt) * 64;
  u32x4 st0 = gld_s<1, 0>(ssrc, (l & 31) * 64), st1 = gld_s<1, 16>(ssrc, (l & 31) * 64), st2 = gld_s<1, 32>(ssrc, (l & 31) * 64), st3 = gld_s<1, 48>(ssrc, (l & 31) * 64);
  float rs, gc0 = GC0; u32x4 xaug;
  u32 wlo = KH * 16384 + vo, wmid = wlo + 65536;
  PIN(wlo); PIN(wmid); PIN(gc0);
  f32x16 kp;
#define E4(F) { _Pragma("unroll") for (int e = 0; e < 4; ++e) { F; } }
#define E2(B, F) { _Pragma("unroll") for (int e = B; e < B + 2; ++e) { F; } }
#define UNITG(u, GO, RG, RU, OUT)                                                                                   \
    { if (u == 0) E4(gv[e] = vadd(kp[GO + e], RG[e]))                                                               \
      if (u == 1) E4(gv[e] = vmul(gv[e], rs))                                                                       \
      if (u == 2) E4(zz[e] = vmul(gv[e], gv[e]))                                                                    \
      if (u == 3) E4(zz[e] = vfma_k(zz[e], GC1, gc0))                                                               \
      if (u == 4) E4(zz[e] = vmul(zz[e], gv[e]))                                                                    \
      if (u == 5) E2(0, zz[e] = vexp(zz[e]))                                                                        \
      if (u == 6) E2(2, zz[e] = vexp(zz[e]))                                                                        \
      if (u == 7) E4(uv[e] = vadd(kp[GO + 4 + e], RU[e]))                                                           \
      if (u == 8) E4(zz[e] = vadd1(zz[e]))                                                                          \
      if (u == 9) E2(0, zz[e] = vrcp(zz[e]))                                                                        \
      if (u == 10) E2(2, zz[e] = vrcp(zz[e]))                                                                       \
      if (u == 11) E4(uv[e] = vmul(uv[e], gv[e]))                                                                   \
      if (u == 12) E4(zz[e] = vmul(zz[e], rs))                                                                      \
      if (u == 13) E4(uv[e] = vmul(uv[e], zz[e]))                                                                   \
      if (u == 14) { OUT[0] = vcvt(uv[0], uv[1]); OUT[1] = vcvt(uv[2], uv[3]); } }
#define UNIT(U) { if ((U) >= 0 && (U) < 15) UNITG((U), 0, r0, r1, q0) else if ((U) >= 15 && (U) < 30) UNITG((U) - 15, 8, r2, r3, q1) }
  constexpr int LG = 6;
#define RD(SL, I) ldsr<SL>(wlo + (I) * 1024, wmid + (I) * 1024)
#define G0(S) ((S) < LG ? (S) : 2 * (S) - LG)
#define STEP(S) { constexpr int s = (S);                                                                           \
      if (s < 16) mfa(a, ra[s & 15], xf[s & 15]);                                                                  \
      if (s + 4 < 16) ra[(s + 4) & 15] = RD(SS, (s + 4) & 15);                                                     \
      if (s == 0) r0 = ldsf4(RB); if (s == 1) r1 = ldsf4(RB + 1024); if (s == 8) r2 = ldsf4(RB + 2048); if (s == 9) r3 = ldsf4(RB + 3072); \
      if (s < 16) { PFM(s); UNIT(G0(s) - 2) }                                                                      \
      if (s >= 18) { constexpr int i4 = 4 * ((s - 18) & 3); stsf4(RB + i4 * 256, f32x4{a[i4], a[i4 + 1], a[i4 + 2], a[i4 + 3]}); } \
      if (s >= LG) mfa(b, rb[(s - LG) & 15], xf[(s - LG) & 15]);                                                   \
      if (s + 3 - LG >= 0 && s + 3 - LG < 16) rb[(s + 3 - LG) & 15] = RD(SK, (s + 3 - LG) & 15);                   \
      if (s >= LG) UNIT((s < 16 ? G0(s) + 1 : 32 - LG + (s - 16)) - 2) }
#define P4_ROUND(AUGS, AUGK, T)                                                                                    \
      f32x16 a, b; u32x4 ra[16], rb[16]; f32x4 r0, r1, r2, r3, gv, uv, zz; u32x2 q0, q1;                           \
      _Pragma("unroll") for (int s = 0; s < 4; ++s) ra[s] = RD(SS, s);                                             \
      mf0(a, AUGS, xaug); mf0(b, AUGK, xaug);                                                                      \
    STEP(0)    STEP(1)    STEP(2)    STEP(3)    STEP(4)    STEP(5)\
    STEP(6)    STEP(7)    STEP(8)    STEP(9)    STEP(10)    STEP(11)\
    STEP(12)    STEP(13)    STEP(14)    STEP(15)    STEP(16)    STEP(17)\
    STEP(18)    STEP(19)    STEP(20)    STEP(21) \
      gst16_sc1(adst + (T) * 1024, vo, u32x4{q0[0], q0[1], q1[0], q1[1]});                                         \
      kp = b;
#define DM4(s, S0, SRC, DST) { if (s == S0) { set_m0(DST); dma1<0>(SRC, vo); } if (s == S0 + 1) dma1<1024>(SRC, vo); if (s == S0 + 2) dma1<2048>(SRC, vo); if (s == S0 + 3) dma1<3072>(SRC, vo); }
  const u32 eA = exbuf(w) + vo, eB = exbuf(w ^ 4) + vo;
  {
    constexpr u32 SS = KH ? SL0 : SL1, SK = KH ? SL1 : SL0;
    f32x16 a, b; u32x4 ra[16], rb[16];
#pragma unroll
    for (int s = 0; s < 3; ++s) { ra[s] = RD(SS, s); rb[s] = RD(SK, s); }
#pragma unroll
    for (int s = 0; s < 16; ++s) {
      if (s == 0) asm volatile("s_waitcnt vmcnt(18)" : "+v"(xf[0]), "+v"(xf[1])::"memory");
      if (s == 2) asm volatile("s_waitcnt vmcnt(16)" : "+v"(xf[2]), "+v"(xf[3])::"memory");
      if (s == 4) asm volatile("s_waitcnt vmcnt(12)" : "+v"(xf[4]), "+v"(xf[5]), "+v"(xf[6]), "+v"(xf[7])::"memory");
      if (s == 8) asm volatile("s_waitcnt vmcnt(8)" : "+v"(xf[8]), "+v"(xf[9]), "+v"(xf[10]), "+v"(xf[11])::"memory");
      if (s == 12) asm volatile("s_waitcnt vmcnt(4)" : "+v"(xf[12]), "+v"(xf[13]), "+v"(xf[14]), "+v"(xf[15])::"memory");
      if (s == 0) mf0(a, ra[0], xf[0]); else mfa(a, ra[s], xf[s]);
      if (s + 3 < 16) { ra[s + 3] = RD(SS, s + 3); rb[s + 3] = RD(SK, s + 3); }
      if (s == 0) mf0(b, rb[0], xf[0]); else if (s < 10) mfa(b, rb[s], xf[s]);
    }
    asm volatile("s_waitcnt vmcnt(0)" : "+v"(st0), "+v"(st1), "+v"(st2), "+v"(st3)::"memory");
    const float s1 = (__uint_as_float(st0[0]) + __uint_as_float(st0[2])) + (__uint_as_float(st1[0]) + __uint_as_float(st1[2])) + (__uint_as_float(st2[0]) + __uint_as_float(st2[2])) + (__uint_as_float(st3[0]) + __uint_as_float(st3[2]));
    const float s2 = (__uint_as_float(st0[1]) + __uint_as_float(st0[3])) + (__uint_as_float(st1[1]) + __uint_as_float(st1[3])) + (__uint_as_float(st2[1]) + __uint_as_float(st2[3])) + (__uint_as_float(st3[1]) + __uint_as_float(st3[3]));
    const float nmu = -s1 * (1.f / DM), var = fmaxf(s2 * (1.f / DM) - nmu * nmu, 0.f) + EPS;
    rs = __builtin_amdgcn_rsqf(var);
    const float sg = var * rs, mh = rbf(nmu), sh = rbf(sg);
    xaug = u32x4{pk(mh, mh), pk(nmu - mh, sh), pk(sh, sg - sh), 0u};
    if (l >= 32) xaug = u32x4{0u, 0u, 0u, 0u};
    asm volatile("s_nop 1" : "+v"(xaug), "+v"(rs));
    mfa(a, aug[KH ? 0 : 1], xaug);
    mfa(b, rb[10], xf[10]); mfa(b, rb[11], xf[11]);
    MFMA_SETTLE();
    stsf4(eA, f32x4{a[0], a[1], a[2], a[3]}); mfa(b, rb[12], xf[12]);
    stsf4(eA + 1024, f32x4{a[4], a[5], a[6], a[7]}); mfa(b, rb[13], xf[13]);
    stsf4(eA + 2048, f32x4{a[8], a[9], a[10], a[11]}); mfa(b, rb[14], xf[14]);
    stsf4(eA + 3072, f32x4{a[12], a[13], a[14], a[15]}); mfa(b, rb[15], xf[15]);
    mfa(b, aug[KH ? 1 : 0], xaug);
    kp = b;
  }
  STAMP(11)
  BAR();
  STAMP(12)
  {
    constexpr u32 SS = KH ? SL2 : SL3, SK = KH ? SL3 : SL2;
    const u32 RB = eB;
#define PFM(s) { DM4(s, 0, wsrc + 4 * 32768, SL0 + w * 4096) DM4(s, 5, wsrc + 5 * 32768, SL1 + w * 4096) if (s == 10) aug[4] = gld_s<1, 0>(asrc + 8192, vo); if (s == 11) aug[5] = gld_s<1, 2048>(asrc + 8192, vo); }
    P4_ROUND(aug[KH ? 2 : 3], aug[KH ? 3 : 2], KH)
#undef PFM
  }
  STAMP(13)
  asm volatile("s_waitcnt vmcnt(1) lgkmcnt(0)\n\ts_barrier" : "+v"(aug[4]), "+v"(aug[5])::"memory");
  STAMP(14)
  {
    constexpr u32 SS = KH ? SL0 : SL1, SK = KH ? SL1 : SL0;
    const u32 RB = eA;
#define PFM(s) { DM4(s, 0, wsrc + 6 * 32768, SL2 + w * 4096) if (s == 5) aug[6] = gld_s<1, 0>(asrc + 12288, vo); DM4(s, 8, dsrc, SL3 + w * 4096) }
    P4_ROUND(aug[KH ? 4 : 5], aug[KH ? 5 : 4], 2 + KH)
#undef PFM
  }
  STAMP(15)
  asm volatile("s_waitcnt vmcnt(5) lgkmcnt(0)\n\ts_barrier" : "+v"(aug[6])::"memory");
  STAMP(16)
  {
    f32x16 a; u32x4 ra[16]; f32x4 r0, r1, r2, r3, gv, uv, zz; u32x2 q0, q1;
#pragma unroll
    for (int s = 0; s < 4; ++s) ra[s] = RD(SL2, s);
    r0 = ldsf4(eB); r1 = ldsf4(eB + 1024);
    mf0(a, aug[6], xaug);
#define STEP3(S) { constexpr int s = (S); mfa(a, ra[s], xf[s]); if (s + 4 < 16) ra[(s + 4) & 15] = RD(SL2, (s + 4) & 15);            \
      if (s == 4) r2 = ldsf4(eB + 2048); if (s == 5) r3 = ldsf4(eB + 3072);                                                         \
      DM4(s, 0, dsrc + 7168, SL0 + w * 4096) DM4(s, 6, dsrc + 14336, SL1 + w * 4096) if (s >= 12) WRL(s >= 12 ? s - 2 : 0); UNIT(s - 1) }
    STEP3(0) STEP3(1) STEP3(2) STEP3(3) STEP3(4) STEP3(5) STEP3(6) STEP3(7) STEP3(8) STEP3(9) STEP3(10) STEP3(11) STEP3(12) STEP3(13) STEP3(14) STEP3(15)
#undef STEP3
    constexpr int K8 = KH * 8, S8 = 8 - K8;
    UNIT(15) UNIT(16) UNIT(17) UNIT(18) UNIT(19)
    MFMA_SETTLE();
    stsf4(eB, f32x4{a[S8], a[S8 + 1], a[S8 + 2], a[S8 + 3]}); stsf4(eB + 1024, f32x4{a[S8 + 4], a[S8 + 5], a[S8 + 6], a[S8 + 7]});
    WAITV_BAR(12);
    if (w == 0 && l == 0) put_flag(p.ws + WS_FE + id.G * 256 + id.c * 4, tag);
    f32x4 h0 = ldsf4(eA), h1 = ldsf4(eA + 1024);
    UNIT(20) UNIT(21) UNIT(22) UNIT(23) UNIT(24) UNIT(25) UNIT(26) UNIT(27) UNIT(28) UNIT(29)
    gst16_sc1(adst + (4 + KH) * 1024, vo, u32x4{q0[0], q0[1], q1[0], q1[1]});
    kp = a;
#define UL(U) UNITG(U, K8, h0, h1, q0)
    UL(0) UL(1) UL(2) UL(3) UL(4) UL(5) UL(6) UL(7) UL(8) UL(9) UL(10) UL(11) UL(12) UL(13) UL(14)
#undef UL
    gst8_sc1(adst + 6 * 1024 + KH * 8, vo, q0);
  }
  STAMP(17)
  BAR();
  dma4s(dsrc + 21504, vo, SL2 + w * 4096);
  set_m0(TOPA + w * 3072); dma1<0>(dsrc + 4096, vo); dma1<1024>(dsrc + 4096, vo); dma1<2048>(dsrc + 4096, vo);
  WRL(14); WRL(15); WRL(16); WRL(17);
  asm volatile("global_load_dword %0, %1, %2 sc1" : "=v"(fe) : "v"((l & 3) * 4), "s"(p.ws + WS_FE + (id.g >> 1) * 256 + (w >> 1) * 16) : "memory");
  WAITV_BAR(12);
  STAMP(18)
  if (w == 0 && l == 0) put_flag(p.ws + WS_FA + id.G * 256 + id.c * 4, tag);
#undef P4_ROUND
#undef STEP
#undef UNIT
#undef UNITG
}

__global__ __launch_bounds__(512, 2) void k_main(Params p) {
  extern __shared__ __attribute__((aligned(16))) char smem[];
  const int tid = threadIdx.x, l = tid & 63, w = rfl(tid >> 6);
  const Ids id = ids();
  const int g = id.g, j = id.j, h = l >> 5;
  const u32 vo = l * 16;
  const u32 epoch = *(const u32*)(p.ws + WS_EPOCH);
  const u32 tag = epoch * 0x9E3779B1u + 0x7F4A7C15u;
  const int tt = w & 1, nt = (w >> 1) & 1, kh = w >> 2, kq = w >> 1;
  const char* modr = p.ws + WS_MOD + (blockIdx.x & 7) * 12288;
  STAMP_DECL STAMP(0)
  u32x4 q0, q1;
  {
    const int nh = (l >> 4) & 1, q = (l & 15) >> 2, pp = l & 3;
    const int uid = g * 7 + j - 1, uT = uid >> 1, ukh = uid & 1;
    const int rk = (blockIdx.x >> 6) * 7 + j - 1;
    const bool gemv = j != 0 && rk < 24;
    const int mu = 4 * ((blockIdx.x & 7) + 8 * (rk >> 2)) + (rk & 3);
    const u32 rawo = (l >> 3) * 1024 + (((l & 7) ^ (((l >> 4) & 1) << 2)) * 16);
    if (w >= 3) { set_m0(JUNK); touch8k(p.ws + (size_t)(w - 3) * (4u << 20), l * 65536); }
    if (w == 2) { set_m0(JUNK); touch8k((const char*)p.out, (l & 31) * 65536); }
    u32x4 w0, w1; u32 c0, c1;
    if (gemv) {
      const u32 mo = (tid >> 1) * 6144 + (tid & 1) * 16;
      w0 = gld_s<0, 0>(p.wm + 16 * mu, mo); w1 = gld_s<0, 0>(p.wm + 16 * mu + 256 * 3072, mo);
      c0 = gld2_s<0>(p.c, (tid >> 1) * 2); c1 = gld2_s<512>(p.c, (tid >> 1) * 2);
      STAMP(10)
      asm volatile("s_waitcnt vmcnt(0)" : "+v"(w0), "+v"(w1), "+v"(c0), "+v"(c1)::"memory");
    }
    u32x4 xv[8];
    {
      const char* xs = (const char*)(p.x + (size_t)(64 * g + 8 * w) * DM);
      xv[0] = gld_s<0, 0>(xs, vo); xv[1] = gld_s<0, 1024>(xs, vo); xv[2] = gld_s<0, 2048>(xs, vo); xv[3] = gld_s<0, 3072>(xs, vo);
      xv[4] = gld_s<0, 0>(xs + 4096, vo); xv[5] = gld_s<0, 1024>(xs + 4096, vo); xv[6] = gld_s<0, 2048>(xs + 4096, vo); xv[7] = gld_s<0, 3072>(xs + 4096, vo);
    }
    if (j == 0) {
      set_m0(F_H + w * 2048); dma<0>(p.wv + w * 1024, vo); dma<1024>(p.wv + w * 1024, vo);
      set_m0(F_H + 16384 + 128 + w * 2048); dma<0>(p.wk + w * 1024, vo); dma<1024>(p.wk + w * 1024, vo);
    }
    if (gemv) {
      const float f0 = bflo(c0), f1 = bflo(c1);
      const float s0 = rbf(f0 * __builtin_amdgcn_rcpf(1.f + EXP2(-1.4426950408889634f * f0))), s1 = rbf(f1 * __builtin_amdgcn_rcpf(1.f + EXP2(-1.4426950408889634f * f1)));
      float a[8];
#pragma unroll
      for (int i = 0; i < 4; ++i) { a[2 * i] = s0 * bflo(w0[i]) + s1 * bflo(w1[i]); a[2 * i + 1] = s0 * bfhi(w0[i]) + s1 * bfhi(w1[i]); }
#pragma unroll
      for (int i = 0; i < 4; ++i) a[i] = halve<0x4E>(l & 2, a[i], a[i + 4]);
#pragma unroll
      for (int i = 0; i < 2; ++i) a[i] = halve<0x124>(l & 4, a[i], a[i + 2]);
      a[0] = halve<0x128>(l & 8, a[0], a[1]);
      a[0] = swap32_sum(swap16_sum(a[0]));
      if (l < 16) *(LAS float*)(uintptr_t)(F_M + w * 64 + ((l & 1) * 8 + (l & 2) * 2 + ((l >> 1) & 2) + (l >> 3)) * 4) = a[0];
      BAR();
      if (w == 7) {
        f32x2 s = {0.f, 0.f};
#pragma unroll
        for (int i = 0; i < 8; ++i) s += ldsf2(F_M + i * 64 + (l & 7) * 8);
        gst8_sc1(p.ws + WS_MOD + mu * 64, (l >> 3) * 12288 + (l & 7) * 8, u32x2{pk(s[0], s[1]), tag});
      }
    }
    STAMP(22)
    if (j == 0) WAITV(4); else if (gemv && w == 7) WAITV(1); else WAITV(0);
    asm volatile("" : "+v"(xv[0]), "+v"(xv[1]), "+v"(xv[2]), "+v"(xv[3]), "+v"(xv[4]), "+v"(xv[5]), "+v"(xv[6]), "+v"(xv[7]));
#define LATE_DMA() {                                                                                                                              \
      _Pragma("unroll") for (int i = 0; i < 8; ++i) { set_m0(F_A + (8 * w + i) * 1024); dma<0>(p.wq + (size_t)(64 * w + 8 * i) * DM + 64 * j, rawo); } \
      if (j != 0) {                                                                                                                               \
        const u32 go = (l >> 1) * (FF * 2) + (l & 1) * 16;                                                                                        \
        set_m0(F_B + w * 1024); dma<0>(p.wg + (size_t)(256 * ukh + 32 * w) * FF + 16 * uT, go);                                                   \
        set_m0(F_B + 8192 + 128 + w * 1024); dma<0>(p.wu + (size_t)(256 * ukh + 32 * w) * FF + 16 * uT, go);                                      \
        set_m0(F_D + w * 1024); dma<0>(p.wd + (size_t)(64 * (uid >> 3) + 8 * w) * DM + 64 * (uid & 7), rawo);                                     \
      }                                                                                                                                           \
      if (w >= 6) { set_m0(F_D + 8192 + (w - 6) * 1024); dma<0>(p.wo + (size_t)(16 * g + 8 * (w - 6)) * DM + 64 * j, rawo); } }
    if (w > 1 && j != 0) LATE_DMA()
    float xh[8][8];
    {
      float s1[8], s2[8];
#pragma unroll
      for (int i = 0; i < 8; ++i) {
        s1[i] = 0.f; s2[i] = 0.f;
#pragma unroll
        for (int e = 0; e < 4; ++e) { const u32 v = xv[i][e]; s1[i] = dot2(v, 0x3f803f80u, s1[i]); s2[i] = dot2(v, v, s2[i]); }
      }
#pragma unroll
      for (int i = 0; i < 4; ++i) { s1[i] = halve<0xB1>(l & 1, s1[i], s1[i + 4]); s2[i] = halve<0xB1>(l & 1, s2[i], s2[i + 4]); }
#pragma unroll
      for (int i = 0; i < 2; ++i) { s1[i] = halve<0x4E>(l & 2, s1[i], s1[i + 2]); s2[i] = halve<0x4E>(l & 2, s2[i], s2[i + 2]); }
      float t1 = halve<0x124>(l & 4, s1[0], s1[1]), t2 = halve<0x124>(l & 4, s2[0], s2[1]);
      t1 += dpp<0x128>(t1); t2 += dpp<0x128>(t2);
      t1 = swap32_sum(swap16_sum(t1)); t2 = swap32_sum(swap16_sum(t2));
      const float m = t1 * (1.f / DM), r = __builtin_amdgcn_rsqf(fmaxf(t2 * (1.f / DM) - m * m, 0.f) + EPS), nm = -m * r;
#define LNROW(I, LN) { const float ri = dpp<0x150 + (LN)>(r), ni = dpp<0x150 + (LN)>(nm); _Pragma("unroll") for (int e = 0; e < 4; ++e) { xh[I][2 * e] = fmaf(bflo(xv[I][e]), ri, ni); xh[I][2 * e + 1] = fmaf(bfhi(xv[I][e]), ri, ni); } }
      LNROW(0, 0) LNROW(1, 4) LNROW(2, 2) LNROW(3, 6) LNROW(4, 1) LNROW(5, 5) LNROW(6, 3) LNROW(7, 7)
#undef LNROW
#pragma unroll
      for (int i = 0; i < 8; ++i) asm volatile("" : "+v"(xh[i][0]), "+v"(xh[i][1]), "+v"(xh[i][2]), "+v"(xh[i][3]), "+v"(xh[i][4]), "+v"(xh[i][5]), "+v"(xh[i][6]), "+v"(xh[i][7]));
    }
    STAMP(23)
    u32x4 wkvf[16];
    if (j == 0) {
      WAITV_BAR(0);
      const u32 a0 = F_H + nh * (16384 + 128) + (256 * kh + 8 * h + q) * 32 + pp * 8;
#pragma unroll
      for (int s = 0; s < 16; ++s) wkvf[s] = ldstr2(a0 + s * 512, a0 + s * 512 + 128);
      if (w > 1) LATE_DMA()
    }
    STAMP(24)
    if (w == 0) {
      u32x4 a0, a1, b0, b1;
#pragma unroll 1
      for (int it = 0; it < SPIN_CAP; ++it) {
        asm volatile("global_load_dwordx4 %0, %4, %5 sc1\n\tglobal_load_dwordx4 %1, %4, %5 offset:16 sc1\n\tglobal_load_dwordx4 %2, %4, %5 offset:2048 sc1\n\t"
                     "global_load_dwordx4 %3, %4, %5 offset:2064 sc1\n\ts_waitcnt vmcnt(0)" : "=&v"(b0), "=&v"(b1), "=&v"(a0), "=&v"(a1) : "v"(l * 32), "s"(modr) : "memory");
        if (__all(a0[1] == tag && a0[3] == tag && a1[1] == tag && a1[3] == tag && b0[1] == tag && b0[3] == tag && b1[1] == tag && b1[3] == tag)) break;
      }
      sts128(F_M + 512 + vo, u32x4{pk(1.f + bflo(a0[0]), 1.f + bfhi(a0[0])), pk(1.f + bflo(a0[2]), 1.f + bfhi(a0[2])), pk(1.f + bflo(a1[0]), 1.f + bfhi(a1[0])), pk(1.f + bflo(a1[2]), 1.f + bfhi(a1[2]))});
      sts128(F_M + 1536 + vo, u32x4{b0[0], b0[2], b1[0], b1[2]});
    } else if (w == 1) {
      if (j != 0) {
        u32x4 a0, b0;
#pragma unroll 1
        for (int it = 0; it < SPIN_CAP; ++it) {
          asm volatile("global_load_dwordx4 %0, %2, %3 sc1\n\tglobal_load_dwordx4 %1, %2, %3 offset:2048 sc1\n\ts_waitcnt vmcnt(0)" : "=&v"(b0), "=&v"(a0) : "v"(vo), "s"(modr + 6144 + ukh * 1024) : "memory");
          if (__all(a0[1] == tag && a0[3] == tag && b0[1] == tag && b0[3] == tag)) break;
          __builtin_amdgcn_s_sleep(2);
        }
        stsf4(F_M + 2560 + vo, f32x4{rbf(1.f + bflo(a0[0])), rbf(1.f + bfhi(a0[0])), rbf(1.f + bflo(a0[2])), rbf(1.f + bfhi(a0[2]))});
        stsf4(F_M + 3584 + vo, f32x4{bflo(b0[0]), bfhi(b0[0]), bflo(b0[2]), bfhi(b0[2])});
      }
    }
    BAR();
    STAMP(25)
    if (w <= 1) LATE_DMA()
    {
      const u32x4 ca = lds128(F_M + 512 + vo), sa = lds128(F_M + 1536 + vo);
      float A[8], B[8];
#pragma unroll
      for (int e = 0; e < 4; ++e) { A[2 * e] = bflo(ca[e]); A[2 * e + 1] = bfhi(ca[e]); B[2 * e] = bflo(sa[e]); B[2 * e + 1] = bfhi(sa[e]); }
#pragma unroll
      for (int i = 0; i < 8; ++i) {
        u32x4 o;
#pragma unroll
        for (int e = 0; e < 4; ++e) o[e] = pk(fmaf(xh[i][2 * e], A[2 * e], B[2 * e]), fmaf(xh[i][2 * e + 1], A[2 * e + 1], B[2 * e + 1]));
        const int tk = 8 * w + i;
        sts128(F_H + ((tk >> 5) * 32 + (l >> 1)) * 1024 + (l & 1) * 512 + (((tk & 31) ^ (l & 7)) * 16), o);
      }
    }
    BAR();
    STAMP(26)
    u32x4 xf[16];
#pragma unroll
    for (int s = 0; s < 16; ++s) xf[s] = lds128(F_H + (tt * 32 + 16 * kh + s) * 1024 + h * 512 + (((l & 31) ^ ((2 * s + h) & 7)) * 16));
    if (j == 0) {
      const u32 xs = F_B + w * 2048 + vo, xr = F_B + (w ^ 4) * 2048 + vo;
      f32x16 acc = {};
      if (nt == 0) {
#pragma unroll
        for (int s = 0; s < 16; ++s) acc = mfma(wkvf[s], xf[s], acc);
      } else {
#pragma unroll
        for (int s = 0; s < 16; ++s) acc = mfma(xf[s], wkvf[s], acc);
      }
      float keep[8];
      if (kh == 0) { stsf4(xs, f32x4{acc[8], acc[9], acc[10], acc[11]}); stsf4(xs + 1024, f32x4{acc[12], acc[13], acc[14], acc[15]});
#pragma unroll
        for (int e = 0; e < 8; ++e) keep[e] = acc[e];
      } else { stsf4(xs, f32x4{acc[0], acc[1], acc[2], acc[3]}); stsf4(xs + 1024, f32x4{acc[4], acc[5], acc[6], acc[7]});
#pragma unroll
        for (int e = 0; e < 8; ++e) keep[e] = acc[8 + e];
      }
      BAR();
      const f32x4 r0 = ldsf4(xr), r1 = ldsf4(xr + 1024);
      u32x4 o = {pk(keep[0] + r0[0], keep[1] + r0[1]), pk(keep[2] + r0[2], keep[3] + r0[3]), pk(keep[4] + r1[0], keep[5] + r1[1]), pk(keep[6] + r1[2], keep[7] + r1[3])};
      char* img = p.ws + WS_KV + (size_t)(2 * g + tt) * TS;
      if (nt == 0) { if (kh == 1) gst16_sc1(img, vo, o); }
      else if ((l & 31) <= 17) {
        if ((l & 31) == 16) o = u32x4{0x3f803f80u, 0x3f803f80u, 0x3f803f80u, 0x3f803f80u};
        if ((l & 31) == 17) o = u32x4{0u, 0u, 0u, 0u};
        gst16_sc1(img + 1024 + kh * 576, h * 288 + (l & 31) * 16, o);
      }
      WAITV_BAR(0);
      if (w == 0 && l < 32) put_flag(p.ws + WS_FKV + l * 128 + g * 4, tag);
    }
    STAMP(21)
    WAITV_BAR(0);
    const u32 rawr = (((nh * 2 + (pp >> 1)) ^ (((q >> 1) & 1) << 2)) * 16) + (pp & 1) * 8;
    f32x16 acc = {};
    {
      const u32 a0 = F_A + (256 * kh + 8 * h + q) * 128 + (rawr ^ (nt * 64));
#pragma unroll
      for (int s = 0; s < 16; ++s) acc = mfma(ldstr2(a0 + s * 2048, a0 + s * 2048 + 512), xf[s], acc);
    }
    const u32 xs = F_H + w * 4096 + vo, xr = F_H + (w ^ 4) * 4096 + vo;
    stsf4(xs, f32x4{acc[0], acc[1], acc[2], acc[3]}); stsf4(xs + 1024, f32x4{acc[4], acc[5], acc[6], acc[7]});
    stsf4(xs + 2048, f32x4{acc[8], acc[9], acc[10], acc[11]}); stsf4(xs + 3072, f32x4{acc[12], acc[13], acc[14], acc[15]});
    STAMP(27)
    u32x4 fo_, fd_, fg[2]; f32x4 cs[2][4];
    if (w >= 6) { const u32 a0 = F_D + 8192 + (4 * h + q) * 128 + (rawr ^ (tt * 64)); fo_ = ldstr2(a0, a0 + 1024); }
    if (j != 0) {
      const u32 a0 = F_D + (16 * kq + 4 * h + q) * 128 + (rawr ^ (tt * 64));
      fd_ = ldstr2(a0, a0 + 1024);
#pragma unroll
      for (int i = 0; i < 2; ++i) {
        const int s = 2 * w + i;
        const u32 a1 = F_B + (pp >> 1) * (8192 + 128) + (16 * s + 4 * h + q) * 32 + nh * 16 + (pp & 1) * 8;
        fg[i] = ldstr2(a1, a1 + 256);
        cs[i][0] = ldsf4(F_M + 2560 + (16 * s + 4 * h) * 4); cs[i][1] = ldsf4(F_M + 2560 + (16 * s + 8 + 4 * h) * 4);
        cs[i][2] = ldsf4(F_M + 3584 + (16 * s + 4 * h) * 4); cs[i][3] = ldsf4(F_M + 3584 + (16 * s + 8 + 4 * h) * 4);
      }
    }
    if (w == 2) poll_flags<32>(p.ws + WS_FKV + (blockIdx.x & 31) * 128, tag, l);
    if (w == 3) {
#pragma unroll 1
      for (int it = 0; it < SPIN_CAP; ++it) {
        const u32 v = __hip_atomic_load((const GAS u32*)(modr + 4096 + j * 256 + h * 6144 + (l & 31) * 8 + 4), RLX_AGENT);
        if (__all(v == tag)) break;
        __builtin_amdgcn_s_sleep(2);
      }
    }
    BAR();
    STAMP(28)
    const f32x4 r0 = ldsf4(xr), r1 = ldsf4(xr + 1024), r2 = ldsf4(xr + 2048), r3 = ldsf4(xr + 3072);
    q0 = u32x4{pk((acc[0] + r0[0]) * C2, (acc[1] + r0[1]) * C2), pk((acc[2] + r0[2]) * C2, (acc[3] + r0[3]) * C2), pk((acc[4] + r1[0]) * C2, (acc[5] + r1[1]) * C2), pk((acc[6] + r1[2]) * C2, (acc[7] + r1[3]) * C2)};
    q1 = u32x4{pk((acc[8] + r2[0]) * C2, (acc[9] + r2[1]) * C2), pk((acc[10] + r2[2]) * C2, (acc[11] + r2[3]) * C2), pk((acc[12] + r3[0]) * C2, (acc[13] + r3[1]) * C2), pk((acc[14] + r3[2]) * C2, (acc[15] + r3[3]) * C2)};
    BAR();
    STAMP(29)
    { const u32 o = (w & 1) * 69632 + (w >> 1) * 6144;
      dma4s(p.ws + WS_KV + o, vo, o); set_m0(o + 4096); dma1<0>(p.ws + WS_KV + o + 4096, vo); dma1<1024>(p.ws + WS_KV + o + 4096, vo); }
    if (w >= 6) gst16_sc1(p.ws + WS_WO + ((size_t)(2 * j + tt) * 32 + g) * 1024, vo, fo_);
    if (j != 0) {
      gst16_sc1(p.ws + WS_WD + ((size_t)(2 * (uid & 7) + tt) * 112 + 4 * (uid >> 3) + kq) * 1024, vo, fd_);
      float us = 0.f, wsum = 0.f;
#pragma unroll
      for (int i = 0; i < 2; ++i) {
        const u32x4 f = fg[i];
        const f32x4 c0 = cs[i][0], c1 = cs[i][1], s0 = cs[i][2], s1 = cs[i][3];
        u32x4 o;
        o[0] = pk(bflo(f[0]) * c0[0], bfhi(f[0]) * c0[1]); o[1] = pk(bflo(f[1]) * c0[2], bfhi(f[1]) * c0[3]);
        o[2] = pk(bflo(f[2]) * c1[0], bfhi(f[2]) * c1[1]); o[3] = pk(bflo(f[3]) * c1[2], bfhi(f[3]) * c1[3]);
#pragma unroll
        for (int e = 0; e < 4; ++e) { const u32 v = o[e]; us = dot2(v, 0x3f803f80u, us); }
        wsum += bflo(f[0]) * s0[0] + bfhi(f[0]) * s0[1] + bflo(f[1]) * s0[2] + bfhi(f[1]) * s0[3] + bflo(f[2]) * s1[0] + bfhi(f[2]) * s1[1] + bflo(f[3]) * s1[2] + bfhi(f[3]) * s1[3];
        gst16_sc1(p.ws + WS_WGU + ((size_t)uT * 32 + 16 * ukh + 2 * w + i) * 1024, vo, o);
      }
      us = swap32_sum(us); wsum = swap32_sum(wsum);
      if (l < 32) *(LAS f32x2*)(uintptr_t)(OEX + w * 256 + l * 8) = f32x2{us, wsum};
      WAITV_BAR(3);
      if (w == 7) {
        f32x2 s = {0.f, 0.f};
#pragma unroll
        for (int i = 0; i < 8; ++i) s += ldsf2(OEX + i * 256 + (l & 31) * 8);
        const float uh = rbf(s[0]), wh = rbf(s[1]);
        u32x4 o = {pk(uh, s[0] - uh), pk(uh, wh), pk(s[1] - wh, wh), 0u};
        if (l >= 32) o = u32x4{0u, 0u, 0u, 0u};
        gst16_sc1(p.ws + WS_AUG + (size_t)(uT * 2 + ukh) * 1024, vo, o);
      }
    } else {
      WAITV_BAR(0);
    }
  }
  const size_t fo = ((size_t)(2 * g + tt) * 32 + 4 * j + 2 * nt) * 1024;
  STAMP(1)
  f32x16 nm0, nm1;
  {
    const u32x4 k0 = lds128(vo);
    const float m0 = rowmax16(mfma(k0, q0, f32x16{})), m1 = rowmax16(mfma(k0, q1, f32x16{}));
#pragma unroll
    for (int r = 0; r < 16; ++r) { nm0[r] = -m0; nm1[r] = -m1; }
    PIN(nm0); PIN(nm1);
  }
  const u32 vrow = 1024 + h * 288 + min(l & 31, 17) * 16;
  u32 ka = kh * 32 * TS + vo, va = kh * 32 * TS + vrow;
  f32x16 o0 = {}, o1 = {};
  u32x4 kA = lds128(ka), kB = lds128(ka + TS);
  f32x16 s0 = mfma(kA, q0, nm0), s1;
  u32x4 vA0 = lds128(va), vA1 = lds128(va + 576), vB0 = {}, vB1 = {};
  u32x4 pa0, pa1, pb0 = {}, pb1 = {};
  PIN(s0);
  if (kh) {
    const u32 o = nt * 69632 + 24576 + tt * 22528;
#pragma unroll
    for (int i = 0; i < 5; ++i) dma4s(p.ws + WS_KV + o + i * 4096, vo, o + i * 4096);
    set_m0(o + 20480); dma1<0>(p.ws + WS_KV + o + 20480, vo); dma1<1024>(p.ws + WS_KV + o + 20480, vo);
  }
  u32x4 wf[16];
#define HALF(SC, N0, N1, MF, OP, VP0, P0, VP1, P1, LD1, LD2)                                              \
  {                                                                                                       \
    MF; LD1; SB();                                                                                        \
    float e0 = EXP2(SC[0]), e1 = EXP2(SC[1]), e2 = EXP2(SC[2]), e3 = EXP2(SC[3]), e4 = EXP2(SC[4]), e5 = EXP2(SC[5]), e6 = EXP2(SC[6]), e7 = EXP2(SC[7]); SB(); \
    OP = mfma(VP0, P0, OP); SB();                                                                         \
    float e8 = EXP2(SC[8]), e9 = EXP2(SC[9]), e10 = EXP2(SC[10]), e11 = EXP2(SC[11]), e12 = EXP2(SC[12]), e13 = EXP2(SC[13]), e14 = EXP2(SC[14]), e15 = EXP2(SC[15]); SB(); \
    OP = mfma(VP1, P1, OP); LD2; SB();                                                                    \
    N0[0] = pk(e0, e1); N0[1] = pk(e2, e3); N0[2] = pk(e4, e5); N0[3] = pk(e6, e7); N1[0] = pk(e8, e9);   \
    N1[1] = pk(e10, e11); N1[2] = pk(e12, e13); N1[3] = pk(e14, e15); SB();                               \
  }
#define TILE(T, KC, KN, VC0, VC1, VP0, VP1)                                                               \
    HALF(s0, pa0, pa1, s1 = mfma(KC, q1, nm1), o1, VP0, pb0, VP1, pb1, KC = lds128(ka + ((T) + 2) * TS),  \
         (VP0 = lds128(va + ((T) + 1) * TS), VP1 = lds128(va + ((T) + 1) * TS + 576)))                    \
    HALF(s1, pb0, pb1, s0 = mfma(KN, q0, nm0), o0, VC0, pa0, VC1, pa1, , )
  const char* wgu = p.ws + WS_WGU + (size_t)id.c * 7 * 32768;
  const char* wob = p.ws + WS_WO + ((size_t)(2 * j) * 32 + 8 * kq) * 1024;
#define LDWF(SEL)                                                                                                     \
  asm volatile("s_cmp_lg_u32 %21, 0\n\ts_cbranch_scc1 .Lwf%=\n\t"                                                     \
               "global_load_dwordx4 %0, %16, %17 sc1\n\tglobal_load_dwordx4 %1, %16, %17 offset:1024 sc1\n\tglobal_load_dwordx4 %2, %16, %17 offset:2048 sc1\n\tglobal_load_dwordx4 %3, %16, %17 offset:3072 sc1\n\t"     \
               "global_load_dwordx4 %4, %16, %18 sc1\n\tglobal_load_dwordx4 %5, %16, %18 offset:1024 sc1\n\tglobal_load_dwordx4 %6, %16, %18 offset:2048 sc1\n\tglobal_load_dwordx4 %7, %16, %18 offset:3072 sc1\n\t"     \
               "global_load_dwordx4 %8, %16, %19 sc1\n\tglobal_load_dwordx4 %9, %16, %19 offset:1024 sc1\n\tglobal_load_dwordx4 %10, %16, %19 offset:2048 sc1\n\tglobal_load_dwordx4 %11, %16, %19 offset:3072 sc1\n\t"   \
               "global_load_dwordx4 %12, %16, %20 sc1\n\tglobal_load_dwordx4 %13, %16, %20 offset:1024 sc1\n\tglobal_load_dwordx4 %14, %16, %20 offset:2048 sc1\n\tglobal_load_dwordx4 %15, %16, %20 offset:3072 sc1\n"   \
               ".Lwf%=:"                                                                                              \
               : "+v"(wf[0]), "+v"(wf[1]), "+v"(wf[2]), "+v"(wf[3]), "+v"(wf[4]), "+v"(wf[5]), "+v"(wf[6]), "+v"(wf[7]), "+v"(wf[8]), "+v"(wf[9]), "+v"(wf[10]), \
                 "+v"(wf[11]), "+v"(wf[12]), "+v"(wf[13]), "+v"(wf[14]), "+v"(wf[15])                                  \
               : "v"(vo), "s"(wob), "s"(wob + 4096), "s"(wob + 32768), "s"(wob + 36864), "s"(SEL) : "memory", "scc")
#pragma unroll
  for (int s4 = 0; s4 < 16; ++s4) asm volatile("" : "=v"(wf[s4]));
  const int qt = blockIdx.x >> 6;
#pragma unroll 1
  for (int seg = 0; seg < 4; ++seg) {
    if (seg == 1) {
      ASTAMP(10)
      WAITV_BAR(0);
      ASTAMP(24)
      if (w == 0 && l < 8) put_flag(p.ws + WS_FP + l * 1024 + blockIdx.x * 4, tag);
    } else if (seg == 2) {
      ASTAMP(21)
      if (w == 0) {
        u32x4 f;
#pragma unroll 1
        for (int it = 0; it < SPIN_CAP; ++it) {
          asm volatile("global_load_dwordx4 %0, %1, %2 sc1\n\ts_waitcnt vmcnt(0)" : "=&v"(f) : "v"(vo), "s"(p.ws + WS_FP + (blockIdx.x & 7) * 1024) : "memory");
          if (__all(f[0] == tag && f[1] == tag && f[2] == tag && f[3] == tag)) break;
          __builtin_amdgcn_s_sleep(1);
        }
        if (blockIdx.x == 0 && l == 0) put_flag(p.ws + WS_EPOCH, epoch + 1u);
      }
      BAR();
      ASTAMP(23)
    } else if (seg == 3) {
      ASTAMP(22)
      if (!kh) {
        dma4s(wgu + w * 8192, vo, SL0 + w * 8192); dma4s(wgu + w * 8192 + 4096, vo, SL0 + w * 8192 + 4096);
        dma4s(wgu + 32768 + w * 8192, vo, SL1 + w * 8192); dma4s(wgu + 32768 + w * 8192 + 4096, vo, SL1 + w * 8192 + 4096);
        set_m0(JUNK);
        touch8k(wgu + 65536 + qt * 40960 + w * 8192, l * 128);
        if (w < 2) touch8k(w == 0 ? wgu + 65536 + qt * 40960 + 32768 : p.ws + WS_AUG + (size_t)id.c * 14336 + (qt & 1) * 6144, l * 128);
      }
    }
    LDWF((seg ^ 2) | (kh ^ 1));
    TILE(0, kA, kB, vA0, vA1, vB0, vB1) TILE(1, kB, kA, vB0, vB1, vA0, vA1) TILE(2, kA, kB, vA0, vA1, vB0, vB1) TILE(3, kB, kA, vB0, vB1, vA0, vA1)
    TILE(4, kA, kB, vA0, vA1, vB0, vB1) TILE(5, kB, kA, vB0, vB1, vA0, vA1) TILE(6, kA, kB, vA0, vA1, vB0, vB1) TILE(7, kB, kA, vB0, vB1, vA0, vA1)
    ka += 8 * TS; va += 8 * TS; PIN(ka); PIN(va);
  }
  o1 = mfma(vB0, pb0, o1); o1 = mfma(vB1, pb1, o1);
  LDWF(kh);
  if (!kh) {
    set_m0(JUNK);
    touch8k(p.ws + WS_WD + (size_t)(2 * j) * 114688 + qt * 57344 + w * 8192, l * 128);
    if (w < 3) touch8k(p.ws + WS_WD + (size_t)(2 * j) * 114688 + qt * 57344 + (w + 4) * 8192, l * 128);
  }
#undef HALF
#undef TILE
  STAMP(2)
  const u32 xo = (u32)((l & 31) * DM + 4 * h) * 2;
  const char* xres = (const char*)(p.x + (size_t)(64 * g + 32 * tt) * DM + 64 * j + 32 * nt + 16 * kh);
  u32x2 xa = gld8_s<0>(xres, xo), xb = gld8_s<16>(xres, xo);
  u32x4 ga = gld_s<1, 0>(modr + 4096 + (64 * j + 32 * nt + 16 * kh) * 4, h * 16), gb = gld_s<1, 32>(modr + 4096 + (64 * j + 32 * nt + 16 * kh) * 4, h * 16);
  {
    const u32 xs = OEX + w * 2304, xr = OEX + (w ^ 4) * 2304;
    float o[9];
    if (kh) { stsf4(xs + vo, f32x4{o0[0], o0[1], o0[2], o0[3]}); stsf4(xs + 1024 + vo, f32x4{o0[4], o0[5], o0[6], o0[7]}); *(LAS float*)(uintptr_t)(xs + 2048 + l * 4) = o0[8];
#pragma unroll
      for (int e = 0; e < 9; ++e) o[e] = o1[e];
    } else { stsf4(xs + vo, f32x4{o1[0], o1[1], o1[2], o1[3]}); stsf4(xs + 1024 + vo, f32x4{o1[4], o1[5], o1[6], o1[7]}); *(LAS float*)(uintptr_t)(xs + 2048 + l * 4) = o1[8];
#pragma unroll
      for (int e = 0; e < 9; ++e) o[e] = o0[e];
    }
    BAR();
    const f32x4 r0 = ldsf4(xr + vo), r1 = ldsf4(xr + 1024 + vo);
#pragma unroll
    for (int e = 0; e < 4; ++e) { o[e] += r0[e]; o[4 + e] += r1[e]; }
    float ls = lowhalf(o[8] + *(const LAS float*)(uintptr_t)(xr + 2048 + l * 4));
    bool bad = !(ls < 1.2676506e30f);
#pragma unroll
    for (int e = 0; e < 8; ++e) bad |= !(fabsf(o[e]) < 3.0e38f);
    if (__builtin_expect(__any(bad), 0)) {
      const u32x4 qf = kh ? q1 : q0;
      float m = -INFINITY;
      f32x16 oo = {};
#pragma unroll 1
      for (int t = 0; t < 64; ++t) {
        u32x4 kf, v0, v1;
        asm volatile("global_load_dwordx4 %0, %3, %5 sc1\n\tglobal_load_dwordx4 %1, %4, %5 sc1\n\tglobal_load_dwordx4 %2, %4, %5 offset:576 sc1\n\ts_waitcnt vmcnt(0)"
                     : "=&v"(kf), "=&v"(v0), "=&v"(v1) : "v"(vo), "v"(vrow), "s"(p.ws + WS_KV + t * TS) : "memory");
        const f32x16 s = mfma(kf, qf, f32x16{});
        const float mn = fmaxf(m, rowmax16(s)), al = EXP2(m - mn);
        m = mn;
        u32x4 pa, pb;
#pragma unroll
        for (int r = 0; r < 4; ++r) { pa[r] = pk(EXP2(s[2 * r] - mn), EXP2(s[2 * r + 1] - mn)); pb[r] = pk(EXP2(s[8 + 2 * r] - mn), EXP2(s[9 + 2 * r] - mn)); }
#pragma unroll
        for (int r = 0; r < 16; ++r) oo[r] *= al;
        oo = mfma(v0, pa, oo); oo = mfma(v1, pb, oo);
      }
#pragma unroll
      for (int e = 0; e < 8; ++e) o[e] = oo[e];
      ls = lowhalf(oo[8]);
    }
    const float rl = __builtin_amdgcn_rcpf(ls);
    gst16_sc1(p.ws + WS_O + fo + kh * 1024, vo, u32x4{pk(o[0] * rl, o[1] * rl), pk(o[2] * rl, o[3] * rl), pk(o[4] * rl, o[5] * rl), pk(o[6] * rl, o[7] * rl)});
  }
  asm volatile("s_waitcnt vmcnt(0)\n\ts_barrier"
               : "+v"(wf[0]), "+v"(wf[1]), "+v"(wf[2]), "+v"(wf[3]), "+v"(wf[4]), "+v"(wf[5]), "+v"(wf[6]), "+v"(wf[7]), "+v"(wf[8]), "+v"(wf[9]),
                 "+v"(wf[10]), "+v"(wf[11]), "+v"(wf[12]), "+v"(wf[13]), "+v"(wf[14]), "+v"(wf[15]), "+v"(xa), "+v"(xb), "+v"(ga), "+v"(gb)::"memory");
  STAMP(3)
  if (w == 0) {
    if (l == 0) put_flag(p.ws + WS_FO + g * 256 + j * 4, tag);
    poll_flags<8>(p.ws + WS_FO + g * 256, tag, l);
    set_m0(JUNK); touch8k(p.ws + WS_O + (size_t)g * 65536 + j * 8192, l * 128);
  }
  BAR();
  STAMP(4)
  u32x4 x1;
  {
    const char* osrc = p.ws + WS_O + ((size_t)(2 * g + tt) * 32 + 8 * kq) * 1024;
    u32x4 xf[8];
    xf[0] = gld_s<1, 0>(osrc, vo); xf[1] = gld_s<1, 1024>(osrc, vo); xf[2] = gld_s<1, 2048>(osrc, vo); xf[3] = gld_s<1, 3072>(osrc, vo);
    xf[4] = gld_s<1, 0>(osrc + 4096, vo); xf[5] = gld_s<1, 1024>(osrc + 4096, vo); xf[6] = gld_s<1, 2048>(osrc + 4096, vo); xf[7] = gld_s<1, 3072>(osrc + 4096, vo);
    f32x16 acc0 = {}, acc1 = {};
#define P3_STEP(S) { waitv_pin<7 - (S)>(xf[S]); acc0 = mfma(wf[S], xf[S], acc0); acc1 = mfma(wf[8 + (S)], xf[S], acc1); }
    P3_STEP(0) P3_STEP(1) P3_STEP(2) P3_STEP(3) P3_STEP(4) P3_STEP(5) P3_STEP(6) P3_STEP(7)
#undef P3_STEP
    STAMP(5)
    float r[8];
    reduce4(acc0, acc1, tt, kq, SL3, vo, r);
    STAMP(6)
    x1 = u32x4{pk(fmaf(bflo(ga[0]), r[0], bflo(xa[0])), fmaf(bfhi(ga[0]), r[1], bfhi(xa[0]))), pk(fmaf(bflo(ga[2]), r[2], bflo(xa[1])), fmaf(bfhi(ga[2]), r[3], bfhi(xa[1]))),
               pk(fmaf(bflo(gb[0]), r[4], bflo(xb[0])), fmaf(bfhi(gb[0]), r[5], bfhi(xb[0]))), pk(fmaf(bflo(gb[2]), r[6], bflo(xb[1])), fmaf(bfhi(gb[2]), r[7], bfhi(xb[1])))};
    gst16_sc1(p.ws + WS_X1 + fo + kh * 1024, vo, x1);
    if (w != 0) dma4s(wgu + 2 * 32768 + w * 4096, vo, SL2 + w * 4096);
    float s1 = 0.f, s2 = 0.f;
#pragma unroll
    for (int e = 0; e < 4; ++e) { const u32 v = x1[e]; s1 = dot2(v, 0x3f803f80u, s1); s2 = dot2(v, v, s2); }
    s1 = swap32_sum(s1); s2 = swap32_sum(s2);
    if (l < 32) *(LAS f32x2*)(uintptr_t)(GAPA + kq * 512 + tt * 256 + l * 8) = f32x2{s1, s2};
    BAR();
    STAMP(7)
    if (w == 0) {
      f32x2 s = {0.f, 0.f};
#pragma unroll
      for (int i = 0; i < 4; ++i) s += ldsf2(GAPA + i * 512 + l * 8);
      gst8_sc1(p.ws + WS_STAT + ((size_t)64 * g * 8 + j) * 8, l * 64, u32x2{__float_as_uint(s[0]), __float_as_uint(s[1])});
      dma4s(wgu + 2 * 32768, vo, SL2);
    }
    dma4s(wgu + 3 * 32768 + w * 4096, vo, SL3 + w * 4096);
    WAITV_BAR(8);
    STAMP(8)
    if (w == 0 && l == 0) put_flag(p.ws + WS_FX + (g >> 1) * 256 + ((g & 1) * 8 + j) * 4, tag);
  }
  u32x4 wr[9][2]; u32 fe;
  if (kh == 0) p4_body<0>(p, id, l, w, tag, wr, fe STAMP_PASS); else p4_body<1>(p, id, l, w, tag, wr, fe STAMP_PASS);
  {
    int ly = l; PIN(ly);
    asm volatile("s_waitcnt vmcnt(0)"
                 : "+v"(wr[0][0]), "+v"(wr[0][1]), "+v"(wr[1][0]), "+v"(wr[1][1]), "+v"(wr[2][0]), "+v"(wr[2][1]), "+v"(wr[3][0]), "+v"(wr[3][1]), "+v"(wr[4][0]),
                   "+v"(wr[4][1]), "+v"(wr[5][0]), "+v"(wr[5][1]), "+v"(wr[6][0]), "+v"(wr[6][1]), "+v"(wr[7][0]), "+v"(wr[7][1]), "+v"(wr[8][0]), "+v"(wr[8][1]), "+v"(fe)::"memory");
    if (!__all(fe == tag)) poll_flags<4>(p.ws + WS_FE + (g >> 1) * 256 + kq * 16, tag, ly);
    STAMP(19)
    const char* xsrc = p.ws + WS_ACT + ((size_t)(2 * g + tt) * 112 + 28 * kq) * 1024;
    u32x4 xf[20]; u32 fa;
#define XL(I) (xf[(I) % 20] = gld_s<1, 0>(xsrc + dks(I) * 1024, vo))
#define XL4(I) { xf[I] = gld_s<1, 0>(xsrc + dks(I) * 1024, vo); xf[I + 1] = gld_s<1, 1024>(xsrc + dks(I) * 1024, vo); xf[I + 2] = gld_s<1, 2048>(xsrc + dks(I) * 1024, vo); xf[I + 3] = gld_s<1, 3072>(xsrc + dks(I) * 1024, vo); }
    XL4(0) XL4(4)
    asm volatile("global_load_dword %0, %1, %2 sc1" : "=v"(fa) : "v"((ly & 3) * 4), "s"(p.ws + WS_FA + (g >> 1) * 256 + kq * 16) : "memory");
    XL4(8) XL4(12)
    u32x4 g0 = gld_s<1, 0>(modr + 10240 + (64 * j + 32 * nt + 16 * kh) * 4, (ly >> 5) * 16), g1 = gld_s<1, 32>(modr + 10240 + (64 * j + 32 * nt + 16 * kh) * 4, (ly >> 5) * 16);
    f32x16 acc0, acc1; u32x4 wa[19], wb[19];
    u32 blo = kq * 8192 + vo, bmid = blo + 65536, bhi = TOPA + kq * 6144 + vo;
    PIN(blo); PIN(bmid); PIN(bhi);
#define K5_W(S, NT) ((S) < 16 ? ldsr<((S) < 4 ? SL3 : (S) < 8 ? SL0 : (S) < 12 ? SL1 : SL2) + ((S) & 3) * 1024 + (NT) * 4096>(blo, bmid) : lds128(bhi + ((S) & 3) * 1024 + (NT) * 3072))
#define K5_RD(S) { if ((S) < 19) { wa[(S) < 19 ? (S) : 0] = K5_W(S, 0); wb[(S) < 19 ? (S) : 0] = K5_W(S, 1); } }
#define K5_STEP(S, N, RDN)                                                                           \
  {                                                                                                  \
    waitv_pin<N>(xf[(S) % 20]);                                                                      \
    if ((S) == 0) mf0(acc0, wa[0], xf[0]); else mfa(acc0, (S) < 19 ? wa[(S) < 19 ? (S) : 0] : wr[(S) < 19 ? 0 : (S) - 19][0], xf[(S) % 20]); \
    if (RDN) K5_RD((S) + 2)                                                                          \
    if ((S) == 0) mf0(acc1, wb[0], xf[0]); else mfa(acc1, (S) < 19 ? wb[(S) < 19 ? (S) : 0] : wr[(S) < 19 ? 0 : (S) - 19][1], xf[(S) % 20]); \
  }
    K5_RD(0) K5_RD(1)
    K5_STEP(0, 18, 1) K5_STEP(1, 17, 1) K5_STEP(2, 16, 1) K5_STEP(3, 15, 1) K5_STEP(4, 14, 1) K5_STEP(5, 13, 1) K5_STEP(6, 12, 1) K5_STEP(7, 11, 1)
    asm volatile("s_waitcnt vmcnt(10)" : "+v"(fa)::"memory");
    if (!__all(fa == tag)) poll_flags<4>(p.ws + WS_FA + (g >> 1) * 256 + kq * 16, tag, ly);
    XL(16); XL(17); XL(18); XL(19); XL(20); XL(21); XL(22); XL(23); XL(24); XL(25); XL(26); XL(27);
    K5_STEP(8, 21, 1) K5_STEP(9, 20, 1)
    asm volatile("s_barrier" ::: "memory");
    K5_RD(12) K5_RD(13)
    K5_STEP(10, 19, 0) K5_STEP(11, 18, 0) K5_STEP(12, 17, 1) K5_STEP(13, 16, 1) K5_STEP(14, 15, 1) K5_STEP(15, 14, 1)
    K5_STEP(16, 11, 1) K5_STEP(17, 10, 0) K5_STEP(18, 9, 0)
    BAR();
    K5_STEP(19, 8, 0) K5_STEP(20, 7, 0) K5_STEP(21, 6, 0) K5_STEP(22, 5, 0) K5_STEP(23, 4, 0) K5_STEP(24, 3, 0) K5_STEP(25, 2, 0) K5_STEP(26, 1, 0) K5_STEP(27, 0, 0)
#undef K5_STEP
#undef K5_RD
#undef K5_W
    MFMA_SETTLE();
    PIN(g0); PIN(g1); PIN(acc0); PIN(acc1);
    STAMP(20)
    float r[8];
    reduce4(acc0, acc1, tt, kq, 0, vo, r);
    const u32 a0 = pk(fmaf(bflo(g0[0]), r[0], bflo(x1[0])), fmaf(bfhi(g0[0]), r[1], bfhi(x1[0]))), a1 = pk(fmaf(bflo(g0[2]), r[2], bflo(x1[1])), fmaf(bfhi(g0[2]), r[3], bfhi(x1[1])));
    const u32 b0 = pk(fmaf(bflo(g1[0]), r[4], bflo(x1[2])), fmaf(bfhi(g1[0]), r[5], bfhi(x1[2]))), b1 = pk(fmaf(bflo(g1[2]), r[6], bflo(x1[3])), fmaf(bfhi(g1[2]), r[7], bfhi(x1[3])));
    const auto s0 = __builtin_amdgcn_permlane32_swap(a0, b0, false, false);
    const auto s1 = __builtin_amdgcn_permlane32_swap(a1, b1, false, false);
    int lz = l; PIN(lz);
    *(u32x4*)(p.out + (size_t)(64 * g + 32 * tt + (lz & 31)) * DM + 64 * j + 32 * nt + 16 * kh + 8 * (lz >> 5)) = u32x4{s0[0], s1[0], s0[1], s1[1]};
  }
  STAMP_FLUSH(0)
}

extern "C" void kernel_launch(void* const* d_in, const int* in_sizes, int n_in, void* d_out, int out_size, void* d_ws, size_t ws_size, hipStream_t stream) {
  Params p;
  p.x = (const u16*)d_in[0]; p.wq = (const u16*)d_in[1]; p.wk = (const u16*)d_in[2]; p.wv = (const u16*)d_in[3]; p.wo = (const u16*)d_in[4];
  p.wg = (const u16*)d_in[6]; p.wu = (const u16*)d_in[7]; p.wd = (const u16*)d_in[8]; p.c = (const u16*)d_in[9]; p.wm = (const u16*)d_in[10];
  p.out = (u16*)d_out; p.ws = (char*)d_ws;
  (void)hipFuncSetAttribute((const void*)k_main, hipFuncAttributeMaxDynamicSharedMemorySize, M_LDS);
  k_main<<<256, 512, M_LDS, stream>>>(p);
}
```

```cpp
#define XSEL 99
#include <hip/hip_runtime.h>
#include <stdint.h>

typedef unsigned short u16;
typedef unsigned int u32;
typedef __attribute__((ext_vector_type(8))) __bf16 bf16x8;
typedef __attribute__((ext_vector_type(2))) __bf16 bf16x2;
typedef __attribute__((ext_vector_type(2))) float f32x2;
typedef __attribute__((ext_vector_type(4))) float f32x4;
typedef __attribute__((ext_vector_type(16))) float f32x16;
typedef __attribute__((ext_vector_type(4))) u32 u32x4;
typedef __attribute__((ext_vector_type(2))) u32 u32x2;
typedef __attribute__((ext_vector_type(4))) short s16x4;
#define LAS __attribute__((address_space(3)))
#define GAS __attribute__((address_space(1)))
#define DI __device__ __forceinline__

constexpr int DM = 512, FF = 1792;
constexpr float EPS = 1e-5f;
constexpr float C2 = 0.25f * 1.4426950408889634f;

constexpr size_t WS_EPOCH = 0;
constexpr size_t WS_FO = 4096;
constexpr size_t WS_FX = WS_FO + 8192;
constexpr size_t WS_FA = WS_FX + 4096;
constexpr size_t WS_FE = WS_FA + 128;
constexpr size_t WS_FKV = WS_FA + 4096;
constexpr size_t WS_FP = WS_FKV + 4096;
constexpr size_t WS_MOD = 65536;
constexpr size_t WS_KV = WS_MOD + 8 * 12288;
constexpr size_t WS_STAT = WS_KV + 139264;
constexpr size_t WS_AUG = WS_STAT + 131072;
constexpr size_t WS_WO = 1u << 20;
constexpr size_t WS_WD = WS_WO + (512u << 10);
constexpr size_t WS_WGU = WS_WD + (1792u << 10);
constexpr size_t WS_O = WS_WGU + (3584u << 10);
constexpr size_t WS_X1 = WS_O + (2u << 20);
constexpr size_t WS_ACT = WS_X1 + (2u << 20);
static_assert(WS_AUG + 229376 <= WS_WO, "workspace map");
constexpr int TS = 2176;

struct Params {
  const u16 *x, *wq, *wk, *wv, *wo, *wg, *wu, *wd, *c, *wm;
  u16* out;
  char* ws;
};

DI f32x16 mfma(u32x4 a, u32x4 b, f32x16 c) {
  return __builtin_amdgcn_mfma_f32_32x32x16_bf16(__builtin_bit_cast(bf16x8, a), __builtin_bit_cast(bf16x8, b), c, 0, 0, 0);
}
DI u32 pk(float lo, float hi) {
  f32x2 v = {lo, hi};
  return __builtin_bit_cast(u32, __builtin_convertvector(v, bf16x2));
}
DI float bflo(u32 u) { return __uint_as_float(u << 16); }
DI float bfhi(u32 u) { return __uint_as_float(u & 0xffff0000u); }
DI float rbf(float f) { return bflo(pk(f, 0.f)); }
DI float dot2(u32 a, u32 b, float c) { return __builtin_amdgcn_fdot2_f32_bf16(__builtin_bit_cast(bf16x2, a), __builtin_bit_cast(bf16x2, b), c, false); }
DI void set_m0(u32 lds) { asm volatile("s_mov_b32 m0, %0\n\ts_nop 0" ::"s"(lds) : "memory"); }
template <int OFF> DI void dma(const void* base, u32 vo) { asm volatile("global_load_lds_dwordx4 %0, %1 offset:%2" ::"v"(vo), "s"(base), "n"(OFF) : "memory"); }
template <int OFF> DI void dma1(const void* base, u32 vo) { asm volatile("global_load_lds_dwordx4 %0, %1 offset:%2 sc1" ::"v"(vo), "s"(base), "n"(OFF) : "memory"); }
DI void dma4(const void* base, u32 vo, u32 lds) { set_m0(lds); dma<0>(base, vo); dma<1024>(base, vo); dma<2048>(base, vo); dma<3072>(base, vo); }
DI void dma4s(const void* base, u32 vo, u32 lds) { set_m0(lds); dma1<0>(base, vo); dma1<1024>(base, vo); dma1<2048>(base, vo); dma1<3072>(base, vo); }
template <int SC, int OFF> DI u32x4 gld_s(const void* base, u32 vo) {
  u32x4 r;
  if (SC) asm volatile("global_load_dwordx4 %0, %1, %2 offset:%3 sc1" : "=v"(r) : "v"(vo), "s"(base), "n"(OFF) : "memory");
  else asm volatile("global_load_dwordx4 %0, %1, %2 offset:%3" : "=v"(r) : "v"(vo), "s"(base), "n"(OFF) : "memory");
  return r;
}
template <int OFF> DI u32x2 gld8_s(const void* base, u32 vo) { u32x2 r; asm volatile("global_load_dwordx2 %0, %1, %2 offset:%3" : "=v"(r) : "v"(vo), "s"(base), "n"(OFF) : "memory"); return r; }
template <int OFF> DI u32 gld2_s(const void* base, u32 vo) { u32 r; asm volatile("global_load_ushort %0, %1, %2 offset:%3" : "=v"(r) : "v"(vo), "s"(base), "n"(OFF) : "memory"); return r; }
DI void touch8k(const char* base, u32 l128) { asm volatile("global_load_lds_dword %0, %1 sc1" ::"v"(l128), "s"(base) : "memory"); }
DI void gst16_sc1(char* base, u32 vo, u32x4 v) { asm volatile("global_store_dwordx4 %0, %1, %2 sc1\n\ts_nop 1" ::"v"(vo), "v"(v), "s"(base) : "memory"); }
DI void gst8_sc1(char* base, u32 vo, u32x2 v) { asm volatile("global_store_dwordx2 %0, %1, %2 sc1\n\ts_nop 1" ::"v"(vo), "v"(v), "s"(base) : "memory"); }
#define RLX_AGENT __ATOMIC_RELAXED, __HIP_MEMORY_SCOPE_AGENT
DI void put_flag(char* f, u32 tag) { __hip_atomic_store((GAS u32*)f, tag, RLX_AGENT); }
constexpr int SPIN_CAP = 1 << 20;
template <int N> DI void poll_flags(const char* f, u32 tag, int l) {
#pragma unroll 1
  for (int it = 0; it < SPIN_CAP; ++it) {
    const u32 v = __hip_atomic_load((const GAS u32*)f + (l < N ? l : 0), RLX_AGENT);
    if (__all(v == tag)) break;
    __builtin_amdgcn_s_sleep(1);
  }
  __builtin_amdgcn_fence(__ATOMIC_ACQUIRE, "wavefront");
}
#define SB() __builtin_amdgcn_sched_barrier(0)
#define PIN(x) asm volatile("" : "+v"(x))
#define WAITV(N) asm volatile("s_waitcnt vmcnt(" #N ")" ::: "memory")
#define WAITV_BAR(N) asm volatile("s_waitcnt vmcnt(" #N ") lgkmcnt(0)\n\ts_barrier" ::: "memory")
#define BAR() asm volatile("s_waitcnt lgkmcnt(0)\n\ts_barrier" ::: "memory")
template <int N> DI void waitv_pin(u32x4& v) { asm volatile("s_waitcnt vmcnt(%1)" : "+v"(v) : "n"(N) : "memory"); }
DI u32x4 lds128(u32 a) { return *(const LAS u32x4*)(uintptr_t)a; }
DI f32x4 ldsf4(u32 a) { return *(const LAS f32x4*)(uintptr_t)a; }
DI f32x2 ldsf2(u32 a) { return *(const LAS f32x2*)(uintptr_t)a; }
DI void sts128(u32 a, u32x4 v) { *(LAS u32x4*)(uintptr_t)a = v; }
DI void stsf4(u32 a, f32x4 v) { *(LAS f32x4*)(uintptr_t)a = v; }
DI u32x2 ldstr(u32 a) { return __builtin_bit_cast(u32x2, __builtin_amdgcn_ds_read_tr16_b64_v4i16((LAS s16x4*)(uintptr_t)a)); }
DI u32x4 ldstr2(u32 a0, u32 a1) { u32x2 p = ldstr(a0), q = ldstr(a1); return u32x4{p[0], p[1], q[0], q[1]}; }
template <int CTRL> DI float dpp(float v) { return __int_as_float(__builtin_amdgcn_mov_dpp(__float_as_int(v), CTRL, 0xf, 0xf, true)); }
DI float swap32_sum(float v) { auto r = __builtin_amdgcn_permlane32_swap(__float_as_uint(v), __float_as_uint(v), false, false); return __uint_as_float(r[0]) + __uint_as_float(r[1]); }
DI float swap32_max(float v) { auto r = __builtin_amdgcn_permlane32_swap(__float_as_uint(v), __float_as_uint(v), false, false); return fmaxf(__uint_as_float(r[0]), __uint_as_float(r[1])); }
DI float swap16_sum(float v) { auto r = __builtin_amdgcn_permlane16_swap(__float_as_uint(v), __float_as_uint(v), false, false); return __uint_as_float(r[0]) + __uint_as_float(r[1]); }
DI float lowhalf(float v) { auto r = __builtin_amdgcn_permlane32_swap(__float_as_uint(v), __float_as_uint(v), false, false); return __uint_as_float(r[0]); }
template <int CTRL> DI float halve(bool bit, float a, float b) { return (bit ? b : a) + dpp<CTRL>(bit ? a : b); }
DI int rfl(int v) { return __builtin_amdgcn_readfirstlane(v); }
#define EXP2(v) __builtin_amdgcn_exp2f(v)

struct Ids { int G, c, g, j; };
DI Ids ids() { const int b = blockIdx.x, x = b & 7, i = b >> 3; Ids r; r.G = (x >> 1) * 4 + (i >> 3); r.c = (x & 1) * 8 + (i & 7); r.g = 4 * x + (i >> 3); r.j = i & 7; return r; }

#define STAMP_DECL const unsigned long long xt0 = __builtin_amdgcn_s_memrealtime(); unsigned long long xt1 = xt0;
#define STAMP(I) { if ((I) == XSEL) xt1 = __builtin_amdgcn_s_memrealtime(); }
#define STAMP_FLUSH(K) { STAMP(30) const unsigned long long lim = 7000ull + ((blockIdx.x == 17 && threadIdx.x >= 448) ? xt1 - xt0 : 0ull); while (__builtin_amdgcn_s_memrealtime() - xt0 < lim) __builtin_amdgcn_s_sleep(1); }
#define STAMP_ARG , unsigned long long& xt1
#define STAMP_PASS , xt1

#define ASTAMP(I)
constexpr u32 M_LDS = 163840;
constexpr u32 F_A = 0;
constexpr u32 F_H = 65536;
constexpr u32 F_B = 131072;
constexpr u32 F_D = F_B + 16640;
constexpr u32 F_M = F_D + 10240;
static_assert(F_M + 4608 <= M_LDS, "front-end LDS map");
constexpr u32 SL0 = 0, SL1 = 69632, SL2 = 32768, SL3 = 102400, TOPA = 135168, GAPA = 65536, OEX = 143616, JUNK = M_LDS - 256;

DI void mfa(f32x16& c, u32x4 a, u32x4 b) { asm volatile("v_mfma_f32_32x32x16_bf16 %0, %1, %2, %0" : "+v"(c) : "v"(a), "v"(b) : "memory"); }
DI void mf0(f32x16& c, u32x4 a, u32x4 b) { asm volatile("v_mfma_f32_32x32x16_bf16 %0, %1, %2, 0" : "=&v"(c) : "v"(a), "v"(b) : "memory"); }
DI float vexp(float a) { float r; asm volatile("v_exp_f32 %0, %1" : "=v"(r) : "v"(a)); return r; }
DI float vrcp(float a) { float r; asm volatile("v_rcp_f32 %0, %1" : "=v"(r) : "v"(a)); return r; }
DI u32 vcvt(float a, float b) { u32 r; asm volatile("v_cvt_pk_bf16_f32 %0, %1, %2" : "=v"(r) : "v"(a), "v"(b)); return r; }
DI float vadd(float a, float b) { float r; asm volatile("v_add_f32 %0, %1, %2" : "=v"(r) : "v"(a), "v"(b)); return r; }
DI float vmul(float a, float b) { float r; asm volatile("v_mul_f32 %0, %1, %2" : "=v"(r) : "v"(a), "v"(b)); return r; }
DI float vfma_k(float a, float k, float c) { float r; asm volatile("v_fma_f32 %0, %1, %2, %3" : "=v"(r) : "v"(a), "s"(k), "v"(c)); return r; }
DI float vadd1(float a) { float r; asm volatile("v_add_f32 %0, 1.0, %1" : "=v"(r) : "v"(a)); return r; }
#define MFMA_SETTLE() asm volatile("s_nop 15\n\ts_nop 3" ::: "memory")
DI float rowmax16(const f32x16& s) {
  float m = fmaxf(fmaxf(s[0], s[1]), s[2]);
#pragma unroll
  for (int r = 3; r < 15; r += 2) m = fmaxf(fmaxf(m, s[r]), s[r + 1]);
  return swap32_max(fmaxf(m, s[15]));
}
constexpr float GC0 = -2.f * 0.7978845608028654f * 1.4426950408889634f, GC1 = GC0 * 0.044715f;
DI u32 exbuf(int i) { return i < 7 ? TOPA + i * 4096 : GAPA; }
template <u32 A> DI u32x4 ldsr(u32 lo, u32 mid) { return A < 65536 ? lds128(lo + A) : lds128(mid + (A - 65536)); }

DI void reduce4(const f32x16& acc0, const f32x16& acc1, int tt, int kq, u32 buf, u32 vo, float (&r)[8]) {
#pragma unroll
  for (int pc = 0; pc < 4; ++pc) {
    const f32x16& a = (pc & 1) ? acc1 : acc0;
    const int o8 = (pc >> 1) * 8;
    if (pc != kq) {
      const u32 d = buf + ((tt * 4 + pc) * 3 + ((kq - pc - 1) & 3)) * 2048 + vo;
      stsf4(d, f32x4{a[o8], a[o8 + 1], a[o8 + 2], a[o8 + 3]}); stsf4(d + 1024, f32x4{a[o8 + 4], a[o8 + 5], a[o8 + 6], a[o8 + 7]});
    } else {
#pragma unroll
      for (int e = 0; e < 8; ++e) r[e] = a[o8 + e];
    }
  }
  BAR();
#pragma unroll
  for (int i = 0; i < 3; ++i) {
    const u32 d = buf + ((tt * 4 + kq) * 3 + i) * 2048 + vo;
    const f32x4 u = ldsf4(d), v = ldsf4(d + 1024);
#pragma unroll
    for (int e = 0; e < 4; ++e) { r[e] += u[e]; r[4 + e] += v[e]; }
  }
}

DI constexpr int dks(int i) { return i < 16 ? 7 * (i >> 2) + (i & 3) : 7 * ((i - 16) / 3) + 4 + (i - 16) % 3; }
template <int KH> DI void p4_body(const Params& p, const Ids& id, int l, int w, u32 tag, u32x4 (&wr)[9][2], u32& fe STAMP_ARG) {
  const int tt = w & 3;
  const u32 vo = l * 16;
  const char* wsrc = p.ws + WS_WGU + (size_t)id.c * 7 * 32768 + w * 4096;
  const char* dsrc = p.ws + WS_WD + ((size_t)(2 * id.j + (w & 1)) * 112 + 28 * (w >> 1)) * 1024;
  const char* dsrc0 = p.ws + WS_WD + ((size_t)(2 * id.j) * 112 + 28 * (w >> 1)) * 1024;
  char* adst = p.ws + WS_ACT + ((size_t)(4 * id.G + tt) * 112 + 7 * id.c) * 1024;
  const char* asrc = p.ws + WS_AUG + (size_t)(id.c * 14 + KH) * 1024;
#define WRL(I) (wr[(I) >> 1][(I) & 1] = gld_s<1, 0>(dsrc0 + (dks(19 + ((I) >> 1)) + ((I) & 1) * 112) * 1024, vo))
  u32x4 aug[7];
  aug[0] = gld_s<1, 0>(asrc, vo); aug[1] = gld_s<1, 2048>(asrc, vo); aug[2] = gld_s<1, 0>(asrc + 4096, vo); aug[3] = gld_s<1, 2048>(asrc + 4096, vo);
  WRL(0); WRL(1); WRL(2); WRL(3); WRL(4); WRL(5); WRL(6); WRL(7); WRL(8); WRL(9);
  if (w == 0) poll_flags<16>(p.ws + WS_FX + id.G * 256, tag, l);
  WAITV(10);
  asm volatile("s_barrier" : "+v"(aug[0]), "+v"(aug[1]), "+v"(aug[2]), "+v"(aug[3])::"memory");
  STAMP(9)
  const char* xsrc = p.ws + WS_X1 + ((size_t)(4 * id.G + tt) * 32 + 16 * KH) * 1024;
  u32x4 xf[16];
#pragma unroll
  for (int s4 = 0; s4 < 4; ++s4) {
    xf[4 * s4] = gld_s<1, 0>(xsrc + s4 * 4096, vo); xf[4 * s4 + 1] = gld_s<1, 1024>(xsrc + s4 * 4096, vo);
    xf[4 * s4 + 2] = gld_s<1, 2048>(xsrc + s4 * 4096, vo); xf[4 * s4 + 3] = gld_s<1, 3072>(xsrc + s4 * 4096, vo);
  }
  const char* ssrc = p.ws + WS_STAT + (size_t)(128 * id.G + 32 * tt) * 64;
  u32x4 st0 = gld_s<1, 0>(ssrc, (l & 31) * 64), st1 = gld_s<1, 16>(ssrc, (l & 31) * 64), st2 = gld_s<1, 32>(ssrc, (l & 31) * 64), st3 = gld_s<1, 48>(ssrc, (l & 31) * 64);
  float rs, gc0 = GC0; u32x4 xaug;
  u32 wlo = KH * 16384 + vo, wmid = wlo + 65536;
  PIN(wlo); PIN(wmid); PIN(gc0);
  f32x16 kp;
#define E4(F) { _Pragma("unroll") for (int e = 0; e < 4; ++e) { F; } }
#define E2(B, F) { _Pragma("unroll") for (int e = B; e < B + 2; ++e) { F; } }
#define UNITG(u, GO, RG, RU, OUT)                                                                                   \
    { if (u == 0) E4(gv[e] = vadd(kp[GO + e], RG[e]))                                                               \
      if (u == 1) E4(gv[e] = vmul(gv[e], rs))                                                                       \
      if (u == 2) E4(zz[e] = vmul(gv[e], gv[e]))                                                                    \
      if (u == 3) E4(zz[e] = vfma_k(zz[e], GC1, gc0))                                                               \
      if (u == 4) E4(zz[e] = vmul(zz[e], gv[e]))                                                                    \
      if (u == 5) E2(0, zz[e] = vexp(zz[e]))                                                                        \
      if (u == 6) E2(2, zz[e] = vexp(zz[e]))                                                                        \
      if (u == 7) E4(uv[e] = vadd(kp[GO + 4 + e], RU[e]))                                                           \
      if (u == 8) E4(zz[e] = vadd1(zz[e]))                                                                          \
      if (u == 9) E2(0, zz[e] = vrcp(zz[e]))                                                                        \
      if (u == 10) E2(2, zz[e] = vrcp(zz[e]))                                                                       \
      if (u == 11) E4(uv[e] = vmul(uv[e], gv[e]))                                                                   \
      if (u == 12) E4(zz[e] = vmul(zz[e], rs))                                                                      \
      if (u == 13) E4(uv[e] = vmul(uv[e], zz[e]))                                                                   \
      if (u == 14) { OUT[0] = vcvt(uv[0], uv[1]); OUT[1] = vcvt(uv[2], uv[3]); } }
#define UNIT(U) { if ((U) >= 0 && (U) < 15) UNITG((U), 0, r0, r1, q0) else if ((U) >= 15 && (U) < 30) UNITG((U) - 15, 8, r2, r3, q1) }
  constexpr int LG = 6;
#define RD(SL, I) ldsr<SL>(wlo + (I) * 1024, wmid + (I) * 1024)
#define G0(S) ((S) < LG ? (S) : 2 * (S) - LG)
#define STEP(S) { constexpr int s = (S);                                                                           \
      if (s < 16) mfa(a, ra[s & 15], xf[s & 15]);                                                                  \
      if (s + 4 < 16) ra[(s + 4) & 15] = RD(SS, (s + 4) & 15);                                                     \
      if (s == 0) r0 = ldsf4(RB); if (s == 1) r1 = ldsf4(RB + 1024); if (s == 8) r2 = ldsf4(RB + 2048); if (s == 9) r3 = ldsf4(RB + 3072); \
      if (s < 16) { PFM(s); UNIT(G0(s) - 2) }                                                                      \
      if (s >= 18) { constexpr int i4 = 4 * ((s - 18) & 3); stsf4(RB + i4 * 256, f32x4{a[i4], a[i4 + 1], a[i4 + 2], a[i4 + 3]}); } \
      if (s >= LG) mfa(b, rb[(s - LG) & 15], xf[(s - LG) & 15]);                                                   \
      if (s + 3 - LG >= 0 && s + 3 - LG < 16) rb[(s + 3 - LG) & 15] = RD(SK, (s + 3 - LG) & 15);                   \
      if (s >= LG) UNIT((s < 16 ? G0(s) + 1 : 32 - LG + (s - 16)) - 2) }
#define P4_ROUND(AUGS, AUGK, T)                                                                                    \
      f32x16 a, b; u32x4 ra[16], rb[16]; f32x4 r0, r1, r2, r3, gv, uv, zz; u32x2 q0, q1;                           \
      _Pragma("unroll") for (int s = 0; s < 4; ++s) ra[s] = RD(SS, s);                                             \
      mf0(a, AUGS, xaug); mf0(b, AUGK, xaug);                                                                      \
    STEP(0)    STEP(1)    STEP(2)    STEP(3)    STEP(4)    STEP(5)\
    STEP(6)    STEP(7)    STEP(8)    STEP(9)    STEP(10)    STEP(11)\
    STEP(12)    STEP(13)    STEP(14)    STEP(15)    STEP(16)    STEP(17)\
    STEP(18)    STEP(19)    STEP(20)    STEP(21) \
      gst16_sc1(adst + (T) * 1024, vo, u32x4{q0[0], q0[1], q1[0], q1[1]});                                         \
      kp = b;
#define DM4(s, S0, SRC, DST) { if (s == S0) { set_m0(DST); dma1<0>(SRC, vo); } if (s == S0 + 1) dma1<1024>(SRC, vo); if (s == S0 + 2) dma1<2048>(SRC, vo); if (s == S0 + 3) dma1<3072>(SRC, vo); }
  const u32 eA = exbuf(w) + vo, eB = exbuf(w ^ 4) + vo;
  {
    constexpr u32 SS = KH ? SL0 : SL1, SK = KH ? SL1 : SL0;
    f32x16 a, b; u32x4 ra[16], rb[16];
#pragma unroll
    for (int s = 0; s < 3; ++s) { ra[s] = RD(SS, s); rb[s] = RD(SK, s); }
#pragma unroll
    for (int s = 0; s < 16; ++s) {
      if (s == 0) asm volatile("s_waitcnt vmcnt(18)" : "+v"(xf[0]), "+v"(xf[1])::"memory");
      if (s == 2) asm volatile("s_waitcnt vmcnt(16)" : "+v"(xf[2]), "+v"(xf[3])::"memory");
      if (s == 4) asm volatile("s_waitcnt vmcnt(12)" : "+v"(xf[4]), "+v"(xf[5]), "+v"(xf[6]), "+v"(xf[7])::"memory");
      if (s == 8) asm volatile("s_waitcnt vmcnt(8)" : "+v"(xf[8]), "+v"(xf[9]), "+v"(xf[10]), "+v"(xf[11])::"memory");
      if (s == 12) asm volatile("s_waitcnt vmcnt(4)" : "+v"(xf[12]), "+v"(xf[13]), "+v"(xf[14]), "+v"(xf[15])::"memory");
      if (s == 0) mf0(a, ra[0], xf[0]); else mfa(a, ra[s], xf[s]);
      if (s + 3 < 16) { ra[s + 3] = RD(SS, s + 3); rb[s + 3] = RD(SK, s + 3); }
      if (s == 0) mf0(b, rb[0], xf[0]); else if (s < 10) mfa(b, rb[s], xf[s]);
    }
    asm volatile("s_waitcnt vmcnt(0)" : "+v"(st0), "+v"(st1), "+v"(st2), "+v"(st3)::"memory");
    const float s1 = (__uint_as_float(st0[0]) + __uint_as_float(st0[2])) + (__uint_as_float(st1[0]) + __uint_as_float(st1[2])) + (__uint_as_float(st2[0]) + __uint_as_float(st2[2])) + (__uint_as_float(st3[0]) + __uint_as_float(st3[2]));
    const float s2 = (__uint_as_float(st0[1]) + __uint_as_float(st0[3])) + (__uint_as_float(st1[1]) + __uint_as_float(st1[3])) + (__uint_as_float(st2[1]) + __uint_as_float(st2[3])) + (__uint_as_float(st3[1]) + __uint_as_float(st3[3]));
    const float nmu = -s1 * (1.f / DM), var = fmaxf(s2 * (1.f / DM) - nmu * nmu, 0.f) + EPS;
    rs = __builtin_amdgcn_rsqf(var);
    const float sg = var * rs, mh = rbf(nmu), sh = rbf(sg);
    xaug = u32x4{pk(mh, mh), pk(nmu - mh, sh), pk(sh, sg - sh), 0u};
    if (l >= 32) xaug = u32x4{0u, 0u, 0u, 0u};
    asm volatile("s_nop 1" : "+v"(xaug), "+v"(rs));
    mfa(a, aug[KH ? 0 : 1], xaug);
    mfa(b, rb[10], xf[10]); mfa(b, rb[11], xf[11]);
    MFMA_SETTLE();
    stsf4(eA, f32x4{a[0], a[1], a[2], a[3]}); mfa(b, rb[12], xf[12]);
    stsf4(eA + 1024, f32x4{a[4], a[5], a[6], a[7]}); mfa(b, rb[13], xf[13]);
    stsf4(eA + 2048, f32x4{a[8], a[9], a[10], a[11]}); mfa(b, rb[14], xf[14]);
    stsf4(eA + 3072, f32x4{a[12], a[13], a[14], a[15]}); mfa(b, rb[15], xf[15]);
    mfa(b, aug[KH ? 1 : 0], xaug);
    kp = b;
  }
  STAMP(11)
  BAR();
  STAMP(12)
  {
    constexpr u32 SS = KH ? SL2 : SL3, SK = KH ? SL3 : SL2;
    const u32 RB = eB;
#define PFM(s) { DM4(s, 0, wsrc + 4 * 32768, SL0 + w * 4096) DM4(s, 5, wsrc + 5 * 32768, SL1 + w * 4096) if (s == 10) aug[4] = gld_s<1, 0>(asrc + 8192, vo); if (s == 11) aug[5] = gld_s<1, 2048>(asrc + 8192, vo); }
    P4_ROUND(aug[KH ? 2 : 3], aug[KH ? 3 : 2], KH)
#undef PFM
  }
  STAMP(13)
  asm volatile("s_waitcnt vmcnt(1) lgkmcnt(0)\n\ts_barrier" : "+v"(aug[4]), "+v"(aug[5])::"memory");
  STAMP(14)
  {
    constexpr u32 SS = KH ? SL0 : SL1, SK = KH ? SL1 : SL0;
    const u32 RB = eA;
#define PFM(s) { DM4(s, 0, wsrc + 6 * 32768, SL2 + w * 4096) if (s == 5) aug[6] = gld_s<1, 0>(asrc + 12288, vo); DM4(s, 8, dsrc, SL3 + w * 4096) }
    P4_ROUND(aug[KH ? 4 : 5], aug[KH ? 5 : 4], 2 + KH)
#undef PFM
  }
  STAMP(15)
  asm volatile("s_waitcnt vmcnt(5) lgkmcnt(0)\n\ts_barrier" : "+v"(aug[6])::"memory");
  STAMP(16)
  {
    f32x16 a; u32x4 ra[16]; f32x4 r0, r1, r2, r3, gv, uv, zz; u32x2 q0, q1;
#pragma unroll
    for (int s = 0; s < 4; ++s) ra[s] = RD(SL2, s);
    r0 = ldsf4(eB); r1 = ldsf4(eB + 1024);
    mf0(a, aug[6], xaug);
#define STEP3(S) { constexpr int s = (S); mfa(a, ra[s], xf[s]); if (s + 4 < 16) ra[(s + 4) & 15] = RD(SL2, (s + 4) & 15);            \
      if (s == 4) r2 = ldsf4(eB + 2048); if (s == 5) r3 = ldsf4(eB + 3072);                                                         \
      DM4(s, 0, dsrc + 7168, SL0 + w * 4096) DM4(s, 6, dsrc + 14336, SL1 + w * 4096) if (s >= 12) WRL(s >= 12 ? s - 2 : 0); UNIT(s - 1) }
    STEP3(0) STEP3(1) STEP3(2) STEP3(3) STEP3(4) STEP3(5) STEP3(6) STEP3(7) STEP3(8) STEP3(9) STEP3(10) STEP3(11) STEP3(12) STEP3(13) STEP3(14) STEP3(15)
#undef STEP3
    constexpr int K8 = KH * 8, S8 = 8 - K8;
    UNIT(15) UNIT(16) UNIT(17) UNIT(18) UNIT(19)
    MFMA_SETTLE();
    stsf4(eB, f32x4{a[S8], a[S8 + 1], a[S8 + 2], a[S8 + 3]}); stsf4(eB + 1024, f32x4{a[S8 + 4], a[S8 + 5], a[S8 + 6], a[S8 + 7]});
    WAITV_BAR(12);
    if (w == 0 && l == 0) put_flag(p.ws + WS_FE + id.G * 256 + id.c * 4, tag);
    f32x4 h0 = ldsf4(eA), h1 = ldsf4(eA + 1024);
    UNIT(20) UNIT(21) UNIT(22) UNIT(23) UNIT(24) UNIT(25) UNIT(26) UNIT(27) UNIT(28) UNIT(29)
    gst16_sc1(adst + (4 + KH) * 1024, vo, u32x4{q0[0], q0[1], q1[0], q1[1]});
    kp = a;
#define UL(U) UNITG(U, K8, h0, h1, q0)
    UL(0) UL(1) UL(2) UL(3) UL(4) UL(5) UL(6) UL(7) UL(8) UL(9) UL(10) UL(11) UL(12) UL(13) UL(14)
#undef UL
    gst8_sc1(adst + 6 * 1024 + KH * 8, vo, q0);
  }
  STAMP(17)
  BAR();
  dma4s(dsrc + 21504, vo, SL2 + w * 4096);
  set_m0(TOPA + w * 3072); dma1<0>(dsrc + 4096, vo); dma1<1024>(dsrc + 4096, vo); dma1<2048>(dsrc + 4096, vo);
  WRL(14); WRL(15); WRL(16); WRL(17);
  asm volatile("global_load_dword %0, %1, %2 sc1" : "=v"(fe) : "v"((l & 3) * 4), "s"(p.ws + WS_FE + (id.g >> 1) * 256 + (w >> 1) * 16) : "memory");
  WAITV_BAR(12);
  STAMP(18)
  if (w == 0 && l == 0) put_flag(p.ws + WS_FA + id.G * 256 + id.c * 4, tag);
#undef P4_ROUND
#undef STEP
#undef UNIT
#undef UNITG
}

__global__ __launch_bounds__(512, 2) void k_main(Params p) {
  extern __shared__ __attribute__((aligned(16))) char smem[];
  const int tid = threadIdx.x, l = tid & 63, w = rfl(tid >> 6);
  const Ids id = ids();
  const int g = id.g, j = id.j, h = l >> 5;
  const u32 vo = l * 16;
  const u32 epoch = *(const u32*)(p.ws + WS_EPOCH);
  const u32 tag = epoch * 0x9E3779B1u + 0x7F4A7C15u;
  const int tt = w & 1, nt = (w >> 1) & 1, kh = w >> 2, kq = w >> 1;
  const char* modr = p.ws + WS_MOD + (blockIdx.x & 7) * 12288;
  STAMP_DECL STAMP(0)
  u32x4 q0, q1;
  {
    const int nh = (l >> 4) & 1, q = (l & 15) >> 2, pp = l & 3;
    const int uid = g * 7 + j - 1, uT = uid >> 1, ukh = uid & 1;
    const int rk = (blockIdx.x >> 6) * 7 + j - 1;
    const bool gemv = j != 0 && rk < 24;
    const int mu = 4 * ((blockIdx.x & 7) + 8 * (rk >> 2)) + (rk & 3);
    const u32 rawo = (l >> 3) * 1024 + (((l & 7) ^ (((l >> 4) & 1) << 2)) * 16);
    u32x4 w0, w1; u32 c0, c1;
    if (gemv) {
      const u32 mo = (tid >> 1) * 6144 + (tid & 1) * 16;
      w0 = gld_s<0, 0>(p.wm + 16 * mu, mo); w1 = gld_s<0, 0>(p.wm + 16 * mu + 256 * 3072, mo);
      c0 = gld2_s<0>(p.c, (tid >> 1) * 2); c1 = gld2_s<512>(p.c, (tid >> 1) * 2);
      STAMP(10)
      asm volatile("s_waitcnt vmcnt(0)" : "+v"(w0), "+v"(w1), "+v"(c0), "+v"(c1)::"memory");
    }
    u32x4 xv[8];
    {
      const char* xs = (const char*)(p.x + (size_t)(64 * g + 8 * w) * DM);
      xv[0] = gld_s<0, 0>(xs, vo); xv[1] = gld_s<0, 1024>(xs, vo); xv[2] = gld_s<0, 2048>(xs, vo); xv[3] = gld_s<0, 3072>(xs, vo);
      xv[4] = gld_s<0, 0>(xs + 4096, vo); xv[5] = gld_s<0, 1024>(xs + 4096, vo); xv[6] = gld_s<0, 2048>(xs + 4096, vo); xv[7] = gld_s<0, 3072>(xs + 4096, vo);
    }
    if (j == 0) {
      set_m0(F_H + w * 2048); dma<0>(p.wv + w * 1024, vo); dma<1024>(p.wv + w * 1024, vo);
      set_m0(F_H + 16384 + 128 + w * 2048); dma<0>(p.wk + w * 1024, vo); dma<1024>(p.wk + w * 1024, vo);
    }
    if (gemv) {
      const float f0 = bflo(c0), f1 = bflo(c1);
      const float s0 = rbf(f0 * __builtin_amdgcn_rcpf(1.f + EXP2(-1.4426950408889634f * f0))), s1 = rbf(f1 * __builtin_amdgcn_rcpf(1.f + EXP2(-1.4426950408889634f * f1)));
      float a[8];
#pragma unroll
      for (int i = 0; i < 4; ++i) { a[2 * i] = s0 * bflo(w0[i]) + s1 * bflo(w1[i]); a[2 * i + 1] = s0 * bfhi(w0[i]) + s1 * bfhi(w1[i]); }
#pragma unroll
      for (int i = 0; i < 4; ++i) a[i] = halve<0x4E>(l & 2, a[i], a[i + 4]);
#pragma unroll
      for (int i = 0; i < 2; ++i) a[i] = halve<0x124>(l & 4, a[i], a[i + 2]);
      a[0] = halve<0x128>(l & 8, a[0], a[1]);
      a[0] = swap32_sum(swap16_sum(a[0]));
      if (l < 16) *(LAS float*)(uintptr_t)(F_M + w * 64 + ((l & 1) * 8 + (l & 2) * 2 + ((l >> 1) & 2) + (l >> 3)) * 4) = a[0];
      BAR();
      if (w == 7) {
        f32x2 s = {0.f, 0.f};
#pragma unroll
        for (int i = 0; i < 8; ++i) s += ldsf2(F_M + i * 64 + (l & 7) * 8);
        gst8_sc1(p.ws + WS_MOD + mu * 64, (l >> 3) * 12288 + (l & 7) * 8, u32x2{pk(s[0], s[1]), tag});
      }
    }
    STAMP(22)
    if (j == 0) WAITV(4); else if (gemv && w == 7) WAITV(1); else WAITV(0);
    asm volatile("" : "+v"(xv[0]), "+v"(xv[1]), "+v"(xv[2]), "+v"(xv[3]), "+v"(xv[4]), "+v"(xv[5]), "+v"(xv[6]), "+v"(xv[7]));
#define LATE_DMA() {                                                                                                                              \
      _Pragma("unroll") for (int i = 0; i < 8; ++i) { set_m0(F_A + (8 * w + i) * 1024); dma<0>(p.wq + (size_t)(64 * w + 8 * i) * DM + 64 * j, rawo); } \
      if (j != 0) {                                                                                                                               \
        const u32 go = (l >> 1) * (FF * 2) + (l & 1) * 16;                                                                                        \
        set_m0(F_B + w * 1024); dma<0>(p.wg + (size_t)(256 * ukh + 32 * w) * FF + 16 * uT, go);                                                   \
        set_m0(F_B + 8192 + 128 + w * 1024); dma<0>(p.wu + (size_t)(256 * ukh + 32 * w) * FF + 16 * uT, go);                                      \
        set_m0(F_D + w * 1024); dma<0>(p.wd + (size_t)(64 * (uid >> 3) + 8 * w) * DM + 64 * (uid & 7), rawo);                                     \
      }                                                                                                                                           \
      if (w >= 6) { set_m0(F_D + 8192 + (w - 6) * 1024); dma<0>(p.wo + (size_t)(16 * g + 8 * (w - 6)) * DM + 64 * j, rawo); } }
    if (w > 1 && j != 0) LATE_DMA()
    float xh[8][8];
    {
      float s1[8], s2[8];
#pragma unroll
      for (int i = 0; i < 8; ++i) {
        s1[i] = 0.f; s2[i] = 0.f;
#pragma unroll
        for (int e = 0; e < 4; ++e) { const u32 v = xv[i][e]; s1[i] = dot2(v, 0x3f803f80u, s1[i]); s2[i] = dot2(v, v, s2[i]); }
      }
#pragma unroll
      for (int i = 0; i < 4; ++i) { s1[i] = halve<0xB1>(l & 1, s1[i], s1[i + 4]); s2[i] = halve<0xB1>(l & 1, s2[i], s2[i + 4]); }
#pragma unroll
      for (int i = 0; i < 2; ++i) { s1[i] = halve<0x4E>(l & 2, s1[i], s1[i + 2]); s2[i] = halve<0x4E>(l & 2, s2[i], s2[i + 2]); }
      float t1 = halve<0x124>(l & 4, s1[0], s1[1]), t2 = halve<0x124>(l & 4, s2[0], s2[1]);
      t1 += dpp<0x128>(t1); t2 += dpp<0x128>(t2);
      t1 = swap32_sum(swap16_sum(t1)); t2 = swap32_sum(swap16_sum(t2));
      const float m = t1 * (1.f / DM), r = __builtin_amdgcn_rsqf(fmaxf(t2 * (1.f / DM) - m * m, 0.f) + EPS), nm = -m * r;
#define LNROW(I, LN) { const float ri = dpp<0x150 + (LN)>(r), ni = dpp<0x150 + (LN)>(nm); _Pragma("unroll") for (int e = 0; e < 4; ++e) { xh[I][2 * e] = fmaf(bflo(xv[I][e]), ri, ni); xh[I][2 * e + 1] = fmaf(bfhi(xv[I][e]), ri, ni); } }
      LNROW(0, 0) LNROW(1, 4) LNROW(2, 2) LNROW(3, 6) LNROW(4, 1) LNROW(5, 5) LNROW(6, 3) LNROW(7, 7)
#undef LNROW
#pragma unroll
      for (int i = 0; i < 8; ++i) asm volatile("" : "+v"(xh[i][0]), "+v"(xh[i][1]), "+v"(xh[i][2]), "+v"(xh[i][3]), "+v"(xh[i][4]), "+v"(xh[i][5]), "+v"(xh[i][6]), "+v"(xh[i][7]));
    }
    STAMP(23)
    u32x4 wkvf[16];
    if (j == 0) {
      WAITV_BAR(0);
      const u32 a0 = F_H + nh * (16384 + 128) + (256 * kh + 8 * h + q) * 32 + pp * 8;
#pragma unroll
      for (int s = 0; s < 16; ++s) wkvf[s] = ldstr2(a0 + s * 512, a0 + s * 512 + 128);
      if (w > 1) LATE_DMA()
    }
    STAMP(24)
    if (w == 0) {
      u32x4 a0, a1, b0, b1;
#pragma unroll 1
      for (int it = 0; it < SPIN_CAP; ++it) {
        asm volatile("global_load_dwordx4 %0, %4, %5 sc1\n\tglobal_load_dwordx4 %1, %4, %5 offset:16 sc1\n\tglobal_load_dwordx4 %2, %4, %5 offset:2048 sc1\n\t"
                     "global_load_dwordx4 %3, %4, %5 offset:2064 sc1\n\ts_waitcnt vmcnt(0)" : "=&v"(b0), "=&v"(b1), "=&v"(a0), "=&v"(a1) : "v"(l * 32), "s"(modr) : "memory");
        if (__all(a0[1] == tag && a0[3] == tag && a1[1] == tag && a1[3] == tag && b0[1] == tag && b0[3] == tag && b1[1] == tag && b1[3] == tag)) break;
      }
      sts128(F_M + 512 + vo, u32x4{pk(1.f + bflo(a0[0]), 1.f + bfhi(a0[0])), pk(1.f + bflo(a0[2]), 1.f + bfhi(a0[2])), pk(1.f + bflo(a1[0]), 1.f + bfhi(a1[0])), pk(1.f + bflo(a1[2]), 1.f + bfhi(a1[2]))});
      sts128(F_M + 1536 + vo, u32x4{b0[0], b0[2], b1[0], b1[2]});
    } else if (w == 1) {
      if (j != 0) {
        u32x4 a0, b0;
#pragma unroll 1
        for (int it = 0; it < SPIN_CAP; ++it) {
          asm volatile("global_load_dwordx4 %0, %2, %3 sc1\n\tglobal_load_dwordx4 %1, %2, %3 offset:2048 sc1\n\ts_waitcnt vmcnt(0)" : "=&v"(b0), "=&v"(a0) : "v"(vo), "s"(modr + 6144 + ukh * 1024) : "memory");
          if (__all(a0[1] == tag && a0[3] == tag && b0[1] == tag && b0[3] == tag)) break;
          __builtin_amdgcn_s_sleep(2);
        }
        stsf4(F_M + 2560 + vo, f32x4{rbf(1.f + bflo(a0[0])), rbf(1.f + bfhi(a0[0])), rbf(1.f + bflo(a0[2])), rbf(1.f + bfhi(a0[2]))});
        stsf4(F_M + 3584 + vo, f32x4{bflo(b0[0]), bfhi(b0[0]), bflo(b0[2]), bfhi(b0[2])});
      }
    }
    BAR();
    STAMP(25)
    if (w <= 1) LATE_DMA()
    {
      const u32x4 ca = lds128(F_M + 512 + vo), sa = lds128(F_M + 1536 + vo);
      float A[8], B[8];
#pragma unroll
      for (int e = 0; e < 4; ++e) { A[2 * e] = bflo(ca[e]); A[2 * e + 1] = bfhi(ca[e]); B[2 * e] = bflo(sa[e]); B[2 * e + 1] = bfhi(sa[e]); }
#pragma unroll
      for (int i = 0; i < 8; ++i) {
        u32x4 o;
#pragma unroll
        for (int e = 0; e < 4; ++e) o[e] = pk(fmaf(xh[i][2 * e], A[2 * e], B[2 * e]), fmaf(xh[i][2 * e + 1], A[2 * e + 1], B[2 * e + 1]));
        const int tk = 8 * w + i;
        sts128(F_H + ((tk >> 5) * 32 + (l >> 1)) * 1024 + (l & 1) * 512 + (((tk & 31) ^ (l & 7)) * 16), o);
      }
    }
    BAR();
    STAMP(26)
    u32x4 xf[16];
#pragma unroll
    for (int s = 0; s < 16; ++s) xf[s] = lds128(F_H + (tt * 32 + 16 * kh + s) * 1024 + h * 512 + (((l & 31) ^ ((2 * s + h) & 7)) * 16));
    if (j == 0) {
      const u32 xs = F_B + w * 2048 + vo, xr = F_B + (w ^ 4) * 2048 + vo;
      f32x16 acc = {};
      if (nt == 0) {
#pragma unroll
        for (int s = 0; s < 16; ++s) acc = mfma(wkvf[s], xf[s], acc);
      } else {
#pragma unroll
        for (int s = 0; s < 16; ++s) acc = mfma(xf[s], wkvf[s], acc);
      }
      float keep[8];
      if (kh == 0) { stsf4(xs, f32x4{acc[8], acc[9], acc[10], acc[11]}); stsf4(xs + 1024, f32x4{acc[12], acc[13], acc[14], acc[15]});
#pragma unroll
        for (int e = 0; e < 8; ++e) keep[e] = acc[e];
      } else { stsf4(xs, f32x4{acc[0], acc[1], acc[2], acc[3]}); stsf4(xs + 1024, f32x4{acc[4], acc[5], acc[6], acc[7]});
#pragma unroll
        for (int e = 0; e < 8; ++e) keep[e] = acc[8 + e];
      }
      BAR();
      const f32x4 r0 = ldsf4(xr), r1 = ldsf4(xr + 1024);
      u32x4 o = {pk(keep[0] + r0[0], keep[1] + r0[1]), pk(keep[2] + r0[2], keep[3] + r0[3]), pk(keep[4] + r1[0], keep[5] + r1[1]), pk(keep[6] + r1[2], keep[7] + r1[3])};
      char* img = p.ws + WS_KV + (size_t)(2 * g + tt) * TS;
      if (nt == 0) { if (kh == 1) gst16_sc1(img, vo, o); }
      else if ((l & 31) <= 17) {
        if ((l & 31) == 16) o = u32x4{0x3f803f80u, 0x3f803f80u, 0x3f803f80u, 0x3f803f80u};
        if ((l & 31) == 17) o = u32x4{0u, 0u, 0u, 0u};
        gst16_sc1(img + 1024 + kh * 576, h * 288 + (l & 31) * 16, o);
      }
      WAITV_BAR(0);
      if (w == 0 && l < 32) put_flag(p.ws + WS_FKV + l * 128 + g * 4, tag);
    }
    STAMP(21)
    WAITV_BAR(0);
    const u32 rawr = (((nh * 2 + (pp >> 1)) ^ (((q >> 1) & 1) << 2)) * 16) + (pp & 1) * 8;
    f32x16 acc = {};
    {
      const u32 a0 = F_A + (256 * kh + 8 * h + q) * 128 + (rawr ^ (nt * 64));
#pragma unroll
      for (int s = 0; s < 16; ++s) acc = mfma(ldstr2(a0 + s * 2048, a0 + s * 2048 + 512), xf[s], acc);
    }
    const u32 xs = F_H + w * 4096 + vo, xr = F_H + (w ^ 4) * 4096 + vo;
    stsf4(xs, f32x4{acc[0], acc[1], acc[2], acc[3]}); stsf4(xs + 1024, f32x4{acc[4], acc[5], acc[6], acc[7]});
    stsf4(xs + 2048, f32x4{acc[8], acc[9], acc[10], acc[11]}); stsf4(xs + 3072, f32x4{acc[12], acc[13], acc[14], acc[15]});
    STAMP(27)
    u32x4 fo_, fd_, fg[2]; f32x4 cs[2][4];
    if (w >= 6) { const u32 a0 = F_D + 8192 + (4 * h + q) * 128 + (rawr ^ (tt * 64)); fo_ = ldstr2(a0, a0 + 1024); }
    if (j != 0) {
      const u32 a0 = F_D + (16 * kq + 4 * h + q) * 128 + (rawr ^ (tt * 64));
      fd_ = ldstr2(a0, a0 + 1024);
#pragma unroll
      for (int i = 0; i < 2; ++i) {
        const int s = 2 * w + i;
        const u32 a1 = F_B + (pp >> 1) * (8192 + 128) + (16 * s + 4 * h + q) * 32 + nh * 16 + (pp & 1) * 8;
        fg[i] = ldstr2(a1, a1 + 256);
        cs[i][0] = ldsf4(F_M + 2560 + (16 * s + 4 * h) * 4); cs[i][1] = ldsf4(F_M + 2560 + (16 * s + 8 + 4 * h) * 4);
        cs[i][2] = ldsf4(F_M + 3584 + (16 * s + 4 * h) * 4); cs[i][3] = ldsf4(F_M + 3584 + (16 * s + 8 + 4 * h) * 4);
      }
    }
    if (w == 2) poll_flags<32>(p.ws + WS_FKV + (blockIdx.x & 31) * 128, tag, l);
    if (w == 3) {
#pragma unroll 1
      for (int it = 0; it < SPIN_CAP; ++it) {
        const u32 v = __hip_atomic_load((const GAS u32*)(modr + 4096 + j * 256 + h * 6144 + (l & 31) * 8 + 4), RLX_AGENT);
        if (__all(v == tag)) break;
        __builtin_amdgcn_s_sleep(2);
      }
    }
    BAR();
    STAMP(28)
    const f32x4 r0 = ldsf4(xr), r1 = ldsf4(xr + 1024), r2 = ldsf4(xr + 2048), r3 = ldsf4(xr + 3072);
    q0 = u32x4{pk((acc[0] + r0[0]) * C2, (acc[1] + r0[1]) * C2), pk((acc[2] + r0[2]) * C2, (acc[3] + r0[3]) * C2), pk((acc[4] + r1[0]) * C2, (acc[5] + r1[1]) * C2), pk((acc[6] + r1[2]) * C2, (acc[7] + r1[3]) * C2)};
    q1 = u32x4{pk((acc[8] + r2[0]) * C2, (acc[9] + r2[1]) * C2), pk((acc[10] + r2[2]) * C2, (acc[11] + r2[3]) * C2), pk((acc[12] + r3[0]) * C2, (acc[13] + r3[1]) * C2), pk((acc[14] + r3[2]) * C2, (acc[15] + r3[3]) * C2)};
    BAR();
    STAMP(29)
    { const u32 o = (w & 1) * 69632 + (w >> 1) * 6144;
      dma4s(p.ws + WS_KV + o, vo, o); set_m0(o + 4096); dma1<0>(p.ws + WS_KV + o + 4096, vo); dma1<1024>(p.ws + WS_KV + o + 4096, vo); }
    if (w >= 6) gst16_sc1(p.ws + WS_WO + ((size_t)(2 * j + tt) * 32 + g) * 1024, vo, fo_);
    if (j != 0) {
      gst16_sc1(p.ws + WS_WD + ((size_t)(2 * (uid & 7) + tt) * 112 + 4 * (uid >> 3) + kq) * 1024, vo, fd_);
      float us = 0.f, wsum = 0.f;
#pragma unroll
      for (int i = 0; i < 2; ++i) {
        const u32x4 f = fg[i];
        const f32x4 c0 = cs[i][0], c1 = cs[i][1], s0 = cs[i][2], s1 = cs[i][3];
        u32x4 o;
        o[0] = pk(bflo(f[0]) * c0[0], bfhi(f[0]) * c0[1]); o[1] = pk(bflo(f[1]) * c0[2], bfhi(f[1]) * c0[3]);
        o[2] = pk(bflo(f[2]) * c1[0], bfhi(f[2]) * c1[1]); o[3] = pk(bflo(f[3]) * c1[2], bfhi(f[3]) * c1[3]);
#pragma unroll
        for (int e = 0; e < 4; ++e) { const u32 v = o[e]; us = dot2(v, 0x3f803f80u, us); }
        wsum += bflo(f[0]) * s0[0] + bfhi(f[0]) * s0[1] + bflo(f[1]) * s0[2] + bfhi(f[1]) * s0[3] + bflo(f[2]) * s1[0] + bfhi(f[2]) * s1[1] + bflo(f[3]) * s1[2] + bfhi(f[3]) * s1[3];
        gst16_sc1(p.ws + WS_WGU + ((size_t)uT * 32 + 16 * ukh + 2 * w + i) * 1024, vo, o);
      }
      us = swap32_sum(us); wsum = swap32_sum(wsum);
      if (l < 32) *(LAS f32x2*)(uintptr_t)(OEX + w * 256 + l * 8) = f32x2{us, wsum};
      WAITV_BAR(3);
      if (w == 7) {
        f32x2 s = {0.f, 0.f};
#pragma unroll
        for (int i = 0; i < 8; ++i) s += ldsf2(OEX + i * 256 + (l & 31) * 8);
        const float uh = rbf(s[0]), wh = rbf(s[1]);
        u32x4 o = {pk(uh, s[0] - uh), pk(uh, wh), pk(s[1] - wh, wh), 0u};
        if (l >= 32) o = u32x4{0u, 0u, 0u, 0u};
        gst16_sc1(p.ws + WS_AUG + (size_t)(uT * 2 + ukh) * 1024, vo, o);
      }
    } else {
      WAITV_BAR(0);
    }
  }
  const size_t fo = ((size_t)(2 * g + tt) * 32 + 4 * j + 2 * nt) * 1024;
  STAMP(1)
  f32x16 nm0, nm1;
  {
    const u32x4 k0 = lds128(vo);
    const float m0 = rowmax16(mfma(k0, q0, f32x16{})), m1 = rowmax16(mfma(k0, q1, f32x16{}));
#pragma unroll
    for (int r = 0; r < 16; ++r) { nm0[r] = -m0; nm1[r] = -m1; }
    PIN(nm0); PIN(nm1);
  }
  const u32 vrow = 1024 + h * 288 + min(l & 31, 17) * 16;
  u32 ka = kh * 32 * TS + vo, va = kh * 32 * TS + vrow;
  f32x16 o0 = {}, o1 = {};
  u32x4 kA = lds128(ka), kB = lds128(ka + TS);
  f32x16 s0 = mfma(kA, q0, nm0), s1;
  u32x4 vA0 = lds128(va), vA1 = lds128(va + 576), vB0 = {}, vB1 = {};
  u32x4 pa0, pa1, pb0 = {}, pb1 = {};
  PIN(s0);
  if (kh) {
    const u32 o = nt * 69632 + 24576 + tt * 22528;
#pragma unroll
    for (int i = 0; i < 5; ++i) dma4s(p.ws + WS_KV + o + i * 4096, vo, o + i * 4096);
    set_m0(o + 20480); dma1<0>(p.ws + WS_KV + o + 20480, vo); dma1<1024>(p.ws + WS_KV + o + 20480, vo);
  }
  u32x4 wf[16];
#define HALF(SC, N0, N1, MF, OP, VP0, P0, VP1, P1, LD1, LD2)                                              \
  {                                                                                                       \
    MF; LD1; SB();                                                                                        \
    float e0 = EXP2(SC[0]), e1 = EXP2(SC[1]), e2 = EXP2(SC[2]), e3 = EXP2(SC[3]), e4 = EXP2(SC[4]), e5 = EXP2(SC[5]), e6 = EXP2(SC[6]), e7 = EXP2(SC[7]); SB(); \
    OP = mfma(VP0, P0, OP); SB();                                                                         \
    float e8 = EXP2(SC[8]), e9 = EXP2(SC[9]), e10 = EXP2(SC[10]), e11 = EXP2(SC[11]), e12 = EXP2(SC[12]), e13 = EXP2(SC[13]), e14 = EXP2(SC[14]), e15 = EXP2(SC[15]); SB(); \
    OP = mfma(VP1, P1, OP); LD2; SB();                                                                    \
    N0[0] = pk(e0, e1); N0[1] = pk(e2, e3); N0[2] = pk(e4, e5); N0[3] = pk(e6, e7); N1[0] = pk(e8, e9);   \
    N1[1] = pk(e10, e11); N1[2] = pk(e12, e13); N1[3] = pk(e14, e15); SB();                               \
  }
#define TILE(T, KC, KN, VC0, VC1, VP0, VP1)                                                               \
    HALF(s0, pa0, pa1, s1 = mfma(KC, q1, nm1), o1, VP0, pb0, VP1, pb1, KC = lds128(ka + ((T) + 2) * TS),  \
         (VP0 = lds128(va + ((T) + 1) * TS), VP1 = lds128(va + ((T) + 1) * TS + 576)))                    \
    HALF(s1, pb0, pb1, s0 = mfma(KN, q0, nm0), o0, VC0, pa0, VC1, pa1, , )
  const char* wgu = p.ws + WS_WGU + (size_t)id.c * 7 * 32768;
  const char* wob = p.ws + WS_WO + ((size_t)(2 * j) * 32 + 8 * kq) * 1024;
#define LDWF(SEL)                                                                                                     \
  asm volatile("s_cmp_lg_u32 %21, 0\n\ts_cbranch_scc1 .Lwf%=\n\t"                                                     \
               "global_load_dwordx4 %0, %16, %17 sc1\n\tglobal_load_dwordx4 %1, %16, %17 offset:1024 sc1\n\tglobal_load_dwordx4 %2, %16, %17 offset:2048 sc1\n\tglobal_load_dwordx4 %3, %16, %17 offset:3072 sc1\n\t"     \
               "global_load_dwordx4 %4, %16, %18 sc1\n\tglobal_load_dwordx4 %5, %16, %18 offset:1024 sc1\n\tglobal_load_dwordx4 %6, %16, %18 offset:2048 sc1\n\tglobal_load_dwordx4 %7, %16, %18 offset:3072 sc1\n\t"     \
               "global_load_dwordx4 %8, %16, %19 sc1\n\tglobal_load_dwordx4 %9, %16, %19 offset:1024 sc1\n\tglobal_load_dwordx4 %10, %16, %19 offset:2048 sc1\n\tglobal_load_dwordx4 %11, %16, %19 offset:3072 sc1\n\t"   \
               "global_load_dwordx4 %12, %16, %20 sc1\n\tglobal_load_dwordx4 %13, %16, %20 offset:1024 sc1\n\tglobal_load_dwordx4 %14, %16, %20 offset:2048 sc1\n\tglobal_load_dwordx4 %15, %16, %20 offset:3072 sc1\n"   \
               ".Lwf%=:"                                                                                              \
               : "+v"(wf[0]), "+v"(wf[1]), "+v"(wf[2]), "+v"(wf[3]), "+v"(wf[4]), "+v"(wf[5]), "+v"(wf[6]), "+v"(wf[7]), "+v"(wf[8]), "+v"(wf[9]), "+v"(wf[10]), \
                 "+v"(wf[11]), "+v"(wf[12]), "+v"(wf[13]), "+v"(wf[14]), "+v"(wf[15])                                  \
               : "v"(vo), "s"(wob), "s"(wob + 4096), "s"(wob + 32768), "s"(wob + 36864), "s"(SEL) : "memory", "scc")
#pragma unroll
  for (int s4 = 0; s4 < 16; ++s4) asm volatile("" : "=v"(wf[s4]));
  const int qt = blockIdx.x >> 6;
#pragma unroll 1
  for (int seg = 0; seg < 4; ++seg) {
    if (seg == 1) {
      ASTAMP(10)
      WAITV_BAR(0);
      ASTAMP(24)
      if (w == 0 && l < 8) put_flag(p.ws + WS_FP + l * 1024 + blockIdx.x * 4, tag);
    } else if (seg == 2) {
      ASTAMP(21)
      if (w == 0) {
        u32x4 f;
#pragma unroll 1
        for (int it = 0; it < SPIN_CAP; ++it) {
          asm volatile("global_load_dwordx4 %0, %1, %2 sc1\n\ts_waitcnt vmcnt(0)" : "=&v"(f) : "v"(vo), "s"(p.ws + WS_FP + (blockIdx.x & 7) * 1024) : "memory");
          if (__all(f[0] == tag && f[1] == tag && f[2] == tag && f[3] == tag)) break;
          __builtin_amdgcn_s_sleep(1);
        }
        if (blockIdx.x == 0 && l == 0) put_flag(p.ws + WS_EPOCH, epoch + 1u);
      }
      BAR();
      ASTAMP(23)
    } else if (seg == 3) {
      ASTAMP(22)
      if (!kh) {
        dma4s(wgu + w * 8192, vo, SL0 + w * 8192); dma4s(wgu + w * 8192 + 4096, vo, SL0 + w * 8192 + 4096);
        dma4s(wgu + 32768 + w * 8192, vo, SL1 + w * 8192); dma4s(wgu + 32768 + w * 8192 + 4096, vo, SL1 + w * 8192 + 4096);
        set_m0(JUNK);
        touch8k(wgu + 65536 + qt * 40960 + w * 8192, l * 128);
        if (w < 2) touch8k(w == 0 ? wgu + 65536 + qt * 40960 + 32768 : p.ws + WS_AUG + (size_t)id.c * 14336 + (qt & 1) * 6144, l * 128);
      }
    }
    LDWF((seg ^ 2) | (kh ^ 1));
    TILE(0, kA, kB, vA0, vA1, vB0, vB1) TILE(1, kB, kA, vB0, vB1, vA0, vA1) TILE(2, kA, kB, vA0, vA1, vB0, vB1) TILE(3, kB, kA, vB0, vB1, vA0, vA1)
    TILE(4, kA, kB, vA0, vA1, vB0, vB1) TILE(5, kB, kA, vB0, vB1, vA0, vA1) TILE(6, kA, kB, vA0, vA1, vB0, vB1) TILE(7, kB, kA, vB0, vB1, vA0, vA1)
    ka += 8 * TS; va += 8 * TS; PIN(ka); PIN(va);
  }
  o1 = mfma(vB0, pb0, o1); o1 = mfma(vB1, pb1, o1);
  LDWF(kh);
  if (!kh) {
    set_m0(JUNK);
    touch8k(p.ws + WS_WD + (size_t)(2 * j) * 114688 + qt * 57344 + w * 8192, l * 128);
    if (w < 3) touch8k(p.ws + WS_WD + (size_t)(2 * j) * 114688 + qt * 57344 + (w + 4) * 8192, l * 128);
  }
#undef HALF
#undef TILE
  STAMP(2)
  const u32 xo = (u32)((l & 31) * DM + 4 * h) * 2;
  const char* xres = (const char*)(p.x + (size_t)(64 * g + 32 * tt) * DM + 64 * j + 32 * nt + 16 * kh);
  u32x2 xa = gld8_s<0>(xres, xo), xb = gld8_s<16>(xres, xo);
  u32x4 ga = gld_s<1, 0>(modr + 4096 + (64 * j + 32 * nt + 16 * kh) * 4, h * 16), gb = gld_s<1, 32>(modr + 4096 + (64 * j + 32 * nt + 16 * kh) * 4, h * 16);
  {
    const u32 xs = OEX + w * 2304, xr = OEX + (w ^ 4) * 2304;
    float o[9];
    if (kh) { stsf4(xs + vo, f32x4{o0[0], o0[1], o0[2], o0[3]}); stsf4(xs + 1024 + vo, f32x4{o0[4], o0[5], o0[6], o0[7]}); *(LAS float*)(uintptr_t)(xs + 2048 + l * 4) = o0[8];
#pragma unroll
      for (int e = 0; e < 9; ++e) o[e] = o1[e];
    } else { stsf4(xs + vo, f32x4{o1[0], o1[1], o1[2], o1[3]}); stsf4(xs + 1024 + vo, f32x4{o1[4], o1[5], o1[6], o1[7]}); *(LAS float*)(uintptr_t)(xs + 2048 + l * 4) = o1[8];
#pragma unroll
      for (int e = 0; e < 9; ++e) o[e] = o0[e];
    }
    BAR();
    const f32x4 r0 = ldsf4(xr + vo), r1 = ldsf4(xr + 1024 + vo);
#pragma unroll
    for (int e = 0; e < 4; ++e) { o[e] += r0[e]; o[4 + e] += r1[e]; }
    float ls = lowhalf(o[8] + *(const LAS float*)(uintptr_t)(xr + 2048 + l * 4));
    bool bad = !(ls < 1.2676506e30f);
#pragma unroll
    for (int e = 0; e < 8; ++e) bad |= !(fabsf(o[e]) < 3.0e38f);
    if (__builtin_expect(__any(bad), 0)) {
      const u32x4 qf = kh ? q1 : q0;
      float m = -INFINITY;
      f32x16 oo = {};
#pragma unroll 1
      for (int t = 0; t < 64; ++t) {
        u32x4 kf, v0, v1;
        asm volatile("global_load_dwordx4 %0, %3, %5 sc1\n\tglobal_load_dwordx4 %1, %4, %5 sc1\n\tglobal_load_dwordx4 %2, %4, %5 offset:576 sc1\n\ts_waitcnt vmcnt(0)"
                     : "=&v"(kf), "=&v"(v0), "=&v"(v1) : "v"(vo), "v"(vrow), "s"(p.ws + WS_KV + t * TS) : "memory");
        const f32x16 s = mfma(kf, qf, f32x16{});
        const float mn = fmaxf(m, rowmax16(s)), al = EXP2(m - mn);
        m = mn;
        u32x4 pa, pb;
#pragma unroll
        for (int r = 0; r < 4; ++r) { pa[r] = pk(EXP2(s[2 * r] - mn), EXP2(s[2 * r + 1] - mn)); pb[r] = pk(EXP2(s[8 + 2 * r] - mn), EXP2(s[9 + 2 * r] - mn)); }
#pragma unroll
        for (int r = 0; r < 16; ++r) oo[r] *= al;
        oo = mfma(v0, pa, oo); oo = mfma(v1, pb, oo);
      }
#pragma unroll
      for (int e = 0; e < 8; ++e) o[e] = oo[e];
      ls = lowhalf(oo[8]);
    }
    const float rl = __builtin_amdgcn_rcpf(ls);
    gst16_sc1(p.ws + WS_O + fo + kh * 1024, vo, u32x4{pk(o[0] * rl, o[1] * rl), pk(o[2] * rl, o[3] * rl), pk(o[4] * rl, o[5] * rl), pk(o[6] * rl, o[7] * rl)});
  }
  asm volatile("s_waitcnt vmcnt(0)\n\ts_barrier"
               : "+v"(wf[0]), "+v"(wf[1]), "+v"(wf[2]), "+v"(wf[3]), "+v"(wf[4]), "+v"(wf[5]), "+v"(wf[6]), "+v"(wf[7]), "+v"(wf[8]), "+v"(wf[9]),
                 "+v"(wf[10]), "+v"(wf[11]), "+v"(wf[12]), "+v"(wf[13]), "+v"(wf[14]), "+v"(wf[15]), "+v"(xa), "+v"(xb), "+v"(ga), "+v"(gb)::"memory");
  STAMP(3)
  if (w == 0) {
    if (l == 0) put_flag(p.ws + WS_FO + g * 256 + j * 4, tag);
    poll_flags<8>(p.ws + WS_FO + g * 256, tag, l);
    set_m0(JUNK); touch8k(p.ws + WS_O + (size_t)g * 65536 + j * 8192, l * 128);
  }
  BAR();
  STAMP(4)
  u32x4 x1;
  {
    const char* osrc = p.ws + WS_O + ((size_t)(2 * g + tt) * 32 + 8 * kq) * 1024;
    u32x4 xf[8];
    xf[0] = gld_s<1, 0>(osrc, vo); xf[1] = gld_s<1, 1024>(osrc, vo); xf[2] = gld_s<1, 2048>(osrc, vo); xf[3] = gld_s<1, 3072>(osrc, vo);
    xf[4] = gld_s<1, 0>(osrc + 4096, vo); xf[5] = gld_s<1, 1024>(osrc + 4096, vo); xf[6] = gld_s<1, 2048>(osrc + 4096, vo); xf[7] = gld_s<1, 3072>(osrc + 4096, vo);
    f32x16 acc0 = {}, acc1 = {};
#define P3_STEP(S) { waitv_pin<7 - (S)>(xf[S]); acc0 = mfma(wf[S], xf[S], acc0); acc1 = mfma(wf[8 + (S)], xf[S], acc1); }
    P3_STEP(0) P3_STEP(1) P3_STEP(2) P3_STEP(3) P3_STEP(4) P3_STEP(5) P3_STEP(6) P3_STEP(7)
#undef P3_STEP
    STAMP(5)
    float r[8];
    reduce4(acc0, acc1, tt, kq, SL3, vo, r);
    STAMP(6)
    x1 = u32x4{pk(fmaf(bflo(ga[0]), r[0], bflo(xa[0])), fmaf(bfhi(ga[0]), r[1], bfhi(xa[0]))), pk(fmaf(bflo(ga[2]), r[2], bflo(xa[1])), fmaf(bfhi(ga[2]), r[3], bfhi(xa[1]))),
               pk(fmaf(bflo(gb[0]), r[4], bflo(xb[0])), fmaf(bfhi(gb[0]), r[5], bfhi(xb[0]))), pk(fmaf(bflo(gb[2]), r[6], bflo(xb[1])), fmaf(bfhi(gb[2]), r[7], bfhi(xb[1])))};
    gst16_sc1(p.ws + WS_X1 + fo + kh * 1024, vo, x1);
    if (w != 0) dma4s(wgu + 2 * 32768 + w * 4096, vo, SL2 + w * 4096);
    float s1 = 0.f, s2 = 0.f;
#pragma unroll
    for (int e = 0; e < 4; ++e) { const u32 v = x1[e]; s1 = dot2(v, 0x3f803f80u, s1); s2 = dot2(v, v, s2); }
    s1 = swap32_sum(s1); s2 = swap32_sum(s2);
    if (l < 32) *(LAS f32x2*)(uintptr_t)(GAPA + kq * 512 + tt * 256 + l * 8) = f32x2{s1, s2};
    BAR();
    STAMP(7)
    if (w == 0) {
      f32x2 s = {0.f, 0.f};
#pragma unroll
      for (int i = 0; i < 4; ++i) s += ldsf2(GAPA + i * 512 + l * 8);
      gst8_sc1(p.ws + WS_STAT + ((size_t)64 * g * 8 + j) * 8, l * 64, u32x2{__float_as_uint(s[0]), __float_as_uint(s[1])});
      dma4s(wgu + 2 * 32768, vo, SL2);
    }
    dma4s(wgu + 3 * 32768 + w * 4096, vo, SL3 + w * 4096);
    WAITV_BAR(8);
    STAMP(8)
    if (w == 0 && l == 0) put_flag(p.ws + WS_FX + (g >> 1) * 256 + ((g & 1) * 8 + j) * 4, tag);
  }
  u32x4 wr[9][2]; u32 fe;
  if (kh == 0) p4_body<0>(p, id, l, w, tag, wr, fe STAMP_PASS); else p4_body<1>(p, id, l, w, tag, wr, fe STAMP_PASS);
  {
    int ly = l; PIN(ly);
    asm volatile("s_waitcnt vmcnt(0)"
                 : "+v"(wr[0][0]), "+v"(wr[0][1]), "+v"(wr[1][0]), "+v"(wr[1][1]), "+v"(wr[2][0]), "+v"(wr[2][1]), "+v"(wr[3][0]), "+v"(wr[3][1]), "+v"(wr[4][0]),
                   "+v"(wr[4][1]), "+v"(wr[5][0]), "+v"(wr[5][1]), "+v"(wr[6][0]), "+v"(wr[6][1]), "+v"(wr[7][0]), "+v"(wr[7][1]), "+v"(wr[8][0]), "+v"(wr[8][1]), "+v"(fe)::"memory");
    if (!__all(fe == tag)) poll_flags<4>(p.ws + WS_FE + (g >> 1) * 256 + kq * 16, tag, ly);
    STAMP(19)
    const char* xsrc = p.ws + WS_ACT + ((size_t)(2 * g + tt) * 112 + 28 * kq) * 1024;
    u32x4 xf[20]; u32 fa;
#define XL(I) (xf[(I) % 20] = gld_s<1, 0>(xsrc + dks(I) * 1024, vo))
#define XL4(I) { xf[I] = gld_s<1, 0>(xsrc + dks(I) * 1024, vo); xf[I + 1] = gld_s<1, 1024>(xsrc + dks(I) * 1024, vo); xf[I + 2] = gld_s<1, 2048>(xsrc + dks(I) * 1024, vo); xf[I + 3] = gld_s<1, 3072>(xsrc + dks(I) * 1024, vo); }
    XL4(0) XL4(4)
    asm volatile("global_load_dword %0, %1, %2 sc1" : "=v"(fa) : "v"((ly & 3) * 4), "s"(p.ws + WS_FA + (g >> 1) * 256 + kq * 16) : "memory");
    XL4(8) XL4(12)
    u32x4 g0 = gld_s<1, 0>(modr + 10240 + (64 * j + 32 * nt + 16 * kh) * 4, (ly >> 5) * 16), g1 = gld_s<1, 32>(modr + 10240 + (64 * j + 32 * nt + 16 * kh) * 4, (ly >> 5) * 16);
    f32x16 acc0, acc1; u32x4 wa[19], wb[19];
    u32 blo = kq * 8192 + vo, bmid = blo + 65536, bhi = TOPA + kq * 6144 + vo;
    PIN(blo); PIN(bmid); PIN(bhi);
#define K5_W(S, NT) ((S) < 16 ? ldsr<((S) < 4 ? SL3 : (S) < 8 ? SL0 : (S) < 12 ? SL1 : SL2) + ((S) & 3) * 1024 + (NT) * 4096>(blo, bmid) : lds128(bhi + ((S) & 3) * 1024 + (NT) * 3072))
#define K5_RD(S) { if ((S) < 19) { wa[(S) < 19 ? (S) : 0] = K5_W(S, 0); wb[(S) < 19 ? (S) : 0] = K5_W(S, 1); } }
#define K5_STEP(S, N, RDN)                                                                           \
  {                                                                                                  \
    waitv_pin<N>(xf[(S) % 20]);                                                                      \
    if ((S) == 0) mf0(acc0, wa[0], xf[0]); else mfa(acc0, (S) < 19 ? wa[(S) < 19 ? (S) : 0] : wr[(S) < 19 ? 0 : (S) - 19][0], xf[(S) % 20]); \
    if (RDN) K5_RD((S) + 2)                                                                          \
    if ((S) == 0) mf0(acc1, wb[0], xf[0]); else mfa(acc1, (S) < 19 ? wb[(S) < 19 ? (S) : 0] : wr[(S) < 19 ? 0 : (S) - 19][1], xf[(S) % 20]); \
  }
    K5_RD(0) K5_RD(1)
    K5_STEP(0, 18, 1) K5_STEP(1, 17, 1) K5_STEP(2, 16, 1) K5_STEP(3, 15, 1) K5_STEP(4, 14, 1) K5_STEP(5, 13, 1) K5_STEP(6, 12, 1) K5_STEP(7, 11, 1)
    asm volatile("s_waitcnt vmcnt(10)" : "+v"(fa)::"memory");
    if (!__all(fa == tag)) poll_flags<4>(p.ws + WS_FA + (g >> 1) * 256 + kq * 16, tag, ly);
    XL(16); XL(17); XL(18); XL(19); XL(20); XL(21); XL(22); XL(23); XL(24); XL(25); XL(26); XL(27);
    K5_STEP(8, 21, 1) K5_STEP(9, 20, 1)
    asm volatile("s_barrier" ::: "memory");
    K5_RD(12) K5_RD(13)
    K5_STEP(10, 19, 0) K5_STEP(11, 18, 0) K5_STEP(12, 17, 1) K5_STEP(13, 16, 1) K5_STEP(14, 15, 1) K5_STEP(15, 14, 1)
    K5_STEP(16, 11, 1) K5_STEP(17, 10, 0) K5_STEP(18, 9, 0)
    BAR();
    K5_STEP(19, 8, 0) K5_STEP(20, 7, 0) K5_STEP(21, 6, 0) K5_STEP(22, 5, 0) K5_STEP(23, 4, 0) K5_STEP(24, 3, 0) K5_STEP(25, 2, 0) K5_STEP(26, 1, 0) K5_STEP(27, 0, 0)
#undef K5_STEP
#undef K5_RD
#undef K5_W
    MFMA_SETTLE();
    PIN(g0); PIN(g1); PIN(acc0); PIN(acc1);
    STAMP(20)
    float r[8];
    reduce4(acc0, acc1, tt, kq, 0, vo, r);
    const u32 a0 = pk(fmaf(bflo(g0[0]), r[0], bflo(x1[0])), fmaf(bfhi(g0[0]), r[1], bfhi(x1[0]))), a1 = pk(fmaf(bflo(g0[2]), r[2], bflo(x1[1])), fmaf(bfhi(g0[2]), r[3], bfhi(x1[1])));
    const u32 b0 = pk(fmaf(bflo(g1[0]), r[4], bflo(x1[2])), fmaf(bfhi(g1[0]), r[5], bfhi(x1[2]))), b1 = pk(fmaf(bflo(g1[2]), r[6], bflo(x1[3])), fmaf(bfhi(g1[2]), r[7], bfhi(x1[3])));
    const auto s0 = __builtin_amdgcn_permlane32_swap(a0, b0, false, false);
    const auto s1 = __builtin_amdgcn_permlane32_swap(a1, b1, false, false);
    int lz = l; PIN(lz);
    *(u32x4*)(p.out + (size_t)(64 * g + 32 * tt + (lz & 31)) * DM + 64 * j + 32 * nt + 16 * kh + 8 * (lz >> 5)) = u32x4{s0[0], s1[0], s0[1], s1[1]};
  }
  STAMP_FLUSH(0)
}

extern "C" void kernel_launch(void* const* d_in, const int* in_sizes, int n_in, void* d_out, int out_size, void* d_ws, size_t ws_size, hipStream_t stream) {
  Params p;
  p.x = (const u16*)d_in[0]; p.wq = (const u16*)d_in[1]; p.wk = (const u16*)d_in[2]; p.wv = (const u16*)d_in[3]; p.wo = (const u16*)d_in[4];
  p.wg = (const u16*)d_in[6]; p.wu = (const u16*)d_in[7]; p.wd = (const u16*)d_in[8]; p.c = (const u16*)d_in[9]; p.wm = (const u16*)d_in[10];
  p.out = (u16*)d_out; p.ws = (char*)d_ws;
  (void)hipFuncSetAttribute((const void*)k_main, hipFuncAttributeMaxDynamicSharedMemorySize, M_LDS);
  k_main<<<256, 512, M_LDS, stream>>>(p);
}
```

```cpp
#include <hip/hip_runtime.h>
#include <stdint.h>

typedef unsigned short u16;
typedef unsigned int u32;
typedef __attribute__((ext_vector_type(8))) __bf16 bf16x8;
typedef __attribute__((ext_vector_type(2))) __bf16 bf16x2;
typedef __attribute__((ext_vector_type(2))) float f32x2;
typedef __attribute__((ext_vector_type(4))) float f32x4;
typedef __attribute__((ext_vector_type(16))) float f32x16;
typedef __attribute__((ext_vector_type(4))) u32 u32x4;
typedef __attribute__((ext_vector_type(2))) u32 u32x2;
typedef __attribute__((ext_vector_type(4))) short s16x4;
#define LAS __attribute__((address_space(3)))
#define GAS __attribute__((address_space(1)))
#define DI __device__ __forceinline__

constexpr int DM = 512, FF = 1792;
constexpr float EPS = 1e-5f;
constexpr float C2 = 0.25f * 1.4426950408889634f;

constexpr size_t WS_EPOCH = 0;
constexpr size_t WS_FO = 4096;
constexpr size_t WS_FX = WS_FO + 8192;
constexpr size_t WS_FA = WS_FX + 4096;
constexpr size_t WS_FE = WS_FA + 128;
constexpr size_t WS_FKV = WS_FA + 4096;
constexpr size_t WS_FP = WS_FKV + 4096;
constexpr size_t WS_MOD = 65536;
constexpr size_t WS_KV = WS_MOD + 8 * 12288;
constexpr size_t WS_STAT = WS_KV + 139264;
constexpr size_t WS_AUG = WS_STAT + 131072;
constexpr size_t WS_WO = 1u << 20;
constexpr size_t WS_WD = WS_WO + (512u << 10);
constexpr size_t WS_WGU = WS_WD + (1792u << 10);
constexpr size_t WS_O = WS_WGU + (3584u << 10);
constexpr size_t WS_X1 = WS_O + (2u << 20);
constexpr size_t WS_ACT = WS_X1 + (2u << 20);
static_assert(WS_AUG + 229376 <= WS_WO, "workspace map");
constexpr int TS = 2176;

struct Params {
  const u16 *x, *wq, *wk, *wv, *wo, *wg, *wu, *wd, *c, *wm;
  u16* out;
  char* ws;
};

DI f32x16 mfma(u32x4 a, u32x4 b, f32x16 c) {
  return __builtin_amdgcn_mfma_f32_32x32x16_bf16(__builtin_bit_cast(bf16x8, a), __builtin_bit_cast(bf16x8, b), c, 0, 0, 0);
}
DI u32 pk(float lo, float hi) {
  f32x2 v = {lo, hi};
  return __builtin_bit_cast(u32, __builtin_convertvector(v, bf16x2));
}
DI float bflo(u32 u) { return __uint_as_float(u << 16); }
DI float bfhi(u32 u) { return __uint_as_float(u & 0xffff0000u); }
DI float rbf(float f) { return bflo(pk(f, 0.f)); }
DI float dot2(u32 a, u32 b, float c) { return __builtin_amdgcn_fdot2_f32_bf16(__builtin_bit_cast(bf16x2, a), __builtin_bit_cast(bf16x2, b), c, false); }
DI void set_m0(u32 lds) { asm volatile("s_mov_b32 m0, %0\n\ts_nop 0" ::"s"(lds) : "memory"); }
template <int OFF> DI void dma(const void* base, u32 vo) { asm volatile("global_load_lds_dwordx4 %0, %1 offset:%2" ::"v"(vo), "s"(base), "n"(OFF) : "memory"); }
template <int OFF> DI void dma1(const void* base, u32 vo) { asm volatile("global_load_lds_dwordx4 %0, %1 offset:%2 sc1" ::"v"(vo), "s"(base), "n"(OFF) : "memory"); }
DI void dma4(const void* base, u32 vo, u32 lds) { set_m0(lds); dma<0>(base, vo); dma<1024>(base, vo); dma<2048>(base, vo); dma<3072>(base, vo); }
DI void dma4s(const void* base, u32 vo, u32 lds) { set_m0(lds); dma1<0>(base, vo); dma1<1024>(base, vo); dma1<2048>(base, vo); dma1<3072>(base, vo); }
template <int SC, int OFF> DI u32x4 gld_s(const void* base, u32 vo) {
  u32x4 r;
  if (SC) asm volatile("global_load_dwordx4 %0, %1, %2 offset:%3 sc1" : "=v"(r) : "v"(vo), "s"(base), "n"(OFF) : "memory");
  else asm volatile("global_load_dwordx4 %0, %1, %2 offset:%3" : "=v"(r) : "v"(vo), "s"(base), "n"(OFF) : "memory");
  return r;
}
template <int OFF> DI u32x2 gld8_s(const void* base, u32 vo) { u32x2 r; asm volatile("global_load_dwordx2 %0, %1, %2 offset:%3" : "=v"(r) : "v"(vo), "s"(base), "n"(OFF) : "memory"); return r; }
template <int OFF> DI u32 gld2_s(const void* base, u32 vo) { u32 r; asm volatile("global_load_ushort %0, %1, %2 offset:%3" : "=v"(r) : "v"(vo), "s"(base), "n"(OFF) : "memory"); return r; }
DI void touch8k(const char* base, u32 l128) { asm volatile("global_load_lds_dword %0, %1 sc1" ::"v"(l128), "s"(base) : "memory"); }
DI void gst16_sc1(char* base, u32 vo, u32x4 v) { asm volatile("global_store_dwordx4 %0, %1, %2 sc1\n\ts_nop 1" ::"v"(vo), "v"(v), "s"(base) : "memory"); }
DI void gst8_sc1(char* base, u32 vo, u32x2 v) { asm volatile("global_store_dwordx2 %0, %1, %2 sc1\n\ts_nop 1" ::"v"(vo), "v"(v), "s"(base) : "memory"); }
#define RLX_AGENT __ATOMIC_RELAXED, __HIP_MEMORY_SCOPE_AGENT
DI void put_flag(char* f, u32 tag) { __hip_atomic_store((GAS u32*)f, tag, RLX_AGENT); }
#define SLP 16
constexpr int SPIN_CAP = 1 << 20;
template <int N> DI void poll_flags(const char* f, u32 tag, int l) {
#pragma unroll 1
  for (int it = 0; it < SPIN_CAP; ++it) {
    const u32 v = __hip_atomic_load((const GAS u32*)f + (l < N ? l : 0), RLX_AGENT);
    if (__all(v == tag)) break;

  }
  __builtin_amdgcn_fence(__ATOMIC_ACQUIRE, "wavefront");
}
#define SB() __builtin_amdgcn_sched_barrier(0)
#define PIN(x) asm volatile("" : "+v"(x))
#define WAITV(N) asm volatile("s_waitcnt vmcnt(" #N ")" ::: "memory")
#define WAITV_BAR(N) asm volatile("s_waitcnt vmcnt(" #N ") lgkmcnt(0)\n\ts_barrier" ::: "memory")
#define BAR() asm volatile("s_waitcnt lgkmcnt(0)\n\ts_barrier" ::: "memory")
template <int N> DI void waitv_pin(u32x4& v) { asm volatile("s_waitcnt vmcnt(%1)" : "+v"(v) : "n"(N) : "memory"); }
DI u32x4 lds128(u32 a) { return *(const LAS u32x4*)(uintptr_t)a; }
DI f32x4 ldsf4(u32 a) { return *(const LAS f32x4*)(uintptr_t)a; }
DI f32x2 ldsf2(u32 a) { return *(const LAS f32x2*)(uintptr_t)a; }
DI void sts128(u32 a, u32x4 v) { *(LAS u32x4*)(uintptr_t)a = v; }
DI void stsf4(u32 a, f32x4 v) { *(LAS f32x4*)(uintptr_t)a = v; }
DI u32x2 ldstr(u32 a) { return __builtin_bit_cast(u32x2, __builtin_amdgcn_ds_read_tr16_b64_v4i16((LAS s16x4*)(uintptr_t)a)); }
DI u32x4 ldstr2(u32 a0, u32 a1) { u32x2 p = ldstr(a0), q = ldstr(a1); return u32x4{p[0], p[1], q[0], q[1]}; }
template <int CTRL> DI float dpp(float v) { return __int_as_float(__builtin_amdgcn_mov_dpp(__float_as_int(v), CTRL, 0xf, 0xf, true)); }
DI float swap32_sum(float v) { auto r = __builtin_amdgcn_permlane32_swap(__float_as_uint(v), __float_as_uint(v), false, false); return __uint_as_float(r[0]) + __uint_as_float(r[1]); }
DI float swap32_max(float v) { auto r = __builtin_amdgcn_permlane32_swap(__float_as_uint(v), __float_as_uint(v), false, false); return fmaxf(__uint_as_float(r[0]), __uint_as_float(r[1])); }
DI float swap16_sum(float v) { auto r = __builtin_amdgcn_permlane16_swap(__float_as_uint(v), __float_as_uint(v), false, false); return __uint_as_float(r[0]) + __uint_as_float(r[1]); }
DI float lowhalf(float v) { auto r = __builtin_amdgcn_permlane32_swap(__float_as_uint(v), __float_as_uint(v), false, false); return __uint_as_float(r[0]); }
template <int CTRL> DI float halve(bool bit, float a, float b) { return (bit ? b : a) + dpp<CTRL>(bit ? a : b); }
DI int rfl(int v) { return __builtin_amdgcn_readfirstlane(v); }
#define EXP2(v) __builtin_amdgcn_exp2f(v)

struct Ids { int G, c, g, j; };
DI Ids ids() { const int b = blockIdx.x, x = b & 7, i = b >> 3; Ids r; r.G = (x >> 1) * 4 + (i >> 3); r.c = (x & 1) * 8 + (i & 7); r.g = 4 * x + (i >> 3); r.j = i & 7; return r; }

#define STAMP_DECL
#define STAMP(I)
#define STAMP_FLUSH(K)
#define STAMP_ARG
#define STAMP_PASS

#define PSTAMP(I)
#define ASTAMP(I)
constexpr u32 M_LDS = 163840;
constexpr u32 F_A = 0;
constexpr u32 F_H = 65536;
constexpr u32 F_B = 131072;
constexpr u32 F_D = F_B + 16640;
constexpr u32 F_M = F_D + 10240;
static_assert(F_M + 4608 <= M_LDS, "front-end LDS map");
constexpr u32 SL0 = 0, SL1 = 69632, SL2 = 32768, SL3 = 102400, TOPA = 135168, GAPA = 65536, OEX = 143616, JUNK = M_LDS - 256;

DI void mfa(f32x16& c, u32x4 a, u32x4 b) { asm volatile("v_mfma_f32_32x32x16_bf16 %0, %1, %2, %0" : "+v"(c) : "v"(a), "v"(b) : "memory"); }
DI void mf0(f32x16& c, u32x4 a, u32x4 b) { asm volatile("v_mfma_f32_32x32x16_bf16 %0, %1, %2, 0" : "=&v"(c) : "v"(a), "v"(b) : "memory"); }
DI float vexp(float a) { float r; asm volatile("v_exp_f32 %0, %1" : "=v"(r) : "v"(a)); return r; }
DI float vrcp(float a) { float r; asm volatile("v_rcp_f32 %0, %1" : "=v"(r) : "v"(a)); return r; }
DI u32 vcvt(float a, float b) { u32 r; asm volatile("v_cvt_pk_bf16_f32 %0, %1, %2" : "=v"(r) : "v"(a), "v"(b)); return r; }
DI float vadd(float a, float b) { float r; asm volatile("v_add_f32 %0, %1, %2" : "=v"(r) : "v"(a), "v"(b)); return r; }
DI float vmul(float a, float b) { float r; asm volatile("v_mul_f32 %0, %1, %2" : "=v"(r) : "v"(a), "v"(b)); return r; }
DI float vfma_k(float a, float k, float c) { float r; asm volatile("v_fma_f32 %0, %1, %2, %3" : "=v"(r) : "v"(a), "s"(k), "v"(c)); return r; }
DI float vadd1(float a) { float r; asm volatile("v_add_f32 %0, 1.0, %1" : "=v"(r) : "v"(a)); return r; }
#define MFMA_SETTLE() asm volatile("s_nop 15\n\ts_nop 3" ::: "memory")
DI float rowmax16(const f32x16& s) {
  float m = fmaxf(fmaxf(s[0], s[1]), s[2]);
#pragma unroll
  for (int r = 3; r < 15; r += 2) m = fmaxf(fmaxf(m, s[r]), s[r + 1]);
  return swap32_max(fmaxf(m, s[15]));
}
constexpr float GC0 = -2.f * 0.7978845608028654f * 1.4426950408889634f, GC1 = GC0 * 0.044715f;
DI u32 exbuf(int i) { return i < 7 ? TOPA + i * 4096 : GAPA; }
template <u32 A> DI u32x4 ldsr(u32 lo, u32 mid) { return A < 65536 ? lds128(lo + A) : lds128(mid + (A - 65536)); }

DI void reduce4(const f32x16& acc0, const f32x16& acc1, int tt, int kq, u32 buf, u32 vo, float (&r)[8]) {
#pragma unroll
  for (int pc = 0; pc < 4; ++pc) {
    const f32x16& a = (pc & 1) ? acc1 : acc0;
    const int o8 = (pc >> 1) * 8;
    if (pc != kq) {
      const u32 d = buf + ((tt * 4 + pc) * 3 + ((kq - pc - 1) & 3)) * 2048 + vo;
      stsf4(d, f32x4{a[o8], a[o8 + 1], a[o8 + 2], a[o8 + 3]}); stsf4(d + 1024, f32x4{a[o8 + 4], a[o8 + 5], a[o8 + 6], a[o8 + 7]});
    } else {
#pragma unroll
      for (int e = 0; e < 8; ++e) r[e] = a[o8 + e];
    }
  }
  BAR();
#pragma unroll
  for (int i = 0; i < 3; ++i) {
    const u32 d = buf + ((tt * 4 + kq) * 3 + i) * 2048 + vo;
    const f32x4 u = ldsf4(d), v = ldsf4(d + 1024);
#pragma unroll
    for (int e = 0; e < 4; ++e) { r[e] += u[e]; r[4 + e] += v[e]; }
  }
}

DI constexpr int dks(int i) { return i < 16 ? 7 * (i >> 2) + (i & 3) : 7 * ((i - 16) / 3) + 4 + (i - 16) % 3; }
template <int KH> DI void p4_body(const Params& p, const Ids& id, int l, int w, u32 tag, u32x4 (&wr)[9][2], u32& fe STAMP_ARG) {
  const int tt = w & 3;
  const u32 vo = l * 16;
  const char* wsrc = p.ws + WS_WGU + (size_t)id.c * 7 * 32768 + w * 4096;
  const char* dsrc = p.ws + WS_WD + ((size_t)(2 * id.j + (w & 1)) * 112 + 28 * (w >> 1)) * 1024;
  const char* dsrc0 = p.ws + WS_WD + ((size_t)(2 * id.j) * 112 + 28 * (w >> 1)) * 1024;
  char* adst = p.ws + WS_ACT + ((size_t)(4 * id.G + tt) * 112 + 7 * id.c) * 1024;
  const char* asrc = p.ws + WS_AUG + (size_t)(id.c * 14 + KH) * 1024;
#define WRL(I) (wr[(I) >> 1][(I) & 1] = gld_s<1, 0>(dsrc0 + (dks(19 + ((I) >> 1)) + ((I) & 1) * 112) * 1024, vo))
  u32x4 aug[7];
  aug[0] = gld_s<1, 0>(asrc, vo); aug[1] = gld_s<1, 2048>(asrc, vo); aug[2] = gld_s<1, 0>(asrc + 4096, vo); aug[3] = gld_s<1, 2048>(asrc + 4096, vo);
  WRL(0); WRL(1); WRL(2); WRL(3); WRL(4); WRL(5); WRL(6); WRL(7); WRL(8); WRL(9);
  if (w == 0) poll_flags<16>(p.ws + WS_FX + id.G * 256, tag, l);
  WAITV(10);
  asm volatile("s_barrier" : "+v"(aug[0]), "+v"(aug[1]), "+v"(aug[2]), "+v"(aug[3])::"memory");
  STAMP(9)
  const char* xsrc = p.ws + WS_X1 + ((size_t)(4 * id.G + tt) * 32 + 16 * KH) * 1024;
  u32x4 xf[16];
#pragma unroll
  for (int s4 = 0; s4 < 4; ++s4) {
    xf[4 * s4] = gld_s<1, 0>(xsrc + s4 * 4096, vo); xf[4 * s4 + 1] = gld_s<1, 1024>(xsrc + s4 * 4096, vo);
    xf[4 * s4 + 2] = gld_s<1, 2048>(xsrc + s4 * 4096, vo); xf[4 * s4 + 3] = gld_s<1, 3072>(xsrc + s4 * 4096, vo);
  }
  const char* ssrc = p.ws + WS_STAT + (size_t)(128 * id.G + 32 * tt) * 64;
  u32x4 st0 = gld_s<1, 0>(ssrc, (l & 31) * 64), st1 = gld_s<1, 16>(ssrc, (l & 31) * 64), st2 = gld_s<1, 32>(ssrc, (l & 31) * 64), st3 = gld_s<1, 48>(ssrc, (l & 31) * 64);
  float rs, gc0 = GC0; u32x4 xaug;
  u32 wlo = KH * 16384 + vo, wmid = wlo + 65536;
  PIN(wlo); PIN(wmid); PIN(gc0);
  f32x16 kp;
#define E4(F) { _Pragma("unroll") for (int e = 0; e < 4; ++e) { F; } }
#define E2(B, F) { _Pragma("unroll") for (int e = B; e < B + 2; ++e) { F; } }
#define UNITG(u, GO, RG, RU, OUT)                                                                                   \
    { if (u == 0) E4(gv[e] = vadd(kp[GO + e], RG[e]))                                                               \
      if (u == 1) E4(gv[e] = vmul(gv[e], rs))                                                                       \
      if (u == 2) E4(zz[e] = vmul(gv[e], gv[e]))                                                                    \
      if (u == 3) E4(zz[e] = vfma_k(zz[e], GC1, gc0))                                                               \
      if (u == 4) E4(zz[e] = vmul(zz[e], gv[e]))                                                                    \
      if (u == 5) E2(0, zz[e] = vexp(zz[e]))                                                                        \
      if (u == 6) E2(2, zz[e] = vexp(zz[e]))                                                                        \
      if (u == 7) E4(uv[e] = vadd(kp[GO + 4 + e], RU[e]))                                                           \
      if (u == 8) E4(zz[e] = vadd1(zz[e]))                                                                          \
      if (u == 9) E2(0, zz[e] = vrcp(zz[e]))                                                                        \
      if (u == 10) E2(2, zz[e] = vrcp(zz[e]))                                                                       \
      if (u == 11) E4(uv[e] = vmul(uv[e], gv[e]))                                                                   \
      if (u == 12) E4(zz[e] = vmul(zz[e], rs))                                                                      \
      if (u == 13) E4(uv[e] = vmul(uv[e], zz[e]))                                                                   \
      if (u == 14) { OUT[0] = vcvt(uv[0], uv[1]); OUT[1] = vcvt(uv[2], uv[3]); } }
#define UNIT(U) { if ((U) >= 0 && (U) < 15) UNITG((U), 0, r0, r1, q0) else if ((U) >= 15 && (U) < 30) UNITG((U) - 15, 8, r2, r3, q1) }
  constexpr int LG = 6;
#define RD(SL, I) ldsr<SL>(wlo + (I) * 1024, wmid + (I) * 1024)
#define G0(S) ((S) < LG ? (S) : 2 * (S) - LG)
#define STEP(S) { constexpr int s = (S);                                                                           \
      if (s < 16) mfa(a, ra[s & 15], xf[s & 15]);                                                                  \
      if (s + 4 < 16) ra[(s + 4) & 15] = RD(SS, (s + 4) & 15);                                                     \
      if (s == 0) r0 = ldsf4(RB); if (s == 1) r1 = ldsf4(RB + 1024); if (s == 8) r2 = ldsf4(RB + 2048); if (s == 9) r3 = ldsf4(RB + 3072); \
      if (s < 16) { PFM(s); UNIT(G0(s) - 2) }                                                                      \
      if (s >= 18) { constexpr int i4 = 4 * ((s - 18) & 3); stsf4(RB + i4 * 256, f32x4{a[i4], a[i4 + 1], a[i4 + 2], a[i4 + 3]}); } \
      if (s >= LG) mfa(b, rb[(s - LG) & 15], xf[(s - LG) & 15]);                                                   \
      if (s + 3 - LG >= 0 && s + 3 - LG < 16) rb[(s + 3 - LG) & 15] = RD(SK, (s + 3 - LG) & 15);                   \
      if (s >= LG) UNIT((s < 16 ? G0(s) + 1 : 32 - LG + (s - 16)) - 2) }
#define P4_ROUND(AUGS, AUGK, T)                                                                                    \
      f32x16 a, b; u32x4 ra[16], rb[16]; f32x4 r0, r1, r2, r3, gv, uv, zz; u32x2 q0, q1;                           \
      _Pragma("unroll") for (int s = 0; s < 4; ++s) ra[s] = RD(SS, s);                                             \
      mf0(a, AUGS, xaug); mf0(b, AUGK, xaug);                                                                      \
    STEP(0)    STEP(1)    STEP(2)    STEP(3)    STEP(4)    STEP(5)\
    STEP(6)    STEP(7)    STEP(8)    STEP(9)    STEP(10)    STEP(11)\
    STEP(12)    STEP(13)    STEP(14)    STEP(15)    STEP(16)    STEP(17)\
    STEP(18)    STEP(19)    STEP(20)    STEP(21) \
      gst16_sc1(adst + (T) * 1024, vo, u32x4{q0[0], q0[1], q1[0], q1[1]});                                         \
      kp = b;
#define DM4(s, S0, SRC, DST) { if (s == S0) { set_m0(DST); dma1<0>(SRC, vo); } if (s == S0 + 1) dma1<1024>(SRC, vo); if (s == S0 + 2) dma1<2048>(SRC, vo); if (s == S0 + 3) dma1<3072>(SRC, vo); }
  const u32 eA = exbuf(w) + vo, eB = exbuf(w ^ 4) + vo;
  {
    constexpr u32 SS = KH ? SL0 : SL1, SK = KH ? SL1 : SL0;
    f32x16 a, b; u32x4 ra[16], rb[16];
#pragma unroll
    for (int s = 0; s < 3; ++s) { ra[s] = RD(SS, s); rb[s] = RD(SK, s); }
#pragma unroll
    for (int s = 0; s < 16; ++s) {
      if (s == 0) asm volatile("s_waitcnt vmcnt(18)" : "+v"(xf[0]), "+v"(xf[1])::"memory");
      if (s == 2) asm volatile("s_waitcnt vmcnt(16)" : "+v"(xf[2]), "+v"(xf[3])::"memory");
      if (s == 4) asm volatile("s_waitcnt vmcnt(12)" : "+v"(xf[4]), "+v"(xf[5]), "+v"(xf[6]), "+v"(xf[7])::"memory");
      if (s == 8) asm volatile("s_waitcnt vmcnt(8)" : "+v"(xf[8]), "+v"(xf[9]), "+v"(xf[10]), "+v"(xf[11])::"memory");
      if (s == 12) asm volatile("s_waitcnt vmcnt(4)" : "+v"(xf[12]), "+v"(xf[13]), "+v"(xf[14]), "+v"(xf[15])::"memory");
      if (s == 0) mf0(a, ra[0], xf[0]); else mfa(a, ra[s], xf[s]);
      if (s + 3 < 16) { ra[s + 3] = RD(SS, s + 3); rb[s + 3] = RD(SK, s + 3); }
      if (s == 0) mf0(b, rb[0], xf[0]); else if (s < 10) mfa(b, rb[s], xf[s]);
    }
    asm volatile("s_waitcnt vmcnt(0)" : "+v"(st0), "+v"(st1), "+v"(st2), "+v"(st3)::"memory");
    const float s1 = (__uint_as_float(st0[0]) + __uint_as_float(st0[2])) + (__uint_as_float(st1[0]) + __uint_as_float(st1[2])) + (__uint_as_float(st2[0]) + __uint_as_float(st2[2])) + (__uint_as_float(st3[0]) + __uint_as_float(st3[2]));
    const float s2 = (__uint_as_float(st0[1]) + __uint_as_float(st0[3])) + (__uint_as_float(st1[1]) + __uint_as_float(st1[3])) + (__uint_as_float(st2[1]) + __uint_as_float(st2[3])) + (__uint_as_float(st3[1]) + __uint_as_float(st3[3]));
    const float nmu = -s1 * (1.f / DM), var = fmaxf(s2 * (1.f / DM) - nmu * nmu, 0.f) + EPS;
    rs = __builtin_amdgcn_rsqf(var);
    const float sg = var * rs, mh = rbf(nmu), sh = rbf(sg);
    xaug = u32x4{pk(mh, mh), pk(nmu - mh, sh), pk(sh, sg - sh), 0u};
    if (l >= 32) xaug = u32x4{0u, 0u, 0u, 0u};
    asm volatile("s_nop 1" : "+v"(xaug), "+v"(rs));
    mfa(a, aug[KH ? 0 : 1], xaug);
    mfa(b, rb[10], xf[10]); mfa(b, rb[11], xf[11]);
    MFMA_SETTLE();
    stsf4(eA, f32x4{a[0], a[1], a[2], a[3]}); mfa(b, rb[12], xf[12]);
    stsf4(eA + 1024, f32x4{a[4], a[5], a[6], a[7]}); mfa(b, rb[13], xf[13]);
    stsf4(eA + 2048, f32x4{a[8], a[9], a[10], a[11]}); mfa(b, rb[14], xf[14]);
    stsf4(eA + 3072, f32x4{a[12], a[13], a[14], a[15]}); mfa(b, rb[15], xf[15]);
    mfa(b, aug[KH ? 1 : 0], xaug);
    kp = b;
  }
  STAMP(11)
  BAR();
  STAMP(12)
  {
    constexpr u32 SS = KH ? SL2 : SL3, SK = KH ? SL3 : SL2;
    const u32 RB = eB;
#define PFM(s) { DM4(s, 0, wsrc + 4 * 32768, SL0 + w * 4096) DM4(s, 5, wsrc + 5 * 32768, SL1 + w * 4096) if (s == 10) aug[4] = gld_s<1, 0>(asrc + 8192, vo); if (s == 11) aug[5] = gld_s<1, 2048>(asrc + 8192, vo); }
    P4_ROUND(aug[KH ? 2 : 3], aug[KH ? 3 : 2], KH)
#undef PFM
  }
  STAMP(13)
  asm volatile("s_waitcnt vmcnt(1) lgkmcnt(0)\n\ts_barrier" : "+v"(aug[4]), "+v"(aug[5])::"memory");
  STAMP(14)
  {
    constexpr u32 SS = KH ? SL0 : SL1, SK = KH ? SL1 : SL0;
    const u32 RB = eA;
#define PFM(s) { DM4(s, 0, wsrc + 6 * 32768, SL2 + w * 4096) if (s == 5) aug[6] = gld_s<1, 0>(asrc + 12288, vo); DM4(s, 8, dsrc, SL3 + w * 4096) }
    P4_ROUND(aug[KH ? 4 : 5], aug[KH ? 5 : 4], 2 + KH)
#undef PFM
  }
  STAMP(15)
  asm volatile("s_waitcnt vmcnt(5) lgkmcnt(0)\n\ts_barrier" : "+v"(aug[6])::"memory");
  STAMP(16)
  {
    f32x16 a; u32x4 ra[16]; f32x4 r0, r1, r2, r3, gv, uv, zz; u32x2 q0, q1;
#pragma unroll
    for (int s = 0; s < 4; ++s) ra[s] = RD(SL2, s);
    r0 = ldsf4(eB); r1 = ldsf4(eB + 1024);
    mf0(a, aug[6], xaug);
#define STEP3(S) { constexpr int s = (S); mfa(a, ra[s], xf[s]); if (s + 4 < 16) ra[(s + 4) & 15] = RD(SL2, (s + 4) & 15);            \
      if (s == 4) r2 = ldsf4(eB + 2048); if (s == 5) r3 = ldsf4(eB + 3072);                                                         \
      DM4(s, 0, dsrc + 7168, SL0 + w * 4096) DM4(s, 6, dsrc + 14336, SL1 + w * 4096) if (s >= 12) WRL(s >= 12 ? s - 2 : 0); UNIT(s - 1) }
    STEP3(0) STEP3(1) STEP3(2) STEP3(3) STEP3(4) STEP3(5) STEP3(6) STEP3(7) STEP3(8) STEP3(9) STEP3(10) STEP3(11) STEP3(12) STEP3(13) STEP3(14) STEP3(15)
#undef STEP3
    constexpr int K8 = KH * 8, S8 = 8 - K8;
    UNIT(15) UNIT(16) UNIT(17) UNIT(18) UNIT(19)
    MFMA_SETTLE();
    stsf4(eB, f32x4{a[S8], a[S8 + 1], a[S8 + 2], a[S8 + 3]}); stsf4(eB + 1024, f32x4{a[S8 + 4], a[S8 + 5], a[S8 + 6], a[S8 + 7]});
    WAITV_BAR(12);
    if (w == 0 && l == 0) put_flag(p.ws + WS_FE + id.G * 256 + id.c * 4, tag);
    f32x4 h0 = ldsf4(eA), h1 = ldsf4(eA + 1024);
    UNIT(20) UNIT(21) UNIT(22) UNIT(23) UNIT(24) UNIT(25) UNIT(26) UNIT(27) UNIT(28) UNIT(29)
    gst16_sc1(adst + (4 + KH) * 1024, vo, u32x4{q0[0], q0[1], q1[0], q1[1]});
    kp = a;
#define UL(U) UNITG(U, K8, h0, h1, q0)
    UL(0) UL(1) UL(2) UL(3) UL(4) UL(5) UL(6) UL(7) UL(8) UL(9) UL(10) UL(11) UL(12) UL(13) UL(14)
#undef UL
    {
      asm volatile("s_nop 1" : "+v"(q0));
      const auto e0 = __builtin_amdgcn_permlane32_swap(q0[0], q0[0], false, false), e1 = __builtin_amdgcn_permlane32_swap(q0[1], q0[1], false, false);
      if (l < 32) gst16_sc1(adst + 6 * 1024 + KH * 512, vo, u32x4{e0[0], e1[0], e0[1], e1[1]});
    }
  }
  asm volatile("global_load_dword %0, %1, %2 sc1" : "=v"(fe) : "v"((l & 15) * 4), "s"(p.ws + WS_FE + (id.g >> 1) * 256) : "memory");
  STAMP(17)
  BAR();
  dma4s(dsrc + 21504, vo, SL2 + w * 4096);
  set_m0(TOPA + w * 3072); dma1<0>(dsrc + 4096, vo); dma1<1024>(dsrc + 4096, vo); dma1<2048>(dsrc + 4096, vo);
  WRL(14); WRL(15); WRL(16); WRL(17);
  asm volatile("s_waitcnt vmcnt(11)\n\ts_barrier" : "+v"(fe)::"memory");
  STAMP(18)
  if (w == 1 && l == 0) put_flag(p.ws + WS_FA + id.G * 256 + id.c * 4, tag);
#undef P4_ROUND
#undef STEP
#undef UNIT
#undef UNITG
}

__global__ __launch_bounds__(512, 2) void k_main(Params p) {
  extern __shared__ __attribute__((aligned(16))) char smem[];
  const int tid = threadIdx.x, l = tid & 63, w = rfl(tid >> 6);
  const Ids id = ids();
  const int g = id.g, j = id.j, h = l >> 5;
  const u32 vo = l * 16;
  const u32 epoch = *(const u32*)(p.ws + WS_EPOCH);
  const u32 tag = epoch * 0x9E3779B1u + 0x7F4A7C15u;
  const int tt = w & 1, nt = (w >> 1) & 1, kh = w >> 2, kq = w >> 1;
  const char* modr = p.ws + WS_MOD + (blockIdx.x & 7) * 12288;
  STAMP_DECL STAMP(0)
  u32x4 q0, q1, fo_, fd_, fg[2];
  char *pdo, *pdd, *pdg;
  {
    const int nh = (l >> 4) & 1, q = (l & 15) >> 2, pp = l & 3;
    const int uid = g * 7 + j - 1, uT = uid >> 1, ukh = uid & 1;
    const int rk = (blockIdx.x >> 6) * 7 + j - 1;
    const bool gemv = j != 0 && rk < 24;
    const int mu = 4 * ((blockIdx.x & 7) + 8 * (rk >> 2)) + (rk & 3);
    const u32 rawo = (l >> 3) * 1024 + (((l & 7) ^ (((l >> 4) & 1) << 2)) * 16);
#define LATE_DMA() {                                                                                                                              \
      _Pragma("unroll") for (int i = 0; i < 8; ++i) { set_m0(F_A + (8 * w + i) * 1024); dma<0>(p.wq + (size_t)(64 * w + 8 * i) * DM + 64 * j, rawo); } \
      if (j != 0) {                                                                                                                               \
        const u32 go = (l >> 1) * (FF * 2) + (l & 1) * 16;                                                                                        \
        set_m0(F_B + w * 1024); dma<0>(p.wg + (size_t)(256 * ukh + 32 * w) * FF + 16 * uT, go);                                                   \
        set_m0(F_B + 8192 + 128 + w * 1024); dma<0>(p.wu + (size_t)(256 * ukh + 32 * w) * FF + 16 * uT, go);                                      \
        set_m0(F_D + w * 1024); dma<0>(p.wd + (size_t)(64 * (uid >> 3) + 8 * w) * DM + 64 * (uid & 7), rawo);                                     \
      }                                                                                                                                           \
      if (w >= 6) { set_m0(F_D + 8192 + (w - 6) * 1024); dma<0>(p.wo + (size_t)(16 * g + 8 * (w - 6)) * DM + 64 * j, rawo); } }
    u32x4 w0, w1; u32 c0, c1;
    if (gemv) {
      const u32 mo = (tid >> 1) * 6144 + (tid & 1) * 16;
      w0 = gld_s<0, 0>(p.wm + 16 * mu, mo); w1 = gld_s<0, 0>(p.wm + 16 * mu + 256 * 3072, mo);
      c0 = gld2_s<0>(p.c, (tid >> 1) * 2); c1 = gld2_s<512>(p.c, (tid >> 1) * 2);
    }
    u32x4 xv[8];
    {
      const char* xs = (const char*)(p.x + (size_t)(64 * g + 8 * w) * DM);
      xv[0] = gld_s<0, 0>(xs, vo); xv[1] = gld_s<0, 1024>(xs, vo); xv[2] = gld_s<0, 2048>(xs, vo); xv[3] = gld_s<0, 3072>(xs, vo);
      xv[4] = gld_s<0, 0>(xs + 4096, vo); xv[5] = gld_s<0, 1024>(xs + 4096, vo); xv[6] = gld_s<0, 2048>(xs + 4096, vo); xv[7] = gld_s<0, 3072>(xs + 4096, vo);
    }
    if (j == 0) {
      set_m0(F_H + w * 2048); dma<0>(p.wv + w * 1024, vo); dma<1024>(p.wv + w * 1024, vo);
      set_m0(F_H + 16384 + 128 + w * 2048); dma<0>(p.wk + w * 1024, vo); dma<1024>(p.wk + w * 1024, vo);
      LATE_DMA()
    }
    if (gemv) {
      asm volatile("s_waitcnt vmcnt(8)" : "+v"(w0), "+v"(w1), "+v"(c0), "+v"(c1)::"memory");
      const float f0 = bflo(c0), f1 = bflo(c1);
      const float s0 = rbf(f0 * __builtin_amdgcn_rcpf(1.f + EXP2(-1.4426950408889634f * f0))), s1 = rbf(f1 * __builtin_amdgcn_rcpf(1.f + EXP2(-1.4426950408889634f * f1)));
      float a[8];
#pragma unroll
      for (int i = 0; i < 4; ++i) { a[2 * i] = s0 * bflo(w0[i]) + s1 * bflo(w1[i]); a[2 * i + 1] = s0 * bfhi(w0[i]) + s1 * bfhi(w1[i]); }
#pragma unroll
      for (int i = 0; i < 4; ++i) a[i] = halve<0x4E>(l & 2, a[i], a[i + 4]);
#pragma unroll
      for (int i = 0; i < 2; ++i) a[i] = halve<0x124>(l & 4, a[i], a[i + 2]);
      a[0] = halve<0x128>(l & 8, a[0], a[1]);
      a[0] = swap32_sum(swap16_sum(a[0]));
      if (l < 16) *(LAS float*)(uintptr_t)(F_M + w * 64 + ((l & 1) * 8 + (l & 2) * 2 + ((l >> 1) & 2) + (l >> 3)) * 4) = a[0];
      BAR();
      if (w == 7) {
        f32x2 s = {0.f, 0.f};
#pragma unroll
        for (int i = 0; i < 8; ++i) s += ldsf2(F_M + i * 64 + (l & 7) * 8);
        gst8_sc1(p.ws + WS_MOD + mu * 64, (l >> 3) * 12288 + (l & 7) * 8, u32x2{pk(s[0], s[1]), tag});
      }
    }
    STAMP(22)
    if (j == 0) WAITV(12); else if (gemv && w == 7) WAITV(1); else WAITV(0);
    asm volatile("" : "+v"(xv[0]), "+v"(xv[1]), "+v"(xv[2]), "+v"(xv[3]), "+v"(xv[4]), "+v"(xv[5]), "+v"(xv[6]), "+v"(xv[7]));
    float xh[8][8];
    {
      float s1[8], s2[8];
#pragma unroll
      for (int i = 0; i < 8; ++i) {
        s1[i] = 0.f; s2[i] = 0.f;
#pragma unroll
        for (int e = 0; e < 4; ++e) { const u32 v = xv[i][e]; s1[i] = dot2(v, 0x3f803f80u, s1[i]); s2[i] = dot2(v, v, s2[i]); }
      }
#pragma unroll
      for (int i = 0; i < 4; ++i) { s1[i] = halve<0xB1>(l & 1, s1[i], s1[i + 4]); s2[i] = halve<0xB1>(l & 1, s2[i], s2[i + 4]); }
#pragma unroll
      for (int i = 0; i < 2; ++i) { s1[i] = halve<0x4E>(l & 2, s1[i], s1[i + 2]); s2[i] = halve<0x4E>(l & 2, s2[i], s2[i + 2]); }
      float t1 = halve<0x124>(l & 4, s1[0], s1[1]), t2 = halve<0x124>(l & 4, s2[0], s2[1]);
      t1 += dpp<0x128>(t1); t2 += dpp<0x128>(t2);
      t1 = swap32_sum(swap16_sum(t1)); t2 = swap32_sum(swap16_sum(t2));
      const float m = t1 * (1.f / DM), r = __builtin_amdgcn_rsqf(fmaxf(t2 * (1.f / DM) - m * m, 0.f) + EPS), nm = -m * r;
#define LNROW(I, LN) { const float ri = dpp<0x150 + (LN)>(r), ni = dpp<0x150 + (LN)>(nm); _Pragma("unroll") for (int e = 0; e < 4; ++e) { xh[I][2 * e] = fmaf(bflo(xv[I][e]), ri, ni); xh[I][2 * e + 1] = fmaf(bfhi(xv[I][e]), ri, ni); } }
      LNROW(0, 0) LNROW(1, 4) LNROW(2, 2) LNROW(3, 6) LNROW(4, 1) LNROW(5, 5) LNROW(6, 3) LNROW(7, 7)
#undef LNROW
#pragma unroll
      for (int i = 0; i < 8; ++i) asm volatile("" : "+v"(xh[i][0]), "+v"(xh[i][1]), "+v"(xh[i][2]), "+v"(xh[i][3]), "+v"(xh[i][4]), "+v"(xh[i][5]), "+v"(xh[i][6]), "+v"(xh[i][7]));
    }
    STAMP(23)
    if (w > 1 && j != 0) LATE_DMA()
    u32x4 wkvf[16];
    if (j == 0) {
      WAITV_BAR(8);
      const u32 a0 = F_H + nh * (16384 + 128) + (256 * kh + 8 * h + q) * 32 + pp * 8;
#pragma unroll
      for (int s = 0; s < 16; ++s) wkvf[s] = ldstr2(a0 + s * 512, a0 + s * 512 + 128);
    }
    STAMP(24)
    if (w == 0) {
      u32x4 a0, a1, b0, b1;
#pragma unroll 1
      for (int it = 0; it < SPIN_CAP; ++it) {
        asm volatile("global_load_dwordx4 %0, %4, %5 sc1\n\tglobal_load_dwordx4 %1, %4, %5 offset:16 sc1\n\tglobal_load_dwordx4 %2, %4, %5 offset:2048 sc1\n\t"
                     "global_load_dwordx4 %3, %4, %5 offset:2064 sc1\n\ts_waitcnt vmcnt(0)" : "=&v"(b0), "=&v"(b1), "=&v"(a0), "=&v"(a1) : "v"(l * 32), "s"(modr) : "memory");
        if (__all(a0[1] == tag && a0[3] == tag && a1[1] == tag && a1[3] == tag && b0[1] == tag && b0[3] == tag && b1[1] == tag && b1[3] == tag)) break;
      }
      sts128(F_M + 512 + vo, u32x4{pk(1.f + bflo(a0[0]), 1.f + bfhi(a0[0])), pk(1.f + bflo(a0[2]), 1.f + bfhi(a0[2])), pk(1.f + bflo(a1[0]), 1.f + bfhi(a1[0])), pk(1.f + bflo(a1[2]), 1.f + bfhi(a1[2]))});
      sts128(F_M + 1536 + vo, u32x4{b0[0], b0[2], b1[0], b1[2]});
    } else if (w == 1) {
      if (j != 0) {
        u32x4 a0, b0;
#pragma unroll 1
        for (int it = 0; it < SPIN_CAP; ++it) {
          asm volatile("global_load_dwordx4 %0, %2, %3 sc1\n\tglobal_load_dwordx4 %1, %2, %3 offset:2048 sc1\n\ts_waitcnt vmcnt(0)" : "=&v"(b0), "=&v"(a0) : "v"(vo), "s"(modr + 6144 + ukh * 1024) : "memory");
          if (__all(a0[1] == tag && a0[3] == tag && b0[1] == tag && b0[3] == tag)) break;
          __builtin_amdgcn_s_sleep(2);
        }
        stsf4(F_M + 2560 + vo, f32x4{rbf(1.f + bflo(a0[0])), rbf(1.f + bfhi(a0[0])), rbf(1.f + bflo(a0[2])), rbf(1.f + bfhi(a0[2]))});
        stsf4(F_M + 3584 + vo, f32x4{bflo(b0[0]), bfhi(b0[0]), bflo(b0[2]), bfhi(b0[2])});
      }
    }
    BAR();
    STAMP(25)
    if (w <= 1 && j != 0) LATE_DMA()
    {
      const u32x4 ca = lds128(F_M + 512 + vo), sa = lds128(F_M + 1536 + vo);
      float A[8], B[8];
#pragma unroll
      for (int e = 0; e < 4; ++e) { A[2 * e] = bflo(ca[e]); A[2 * e + 1] = bfhi(ca[e]); B[2 * e] = bflo(sa[e]); B[2 * e + 1] = bfhi(sa[e]); }
#pragma unroll
      for (int i = 0; i < 8; ++i) {
        u32x4 o;
#pragma unroll
        for (int e = 0; e < 4; ++e) o[e] = pk(fmaf(xh[i][2 * e], A[2 * e], B[2 * e]), fmaf(xh[i][2 * e + 1], A[2 * e + 1], B[2 * e + 1]));
        const int tk = 8 * w + i;
        sts128(F_H + ((tk >> 5) * 32 + (l >> 1)) * 1024 + (l & 1) * 512 + (((tk & 31) ^ (l & 7)) * 16), o);
      }
    }
    BAR();
    STAMP(26)
    u32x4 xf[16];
#pragma unroll
    for (int s = 0; s < 16; ++s) xf[s] = lds128(F_H + (tt * 32 + 16 * kh + s) * 1024 + h * 512 + (((l & 31) ^ ((2 * s + h) & 7)) * 16));
    if (j == 0) {
      const u32 xs = F_B + w * 2048 + vo, xr = F_B + (w ^ 4) * 2048 + vo;
      f32x16 acc = {};
      if (nt == 0) {
#pragma unroll
        for (int s = 0; s < 16; ++s) acc = mfma(wkvf[s], xf[s], acc);
      } else {
#pragma unroll
        for (int s = 0; s < 16; ++s) acc = mfma(xf[s], wkvf[s], acc);
      }
      float keep[8];
      if (kh == 0) { stsf4(xs, f32x4{acc[8], acc[9], acc[10], acc[11]}); stsf4(xs + 1024, f32x4{acc[12], acc[13], acc[14], acc[15]});
#pragma unroll
        for (int e = 0; e < 8; ++e) keep[e] = acc[e];
      } else { stsf4(xs, f32x4{acc[0], acc[1], acc[2], acc[3]}); stsf4(xs + 1024, f32x4{acc[4], acc[5], acc[6], acc[7]});
#pragma unroll
        for (int e = 0; e < 8; ++e) keep[e] = acc[8 + e];
      }
      BAR();
      const f32x4 r0 = ldsf4(xr), r1 = ldsf4(xr + 1024);
      u32x4 o = {pk(keep[0] + r0[0], keep[1] + r0[1]), pk(keep[2] + r0[2], keep[3] + r0[3]), pk(keep[4] + r1[0], keep[5] + r1[1]), pk(keep[6] + r1[2], keep[7] + r1[3])};
      char* img = p.ws + WS_KV + (size_t)(2 * g + tt) * TS;
      if (nt == 0) { if (kh == 1) gst16_sc1(img, vo, o); }
      else if ((l & 31) <= 17) {
        if ((l & 31) == 16) o = u32x4{0x3f803f80u, 0x3f803f80u, 0x3f803f80u, 0x3f803f80u};
        if ((l & 31) == 17) o = u32x4{0u, 0u, 0u, 0u};
        gst16_sc1(img + 1024 + kh * 576, h * 288 + (l & 31) * 16, o);
      }
      WAITV_BAR(0);
      if (w == 0 && l < 32) put_flag(p.ws + WS_FKV + l * 128 + g * 4, tag);
    }
    STAMP(21)
    WAITV_BAR(0);
    const u32 rawr = (((nh * 2 + (pp >> 1)) ^ (((q >> 1) & 1) << 2)) * 16) + (pp & 1) * 8;
    f32x16 acc = {};
    {
      const u32 a0 = F_A + (256 * kh + 8 * h + q) * 128 + (rawr ^ (nt * 64));
#pragma unroll
      for (int s = 0; s < 16; ++s) acc = mfma(ldstr2(a0 + s * 2048, a0 + s * 2048 + 512), xf[s], acc);
    }
    const u32 xs = F_H + w * 4096 + vo, xr = F_H + (w ^ 4) * 4096 + vo;
    stsf4(xs, f32x4{acc[0], acc[1], acc[2], acc[3]}); stsf4(xs + 1024, f32x4{acc[4], acc[5], acc[6], acc[7]});
    stsf4(xs + 2048, f32x4{acc[8], acc[9], acc[10], acc[11]}); stsf4(xs + 3072, f32x4{acc[12], acc[13], acc[14], acc[15]});
    STAMP(27)
    f32x4 cs[2][4];
    if (w >= 6) { const u32 a0 = F_D + 8192 + (4 * h + q) * 128 + (rawr ^ (tt * 64)); fo_ = ldstr2(a0, a0 + 1024); }
    if (j != 0) {
      const bool nat = (4 * (uid >> 3) + kq) % 7 == 6;
      const u32 a0 = F_D + (16 * kq + (nat ? 8 : 4) * h + q) * 128 + (rawr ^ (tt * 64));
      fd_ = ldstr2(a0, a0 + (nat ? 512 : 1024));
#pragma unroll
      for (int i = 0; i < 2; ++i) {
        const int s = 2 * w + i;
        const u32 a1 = F_B + (pp >> 1) * (8192 + 128) + (16 * s + 4 * h + q) * 32 + nh * 16 + (pp & 1) * 8;
        fg[i] = ldstr2(a1, a1 + 256);
        cs[i][0] = ldsf4(F_M + 2560 + (16 * s + 4 * h) * 4); cs[i][1] = ldsf4(F_M + 2560 + (16 * s + 8 + 4 * h) * 4);
        cs[i][2] = ldsf4(F_M + 3584 + (16 * s + 4 * h) * 4); cs[i][3] = ldsf4(F_M + 3584 + (16 * s + 8 + 4 * h) * 4);
      }
    }
    if (w == 2) poll_flags<32>(p.ws + WS_FKV + (blockIdx.x & 31) * 128, tag, l);
    if (w == 3) {
#pragma unroll 1
      for (int it = 0; it < SPIN_CAP; ++it) {
        const u32 v = __hip_atomic_load((const GAS u32*)(modr + 4096 + j * 256 + h * 6144 + (l & 31) * 8 + 4), RLX_AGENT);
        if (__all(v == tag)) break;
        __builtin_amdgcn_s_sleep(2);
      }
    }
    BAR();
    STAMP(28)
    const f32x4 r0 = ldsf4(xr), r1 = ldsf4(xr + 1024), r2 = ldsf4(xr + 2048), r3 = ldsf4(xr + 3072);
    q0 = u32x4{pk((acc[0] + r0[0]) * C2, (acc[1] + r0[1]) * C2), pk((acc[2] + r0[2]) * C2, (acc[3] + r0[3]) * C2), pk((acc[4] + r1[0]) * C2, (acc[5] + r1[1]) * C2), pk((acc[6] + r1[2]) * C2, (acc[7] + r1[3]) * C2)};
    q1 = u32x4{pk((acc[8] + r2[0]) * C2, (acc[9] + r2[1]) * C2), pk((acc[10] + r2[2]) * C2, (acc[11] + r2[3]) * C2), pk((acc[12] + r3[0]) * C2, (acc[13] + r3[1]) * C2), pk((acc[14] + r3[2]) * C2, (acc[15] + r3[3]) * C2)};
    BAR();
    STAMP(29)
    { const u32 o = (w & 1) * 69632 + (w >> 1) * 6144;
      dma4s(p.ws + WS_KV + o, vo, o); set_m0(o + 4096); dma1<0>(p.ws + WS_KV + o + 4096, vo); dma1<1024>(p.ws + WS_KV + o + 4096, vo); }
    pdo = p.ws + WS_WO + ((size_t)(2 * j + tt) * 32 + g) * 1024;
    pdd = p.ws + WS_WD + ((size_t)(2 * (uid & 7) + tt) * 112 + 4 * (uid >> 3) + kq) * 1024;
    pdg = p.ws + WS_WGU + ((size_t)uT * 32 + 16 * ukh + 2 * w) * 1024;
    if (j != 0) {
      float us = 0.f, wsum = 0.f;
#pragma unroll
      for (int i = 0; i < 2; ++i) {
        const u32x4 f = fg[i];
        const f32x4 c0 = cs[i][0], c1 = cs[i][1], s0 = cs[i][2], s1 = cs[i][3];
        u32x4 o;
        o[0] = pk(bflo(f[0]) * c0[0], bfhi(f[0]) * c0[1]); o[1] = pk(bflo(f[1]) * c0[2], bfhi(f[1]) * c0[3]);
        o[2] = pk(bflo(f[2]) * c1[0], bfhi(f[2]) * c1[1]); o[3] = pk(bflo(f[3]) * c1[2], bfhi(f[3]) * c1[3]);
#pragma unroll
        for (int e = 0; e < 4; ++e) { const u32 v = o[e]; us = dot2(v, 0x3f803f80u, us); }
        wsum += bflo(f[0]) * s0[0] + bfhi(f[0]) * s0[1] + bflo(f[1]) * s0[2] + bfhi(f[1]) * s0[3] + bflo(f[2]) * s1[0] + bfhi(f[2]) * s1[1] + bflo(f[3]) * s1[2] + bfhi(f[3]) * s1[3];
        fg[i] = o;
      }
      us = swap32_sum(us); wsum = swap32_sum(wsum);
      if (l < 32) *(LAS f32x2*)(uintptr_t)(OEX + w * 256 + l * 8) = f32x2{us, wsum};
      WAITV_BAR(0);
      if (w == 7) {
        f32x2 s = {0.f, 0.f};
#pragma unroll
        for (int i = 0; i < 8; ++i) s += ldsf2(OEX + i * 256 + (l & 31) * 8);
        const float uh = rbf(s[0]), wh = rbf(s[1]);
        u32x4 o = {pk(uh, s[0] - uh), pk(uh, wh), pk(s[1] - wh, wh), 0u};
        if (l >= 32) o = u32x4{0u, 0u, 0u, 0u};
        gst16_sc1(p.ws + WS_AUG + (size_t)(uT * 2 + ukh) * 1024, vo, o);
      }
    } else {
      WAITV_BAR(0);
    }
  }
  const size_t fo = ((size_t)(2 * g + tt) * 32 + 4 * j + 2 * nt) * 1024;
  STAMP(1)
  f32x16 nm0, nm1;
  {
    const u32x4 k0 = lds128(vo);
    const float m0 = rowmax16(mfma(k0, q0, f32x16{})), m1 = rowmax16(mfma(k0, q1, f32x16{}));
#pragma unroll
    for (int r = 0; r < 16; ++r) { nm0[r] = -m0; nm1[r] = -m1; }
    PIN(nm0); PIN(nm1);
  }
  const u32 vrow = 1024 + h * 288 + min(l & 31, 17) * 16;
  u32 ka = kh * 32 * TS + vo, va = kh * 32 * TS + vrow;
  f32x16 o0 = {}, o1 = {};
  u32x4 kA = lds128(ka), kB = lds128(ka + TS);
  f32x16 s0 = mfma(kA, q0, nm0), s1;
  u32x4 vA0 = lds128(va), vA1 = lds128(va + 576), vB0 = {}, vB1 = {};
  u32x4 pa0, pa1, pb0 = {}, pb1 = {};
  PIN(s0);
  if (kh) {
    const u32 o = nt * 69632 + 24576 + tt * 22528;
#pragma unroll
    for (int i = 0; i < 5; ++i) dma4s(p.ws + WS_KV + o + i * 4096, vo, o + i * 4096);
    set_m0(o + 20480); dma1<0>(p.ws + WS_KV + o + 20480, vo); dma1<1024>(p.ws + WS_KV + o + 20480, vo);
  }
#define PREP_ST() { if (j != 0) { gst16_sc1(pdd, vo, fd_); gst16_sc1(pdg, vo, fg[0]); gst16_sc1(pdg + 1024, vo, fg[1]); } }
  if (kh) { PREP_ST() if (w >= 6) gst16_sc1(pdo, vo, fo_); }
  u32x4 wf[16];
#define HALF(SC, N0, N1, MF, OP, VP0, P0, VP1, P1, LD1, LD2)                                              \
  {                                                                                                       \
    MF; LD1; SB();                                                                                        \
    float e0 = EXP2(SC[0]), e1 = EXP2(SC[1]), e2 = EXP2(SC[2]), e3 = EXP2(SC[3]), e4 = EXP2(SC[4]), e5 = EXP2(SC[5]), e6 = EXP2(SC[6]), e7 = EXP2(SC[7]); SB(); \
    OP = mfma(VP0, P0, OP); SB();                                                                         \
    float e8 = EXP2(SC[8]), e9 = EXP2(SC[9]), e10 = EXP2(SC[10]), e11 = EXP2(SC[11]), e12 = EXP2(SC[12]), e13 = EXP2(SC[13]), e14 = EXP2(SC[14]), e15 = EXP2(SC[15]); SB(); \
    OP = mfma(VP1, P1, OP); LD2; SB();                                                                    \
    N0[0] = pk(e0, e1); N0[1] = pk(e2, e3); N0[2] = pk(e4, e5); N0[3] = pk(e6, e7); N1[0] = pk(e8, e9);   \
    N1[1] = pk(e10, e11); N1[2] = pk(e12, e13); N1[3] = pk(e14, e15); SB();                               \
  }
#define TILE(T, KC, KN, VC0, VC1, VP0, VP1)                                                               \
    HALF(s0, pa0, pa1, s1 = mfma(KC, q1, nm1), o1, VP0, pb0, VP1, pb1, KC = lds128(ka + ((T) + 2) * TS),  \
         (VP0 = lds128(va + ((T) + 1) * TS), VP1 = lds128(va + ((T) + 1) * TS + 576)))                    \
    HALF(s1, pb0, pb1, s0 = mfma(KN, q0, nm0), o0, VC0, pa0, VC1, pa1, , )
  const char* wgu = p.ws + WS_WGU + (size_t)id.c * 7 * 32768;
  const char* wob = p.ws + WS_WO + ((size_t)(2 * j) * 32 + 8 * kq) * 1024;
#define LDWF(SEL)                                                                                                     \
  asm volatile("s_cmp_lg_u32 %21, 0\n\ts_cbranch_scc1 .Lwf%=\n\t"                                                     \
               "global_load_dwordx4 %0, %16, %17 sc1\n\tglobal_load_dwordx4 %1, %16, %17 offset:1024 sc1\n\tglobal_load_dwordx4 %2, %16, %17 offset:2048 sc1\n\tglobal_load_dwordx4 %3, %16, %17 offset:3072 sc1\n\t"     \
               "global_load_dwordx4 %4, %16, %18 sc1\n\tglobal_load_dwordx4 %5, %16, %18 offset:1024 sc1\n\tglobal_load_dwordx4 %6, %16, %18 offset:2048 sc1\n\tglobal_load_dwordx4 %7, %16, %18 offset:3072 sc1\n\t"     \
               "global_load_dwordx4 %8, %16, %19 sc1\n\tglobal_load_dwordx4 %9, %16, %19 offset:1024 sc1\n\tglobal_load_dwordx4 %10, %16, %19 offset:2048 sc1\n\tglobal_load_dwordx4 %11, %16, %19 offset:3072 sc1\n\t"   \
               "global_load_dwordx4 %12, %16, %20 sc1\n\tglobal_load_dwordx4 %13, %16, %20 offset:1024 sc1\n\tglobal_load_dwordx4 %14, %16, %20 offset:2048 sc1\n\tglobal_load_dwordx4 %15, %16, %20 offset:3072 sc1\n"   \
               ".Lwf%=:"                                                                                              \
               : "+v"(wf[0]), "+v"(wf[1]), "+v"(wf[2]), "+v"(wf[3]), "+v"(wf[4]), "+v"(wf[5]), "+v"(wf[6]), "+v"(wf[7]), "+v"(wf[8]), "+v"(wf[9]), "+v"(wf[10]), \
                 "+v"(wf[11]), "+v"(wf[12]), "+v"(wf[13]), "+v"(wf[14]), "+v"(wf[15])                                  \
               : "v"(vo), "s"(wob), "s"(wob + 4096), "s"(wob + 32768), "s"(wob + 36864), "s"(SEL) : "memory", "scc")
#pragma unroll
  for (int s4 = 0; s4 < 16; ++s4) asm volatile("" : "=v"(wf[s4]));
  const int qt = blockIdx.x >> 6;
#pragma unroll 1
  for (int seg = 0; seg < 4; ++seg) {
    if (seg == 1) {
      if (!kh) PREP_ST()
      ASTAMP(10)
      WAITV_BAR(0);
      ASTAMP(24)
      if (w == 0 && l < 8) put_flag(p.ws + WS_FP + l * 1024 + blockIdx.x * 4, tag);
    } else if (seg == 2) {
      ASTAMP(21)
      if (w == 0) {
        u32x4 f;
#pragma unroll 1
        for (int it = 0; it < SPIN_CAP; ++it) {
          asm volatile("global_load_dwordx4 %0, %1, %2 sc1\n\ts_waitcnt vmcnt(0)" : "=&v"(f) : "v"(vo), "s"(p.ws + WS_FP + (blockIdx.x & 7) * 1024) : "memory");
          if (__all(f[0] == tag && f[1] == tag && f[2] == tag && f[3] == tag)) break;
          __builtin_amdgcn_s_sleep(1);
        }
        if (blockIdx.x == 0 && l == 0) put_flag(p.ws + WS_EPOCH, epoch + 1u);
      }
      BAR();
      ASTAMP(23)
    } else if (seg == 3) {
      ASTAMP(22)
      if (!kh) {
        dma4s(wgu + w * 8192, vo, SL0 + w * 8192); dma4s(wgu + w * 8192 + 4096, vo, SL0 + w * 8192 + 4096);
        dma4s(wgu + 32768 + w * 8192, vo, SL1 + w * 8192); dma4s(wgu + 32768 + w * 8192 + 4096, vo, SL1 + w * 8192 + 4096);
        set_m0(JUNK);
        touch8k(wgu + 65536 + qt * 40960 + w * 8192, l * 128);
        if (w < 2) touch8k(w == 0 ? wgu + 65536 + qt * 40960 + 32768 : p.ws + WS_AUG + (size_t)id.c * 14336 + (qt & 1) * 6144, l * 128);
      }
    }
    LDWF((seg ^ 2) | (kh ^ 1));
    TILE(0, kA, kB, vA0, vA1, vB0, vB1) TILE(1, kB, kA, vB0, vB1, vA0, vA1) TILE(2, kA, kB, vA0, vA1, vB0, vB1) TILE(3, kB, kA, vB0, vB1, vA0, vA1)
    TILE(4, kA, kB, vA0, vA1, vB0, vB1) TILE(5, kB, kA, vB0, vB1, vA0, vA1) TILE(6, kA, kB, vA0, vA1, vB0, vB1) TILE(7, kB, kA, vB0, vB1, vA0, vA1)
    ka += 8 * TS; va += 8 * TS; PIN(ka); PIN(va);
  }
  o1 = mfma(vB0, pb0, o1); o1 = mfma(vB1, pb1, o1);
  LDWF(kh);
  if (!kh) {
    set_m0(JUNK);
    touch8k(p.ws + WS_WD + (size_t)(2 * j) * 114688 + qt * 57344 + w * 8192, l * 128);
    if (w < 3) touch8k(p.ws + WS_WD + (size_t)(2 * j) * 114688 + qt * 57344 + (w + 4) * 8192, l * 128);
  }
#undef HALF
#undef TILE
  STAMP(2)
  const u32 xo = (u32)((l & 31) * DM + 4 * h) * 2;
  const char* xres = (const char*)(p.x + (size_t)(64 * g + 32 * tt) * DM + 64 * j + 32 * nt + 16 * kh);
  u32x2 xa = gld8_s<0>(xres, xo), xb = gld8_s<16>(xres, xo);
  u32x4 ga = gld_s<1, 0>(modr + 4096 + (64 * j + 32 * nt + 16 * kh) * 4, h * 16), gb = gld_s<1, 32>(modr + 4096 + (64 * j + 32 * nt + 16 * kh) * 4, h * 16);
  {
    const u32 xs = OEX + w * 2304, xr = OEX + (w ^ 4) * 2304;
    float o[9];
    if (kh) { stsf4(xs + vo, f32x4{o0[0], o0[1], o0[2], o0[3]}); stsf4(xs + 1024 + vo, f32x4{o0[4], o0[5], o0[6], o0[7]}); *(LAS float*)(uintptr_t)(xs + 2048 + l * 4) = o0[8];
#pragma unroll
      for (int e = 0; e < 9; ++e) o[e] = o1[e];
    } else { stsf4(xs + vo, f32x4{o1[0], o1[1], o1[2], o1[3]}); stsf4(xs + 1024 + vo, f32x4{o1[4], o1[5], o1[6], o1[7]}); *(LAS float*)(uintptr_t)(xs + 2048 + l * 4) = o1[8];
#pragma unroll
      for (int e = 0; e < 9; ++e) o[e] = o0[e];
    }
    BAR();
    const f32x4 r0 = ldsf4(xr + vo), r1 = ldsf4(xr + 1024 + vo);
#pragma unroll
    for (int e = 0; e < 4; ++e) { o[e] += r0[e]; o[4 + e] += r1[e]; }
    float ls = lowhalf(o[8] + *(const LAS float*)(uintptr_t)(xr + 2048 + l * 4));
    bool bad = !(ls < 1.2676506e30f);
#pragma unroll
    for (int e = 0; e < 8; ++e) bad |= !(fabsf(o[e]) < 3.0e38f);
    if (__builtin_expect(__any(bad), 0)) {
      const u32x4 qf = kh ? q1 : q0;
      float m = -INFINITY;
      f32x16 oo = {};
#pragma unroll 1
      for (int t = 0; t < 64; ++t) {
        u32x4 kf, v0, v1;
        asm volatile("global_load_dwordx4 %0, %3, %5 sc1\n\tglobal_load_dwordx4 %1, %4, %5 sc1\n\tglobal_load_dwordx4 %2, %4, %5 offset:576 sc1\n\ts_waitcnt vmcnt(0)"
                     : "=&v"(kf), "=&v"(v0), "=&v"(v1) : "v"(vo), "v"(vrow), "s"(p.ws + WS_KV + t * TS) : "memory");
        const f32x16 s = mfma(kf, qf, f32x16{});
        const float mn = fmaxf(m, rowmax16(s)), al = EXP2(m - mn);
        m = mn;
        u32x4 pa, pb;
#pragma unroll
        for (int r = 0; r < 4; ++r) { pa[r] = pk(EXP2(s[2 * r] - mn), EXP2(s[2 * r + 1] - mn)); pb[r] = pk(EXP2(s[8 + 2 * r] - mn), EXP2(s[9 + 2 * r] - mn)); }
#pragma unroll
        for (int r = 0; r < 16; ++r) oo[r] *= al;
        oo = mfma(v0, pa, oo); oo = mfma(v1, pb, oo);
      }
#pragma unroll
      for (int e = 0; e < 8; ++e) o[e] = oo[e];
      ls = lowhalf(oo[8]);
    }
    const float rl = __builtin_amdgcn_rcpf(ls);
    gst16_sc1(p.ws + WS_O + fo + kh * 1024, vo, u32x4{pk(o[0] * rl, o[1] * rl), pk(o[2] * rl, o[3] * rl), pk(o[4] * rl, o[5] * rl), pk(o[6] * rl, o[7] * rl)});
  }
  asm volatile("s_waitcnt vmcnt(0)\n\ts_barrier"
               : "+v"(wf[0]), "+v"(wf[1]), "+v"(wf[2]), "+v"(wf[3]), "+v"(wf[4]), "+v"(wf[5]), "+v"(wf[6]), "+v"(wf[7]), "+v"(wf[8]), "+v"(wf[9]),
                 "+v"(wf[10]), "+v"(wf[11]), "+v"(wf[12]), "+v"(wf[13]), "+v"(wf[14]), "+v"(wf[15]), "+v"(xa), "+v"(xb), "+v"(ga), "+v"(gb)::"memory");
  STAMP(3)
  if (w == 0) {
    if (l == 0) put_flag(p.ws + WS_FO + g * 256 + j * 4, tag);
    poll_flags<8>(p.ws + WS_FO + g * 256, tag, l);
    set_m0(JUNK); touch8k(p.ws + WS_O + (size_t)g * 65536 + j * 8192, l * 128);
  }
  BAR();
  STAMP(4)
  u32x4 x1;
  {
    const char* osrc = p.ws + WS_O + ((size_t)(2 * g + tt) * 32 + 8 * kq) * 1024;
    u32x4 xf[8];
    xf[0] = gld_s<1, 0>(osrc, vo); xf[1] = gld_s<1, 1024>(osrc, vo); xf[2] = gld_s<1, 2048>(osrc, vo); xf[3] = gld_s<1, 3072>(osrc, vo);
    xf[4] = gld_s<1, 0>(osrc + 4096, vo); xf[5] = gld_s<1, 1024>(osrc + 4096, vo); xf[6] = gld_s<1, 2048>(osrc + 4096, vo); xf[7] = gld_s<1, 3072>(osrc + 4096, vo);
    f32x16 acc0 = {}, acc1 = {};
#define P3_STEP(S) { waitv_pin<7 - (S)>(xf[S]); acc0 = mfma(wf[S], xf[S], acc0); acc1 = mfma(wf[8 + (S)], xf[S], acc1); }
    P3_STEP(0) P3_STEP(1) P3_STEP(2) P3_STEP(3) P3_STEP(4) P3_STEP(5) P3_STEP(6) P3_STEP(7)
#undef P3_STEP
    STAMP(5)
    float r[8];
    reduce4(acc0, acc1, tt, kq, SL3, vo, r);
    STAMP(6)
    x1 = u32x4{pk(fmaf(bflo(ga[0]), r[0], bflo(xa[0])), fmaf(bfhi(ga[0]), r[1], bfhi(xa[0]))), pk(fmaf(bflo(ga[2]), r[2], bflo(xa[1])), fmaf(bfhi(ga[2]), r[3], bfhi(xa[1]))),
               pk(fmaf(bflo(gb[0]), r[4], bflo(xb[0])), fmaf(bfhi(gb[0]), r[5], bfhi(xb[0]))), pk(fmaf(bflo(gb[2]), r[6], bflo(xb[1])), fmaf(bfhi(gb[2]), r[7], bfhi(xb[1])))};
    gst16_sc1(p.ws + WS_X1 + fo + kh * 1024, vo, x1);
    if (w != 0) dma4s(wgu + 2 * 32768 + w * 4096, vo, SL2 + w * 4096);
    float s1 = 0.f, s2 = 0.f;
#pragma unroll
    for (int e = 0; e < 4; ++e) { const u32 v = x1[e]; s1 = dot2(v, 0x3f803f80u, s1); s2 = dot2(v, v, s2); }
    s1 = swap32_sum(s1); s2 = swap32_sum(s2);
    if (l < 32) *(LAS f32x2*)(uintptr_t)(GAPA + kq * 512 + tt * 256 + l * 8) = f32x2{s1, s2};
    BAR();
    STAMP(7)
    if (w == 0) {
      f32x2 s = {0.f, 0.f};
#pragma unroll
      for (int i = 0; i < 4; ++i) s += ldsf2(GAPA + i * 512 + l * 8);
      gst8_sc1(p.ws + WS_STAT + ((size_t)64 * g * 8 + j) * 8, l * 64, u32x2{__float_as_uint(s[0]), __float_as_uint(s[1])});
      dma4s(wgu + 2 * 32768, vo, SL2);
    }
    dma4s(wgu + 3 * 32768 + w * 4096, vo, SL3 + w * 4096);
    WAITV_BAR(8);
    STAMP(8)
    if (w == 0 && l == 0) put_flag(p.ws + WS_FX + (g >> 1) * 256 + ((g & 1) * 8 + j) * 4, tag);
  }
  u32x4 wr[9][2]; u32 fe;
  if (kh == 0) p4_body<0>(p, id, l, w, tag, wr, fe STAMP_PASS); else p4_body<1>(p, id, l, w, tag, wr, fe STAMP_PASS);
  {
    int ly = l; PIN(ly);
    if (w == 0 && !__all(fe == tag)) poll_flags<16>(p.ws + WS_FE + (g >> 1) * 256, tag, ly);
    asm volatile("s_barrier" ::: "memory");
    STAMP(19)
    const char* xsrc = p.ws + WS_ACT + ((size_t)(2 * g + tt) * 112 + 28 * kq) * 1024;
    u32x4 xf[20];
#define XL(I) (xf[(I) % 20] = gld_s<1, 0>(xsrc + dks(I) * 1024, vo))
#define XL4(I) { xf[I] = gld_s<1, 0>(xsrc + dks(I) * 1024, vo); xf[I + 1] = gld_s<1, 1024>(xsrc + dks(I) * 1024, vo); xf[I + 2] = gld_s<1, 2048>(xsrc + dks(I) * 1024, vo); xf[I + 3] = gld_s<1, 3072>(xsrc + dks(I) * 1024, vo); }
    XL4(0) XL4(4) XL4(8) XL4(12)
    u32x4 g0 = gld_s<1, 0>(modr + 10240 + (64 * j + 32 * nt + 16 * kh) * 4, (ly >> 5) * 16), g1 = gld_s<1, 32>(modr + 10240 + (64 * j + 32 * nt + 16 * kh) * 4, (ly >> 5) * 16);
    f32x16 acc0, acc1; u32x4 wa[19], wb[19];
    u32 blo = kq * 8192 + vo, bmid = blo + 65536, bhi = TOPA + kq * 6144 + vo;
    PIN(blo); PIN(bmid); PIN(bhi);
#define K5_W(S, NT) ((S) < 16 ? ldsr<((S) < 4 ? SL3 : (S) < 8 ? SL0 : (S) < 12 ? SL1 : SL2) + ((S) & 3) * 1024 + (NT) * 4096>(blo, bmid) : lds128(bhi + ((S) & 3) * 1024 + (NT) * 3072))
#define K5_RD(S) { if ((S) < 19) { wa[(S) < 19 ? (S) : 0] = K5_W(S, 0); wb[(S) < 19 ? (S) : 0] = K5_W(S, 1); } }
#define K5_STEP(S, N, RDN)                                                                           \
  {                                                                                                  \
    waitv_pin<N>(xf[(S) % 20]);                                                                      \
    if ((S) == 0) mf0(acc0, wa[0], xf[0]); else mfa(acc0, (S) < 19 ? wa[(S) < 19 ? (S) : 0] : wr[(S) < 19 ? 0 : (S) - 19][0], xf[(S) % 20]); \
    if (RDN) K5_RD((S) + 2)                                                                          \
    if ((S) == 0) mf0(acc1, wb[0], xf[0]); else mfa(acc1, (S) < 19 ? wb[(S) < 19 ? (S) : 0] : wr[(S) < 19 ? 0 : (S) - 19][1], xf[(S) % 20]); \
  }
    K5_RD(0) K5_RD(1)
    asm volatile("s_waitcnt vmcnt(18)"
                 : "+v"(wr[0][0]), "+v"(wr[0][1]), "+v"(wr[1][0]), "+v"(wr[1][1]), "+v"(wr[2][0]), "+v"(wr[2][1]), "+v"(wr[3][0]), "+v"(wr[3][1]), "+v"(wr[4][0]),
                   "+v"(wr[4][1]), "+v"(wr[5][0]), "+v"(wr[5][1]), "+v"(wr[6][0]), "+v"(wr[6][1]), "+v"(wr[7][0]), "+v"(wr[7][1]), "+v"(wr[8][0]), "+v"(wr[8][1])::"memory");
    K5_STEP(0, 17, 1) K5_STEP(1, 16, 1) K5_STEP(2, 15, 1) K5_STEP(3, 14, 1) K5_STEP(4, 13, 1) K5_STEP(5, 12, 1) K5_STEP(6, 11, 1) K5_STEP(7, 10, 1)
    PSTAMP(24)
    if (w == 0) poll_flags<16>(p.ws + WS_FA + (g >> 1) * 256, tag, ly);
    asm volatile("s_barrier" ::: "memory");
    PSTAMP(25)
    XL(16); XL(17); XL(18); XL(19); XL(20); XL(21); XL(22); XL(23); XL(24); XL(25); XL(26); XL(27);
    K5_STEP(8, 21, 1) K5_STEP(9, 20, 1) K5_STEP(10, 19, 1) K5_STEP(11, 18, 1) K5_STEP(12, 17, 1) K5_STEP(13, 16, 1) K5_STEP(14, 15, 1) K5_STEP(15, 14, 1)
    K5_STEP(16, 11, 1) K5_STEP(17, 10, 0) K5_STEP(18, 9, 0)
    PSTAMP(26)
    BAR();
    PSTAMP(27)
    K5_STEP(19, 8, 0) K5_STEP(20, 7, 0) K5_STEP(21, 6, 0) K5_STEP(22, 5, 0) K5_STEP(23, 4, 0) K5_STEP(24, 3, 0) K5_STEP(25, 2, 0) K5_STEP(26, 1, 0) K5_STEP(27, 0, 0)
#undef K5_STEP
#undef K5_RD
#undef K5_W
    MFMA_SETTLE();
    PIN(g0); PIN(g1); PIN(acc0); PIN(acc1);
    STAMP(20)
    float r[8];
    reduce4(acc0, acc1, tt, kq, 0, vo, r);
    const u32 a0 = pk(fmaf(bflo(g0[0]), r[0], bflo(x1[0])), fmaf(bfhi(g0[0]), r[1], bfhi(x1[0]))), a1 = pk(fmaf(bflo(g0[2]), r[2], bflo(x1[1])), fmaf(bfhi(g0[2]), r[3], bfhi(x1[1])));
    const u32 b0 = pk(fmaf(bflo(g1[0]), r[4], bflo(x1[2])), fmaf(bfhi(g1[0]), r[5], bfhi(x1[2]))), b1 = pk(fmaf(bflo(g1[2]), r[6], bflo(x1[3])), fmaf(bfhi(g1[2]), r[7], bfhi(x1[3])));
    const auto s0 = __builtin_amdgcn_permlane32_swap(a0, b0, false, false);
    const auto s1 = __builtin_amdgcn_permlane32_swap(a1, b1, false, false);
    int lz = l; PIN(lz);
    *(u32x4*)(p.out + (size_t)(64 * g + 32 * tt + (lz & 31)) * DM + 64 * j + 32 * nt + 16 * kh + 8 * (lz >> 5)) = u32x4{s0[0], s1[0], s0[1], s1[1]};
  }
  STAMP_FLUSH(0)
}

extern "C" void kernel_launch(void* const* d_in, const int* in_sizes, int n_in, void* d_out, int out_size, void* d_ws, size_t ws_size, hipStream_t stream) {
  Params p;
  p.x = (const u16*)d_in[0]; p.wq = (const u16*)d_in[1]; p.wk = (const u16*)d_in[2]; p.wv = (const u16*)d_in[3]; p.wo = (const u16*)d_in[4];
  p.wg = (const u16*)d_in[6]; p.wu = (const u16*)d_in[7]; p.wd = (const u16*)d_in[8]; p.c = (const u16*)d_in[9]; p.wm = (const u16*)d_in[10];
  p.out = (u16*)d_out; p.ws = (char*)d_ws;
  (void)hipFuncSetAttribute((const void*)k_main, hipFuncAttributeMaxDynamicSharedMemorySize, M_LDS);
  k_main<<<256, 512, M_LDS, stream>>>(p);
}
```

```cpp
#include <hip/hip_runtime.h>
#include <stdint.h>

typedef unsigned short u16;
typedef unsigned int u32;
typedef __attribute__((ext_vector_type(8))) __bf16 bf16x8;
typedef __attribute__((ext_vector_type(2))) __bf16 bf16x2;
typedef __attribute__((ext_vector_type(2))) float f32x2;
typedef __attribute__((ext_vector_type(4))) float f32x4;
typedef __attribute__((ext_vector_type(16))) float f32x16;
typedef __attribute__((ext_vector_type(4))) u32 u32x4;
typedef __attribute__((ext_vector_type(2))) u32 u32x2;
typedef __attribute__((ext_vector_type(4))) short s16x4;
#define LAS __attribute__((address_space(3)))
#define GAS __attribute__((address_space(1)))
#define DI __device__ __forceinline__

constexpr int DM = 512, FF = 1792;
constexpr float EPS = 1e-5f;
constexpr float C2 = 0.25f * 1.4426950408889634f;

constexpr size_t WS_EPOCH = 0;
constexpr size_t WS_FO = 4096;
constexpr size_t WS_FX = WS_FO + 8192;
constexpr size_t WS_FA = WS_FX + 4096;
constexpr size_t WS_FE = WS_FA + 4096;
constexpr size_t WS_FKV = WS_FE + 4096;
constexpr size_t WS_FP = WS_FKV + 4096;
constexpr size_t WS_MOD = 65536;
constexpr size_t WS_KV = WS_MOD + 8 * 12288;
constexpr size_t WS_STAT = WS_KV + 139264;
constexpr size_t WS_AUG = WS_STAT + 131072;
constexpr size_t WS_WO = 1u << 20;
constexpr size_t WS_WD = WS_WO + (512u << 10);
constexpr size_t WS_WGU = WS_WD + (1792u << 10);
constexpr size_t WS_O = WS_WGU + (3584u << 10);
constexpr size_t WS_X1 = WS_O + (2u << 20);
constexpr size_t WS_ACT = WS_X1 + (2u << 20);
static_assert(WS_AUG + 229376 <= WS_WO, "workspace map");
constexpr int TS = 2176;

struct Params {
  const u16 *x, *wq, *wk, *wv, *wo, *wg, *wu, *wd, *c, *wm;
  u16* out;
  char* ws;
};

DI f32x16 mfma(u32x4 a, u32x4 b, f32x16 c) {
  return __builtin_amdgcn_mfma_f32_32x32x16_bf16(__builtin_bit_cast(bf16x8, a), __builtin_bit_cast(bf16x8, b), c, 0, 0, 0);
}
DI u32 pk(float lo, float hi) {
  f32x2 v = {lo, hi};
  return __builtin_bit_cast(u32, __builtin_convertvector(v, bf16x2));
}
DI float bflo(u32 u) { return __uint_as_float(u << 16); }
DI float bfhi(u32 u) { return __uint_as_float(u & 0xffff0000u); }
DI float rbf(float f) { return bflo(pk(f, 0.f)); }
DI float dot2(u32 a, u32 b, float c) { return __builtin_amdgcn_fdot2_f32_bf16(__builtin_bit_cast(bf16x2, a), __builtin_bit_cast(bf16x2, b), c, false); }
DI void set_m0(u32 lds) { asm volatile("s_mov_b32 m0, %0\n\ts_nop 0" ::"s"(lds) : "memory"); }
template <int OFF> DI void dma(const void* base, u32 vo) { asm volatile("global_load_lds_dwordx4 %0, %1 offset:%2" ::"v"(vo), "s"(base), "n"(OFF) : "memory"); }
template <int OFF> DI void dma1(const void* base, u32 vo) { asm volatile("global_load_lds_dwordx4 %0, %1 offset:%2 sc1" ::"v"(vo), "s"(base), "n"(OFF) : "memory"); }
DI void dma4(const void* base, u32 vo, u32 lds) { set_m0(lds); dma<0>(base, vo); dma<1024>(base, vo); dma<2048>(base, vo); dma<3072>(base, vo); }
DI void dma4s(const void* base, u32 vo, u32 lds) { set_m0(lds); dma1<0>(base, vo); dma1<1024>(base, vo); dma1<2048>(base, vo); dma1<3072>(base, vo); }
template <int SC, int OFF> DI u32x4 gld_s(const void* base, u32 vo) {
  u32x4 r;
  if (SC) asm volatile("global_load_dwordx4 %0, %1, %2 offset:%3 sc1" : "=v"(r) : "v"(vo), "s"(base), "n"(OFF) : "memory");
  else asm volatile("global_load_dwordx4 %0, %1, %2 offset:%3" : "=v"(r) : "v"(vo), "s"(base), "n"(OFF) : "memory");
  return r;
}
template <int OFF> DI u32x2 gld8_s(const void* base, u32 vo) { u32x2 r; asm volatile("global_load_dwordx2 %0, %1, %2 offset:%3" : "=v"(r) : "v"(vo), "s"(base), "n"(OFF) : "memory"); return r; }
template <int OFF> DI u32 gld2_s(const void* base, u32 vo) { u32 r; asm volatile("global_load_ushort %0, %1, %2 offset:%3" : "=v"(r) : "v"(vo), "s"(base), "n"(OFF) : "memory"); return r; }
DI void touch8k(const char* base, u32 l128) { asm volatile("global_load_lds_dword %0, %1 sc1" ::"v"(l128), "s"(base) : "memory"); }
DI void gst16_sc1(char* base, u32 vo, u32x4 v) { asm volatile("global_store_dwordx4 %0, %1, %2 sc1\n\ts_nop 1" ::"v"(vo), "v"(v), "s"(base) : "memory"); }
DI void gst8_sc1(char* base, u32 vo, u32x2 v) { asm volatile("global_store_dwordx2 %0, %1, %2 sc1\n\ts_nop 1" ::"v"(vo), "v"(v), "s"(base) : "memory"); }
#define RLX_AGENT __ATOMIC_RELAXED, __HIP_MEMORY_SCOPE_AGENT
DI void put_flag(char* f, u32 tag) { __hip_atomic_store((GAS u32*)f, tag, RLX_AGENT); }
#define SLP 16
constexpr int SPIN_CAP = 1 << 20;
template <int N> DI void poll_flags(const char* f, u32 tag, int l) {
#pragma unroll 1
  for (int it = 0; it < SPIN_CAP; ++it) {
    const u32 v = __hip_atomic_load((const GAS u32*)f + (l < N ? l : 0), RLX_AGENT);
    if (__all(v == tag)) break;

  }
  __builtin_amdgcn_fence(__ATOMIC_ACQUIRE, "wavefront");
}
#define SB() __builtin_amdgcn_sched_barrier(0)
#define PIN(x) asm volatile("" : "+v"(x))
#define WAITV(N) asm volatile("s_waitcnt vmcnt(" #N ")" ::: "memory")
#define WAITV_BAR(N) asm volatile("s_waitcnt vmcnt(" #N ") lgkmcnt(0)\n\ts_barrier" ::: "memory")
#define BAR() asm volatile("s_waitcnt lgkmcnt(0)\n\ts_barrier" ::: "memory")
template <int N> DI void waitv_pin(u32x4& v) { asm volatile("s_waitcnt vmcnt(%1)" : "+v"(v) : "n"(N) : "memory"); }
DI u32x4 lds128(u32 a) { return *(const LAS u32x4*)(uintptr_t)a; }
DI f32x4 ldsf4(u32 a) { return *(const LAS f32x4*)(uintptr_t)a; }
DI f32x2 ldsf2(u32 a) { return *(const LAS f32x2*)(uintptr_t)a; }
DI void sts128(u32 a, u32x4 v) { *(LAS u32x4*)(uintptr_t)a = v; }
DI void stsf4(u32 a, f32x4 v) { *(LAS f32x4*)(uintptr_t)a = v; }
DI u32x2 ldstr(u32 a) { return __builtin_bit_cast(u32x2, __builtin_amdgcn_ds_read_tr16_b64_v4i16((LAS s16x4*)(uintptr_t)a)); }
DI u32x4 ldstr2(u32 a0, u32 a1) { u32x2 p = ldstr(a0), q = ldstr(a1); return u32x4{p[0], p[1], q[0], q[1]}; }
template <int CTRL> DI float dpp(float v) { return __int_as_float(__builtin_amdgcn_mov_dpp(__float_as_int(v), CTRL, 0xf, 0xf, true)); }
DI float swap32_sum(float v) { auto r = __builtin_amdgcn_permlane32_swap(__float_as_uint(v), __float_as_uint(v), false, false); return __uint_as_float(r[0]) + __uint_as_float(r[1]); }
DI float swap32_max(float v) { auto r = __builtin_amdgcn_permlane32_swap(__float_as_uint(v), __float_as_uint(v), false, false); return fmaxf(__uint_as_float(r[0]), __uint_as_float(r[1])); }
DI float swap16_sum(float v) { auto r = __builtin_amdgcn_permlane16_swap(__float_as_uint(v), __float_as_uint(v), false, false); return __uint_as_float(r[0]) + __uint_as_float(r[1]); }
DI float lowhalf(float v) { auto r = __builtin_amdgcn_permlane32_swap(__float_as_uint(v), __float_as_uint(v), false, false); return __uint_as_float(r[0]); }
template <int CTRL> DI float halve(bool bit, float a, float b) { return (bit ? b : a) + dpp<CTRL>(bit ? a : b); }
DI int rfl(int v) { return __builtin_amdgcn_readfirstlane(v); }
#define EXP2(v) __builtin_amdgcn_exp2f(v)

struct Ids { int G, c, g, j; };
DI Ids ids() { const int b = blockIdx.x, x = b & 7, i = b >> 3; Ids r; r.G = (x >> 1) * 4 + (i >> 3); r.c = (x & 1) * 8 + (i & 7); r.g = 4 * x + (i >> 3); r.j = i & 7; return r; }

#define STAMP_DECL
#define STAMP(I)
#define STAMP_FLUSH(K)
#define STAMP_ARG
#define STAMP_PASS

#define PSTAMP(I)
#define ASTAMP(I)
constexpr u32 M_LDS = 163840;
constexpr u32 F_A = 0;
constexpr u32 F_H = 65536;
constexpr u32 F_B = 131072;
constexpr u32 F_D = F_B + 16640;
constexpr u32 F_M = F_D + 10240;
static_assert(F_M + 4608 <= M_LDS, "front-end LDS map");
constexpr u32 SL0 = 0, SL1 = 69632, SL2 = 32768, SL3 = 102400, TOPA = 135168, GAPA = 65536, OEX = 143616, JUNK = M_LDS - 256;

DI void mfa(f32x16& c, u32x4 a, u32x4 b) { asm volatile("v_mfma_f32_32x32x16_bf16 %0, %1, %2, %0" : "+v"(c) : "v"(a), "v"(b) : "memory"); }
DI void mf0(f32x16& c, u32x4 a, u32x4 b) { asm volatile("v_mfma_f32_32x32x16_bf16 %0, %1, %2, 0" : "=&v"(c) : "v"(a), "v"(b) : "memory"); }
DI float vexp(float a) { float r; asm volatile("v_exp_f32 %0, %1" : "=v"(r) : "v"(a)); return r; }
DI float vrcp(float a) { float r; asm volatile("v_rcp_f32 %0, %1" : "=v"(r) : "v"(a)); return r; }
DI u32 vcvt(float a, float b) { u32 r; asm volatile("v_cvt_pk_bf16_f32 %0, %1, %2" : "=v"(r) : "v"(a), "v"(b)); return r; }
DI float vadd(float a, float b) { float r; asm volatile("v_add_f32 %0, %1, %2" : "=v"(r) : "v"(a), "v"(b)); return r; }
DI float vmul(float a, float b) { float r; asm volatile("v_mul_f32 %0, %1, %2" : "=v"(r) : "v"(a), "v"(b)); return r; }
DI float vfma_k(float a, float k, float c) { float r; asm volatile("v_fma_f32 %0, %1, %2, %3" : "=v"(r) : "v"(a), "s"(k), "v"(c)); return r; }
DI float vadd1(float a) { float r; asm volatile("v_add_f32 %0, 1.0, %1" : "=v"(r) : "v"(a)); return r; }
#define MFMA_SETTLE() asm volatile("s_nop 15\n\ts_nop 3" ::: "memory")
DI float rowmax16(const f32x16& s) {
  float m = fmaxf(fmaxf(s[0], s[1]), s[2]);
#pragma unroll
  for (int r = 3; r < 15; r += 2) m = fmaxf(fmaxf(m, s[r]), s[r + 1]);
  return swap32_max(fmaxf(m, s[15]));
}
constexpr float GC0 = -2.f * 0.7978845608028654f * 1.4426950408889634f, GC1 = GC0 * 0.044715f;
template <u32 A> DI u32x4 ldsr(u32 lo, u32 mid) { return A < 65536 ? lds128(lo + A) : lds128(mid + (A - 65536)); }

DI void reduce4(const f32x16& acc0, const f32x16& acc1, int tt, int kq, u32 buf, u32 vo, float (&r)[8]) {
#pragma unroll
  for (int pc = 0; pc < 4; ++pc) {
    const f32x16& a = (pc & 1) ? acc1 : acc0;
    const int o8 = (pc >> 1) * 8;
    if (pc != kq) {
      const u32 d = buf + ((tt * 4 + pc) * 3 + ((kq - pc - 1) & 3)) * 2048 + vo;
      stsf4(d, f32x4{a[o8], a[o8 + 1], a[o8 + 2], a[o8 + 3]}); stsf4(d + 1024, f32x4{a[o8 + 4], a[o8 + 5], a[o8 + 6], a[o8 + 7]});
    } else {
#pragma unroll
      for (int e = 0; e < 8; ++e) r[e] = a[o8 + e];
    }
  }
  BAR();
#pragma unroll
  for (int i = 0; i < 3; ++i) {
    const u32 d = buf + ((tt * 4 + kq) * 3 + i) * 2048 + vo;
    const f32x4 u = ldsf4(d), v = ldsf4(d + 1024);
#pragma unroll
    for (int e = 0; e < 4; ++e) { r[e] += u[e]; r[4 + e] += v[e]; }
  }
}

DI void reduce8(const f32x16 (&acc)[4], int w, u32 vo, float (&r)[8]) {
#pragma unroll
  for (int pc = 0; pc < 8; ++pc) {
    const f32x16& a = acc[pc & 3];
    const int o8 = (pc >> 2) * 8;
    if (pc != w) {
      sts128((pc * 7 + ((w - pc - 1) & 7)) * 1024 + vo, u32x4{pk(a[o8], a[o8 + 1]), pk(a[o8 + 2], a[o8 + 3]), pk(a[o8 + 4], a[o8 + 5]), pk(a[o8 + 6], a[o8 + 7])});
    } else {
#pragma unroll
      for (int e = 0; e < 8; ++e) r[e] = a[o8 + e];
    }
  }
  u32 klo = 0x00003f80u, khi = 0x3f800000u;
  asm volatile("" : "+s"(klo), "+s"(khi));
  BAR();
#pragma unroll
  for (int i = 0; i < 7; ++i) {
    const u32x4 u = lds128((w * 7 + i) * 1024 + vo);
#pragma unroll
    for (int e = 0; e < 4; ++e) { const u32 v = u[e]; r[2 * e] = dot2(v, klo, r[2 * e]); r[2 * e + 1] = dot2(v, khi, r[2 * e + 1]); }
  }
}

constexpr u32 SL4 = 0xffffffffu;
template <u32 SL, int S> DI u32x4 wrd(u32 lo, u32 mid, u32 hi) {
  constexpr u32 A = SL == SL4 ? (S < 28 ? TOPA + S * 1024 : GAPA + (S - 28) * 1024) : SL + S * 1024;
  return A < 65536 ? lds128(lo + A) : (A < 131072 ? lds128(mid + (A - 65536)) : lds128(hi + (A - 131072)));
}
DI constexpr int dks(int i) { return i < 6 ? 7 * (i / 3) + i % 3 : 7 * ((i - 6) >> 2) + 3 + ((i - 6) & 3); }

DI void p4_compute(const Params& p, const Ids& id, int l, int w, u32 tag, u32x4 (&wd)[28] STAMP_ARG) {
  const u32 vo = l * 16;
  char* adst = p.ws + WS_ACT + ((size_t)(4 * id.G + w) * 112 + 7 * id.c) * 1024;
  const char* asrc = p.ws + WS_AUG + (size_t)id.c * 7168;
  u32x4 aug[7];
  aug[0] = gld_s<1, 0>(asrc, vo); aug[1] = gld_s<1, 1024>(asrc, vo); aug[2] = gld_s<1, 2048>(asrc, vo);
  asm volatile("s_waitcnt vmcnt(0)\n\ts_barrier" : "+v"(aug[0]), "+v"(aug[1]), "+v"(aug[2])::"memory");
  STAMP(9)
  const char* xsrc = p.ws + WS_X1 + (size_t)(4 * id.G + w) * 32768;
  const char* ssrc = p.ws + WS_STAT + (size_t)(128 * id.G + 32 * w) * 64;
  u32x4 xf[32], st0, st1, st2, st3;
#define XLD(S) (xf[S] = gld_s<1, ((S) & 3) * 1024>(xsrc + ((S) >> 2) * 4096, vo))
  XLD(0); XLD(1); XLD(2); XLD(3); XLD(4); XLD(5); XLD(6); XLD(7); XLD(8); XLD(9); XLD(10); XLD(11); XLD(12); XLD(13); XLD(14); XLD(15);
  float rs, gc0 = GC0; u32x4 xaug;
  u32 lo = vo, mid = vo + 65536, hi = vo + 131072;
  PIN(lo); PIN(mid); PIN(hi); PIN(gc0);
  f32x16 acc[3]; u32x4 ra[32], rb[32];
  ra[0] = wrd<SL0, 0>(lo, mid, hi); rb[0] = wrd<SL1, 0>(lo, mid, hi); ra[1] = wrd<SL0, 1>(lo, mid, hi); rb[1] = wrd<SL1, 1>(lo, mid, hi);
  ra[2] = wrd<SL0, 2>(lo, mid, hi); rb[2] = wrd<SL1, 2>(lo, mid, hi);
#define PH0(S) { constexpr int s = (S);                                                                            \
      waitv_pin<(s < 16 ? 15 : 35 - s)>(xf[s]);                                                                    \
      if (s == 0) mf0(acc[0], ra[0], xf[0]); else mfa(acc[0], ra[s], xf[s]);                                       \
      if (s + 16 < 32) XLD((s + 16) & 31);                                                                         \
      if (s == 15) { st0 = gld_s<1, 0>(ssrc, (l & 31) * 64); st1 = gld_s<1, 16>(ssrc, (l & 31) * 64); st2 = gld_s<1, 32>(ssrc, (l & 31) * 64); st3 = gld_s<1, 48>(ssrc, (l & 31) * 64); } \
      if (s + 3 < 32) { ra[(s + 3) & 31] = wrd<SL0, (s + 3) & 31>(lo, mid, hi); rb[(s + 3) & 31] = wrd<SL1, (s + 3) & 31>(lo, mid, hi); } \
      if (s == 0) mf0(acc[1], rb[0], xf[0]); else mfa(acc[1], rb[s], xf[s]); }
 PH0(0) PH0(1) PH0(2) PH0(3) PH0(4) PH0(5) PH0(6) PH0(7) \
 PH0(8) PH0(9) PH0(10) PH0(11) PH0(12) PH0(13) PH0(14) PH0(15) \
 PH0(16) PH0(17) PH0(18) PH0(19) PH0(20) PH0(21) PH0(22) PH0(23) \
 PH0(24) PH0(25) PH0(26) PH0(27) PH0(28) PH0(29) PH0(30) PH0(31)
#undef PH0
  ra[0] = wrd<SL2, 0>(lo, mid, hi); ra[1] = wrd<SL2, 1>(lo, mid, hi); ra[2] = wrd<SL2, 2>(lo, mid, hi); ra[3] = wrd<SL2, 3>(lo, mid, hi);
  asm volatile("s_waitcnt vmcnt(0)" : "+v"(st0), "+v"(st1), "+v"(st2), "+v"(st3)::"memory");
  {
    const float s1 = (__uint_as_float(st0[0]) + __uint_as_float(st0[2])) + (__uint_as_float(st1[0]) + __uint_as_float(st1[2])) + (__uint_as_float(st2[0]) + __uint_as_float(st2[2])) + (__uint_as_float(st3[0]) + __uint_as_float(st3[2]));
    const float s2 = (__uint_as_float(st0[1]) + __uint_as_float(st0[3])) + (__uint_as_float(st1[1]) + __uint_as_float(st1[3])) + (__uint_as_float(st2[1]) + __uint_as_float(st2[3])) + (__uint_as_float(st3[1]) + __uint_as_float(st3[3]));
    const float nmu = -s1 * (1.f / DM), var = fmaxf(s2 * (1.f / DM) - nmu * nmu, 0.f) + EPS;
    rs = __builtin_amdgcn_rsqf(var);
    const float sg = var * rs, mh = rbf(nmu), sh = rbf(sg);
    xaug = u32x4{pk(mh, mh), pk(nmu - mh, sh), pk(sh, sg - sh), 0u};
    asm volatile("s_nop 1" : "+v"(xaug), "+v"(rs));
  }
  mfa(acc[0], aug[0], xaug); mfa(acc[1], aug[1], xaug);
  STAMP(11)
  asm volatile("s_barrier" ::: "memory");
  f32x4 gv, uv, zz; u32x4 q;
#define E4(F) { _Pragma("unroll") for (int e = 0; e < 4; ++e) { F; } }
#define E2(B, F) { _Pragma("unroll") for (int e = B; e < B + 2; ++e) { F; } }
#define UNIT(T, U) { constexpr int u = (U) % 13, GO = ((U) / 13) * 8; const f32x16& k_ = acc[(T) % 3];              \
      if (u == 0) E4(gv[e] = vmul(k_[GO + e], rs))                                                                  \
      if (u == 1) E4(zz[e] = vmul(gv[e], gv[e]))                                                                    \
      if (u == 2) E4(zz[e] = vfma_k(zz[e], GC1, gc0))                                                               \
      if (u == 3) E4(zz[e] = vmul(zz[e], gv[e]))                                                                    \
      if (u == 4) E2(0, zz[e] = vexp(zz[e]))                                                                        \
      if (u == 5) E2(2, zz[e] = vexp(zz[e]))                                                                        \
      if (u == 6) E4(uv[e] = vmul(k_[GO + 4 + e], rs))                                                              \
      if (u == 7) E4(zz[e] = vadd1(zz[e]))                                                                          \
      if (u == 8) E2(0, zz[e] = vrcp(zz[e]))                                                                        \
      if (u == 9) E2(2, zz[e] = vrcp(zz[e]))                                                                        \
      if (u == 10) E4(uv[e] = vmul(uv[e], gv[e]))                                                                   \
      if (u == 11) E4(uv[e] = vmul(uv[e], zz[e]))                                                                   \
      if (u == 12) { q[GO / 4] = vcvt(uv[0], uv[1]); q[GO / 4 + 1] = vcvt(uv[2], uv[3]); if (GO) gst16_sc1(adst + (T) * 1024, vo, q); } }
#define E0(T) ((T) == 0 ? 4 : (T) == 1 ? 30 : (T) == 2 ? 56 : (T) == 3 ? 82 : (T) == 4 ? 108 : 137)
#define GAP1(T, G) { if ((G) >= E0(T) && (G) < E0(T) + 26) UNIT(T, ((G) - E0(T) + 26) % 26) }
#define GAP(G) { GAP1(0, G) GAP1(1, G) GAP1(2, G) GAP1(3, G) GAP1(4, G) GAP1(5, G) }
#define PH(S) { constexpr int s = (S);                                                                             \
      if (s == 0) mf0(acc[TT % 3], aug[TT], xaug); else mfa(acc[TT % 3], ra[(s - 1) & 31], xf[(s - 1) & 31]);      \
      if (s >= 1) { if (s + 3 < 32) ra[(s + 3) & 31] = wrd<SA, (s + 3) & 31>(lo, mid, hi); else if (TT < 6) ra[(s + 3) & 31] = wrd<SN, (s + 3) & 3>(lo, mid, hi); } \
      if (s == 1 && TT < 6) aug[TT < 6 ? TT + 1 : 0] = gld_s<1, 0>(asrc + (TT + 1) * 1024, vo);                    \
      GAP((TT - 2) * 33 + s) }
#define PHASE  PH(0) PH(1) PH(2) PH(3) PH(4) PH(5) PH(6) PH(7) \
 PH(8) PH(9) PH(10) PH(11) PH(12) PH(13) PH(14) PH(15) \
 PH(16) PH(17) PH(18) PH(19) PH(20) PH(21) PH(22) PH(23) \
 PH(24) PH(25) PH(26) PH(27) PH(28) PH(29) PH(30) PH(31) \
 PH(32)
  { constexpr int TT = 2; constexpr u32 SA = SL2, SN = SL3; PHASE }
  asm volatile("s_waitcnt vmcnt(1)\n\ts_barrier" : "+v"(aug[3])::"memory");
  STAMP(12)
  { constexpr int TT = 3; constexpr u32 SA = SL3, SN = SL4; PHASE }
  asm volatile("s_waitcnt vmcnt(1)\n\ts_barrier" : "+v"(aug[4])::"memory");
  STAMP(13)
  { constexpr int TT = 4; constexpr u32 SA = SL4, SN = SL0; PHASE }
  asm volatile("s_waitcnt vmcnt(1)\n\ts_barrier" : "+v"(aug[5])::"memory");
  STAMP(14)
  { constexpr int TT = 5; constexpr u32 SA = SL0, SN = SL1; PHASE }
  asm volatile("s_waitcnt vmcnt(1)\n\ts_barrier" : "+v"(aug[6])::"memory");
  if (l == 0) put_flag(p.ws + WS_FE + (4 * id.G + w) * 64 + id.c * 4, tag);
  STAMP(15)
  { constexpr int TT = 6; constexpr u32 SA = SL1, SN = SL1; PHASE }
  STAMP(16)
  MFMA_SETTLE();
 UNIT(6, 0) UNIT(6, 1) UNIT(6, 2) UNIT(6, 3) UNIT(6, 4) UNIT(6, 5) UNIT(6, 6) UNIT(6, 7) UNIT(6, 8)
 UNIT(6, 9) UNIT(6, 10) UNIT(6, 11) UNIT(6, 12) UNIT(6, 13) UNIT(6, 14) UNIT(6, 15) UNIT(6, 16) UNIT(6, 17)
 UNIT(6, 18) UNIT(6, 19) UNIT(6, 20) UNIT(6, 21) UNIT(6, 22) UNIT(6, 23) UNIT(6, 24) UNIT(6, 25)
  STAMP(17)
  asm volatile("s_barrier" ::: "memory");
  {
    const u32 b = w * 8192;
#define WDR(F) (wd[F] = (F) < 8 ? lds128(lo + SL2 + b + ((F) & 7) * 1024) : (F) < 16 ? lds128(mid + (SL3 - 65536) + b + ((F) & 7) * 1024) : (F) < 22 ? lds128(hi + (TOPA - 131072) + w * 6144 + (((F) - 16) & 7) * 1024) : lds128(lo + SL0 + w * 6144 + (((F) - 22) & 7) * 1024))
 WDR(0); WDR(1); WDR(2); WDR(3); WDR(4); WDR(5); WDR(6); WDR(7); WDR(8); WDR(9);
 WDR(10); WDR(11); WDR(12); WDR(13); WDR(14); WDR(15); WDR(16); WDR(17); WDR(18); WDR(19);
 WDR(20); WDR(21); WDR(22); WDR(23); WDR(24); WDR(25); WDR(26); WDR(27);
#undef WDR
  }
#undef PHASE
#undef PH
#undef GAP
#undef GAP1
#undef UNIT
#undef XLD
}

DI void p4_helper(const Params& p, const Ids& id, int l, int w, u32 tag, u32x4 (&wd)[28] STAMP_ARG) {
  const u32 vo = l * 16;
  const int hw = w - 4;
  const char* wsrc = p.ws + WS_WGU + (size_t)id.c * 7 * 32768 + hw * 8192;
  const char* dme = p.ws + WS_WD + ((size_t)(2 * id.j) * 112 + 14 * w) * 1024;
  const char* dcw = p.ws + WS_WD + ((size_t)(2 * id.j) * 112 + 14 * hw) * 1024;
#define D8(SRC, DST) { dma4s(SRC, vo, DST); dma4s((SRC) + 4096, vo, (DST) + 4096); }
#define D6(SRC, DST) { dma4s(SRC, vo, DST); set_m0((DST) + 4096); dma1<0>((SRC) + 4096, vo); dma1<1024>((SRC) + 4096, vo); }
#define WDG(F) (wd[F] = gld_s<1, 0>(dme + ((F) < 14 ? (F) : 98 + (F)) * 1024, vo))
  if (w == 4) { poll_flags<16>(p.ws + WS_FX + id.G * 256, tag, l); asm volatile("s_barrier" ::: "memory"); D8(wsrc + 3 * 32768, SL3 + hw * 8192) }
  else { D8(wsrc + 3 * 32768, SL3 + hw * 8192) WAITV_BAR(8); }
  __builtin_amdgcn_s_sleep(32);
  dma4s(wsrc + 4 * 32768, vo, TOPA + hw * 8192); dma4s(wsrc + 4 * 32768 + 4096, vo, hw < 3 ? TOPA + hw * 8192 + 4096 : GAPA);
  WAITV_BAR(8);
  D8(wsrc + 5 * 32768, SL0 + hw * 8192) WDG(0); WDG(1); WDG(2); WDG(3);
  WAITV_BAR(12);
  D8(wsrc + 6 * 32768, SL1 + hw * 8192) dma4s(dcw, vo, SL2 + hw * 8192);
  WAITV_BAR(16);
  dma4s(dcw + 4096, vo, SL2 + hw * 8192 + 4096); D6(dcw + 8192, SL3 + hw * 8192)
  set_m0(SL3 + hw * 8192 + 6144); dma1<0>(dcw + 114688, vo); dma1<1024>(dcw + 114688, vo);
  WAITV_BAR(16);
  D6(dcw + 114688 + 2048, TOPA + hw * 6144) WDG(4); WDG(5); WDG(6); WDG(7); WDG(8); WDG(9);
  asm volatile("s_barrier" ::: "memory");
  D6(dcw + 114688 + 8192, SL0 + hw * 6144) WDG(10); WDG(11); WDG(12); WDG(13); WDG(14); WDG(15);
  WAITV_BAR(6);
  WDG(16); WDG(17); WDG(18); WDG(19); WDG(20); WDG(21); WDG(22); WDG(23); WDG(24); WDG(25); WDG(26); WDG(27);
  poll_flags<32>(p.ws + WS_FE + id.g * 128, tag, l);
  asm volatile("s_waitcnt vmcnt(0)" : "+v"(wd[0]), "+v"(wd[1]), "+v"(wd[2]), "+v"(wd[3]), "+v"(wd[4]), "+v"(wd[5]), "+v"(wd[6]), "+v"(wd[7]), "+v"(wd[8]), "+v"(wd[9]), "+v"(wd[10]),
               "+v"(wd[11]), "+v"(wd[12]), "+v"(wd[13])::"memory");
  asm volatile("" : "+v"(wd[14]), "+v"(wd[15]), "+v"(wd[16]), "+v"(wd[17]), "+v"(wd[18]), "+v"(wd[19]), "+v"(wd[20]), "+v"(wd[21]), "+v"(wd[22]), "+v"(wd[23]), "+v"(wd[24]),
               "+v"(wd[25]), "+v"(wd[26]), "+v"(wd[27])::"memory");
#undef WDG
#undef D8
#undef D6
}

__global__ __launch_bounds__(512, 2) void k_main(Params p) {
  extern __shared__ __attribute__((aligned(16))) char smem[];
  const int tid = threadIdx.x, l = tid & 63, w = rfl(tid >> 6);
  const Ids id = ids();
  const int g = id.g, j = id.j, h = l >> 5;
  const u32 vo = l * 16;
  const u32 epoch = *(const u32*)(p.ws + WS_EPOCH);
  const u32 tag = epoch * 0x9E3779B1u + 0x7F4A7C15u;
  const int tt = w & 1, nt = (w >> 1) & 1, kh = w >> 2, kq = w >> 1;
  const char* modr = p.ws + WS_MOD + (blockIdx.x & 7) * 12288;
  STAMP_DECL STAMP(0)
  u32x4 q0, q1, fo_, fd_, fg[2];
  char *pdo, *pdd, *pdg;
  {
    const int nh = (l >> 4) & 1, q = (l & 15) >> 2, pp = l & 3;
    const int uid = g * 7 + j - 1, uT = uid >> 1, ukh = uid & 1;
    const int rk = (blockIdx.x >> 6) * 7 + j - 1;
    const bool gemv = j != 0 && rk < 24;
    const int mu = 4 * ((blockIdx.x & 7) + 8 * (rk >> 2)) + (rk & 3);
    const u32 rawo = (l >> 3) * 1024 + (((l & 7) ^ (((l >> 4) & 1) << 2)) * 16);
#define WQ_DMA() { _Pragma("unroll") for (int i = 0; i < 8; ++i) { set_m0(F_A + (8 * w + i) * 1024); dma<0>(p.wq + (size_t)(64 * w + 8 * i) * DM + 64 * j, rawo); } }
#define LATE_DMA() {                                                                                                                              \
      if (j != 0) {                                                                                                                               \
        const u32 go = (l >> 1) * (FF * 2) + (l & 1) * 16;                                                                                        \
        set_m0(F_B + w * 1024); dma<0>(p.wg + (size_t)(256 * ukh + 32 * w) * FF + 16 * uT, go);                                                   \
        set_m0(F_B + 8192 + 128 + w * 1024); dma<0>(p.wu + (size_t)(256 * ukh + 32 * w) * FF + 16 * uT, go);                                      \
        set_m0(F_D + w * 1024); dma<0>(p.wd + (size_t)(64 * (uid >> 3) + 8 * w) * DM + 64 * (uid & 7), rawo);                                     \
      }                                                                                                                                           \
      if (w >= 6) { set_m0(F_D + 8192 + (w - 6) * 1024); dma<0>(p.wo + (size_t)(16 * g + 8 * (w - 6)) * DM + 64 * j, rawo); } }
    u32x4 w0, w1; u32 c0, c1;
    if (gemv) {
      const u32 mo = (tid >> 1) * 6144 + (tid & 1) * 16;
      w0 = gld_s<0, 0>(p.wm + 16 * mu, mo); w1 = gld_s<0, 0>(p.wm + 16 * mu + 256 * 3072, mo);
      c0 = gld2_s<0>(p.c, (tid >> 1) * 2); c1 = gld2_s<512>(p.c, (tid >> 1) * 2);
    }
    u32x4 xv[8];
    {
      const char* xs = (const char*)(p.x + (size_t)(64 * g + 8 * w) * DM);
      xv[0] = gld_s<0, 0>(xs, vo); xv[1] = gld_s<0, 1024>(xs, vo); xv[2] = gld_s<0, 2048>(xs, vo); xv[3] = gld_s<0, 3072>(xs, vo);
      xv[4] = gld_s<0, 0>(xs + 4096, vo); xv[5] = gld_s<0, 1024>(xs + 4096, vo); xv[6] = gld_s<0, 2048>(xs + 4096, vo); xv[7] = gld_s<0, 3072>(xs + 4096, vo);
    }
    if (j == 0) {
      set_m0(F_H + w * 2048); dma<0>(p.wv + w * 1024, vo); dma<1024>(p.wv + w * 1024, vo);
      set_m0(F_H + 16384 + 128 + w * 2048); dma<0>(p.wk + w * 1024, vo); dma<1024>(p.wk + w * 1024, vo);
      WQ_DMA() LATE_DMA()
    }
    if (gemv) {
      asm volatile("s_waitcnt vmcnt(8)" : "+v"(w0), "+v"(w1), "+v"(c0), "+v"(c1)::"memory");
      const float f0 = bflo(c0), f1 = bflo(c1);
      const float s0 = rbf(f0 * __builtin_amdgcn_rcpf(1.f + EXP2(-1.4426950408889634f * f0))), s1 = rbf(f1 * __builtin_amdgcn_rcpf(1.f + EXP2(-1.4426950408889634f * f1)));
      float a[8];
#pragma unroll
      for (int i = 0; i < 4; ++i) { a[2 * i] = s0 * bflo(w0[i]) + s1 * bflo(w1[i]); a[2 * i + 1] = s0 * bfhi(w0[i]) + s1 * bfhi(w1[i]); }
#pragma unroll
      for (int i = 0; i < 4; ++i) a[i] = halve<0x4E>(l & 2, a[i], a[i + 4]);
#pragma unroll
      for (int i = 0; i < 2; ++i) a[i] = halve<0x124>(l & 4, a[i], a[i + 2]);
      a[0] = halve<0x128>(l & 8, a[0], a[1]);
      a[0] = swap32_sum(swap16_sum(a[0]));
      if (l < 16) *(LAS float*)(uintptr_t)(F_M + w * 64 + ((l & 1) * 8 + (l & 2) * 2 + ((l >> 1) & 2) + (l >> 3)) * 4) = a[0];
      BAR();
      if (w == 7) {
        f32x2 s = {0.f, 0.f};
#pragma unroll
        for (int i = 0; i < 8; ++i) s += ldsf2(F_M + i * 64 + (l & 7) * 8);
        gst8_sc1(p.ws + WS_MOD + mu * 64, (l >> 3) * 12288 + (l & 7) * 8, u32x2{pk(s[0], s[1]), tag});
        if (rk < 8) { STAMP(10) }
      }
    }
    STAMP(22)
    if (j == 0) WAITV(12); else if (gemv && w == 7) WAITV(1); else WAITV(0);
    asm volatile("" : "+v"(xv[0]), "+v"(xv[1]), "+v"(xv[2]), "+v"(xv[3]), "+v"(xv[4]), "+v"(xv[5]), "+v"(xv[6]), "+v"(xv[7]));
    float xh[8][8];
    {
      float s1[8], s2[8];
#pragma unroll
      for (int i = 0; i < 8; ++i) {
        s1[i] = 0.f; s2[i] = 0.f;
#pragma unroll
        for (int e = 0; e < 4; ++e) { const u32 v = xv[i][e]; s1[i] = dot2(v, 0x3f803f80u, s1[i]); s2[i] = dot2(v, v, s2[i]); }
      }
#pragma unroll
      for (int i = 0; i < 4; ++i) { s1[i] = halve<0xB1>(l & 1, s1[i], s1[i + 4]); s2[i] = halve<0xB1>(l & 1, s2[i], s2[i + 4]); }
#pragma unroll
      for (int i = 0; i < 2; ++i) { s1[i] = halve<0x4E>(l & 2, s1[i], s1[i + 2]); s2[i] = halve<0x4E>(l & 2, s2[i], s2[i + 2]); }
      float t1 = halve<0x124>(l & 4, s1[0], s1[1]), t2 = halve<0x124>(l & 4, s2[0], s2[1]);
      t1 += dpp<0x128>(t1); t2 += dpp<0x128>(t2);
      t1 = swap32_sum(swap16_sum(t1)); t2 = swap32_sum(swap16_sum(t2));
      const float m = t1 * (1.f / DM), r = __builtin_amdgcn_rsqf(fmaxf(t2 * (1.f / DM) - m * m, 0.f) + EPS), nm = -m * r;
#define LNROW(I, LN) { const float ri = dpp<0x150 + (LN)>(r), ni = dpp<0x150 + (LN)>(nm); _Pragma("unroll") for (int e = 0; e < 4; ++e) { xh[I][2 * e] = fmaf(bflo(xv[I][e]), ri, ni); xh[I][2 * e + 1] = fmaf(bfhi(xv[I][e]), ri, ni); } }
      LNROW(0, 0) LNROW(1, 4) LNROW(2, 2) LNROW(3, 6) LNROW(4, 1) LNROW(5, 5) LNROW(6, 3) LNROW(7, 7)
#undef LNROW
#pragma unroll
      for (int i = 0; i < 8; ++i) asm volatile("" : "+v"(xh[i][0]), "+v"(xh[i][1]), "+v"(xh[i][2]), "+v"(xh[i][3]), "+v"(xh[i][4]), "+v"(xh[i][5]), "+v"(xh[i][6]), "+v"(xh[i][7]));
    }
    STAMP(23)
    if (w > 0 && j != 0) WQ_DMA()
    u32x4 wkvf[16];
    if (j == 0) {
      WAITV_BAR(8);
      const u32 a0 = F_H + nh * (16384 + 128) + (256 * kh + 8 * h + q) * 32 + pp * 8;
#pragma unroll
      for (int s = 0; s < 16; ++s) wkvf[s] = ldstr2(a0 + s * 512, a0 + s * 512 + 128);
    }
    STAMP(24)
    if (w == 0) {
      u32x4 a0, a1, b0, b1;
#pragma unroll 1
      for (int it = 0; it < SPIN_CAP; ++it) {
        asm volatile("global_load_dwordx4 %0, %4, %5 sc1\n\tglobal_load_dwordx4 %1, %4, %5 offset:16 sc1\n\tglobal_load_dwordx4 %2, %4, %5 offset:2048 sc1\n\t"
                     "global_load_dwordx4 %3, %4, %5 offset:2064 sc1\n\ts_waitcnt vmcnt(0)" : "=&v"(b0), "=&v"(b1), "=&v"(a0), "=&v"(a1) : "v"(l * 32), "s"(modr) : "memory");
        if (__all(a0[1] == tag && a0[3] == tag && a1[1] == tag && a1[3] == tag && b0[1] == tag && b0[3] == tag && b1[1] == tag && b1[3] == tag)) break;
      }
      sts128(F_M + 512 + vo, u32x4{pk(1.f + bflo(a0[0]), 1.f + bfhi(a0[0])), pk(1.f + bflo(a0[2]), 1.f + bfhi(a0[2])), pk(1.f + bflo(a1[0]), 1.f + bfhi(a1[0])), pk(1.f + bflo(a1[2]), 1.f + bfhi(a1[2]))});
      sts128(F_M + 1536 + vo, u32x4{b0[0], b0[2], b1[0], b1[2]});
    }
    BAR();
    STAMP(25)
    if (w == 0 && j != 0) WQ_DMA()
    {
      const u32x4 ca = lds128(F_M + 512 + vo), sa = lds128(F_M + 1536 + vo);
      float A[8], B[8];
#pragma unroll
      for (int e = 0; e < 4; ++e) { A[2 * e] = bflo(ca[e]); A[2 * e + 1] = bfhi(ca[e]); B[2 * e] = bflo(sa[e]); B[2 * e + 1] = bfhi(sa[e]); }
#pragma unroll
      for (int i = 0; i < 8; ++i) {
        u32x4 o;
#pragma unroll
        for (int e = 0; e < 4; ++e) o[e] = pk(fmaf(xh[i][2 * e], A[2 * e], B[2 * e]), fmaf(xh[i][2 * e + 1], A[2 * e + 1], B[2 * e + 1]));
        const int tk = 8 * w + i;
        sts128(F_H + ((tk >> 5) * 32 + (l >> 1)) * 1024 + (l & 1) * 512 + (((tk & 31) ^ (l & 7)) * 16), o);
      }
    }
    if (j != 0) LATE_DMA()
    BAR();
    STAMP(26)
    u32x4 xf[16];
#pragma unroll
    for (int s = 0; s < 16; ++s) xf[s] = lds128(F_H + (tt * 32 + 16 * kh + s) * 1024 + h * 512 + (((l & 31) ^ ((2 * s + h) & 7)) * 16));
    if (j == 0) {
      const u32 xs = F_B + w * 2048 + vo, xr = F_B + (w ^ 4) * 2048 + vo;
      f32x16 acc = {};
      if (nt == 0) {
#pragma unroll
        for (int s = 0; s < 16; ++s) acc = mfma(wkvf[s], xf[s], acc);
      } else {
#pragma unroll
        for (int s = 0; s < 16; ++s) acc = mfma(xf[s], wkvf[s], acc);
      }
      float keep[8];
      if (kh == 0) { stsf4(xs, f32x4{acc[8], acc[9], acc[10], acc[11]}); stsf4(xs + 1024, f32x4{acc[12], acc[13], acc[14], acc[15]});
#pragma unroll
        for (int e = 0; e < 8; ++e) keep[e] = acc[e];
      } else { stsf4(xs, f32x4{acc[0], acc[1], acc[2], acc[3]}); stsf4(xs + 1024, f32x4{acc[4], acc[5], acc[6], acc[7]});
#pragma unroll
        for (int e = 0; e < 8; ++e) keep[e] = acc[8 + e];
      }
      BAR();
      const f32x4 r0 = ldsf4(xr), r1 = ldsf4(xr + 1024);
      u32x4 o = {pk(keep[0] + r0[0], keep[1] + r0[1]), pk(keep[2] + r0[2], keep[3] + r0[3]), pk(keep[4] + r1[0], keep[5] + r1[1]), pk(keep[6] + r1[2], keep[7] + r1[3])};
      char* img = p.ws + WS_KV + (size_t)(2 * g + tt) * TS;
      if (nt == 0) { if (kh == 1) gst16_sc1(img, vo, o); }
      else if ((l & 31) <= 17) {
        if ((l & 31) == 16) o = u32x4{0x3f803f80u, 0x3f803f80u, 0x3f803f80u, 0x3f803f80u};
        if ((l & 31) == 17) o = u32x4{0u, 0u, 0u, 0u};
        gst16_sc1(img + 1024 + kh * 576, h * 288 + (l & 31) * 16, o);
      }
      WAITV_BAR(0);
      if (w == 0 && l < 32) put_flag(p.ws + WS_FKV + l * 128 + g * 4, tag);
    }
    STAMP(21)
    if (j != 0) { if (w >= 6) WAITV(4); else WAITV(3); }
    BAR();
    const u32 rawr = (((nh * 2 + (pp >> 1)) ^ (((q >> 1) & 1) << 2)) * 16) + (pp & 1) * 8;
    f32x16 acc = {};
    {
      const u32 a0 = F_A + (256 * kh + 8 * h + q) * 128 + (rawr ^ (nt * 64));
#pragma unroll
      for (int s = 0; s < 16; ++s) acc = mfma(ldstr2(a0 + s * 2048, a0 + s * 2048 + 512), xf[s], acc);
    }
    const u32 xs = F_H + w * 4096 + vo, xr = F_H + (w ^ 4) * 4096 + vo;
    stsf4(xs, f32x4{acc[0], acc[1], acc[2], acc[3]}); stsf4(xs + 1024, f32x4{acc[4], acc[5], acc[6], acc[7]});
    stsf4(xs + 2048, f32x4{acc[8], acc[9], acc[10], acc[11]}); stsf4(xs + 3072, f32x4{acc[12], acc[13], acc[14], acc[15]});
    STAMP(27)
    if (w == 1 && j != 0) {
      {
        u32x4 a0, b0;
#pragma unroll 1
        for (int it = 0; it < SPIN_CAP; ++it) {
          asm volatile("global_load_dwordx4 %0, %2, %3 sc1\n\tglobal_load_dwordx4 %1, %2, %3 offset:2048 sc1\n\ts_waitcnt vmcnt(0)" : "=&v"(b0), "=&v"(a0) : "v"(vo), "s"(modr + 6144 + ukh * 1024) : "memory");
          if (__all(a0[1] == tag && a0[3] == tag && b0[1] == tag && b0[3] == tag)) break;
          __builtin_amdgcn_s_sleep(2);
        }
        stsf4(F_M + 2560 + vo, f32x4{rbf(1.f + bflo(a0[0])), rbf(1.f + bfhi(a0[0])), rbf(1.f + bflo(a0[2])), rbf(1.f + bfhi(a0[2]))});
        stsf4(F_M + 3584 + vo, f32x4{bflo(b0[0]), bfhi(b0[0]), bflo(b0[2]), bfhi(b0[2])});
      }
    }
    WAITV_BAR(0);
    f32x4 cs[2][4];
    if (w >= 6) { const u32 a0 = F_D + 8192 + (4 * h + q) * 128 + (rawr ^ (tt * 64)); fo_ = ldstr2(a0, a0 + 1024); }
    if (j != 0) {
      const u32 a0 = F_D + (16 * kq + 4 * h + q) * 128 + (rawr ^ (tt * 64));
      fd_ = ldstr2(a0, a0 + 1024);
#pragma unroll
      for (int i = 0; i < 2; ++i) {
        const int s = 2 * w + i;
        const u32 a1 = F_B + (pp >> 1) * (8192 + 128) + (16 * s + 4 * h + q) * 32 + nh * 16 + (pp & 1) * 8;
        fg[i] = ldstr2(a1, a1 + 256);
        cs[i][0] = ldsf4(F_M + 2560 + (16 * s + 4 * h) * 4); cs[i][1] = ldsf4(F_M + 2560 + (16 * s + 8 + 4 * h) * 4);
        cs[i][2] = ldsf4(F_M + 3584 + (16 * s + 4 * h) * 4); cs[i][3] = ldsf4(F_M + 3584 + (16 * s + 8 + 4 * h) * 4);
      }
    }
    const f32x4 r0 = ldsf4(xr), r1 = ldsf4(xr + 1024), r2 = ldsf4(xr + 2048), r3 = ldsf4(xr + 3072);
    q0 = u32x4{pk((acc[0] + r0[0]) * C2, (acc[1] + r0[1]) * C2), pk((acc[2] + r0[2]) * C2, (acc[3] + r0[3]) * C2), pk((acc[4] + r1[0]) * C2, (acc[5] + r1[1]) * C2), pk((acc[6] + r1[2]) * C2, (acc[7] + r1[3]) * C2)};
    q1 = u32x4{pk((acc[8] + r2[0]) * C2, (acc[9] + r2[1]) * C2), pk((acc[10] + r2[2]) * C2, (acc[11] + r2[3]) * C2), pk((acc[12] + r3[0]) * C2, (acc[13] + r3[1]) * C2), pk((acc[14] + r3[2]) * C2, (acc[15] + r3[3]) * C2)};
    PIN(q0); PIN(q1);
    if (w == 2) poll_flags<32>(p.ws + WS_FKV + (blockIdx.x & 31) * 128, tag, l);
    if (w == 3) {
#pragma unroll 1
      for (int it = 0; it < SPIN_CAP; ++it) {
        const u32 v = __hip_atomic_load((const GAS u32*)(modr + 4096 + j * 256 + h * 6144 + (l & 31) * 8 + 4), RLX_AGENT);
        if (__all(v == tag)) break;
        __builtin_amdgcn_s_sleep(2);
      }
    }
    BAR();
    STAMP(28)
    STAMP(29)
    { const u32 o = (w & 1) * 69632 + (w >> 1) * 6144;
      dma4s(p.ws + WS_KV + o, vo, o); set_m0(o + 4096); dma1<0>(p.ws + WS_KV + o + 4096, vo); dma1<1024>(p.ws + WS_KV + o + 4096, vo); }
    pdo = p.ws + WS_WO + ((size_t)(2 * j + tt) * 32 + g) * 1024;
    pdd = p.ws + WS_WD + ((size_t)(2 * (uid & 7) + tt) * 112 + 4 * (uid >> 3) + kq) * 1024;
    pdg = p.ws + WS_WGU + ((size_t)uT * 32 + 16 * ukh + 2 * w) * 1024;
    if (j != 0) {
      float us = 0.f, wsum = 0.f;
#pragma unroll
      for (int i = 0; i < 2; ++i) {
        const u32x4 f = fg[i];
        const f32x4 c0 = cs[i][0], c1 = cs[i][1], s0 = cs[i][2], s1 = cs[i][3];
        u32x4 o;
        o[0] = pk(bflo(f[0]) * c0[0], bfhi(f[0]) * c0[1]); o[1] = pk(bflo(f[1]) * c0[2], bfhi(f[1]) * c0[3]);
        o[2] = pk(bflo(f[2]) * c1[0], bfhi(f[2]) * c1[1]); o[3] = pk(bflo(f[3]) * c1[2], bfhi(f[3]) * c1[3]);
#pragma unroll
        for (int e = 0; e < 4; ++e) { const u32 v = o[e]; us = dot2(v, 0x3f803f80u, us); }
        wsum += bflo(f[0]) * s0[0] + bfhi(f[0]) * s0[1] + bflo(f[1]) * s0[2] + bfhi(f[1]) * s0[3] + bflo(f[2]) * s1[0] + bfhi(f[2]) * s1[1] + bflo(f[3]) * s1[2] + bfhi(f[3]) * s1[3];
        fg[i] = o;
      }
      us = swap32_sum(us); wsum = swap32_sum(wsum);
      if (l < 32) *(LAS f32x2*)(uintptr_t)(OEX + w * 256 + l * 8) = f32x2{us, wsum};
      WAITV_BAR(0);
      if (w == 7) {
        f32x2 s = {0.f, 0.f};
#pragma unroll
        for (int i = 0; i < 8; ++i) s += ldsf2(OEX + i * 256 + (l & 31) * 8);
        const float uh = rbf(s[0]), wh = rbf(s[1]);
        if (l < 32) gst16_sc1(p.ws + WS_AUG + (size_t)uT * 1024 + ukh * 512, vo, u32x4{pk(uh, s[0] - uh), pk(uh, wh), pk(s[1] - wh, wh), 0u});
      }
    } else {
      WAITV_BAR(0);
    }
  }
  const size_t fo = ((size_t)(2 * g + tt) * 32 + 4 * j + 2 * nt) * 1024;
  STAMP(1)
  f32x16 nm0, nm1;
  {
    const u32x4 k0 = lds128(vo);
    const float m0 = rowmax16(mfma(k0, q0, f32x16{})), m1 = rowmax16(mfma(k0, q1, f32x16{}));
#pragma unroll
    for (int r = 0; r < 16; ++r) { nm0[r] = -m0; nm1[r] = -m1; }
    PIN(nm0); PIN(nm1);
  }
  const u32 vrow = 1024 + h * 288 + min(l & 31, 17) * 16;
  u32 ka = kh * 32 * TS + vo, va = kh * 32 * TS + vrow;
  f32x16 o0 = {}, o1 = {};
  u32x4 kA = lds128(ka), kB = lds128(ka + TS);
  f32x16 s0 = mfma(kA, q0, nm0), s1;
  u32x4 vA0 = lds128(va), vA1 = lds128(va + 576), vB0 = {}, vB1 = {};
  u32x4 pa0, pa1, pb0 = {}, pb1 = {};
  PIN(s0);
  if (kh) {
    const u32 o = nt * 69632 + 24576 + tt * 22528;
#pragma unroll
    for (int i = 0; i < 5; ++i) dma4s(p.ws + WS_KV + o + i * 4096, vo, o + i * 4096);
    set_m0(o + 20480); dma1<0>(p.ws + WS_KV + o + 20480, vo); dma1<1024>(p.ws + WS_KV + o + 20480, vo);
  }
#define PREP_ST() { if (j != 0) { gst16_sc1(pdd, vo, fd_); gst16_sc1(pdg, vo, fg[0]); gst16_sc1(pdg + 1024, vo, fg[1]); } }
  if (kh) { PREP_ST() if (w >= 6) gst16_sc1(pdo, vo, fo_); }
  u32x4 wf[16];
#define HALF(SC, N0, N1, MF, OP, VP0, P0, VP1, P1, LD1, LD2)                                              \
  {                                                                                                       \
    MF; LD1; SB();                                                                                        \
    float e0 = EXP2(SC[0]), e1 = EXP2(SC[1]), e2 = EXP2(SC[2]), e3 = EXP2(SC[3]), e4 = EXP2(SC[4]), e5 = EXP2(SC[5]), e6 = EXP2(SC[6]), e7 = EXP2(SC[7]); SB(); \
    OP = mfma(VP0, P0, OP); SB();                                                                         \
    float e8 = EXP2(SC[8]), e9 = EXP2(SC[9]), e10 = EXP2(SC[10]), e11 = EXP2(SC[11]), e12 = EXP2(SC[12]), e13 = EXP2(SC[13]), e14 = EXP2(SC[14]), e15 = EXP2(SC[15]); SB(); \
    OP = mfma(VP1, P1, OP); LD2; SB();                                                                    \
    N0[0] = pk(e0, e1); N0[1] = pk(e2, e3); N0[2] = pk(e4, e5); N0[3] = pk(e6, e7); N1[0] = pk(e8, e9);   \
    N1[1] = pk(e10, e11); N1[2] = pk(e12, e13); N1[3] = pk(e14, e15); SB();                               \
  }
#define TILE(T, KC, KN, VC0, VC1, VP0, VP1)                                                               \
    HALF(s0, pa0, pa1, s1 = mfma(KC, q1, nm1), o1, VP0, pb0, VP1, pb1, KC = lds128(ka + ((T) + 2) * TS),  \
         (VP0 = lds128(va + ((T) + 1) * TS), VP1 = lds128(va + ((T) + 1) * TS + 576)))                    \
    HALF(s1, pb0, pb1, s0 = mfma(KN, q0, nm0), o0, VC0, pa0, VC1, pa1, , )
  const char* wgu = p.ws + WS_WGU + (size_t)id.c * 7 * 32768;
  const char* wob = p.ws + WS_WO + ((size_t)(2 * j) * 32 + 8 * kq) * 1024;
#define LDWF(SEL)                                                                                                     \
  asm volatile("s_cmp_lg_u32 %21, 0\n\ts_cbranch_scc1 .Lwf%=\n\t"                                                     \
               "global_load_dwordx4 %0, %16, %17 sc1\n\tglobal_load_dwordx4 %1, %16, %17 offset:1024 sc1\n\tglobal_load_dwordx4 %2, %16, %17 offset:2048 sc1\n\tglobal_load_dwordx4 %3, %16, %17 offset:3072 sc1\n\t"     \
               "global_load_dwordx4 %4, %16, %18 sc1\n\tglobal_load_dwordx4 %5, %16, %18 offset:1024 sc1\n\tglobal_load_dwordx4 %6, %16, %18 offset:2048 sc1\n\tglobal_load_dwordx4 %7, %16, %18 offset:3072 sc1\n\t"     \
               "global_load_dwordx4 %8, %16, %19 sc1\n\tglobal_load_dwordx4 %9, %16, %19 offset:1024 sc1\n\tglobal_load_dwordx4 %10, %16, %19 offset:2048 sc1\n\tglobal_load_dwordx4 %11, %16, %19 offset:3072 sc1\n\t"   \
               "global_load_dwordx4 %12, %16, %20 sc1\n\tglobal_load_dwordx4 %13, %16, %20 offset:1024 sc1\n\tglobal_load_dwordx4 %14, %16, %20 offset:2048 sc1\n\tglobal_load_dwordx4 %15, %16, %20 offset:3072 sc1\n"   \
               ".Lwf%=:"                                                                                              \
               : "+v"(wf[0]), "+v"(wf[1]), "+v"(wf[2]), "+v"(wf[3]), "+v"(wf[4]), "+v"(wf[5]), "+v"(wf[6]), "+v"(wf[7]), "+v"(wf[8]), "+v"(wf[9]), "+v"(wf[10]), \
                 "+v"(wf[11]), "+v"(wf[12]), "+v"(wf[13]), "+v"(wf[14]), "+v"(wf[15])                                  \
               : "v"(vo), "s"(wob), "s"(wob + 4096), "s"(wob + 32768), "s"(wob + 36864), "s"(SEL) : "memory", "scc")
#pragma unroll
  for (int s4 = 0; s4 < 16; ++s4) asm volatile("" : "=v"(wf[s4]));
  const int qt = blockIdx.x >> 6;
#pragma unroll 1
  for (int seg = 0; seg < 4; ++seg) {
    if (seg == 1) {
      if (!kh) PREP_ST()
      ASTAMP(10)
      WAITV_BAR(0);
      ASTAMP(24)
      if (w == 0 && l < 8) put_flag(p.ws + WS_FP + l * 1024 + blockIdx.x * 4, tag);
    } else if (seg == 2) {
      ASTAMP(21)
      if (w == 0) {
        u32x4 f;
#pragma unroll 1
        for (int it = 0; it < SPIN_CAP; ++it) {
          asm volatile("global_load_dwordx4 %0, %1, %2 sc1\n\ts_waitcnt vmcnt(0)" : "=&v"(f) : "v"(vo), "s"(p.ws + WS_FP + (blockIdx.x & 7) * 1024) : "memory");
          if (__all(f[0] == tag && f[1] == tag && f[2] == tag && f[3] == tag)) break;
          __builtin_amdgcn_s_sleep(1);
        }
        if (blockIdx.x == 0 && l == 0) put_flag(p.ws + WS_EPOCH, epoch + 1u);
      }
      BAR();
      ASTAMP(23)
    } else if (seg == 3) {
      ASTAMP(22)
      if (!kh) {
        dma4s(wgu + w * 8192, vo, SL0 + w * 8192); dma4s(wgu + w * 8192 + 4096, vo, SL0 + w * 8192 + 4096);
        dma4s(wgu + 32768 + w * 8192, vo, SL1 + w * 8192); dma4s(wgu + 32768 + w * 8192 + 4096, vo, SL1 + w * 8192 + 4096);
        set_m0(JUNK);
        touch8k(wgu + 65536 + qt * 40960 + w * 8192, l * 128);
        if (w < 2) touch8k(w == 0 ? wgu + 65536 + qt * 40960 + 32768 : p.ws + WS_AUG + (size_t)id.c * 7168, l * 128);
      }
    }
    LDWF((seg ^ 2) | (kh ^ 1));
    TILE(0, kA, kB, vA0, vA1, vB0, vB1) TILE(1, kB, kA, vB0, vB1, vA0, vA1) TILE(2, kA, kB, vA0, vA1, vB0, vB1) TILE(3, kB, kA, vB0, vB1, vA0, vA1)
    TILE(4, kA, kB, vA0, vA1, vB0, vB1) TILE(5, kB, kA, vB0, vB1, vA0, vA1) TILE(6, kA, kB, vA0, vA1, vB0, vB1) TILE(7, kB, kA, vB0, vB1, vA0, vA1)
    ka += 8 * TS; va += 8 * TS; PIN(ka); PIN(va);
  }
  o1 = mfma(vB0, pb0, o1); o1 = mfma(vB1, pb1, o1);
  LDWF(kh);
  if (!kh) {
    set_m0(JUNK);
    touch8k(p.ws + WS_WD + (size_t)(2 * j) * 114688 + qt * 57344 + w * 8192, l * 128);
    if (w < 3) touch8k(p.ws + WS_WD + (size_t)(2 * j) * 114688 + qt * 57344 + (w + 4) * 8192, l * 128);
  }
#undef HALF
#undef TILE
  STAMP(2)
  const u32 xo = (u32)((l & 31) * DM + 4 * h) * 2;
  const char* xres = (const char*)(p.x + (size_t)(64 * g + 32 * tt) * DM + 64 * j + 32 * nt + 16 * kh);
  u32x2 xa = gld8_s<0>(xres, xo), xb = gld8_s<16>(xres, xo);
  u32x4 ga = gld_s<1, 0>(modr + 4096 + (64 * j + 32 * nt + 16 * kh) * 4, h * 16), gb = gld_s<1, 32>(modr + 4096 + (64 * j + 32 * nt + 16 * kh) * 4, h * 16);
  {
    const u32 xs = OEX + w * 2304, xr = OEX + (w ^ 4) * 2304;
    float o[9];
    if (kh) { stsf4(xs + vo, f32x4{o0[0], o0[1], o0[2], o0[3]}); stsf4(xs + 1024 + vo, f32x4{o0[4], o0[5], o0[6], o0[7]}); *(LAS float*)(uintptr_t)(xs + 2048 + l * 4) = o0[8];
#pragma unroll
      for (int e = 0; e < 9; ++e) o[e] = o1[e];
    } else { stsf4(xs + vo, f32x4{o1[0], o1[1], o1[2], o1[3]}); stsf4(xs + 1024 + vo, f32x4{o1[4], o1[5], o1[6], o1[7]}); *(LAS float*)(uintptr_t)(xs + 2048 + l * 4) = o1[8];
#pragma unroll
      for (int e = 0; e < 9; ++e) o[e] = o0[e];
    }
    BAR();
    const f32x4 r0 = ldsf4(xr + vo), r1 = ldsf4(xr + 1024 + vo);
#pragma unroll
    for (int e = 0; e < 4; ++e) { o[e] += r0[e]; o[4 + e] += r1[e]; }
    float ls = lowhalf(o[8] + *(const LAS float*)(uintptr_t)(xr + 2048 + l * 4));
    bool bad = !(ls < 1.2676506e30f);
#pragma unroll
    for (int e = 0; e < 8; ++e) bad |= !(fabsf(o[e]) < 3.0e38f);
    if (__builtin_expect(__any(bad), 0)) {
      const u32x4 qf = kh ? q1 : q0;
      float m = -INFINITY;
      f32x16 oo = {};
#pragma unroll 1
      for (int t = 0; t < 64; ++t) {
        u32x4 kf, v0, v1;
        asm volatile("global_load_dwordx4 %0, %3, %5 sc1\n\tglobal_load_dwordx4 %1, %4, %5 sc1\n\tglobal_load_dwordx4 %2, %4, %5 offset:576 sc1\n\ts_waitcnt vmcnt(0)"
                     : "=&v"(kf), "=&v"(v0), "=&v"(v1) : "v"(vo), "v"(vrow), "s"(p.ws + WS_KV + t * TS) : "memory");
        const f32x16 s = mfma(kf, qf, f32x16{});
        const float mn = fmaxf(m, rowmax16(s)), al = EXP2(m - mn);
        m = mn;
        u32x4 pa, pb;
#pragma unroll
        for (int r = 0; r < 4; ++r) { pa[r] = pk(EXP2(s[2 * r] - mn), EXP2(s[2 * r + 1] - mn)); pb[r] = pk(EXP2(s[8 + 2 * r] - mn), EXP2(s[9 + 2 * r] - mn)); }
#pragma unroll
        for (int r = 0; r < 16; ++r) oo[r] *= al;
        oo = mfma(v0, pa, oo); oo = mfma(v1, pb, oo);
      }
#pragma unroll
      for (int e = 0; e < 8; ++e) o[e] = oo[e];
      ls = lowhalf(oo[8]);
    }
    const float rl = __builtin_amdgcn_rcpf(ls);
    gst16_sc1(p.ws + WS_O + fo + kh * 1024, vo, u32x4{pk(o[0] * rl, o[1] * rl), pk(o[2] * rl, o[3] * rl), pk(o[4] * rl, o[5] * rl), pk(o[6] * rl, o[7] * rl)});
  }
  asm volatile("s_waitcnt vmcnt(0)\n\ts_barrier"
               : "+v"(wf[0]), "+v"(wf[1]), "+v"(wf[2]), "+v"(wf[3]), "+v"(wf[4]), "+v"(wf[5]), "+v"(wf[6]), "+v"(wf[7]), "+v"(wf[8]), "+v"(wf[9]),
                 "+v"(wf[10]), "+v"(wf[11]), "+v"(wf[12]), "+v"(wf[13]), "+v"(wf[14]), "+v"(wf[15]), "+v"(xa), "+v"(xb), "+v"(ga), "+v"(gb)::"memory");
  STAMP(3)
  if (w == 0) {
    if (l == 0) put_flag(p.ws + WS_FO + g * 256 + j * 4, tag);
    poll_flags<8>(p.ws + WS_FO + g * 256, tag, l);
  }
  BAR();
  STAMP(4)
  u32x4 x1;
  {
    const char* osrc = p.ws + WS_O + ((size_t)(2 * g + tt) * 32 + 8 * kq) * 1024;
    u32x4 xf[8];
    xf[0] = gld_s<1, 0>(osrc, vo); xf[1] = gld_s<1, 1024>(osrc, vo); xf[2] = gld_s<1, 2048>(osrc, vo); xf[3] = gld_s<1, 3072>(osrc, vo);
    xf[4] = gld_s<1, 0>(osrc + 4096, vo); xf[5] = gld_s<1, 1024>(osrc + 4096, vo); xf[6] = gld_s<1, 2048>(osrc + 4096, vo); xf[7] = gld_s<1, 3072>(osrc + 4096, vo);
    f32x16 acc0 = {}, acc1 = {};
#define P3_STEP(S) { waitv_pin<7 - (S)>(xf[S]); acc0 = mfma(wf[S], xf[S], acc0); acc1 = mfma(wf[8 + (S)], xf[S], acc1); }
    P3_STEP(0) P3_STEP(1) P3_STEP(2) P3_STEP(3) P3_STEP(4) P3_STEP(5) P3_STEP(6) P3_STEP(7)
#undef P3_STEP
    STAMP(5)
    float r[8];
    reduce4(acc0, acc1, tt, kq, SL3, vo, r);
    STAMP(6)
    x1 = u32x4{pk(fmaf(bflo(ga[0]), r[0], bflo(xa[0])), fmaf(bfhi(ga[0]), r[1], bfhi(xa[0]))), pk(fmaf(bflo(ga[2]), r[2], bflo(xa[1])), fmaf(bfhi(ga[2]), r[3], bfhi(xa[1]))),
               pk(fmaf(bflo(gb[0]), r[4], bflo(xb[0])), fmaf(bfhi(gb[0]), r[5], bfhi(xb[0]))), pk(fmaf(bflo(gb[2]), r[6], bflo(xb[1])), fmaf(bfhi(gb[2]), r[7], bfhi(xb[1])))};
    gst16_sc1(p.ws + WS_X1 + fo + kh * 1024, vo, x1);
    if (w != 0) dma4s(wgu + 2 * 32768 + w * 4096, vo, SL2 + w * 4096);
    float s1 = 0.f, s2 = 0.f;
#pragma unroll
    for (int e = 0; e < 4; ++e) { const u32 v = x1[e]; s1 = dot2(v, 0x3f803f80u, s1); s2 = dot2(v, v, s2); }
    s1 = swap32_sum(s1); s2 = swap32_sum(s2);
    if (l < 32) *(LAS f32x2*)(uintptr_t)(GAPA + kq * 512 + tt * 256 + l * 8) = f32x2{s1, s2};
    BAR();
    STAMP(7)
    if (w == 0) {
      f32x2 s = {0.f, 0.f};
#pragma unroll
      for (int i = 0; i < 4; ++i) s += ldsf2(GAPA + i * 512 + l * 8);
      gst8_sc1(p.ws + WS_STAT + ((size_t)64 * g * 8 + j) * 8, l * 64, u32x2{__float_as_uint(s[0]), __float_as_uint(s[1])});
      dma4s(wgu + 2 * 32768, vo, SL2);
    }
    WAITV_BAR(4);
    STAMP(8)
    if (w == 0 && l == 0) put_flag(p.ws + WS_FX + (g >> 1) * 256 + ((g & 1) * 8 + j) * 4, tag);
  }
  u32x4 wd[28];
  if (kh == 0) p4_compute(p, id, l, w, tag, wd STAMP_PASS); else p4_helper(p, id, l, w, tag, wd STAMP_PASS);
  {
    int ly = l; PIN(ly);
    const char* xsrc = p.ws + WS_ACT + ((size_t)(2 * g) * 112 + 14 * w) * 1024;
    u32x4 xf[16];
#define XL(N) (xf[(N) % 16] = gld_s<1, 0>(xsrc + dks((N) >> 1) * 1024 + ((N) & 1) * 114688, vo))
    if (!kh) {
      WAITV(0);
      if (ly == 0) put_flag(p.ws + WS_FA + (4 * id.G + w) * 64 + id.c * 4, tag);
      BAR();
    }
    STAMP(18)
    XL(0); XL(1); XL(2); XL(3); XL(4); XL(5); XL(6); XL(7); XL(8); XL(9); XL(10); XL(11);
    u32x4 g0 = gld_s<1, 0>(modr + 10240 + (64 * j + 32 * nt + 16 * kh) * 4, (ly >> 5) * 16), g1 = gld_s<1, 32>(modr + 10240 + (64 * j + 32 * nt + 16 * kh) * 4, (ly >> 5) * 16);
    if (kh) asm volatile("s_barrier" ::: "memory");
    f32x16 acc[4];
#define K5_X(N, CNT)                                                                                 \
  {                                                                                                  \
    waitv_pin<CNT>(xf[(N) % 16]);                                                                    \
    if ((N) < 2) { mf0(acc[(N) & 1], wd[0], xf[(N) & 1]); mf0(acc[2 + ((N) & 1)], wd[14], xf[(N) & 1]); } \
    else { mfa(acc[(N) & 1], wd[dks((N) >> 1)], xf[(N) % 16]); mfa(acc[2 + ((N) & 1)], wd[14 + dks((N) >> 1)], xf[(N) % 16]); } \
  }
    K5_X(0, 13) K5_X(1, 12) K5_X(2, 11) K5_X(3, 10) K5_X(4, 9) K5_X(5, 8) K5_X(6, 7) K5_X(7, 6) K5_X(8, 5) K5_X(9, 4) K5_X(10, 3) K5_X(11, 2)
    PSTAMP(24)
    if (w == 4) poll_flags<32>(p.ws + WS_FA + g * 128, tag, ly);
    asm volatile("s_barrier" ::: "memory");
    PSTAMP(25)
    XL(12); XL(13); XL(14); XL(15); XL(16); XL(17); XL(18); XL(19); XL(20); XL(21); XL(22); XL(23); XL(24); XL(25); XL(26); XL(27);
    K5_X(12, 15) K5_X(13, 14) K5_X(14, 13) K5_X(15, 12) K5_X(16, 11) K5_X(17, 10) K5_X(18, 9) K5_X(19, 8) K5_X(20, 7) K5_X(21, 6) K5_X(22, 5) K5_X(23, 4)
    K5_X(24, 3) K5_X(25, 2) K5_X(26, 1) K5_X(27, 0)
#undef K5_X
    MFMA_SETTLE();
    PIN(g0); PIN(g1); PIN(acc[0]); PIN(acc[1]); PIN(acc[2]); PIN(acc[3]);
    STAMP(20)
    float r[8];
    reduce8(acc, w, vo, r);
    const u32 a0 = pk(fmaf(bflo(g0[0]), r[0], bflo(x1[0])), fmaf(bfhi(g0[0]), r[1], bfhi(x1[0]))), a1 = pk(fmaf(bflo(g0[2]), r[2], bflo(x1[1])), fmaf(bfhi(g0[2]), r[3], bfhi(x1[1])));
    const u32 b0 = pk(fmaf(bflo(g1[0]), r[4], bflo(x1[2])), fmaf(bfhi(g1[0]), r[5], bfhi(x1[2]))), b1 = pk(fmaf(bflo(g1[2]), r[6], bflo(x1[3])), fmaf(bfhi(g1[2]), r[7], bfhi(x1[3])));
    const auto s0 = __builtin_amdgcn_permlane32_swap(a0, b0, false, false);
    const auto s1 = __builtin_amdgcn_permlane32_swap(a1, b1, false, false);
    int lz = l; PIN(lz);
    *(u32x4*)(p.out + (size_t)(64 * g + 32 * tt + (lz & 31)) * DM + 64 * j + 32 * nt + 16 * kh + 8 * (lz >> 5)) = u32x4{s0[0], s1[0], s0[1], s1[1]};
  }
  STAMP_FLUSH(0)
}

extern "C" void kernel_launch(void* const* d_in, const int* in_sizes, int n_in, void* d_out, int out_size, void* d_ws, size_t ws_size, hipStream_t stream) {
  Params p;
  p.x = (const u16*)d_in[0]; p.wq = (const u16*)d_in[1]; p.wk = (const u16*)d_in[2]; p.wv = (const u16*)d_in[3]; p.wo = (const u16*)d_in[4];
  p.wg = (const u16*)d_in[6]; p.wu = (const u16*)d_in[7]; p.wd = (const u16*)d_in[8]; p.c = (const u16*)d_in[9]; p.wm = (const u16*)d_in[10];
  p.out = (u16*)d_out; p.ws = (char*)d_ws;
  (void)hipFuncSetAttribute((const void*)k_main, hipFuncAttributeMaxDynamicSharedMemorySize, M_LDS);
  k_main<<<256, 512, M_LDS, stream>>>(p);
}
```

```cpp
#include <hip/hip_runtime.h>
#include <stdint.h>

typedef unsigned short u16;
typedef unsigned int u32;
typedef __attribute__((ext_vector_type(8))) __bf16 bf16x8;
typedef __attribute__((ext_vector_type(2))) __bf16 bf16x2;
typedef __attribute__((ext_vector_type(2))) float f32x2;
typedef __attribute__((ext_vector_type(4))) float f32x4;
typedef __attribute__((ext_vector_type(16))) float f32x16;
typedef __attribute__((ext_vector_type(4))) u32 u32x4;
typedef __attribute__((ext_vector_type(2))) u32 u32x2;
typedef __attribute__((ext_vector_type(4))) short s16x4;
#define LAS __attribute__((address_space(3)))
#define GAS __attribute__((address_space(1)))
#define DI __device__ __forceinline__

constexpr int DM = 512, FF = 1792;
constexpr float EPS = 1e-5f;
constexpr float C2 = 0.25f * 1.4426950408889634f;

constexpr size_t WS_EPOCH = 0;
constexpr size_t WS_FO = 4096;
constexpr size_t WS_FX = WS_FO + 16384;
constexpr size_t WS_FS = WS_FX + 8192;
constexpr size_t WS_FE = WS_FS + 8192;
constexpr size_t WS_FA = WS_FE + 8192;
constexpr size_t WS_FKV = WS_FA + 8192;
constexpr size_t WS_FP = WS_FKV + 8192;
constexpr size_t WS_MOD = 81920;
constexpr size_t WS_KV = WS_MOD + 8 * 12288;
constexpr size_t WS_STAT = WS_KV + 139264;
constexpr size_t WS_AUG = WS_STAT + 131072;
constexpr size_t WS_WO = 1u << 20;
constexpr size_t WS_WD = WS_WO + (512u << 10);
constexpr size_t WS_WGU = WS_WD + (1792u << 10);
constexpr size_t WS_O = WS_WGU + (3584u << 10);
constexpr size_t WS_X1 = WS_O + (2u << 20);
constexpr size_t WS_ACT = WS_X1 + (2u << 20);
static_assert(WS_AUG + 229376 <= WS_WO, "workspace map");
constexpr int TS = 2176;

struct Params {
  const u16 *x, *wq, *wk, *wv, *wo, *wg, *wu, *wd, *c, *wm;
  u16* out;
  char* ws;
};

DI f32x16 mfma(u32x4 a, u32x4 b, f32x16 c) {
  return __builtin_amdgcn_mfma_f32_32x32x16_bf16(__builtin_bit_cast(bf16x8, a), __builtin_bit_cast(bf16x8, b), c, 0, 0, 0);
}
DI u32 pk(float lo, float hi) {
  f32x2 v = {lo, hi};
  return __builtin_bit_cast(u32, __builtin_convertvector(v, bf16x2));
}
DI float bflo(u32 u) { return __uint_as_float(u << 16); }
DI float bfhi(u32 u) { return __uint_as_float(u & 0xffff0000u); }
DI float rbf(float f) { return bflo(pk(f, 0.f)); }
DI float dot2(u32 a, u32 b, float c) { return __builtin_amdgcn_fdot2_f32_bf16(__builtin_bit_cast(bf16x2, a), __builtin_bit_cast(bf16x2, b), c, false); }
DI void set_m0(u32 lds) { asm volatile("s_mov_b32 m0, %0\n\ts_nop 0" ::"s"(lds) : "memory"); }
template <int OFF> DI void dma(const void* base, u32 vo) { asm volatile("global_load_lds_dwordx4 %0, %1 offset:%2" ::"v"(vo), "s"(base), "n"(OFF) : "memory"); }
template <int OFF> DI void dma1(const void* base, u32 vo) { asm volatile("global_load_lds_dwordx4 %0, %1 offset:%2 sc1" ::"v"(vo), "s"(base), "n"(OFF) : "memory"); }
DI void dma4(const void* base, u32 vo, u32 lds) { set_m0(lds); dma<0>(base, vo); dma<1024>(base, vo); dma<2048>(base, vo); dma<3072>(base, vo); }
DI void dma4s(const void* base, u32 vo, u32 lds) { set_m0(lds); dma1<0>(base, vo); dma1<1024>(base, vo); dma1<2048>(base, vo); dma1<3072>(base, vo); }
template <int SC, int OFF> DI u32x4 gld_s(const void* base, u32 vo) {
  u32x4 r;
  if (SC) asm volatile("global_load_dwordx4 %0, %1, %2 offset:%3 sc1" : "=v"(r) : "v"(vo), "s"(base), "n"(OFF) : "memory");
  else asm volatile("global_load_dwordx4 %0, %1, %2 offset:%3" : "=v"(r) : "v"(vo), "s"(base), "n"(OFF) : "memory");
  return r;
}
template <int OFF> DI u32x2 gld8_s(const void* base, u32 vo) { u32x2 r; asm volatile("global_load_dwordx2 %0, %1, %2 offset:%3" : "=v"(r) : "v"(vo), "s"(base), "n"(OFF) : "memory"); return r; }
template <int OFF> DI u32 gld2_s(const void* base, u32 vo) { u32 r; asm volatile("global_load_ushort %0, %1, %2 offset:%3" : "=v"(r) : "v"(vo), "s"(base), "n"(OFF) : "memory"); return r; }
DI void touch8k(const char* base, u32 l128) { asm volatile("global_load_lds_dword %0, %1 sc1" ::"v"(l128), "s"(base) : "memory"); }
DI void gst16_sc1(char* base, u32 vo, u32x4 v) { asm volatile("global_store_dwordx4 %0, %1, %2 sc1\n\ts_nop 1" ::"v"(vo), "v"(v), "s"(base) : "memory"); }
DI void gst8_sc1(char* base, u32 vo, u32x2 v) { asm volatile("global_store_dwordx2 %0, %1, %2 sc1\n\ts_nop 1" ::"v"(vo), "v"(v), "s"(base) : "memory"); }
#define RLX_AGENT __ATOMIC_RELAXED, __HIP_MEMORY_SCOPE_AGENT
DI void put_flag(char* f, u32 tag) { __hip_atomic_store((GAS u32*)f, tag, RLX_AGENT); }
#define SLP 16
constexpr int SPIN_CAP = 1 << 20;
template <int PER> DI u32 fofs(int i) { return (u32)((i / PER) * 256 + (i % PER) * 4); }
template <int N, int PER, int NAP = 0> DI void poll_flags(const char* f, u32 tag, int l) {
  const char* a = f + fofs<PER>(l < N ? l : 0);
#pragma unroll 1
  for (int it = 0; it < SPIN_CAP; ++it) {
    const u32 v = __hip_atomic_load((const GAS u32*)a, RLX_AGENT);
    if (__all(v == tag)) break;
    if (NAP) __builtin_amdgcn_s_sleep(NAP);
  }
  __builtin_amdgcn_fence(__ATOMIC_ACQUIRE, "wavefront");
}
#define SB() __builtin_amdgcn_sched_barrier(0)
#define PIN(x) asm volatile("" : "+v"(x))
#define WAITV(N) asm volatile("s_waitcnt vmcnt(" #N ")" ::: "memory")
#define WAITV_BAR(N) asm volatile("s_waitcnt vmcnt(" #N ") lgkmcnt(0)\n\ts_barrier" ::: "memory")
#define BAR() asm volatile("s_waitcnt lgkmcnt(0)\n\ts_barrier" ::: "memory")
template <int N> DI void waitv_pin(u32x4& v) { asm volatile("s_waitcnt vmcnt(%1)" : "+v"(v) : "n"(N) : "memory"); }
DI u32x4 lds128(u32 a) { return *(const LAS u32x4*)(uintptr_t)a; }
DI f32x4 ldsf4(u32 a) { return *(const LAS f32x4*)(uintptr_t)a; }
DI f32x2 ldsf2(u32 a) { return *(const LAS f32x2*)(uintptr_t)a; }
DI void sts128(u32 a, u32x4 v) { *(LAS u32x4*)(uintptr_t)a = v; }
DI void stsf4(u32 a, f32x4 v) { *(LAS f32x4*)(uintptr_t)a = v; }
DI u32x2 ldstr(u32 a) { return __builtin_bit_cast(u32x2, __builtin_amdgcn_ds_read_tr16_b64_v4i16((LAS s16x4*)(uintptr_t)a)); }
DI u32x4 ldstr2(u32 a0, u32 a1) { u32x2 p = ldstr(a0), q = ldstr(a1); return u32x4{p[0], p[1], q[0], q[1]}; }
template <int CTRL> DI float dpp(float v) { return __int_as_float(__builtin_amdgcn_mov_dpp(__float_as_int(v), CTRL, 0xf, 0xf, true)); }
DI float swap32_sum(float v) { auto r = __builtin_amdgcn_permlane32_swap(__float_as_uint(v), __float_as_uint(v), false, false); return __uint_as_float(r[0]) + __uint_as_float(r[1]); }
DI float swap32_max(float v) { auto r = __builtin_amdgcn_permlane32_swap(__float_as_uint(v), __float_as_uint(v), false, false); return fmaxf(__uint_as_float(r[0]), __uint_as_float(r[1])); }
DI float swap16_sum(float v) { auto r = __builtin_amdgcn_permlane16_swap(__float_as_uint(v), __float_as_uint(v), false, false); return __uint_as_float(r[0]) + __uint_as_float(r[1]); }
DI float lowhalf(float v) { auto r = __builtin_amdgcn_permlane32_swap(__float_as_uint(v), __float_as_uint(v), false, false); return __uint_as_float(r[0]); }
template <int CTRL> DI float halve(bool bit, float a, float b) { return (bit ? b : a) + dpp<CTRL>(bit ? a : b); }
DI int rfl(int v) { return __builtin_amdgcn_readfirstlane(v); }
#define EXP2(v) __builtin_amdgcn_exp2f(v)

struct Ids { int G, c, g, j; };
DI Ids ids() { const int b = blockIdx.x, x = b & 7, i = b >> 3; Ids r; r.G = (x >> 1) * 4 + (i >> 3); r.c = (x & 1) * 8 + (i & 7); r.g = 4 * x + (i >> 3); r.j = i & 7; return r; }

#define STAMP_DECL
#define STAMP(I)
#define STAMP_FLUSH(K)
#define STAMP_ARG
#define STAMP_PASS

#define PSTAMP(I)
#define ASTAMP(I)
constexpr u32 M_LDS = 163840;
constexpr u32 F_A = 0;
constexpr u32 F_H = 65536;
constexpr u32 F_B = 131072;
constexpr u32 F_D = F_B + 16640;
constexpr u32 F_M = F_D + 10240;
static_assert(F_M + 4608 <= M_LDS, "front-end LDS map");
constexpr u32 SL0 = 0, SL1 = 69632, SL2 = 32768, SL3 = 102400, TOPA = 135168, GAPA = 65536, OEX = 143616, JUNK = M_LDS - 256;

DI void mfa(f32x16& c, u32x4 a, u32x4 b) { asm volatile("v_mfma_f32_32x32x16_bf16 %0, %1, %2, %0" : "+v"(c) : "v"(a), "v"(b) : "memory"); }
DI void mf0(f32x16& c, u32x4 a, u32x4 b) { asm volatile("v_mfma_f32_32x32x16_bf16 %0, %1, %2, 0" : "=&v"(c) : "v"(a), "v"(b) : "memory"); }
DI float vexp(float a) { float r; asm volatile("v_exp_f32 %0, %1" : "=v"(r) : "v"(a)); return r; }
DI float vrcp(float a) { float r; asm volatile("v_rcp_f32 %0, %1" : "=v"(r) : "v"(a)); return r; }
DI u32 vcvt(float a, float b) { u32 r; asm volatile("v_cvt_pk_bf16_f32 %0, %1, %2" : "=v"(r) : "v"(a), "v"(b)); return r; }
DI float vadd(float a, float b) { float r; asm volatile("v_add_f32 %0, %1, %2" : "=v"(r) : "v"(a), "v"(b)); return r; }
DI float vmul(float a, float b) { float r; asm volatile("v_mul_f32 %0, %1, %2" : "=v"(r) : "v"(a), "v"(b)); return r; }
DI float vfma_k(float a, float k, float c) { float r; asm volatile("v_fma_f32 %0, %1, %2, %3" : "=v"(r) : "v"(a), "s"(k), "v"(c)); return r; }
DI float vadd1(float a) { float r; asm volatile("v_add_f32 %0, 1.0, %1" : "=v"(r) : "v"(a)); return r; }
DI void vdotk(float& c, u32 a, u32 k) { asm volatile("v_dot2c_f32_bf16 %0, %2, %1" : "+v"(c) : "v"(a), "s"(k)); }
DI void vdot(float& c, u32 a) { asm volatile("v_dot2c_f32_bf16 %0, %1, %1" : "+v"(c) : "v"(a)); }
#define MFMA_SETTLE() asm volatile("s_nop 15\n\ts_nop 3" ::: "memory")
DI float rowmax16(const f32x16& s) {
  float m = fmaxf(fmaxf(s[0], s[1]), s[2]);
#pragma unroll
  for (int r = 3; r < 15; r += 2) m = fmaxf(fmaxf(m, s[r]), s[r + 1]);
  return swap32_max(fmaxf(m, s[15]));
}
constexpr float GC0 = -2.f * 0.7978845608028654f * 1.4426950408889634f, GC1 = GC0 * 0.044715f;
template <u32 A> DI u32x4 ldsr(u32 lo, u32 mid) { return A < 65536 ? lds128(lo + A) : lds128(mid + (A - 65536)); }

DI u32x4 pack8(const f32x16& a, int o8) { return u32x4{pk(a[o8], a[o8 + 1]), pk(a[o8 + 2], a[o8 + 3]), pk(a[o8 + 4], a[o8 + 5]), pk(a[o8 + 6], a[o8 + 7])}; }
template <int N> DI void sum_pieces(u32 a, float (&r)[8]) {
  u32 klo = 0x00003f80u, khi = 0x3f800000u;
  asm volatile("" : "+s"(klo), "+s"(khi));
  u32x4 u[N];
#pragma unroll
  for (int i = 0; i < N; ++i) u[i] = lds128(a + i * 1024);
#pragma unroll
  for (int e = 0; e < 4; ++e) { r[2 * e] = bflo(u[0][e]); r[2 * e + 1] = bfhi(u[0][e]); }
#pragma unroll
  for (int i = 1; i < N; ++i)
#pragma unroll
    for (int e = 0; e < 4; ++e) { const u32 v = u[i][e]; r[2 * e] = dot2(v, klo, r[2 * e]); r[2 * e + 1] = dot2(v, khi, r[2 * e + 1]); }
}

constexpr u32 SL4 = 0xffffffffu;
template <u32 SL, int S> DI u32x4 wrd(u32 lo, u32 mid, u32 hi) {
  constexpr u32 A = SL == SL4 ? (S < 28 ? TOPA + S * 1024 : GAPA + (S - 28) * 1024) : SL + S * 1024;
  return A < 65536 ? lds128(lo + A) : (A < 131072 ? lds128(mid + (A - 65536)) : lds128(hi + (A - 131072)));
}
DI constexpr int dks(int i) { return i < 6 ? 7 * (i / 3) + i % 3 : 7 * ((i - 6) >> 2) + 3 + ((i - 6) & 3); }

DI void p4_compute(const Params& p, const Ids& id, int l, int w, u32 tag, u32x4 (&wd)[28] STAMP_ARG) {
  const u32 vo = l * 16;
  char* adst = p.ws + WS_ACT + ((size_t)(4 * id.G + w) * 112 + 7 * id.c) * 1024;
  const char* asrc = p.ws + WS_AUG + (size_t)id.c * 7168;
  u32x4 aug[7];
  aug[0] = gld_s<1, 0>(asrc, vo); aug[1] = gld_s<1, 1024>(asrc, vo); aug[2] = gld_s<1, 2048>(asrc, vo);
  if (w == 0) poll_flags<16, 8>(p.ws + WS_FX + id.G * 512, tag, l);
  asm volatile("s_waitcnt vmcnt(0)\n\ts_barrier" : "+v"(aug[0]), "+v"(aug[1]), "+v"(aug[2])::"memory");
  STAMP(9)
  const char* xsrc = p.ws + WS_X1 + (size_t)(4 * id.G + w) * 32768;
  u32x4 xf[32];
#define XLD(S) (xf[S] = gld_s<1, ((S) & 3) * 1024>(xsrc + ((S) >> 2) * 4096, vo))
  XLD(0); XLD(1); XLD(2); XLD(3); XLD(4); XLD(5); XLD(6); XLD(7); XLD(8); XLD(9); XLD(10); XLD(11); XLD(12); XLD(13); XLD(14); XLD(15);
  float rs, gc0 = GC0; u32x4 xaug;
  u32 lo = vo, mid = vo + 65536, hi = vo + 131072;
  PIN(lo); PIN(mid); PIN(hi); PIN(gc0);
  f32x16 acc[3]; u32x4 ra[32], rb[32];
  ra[0] = wrd<SL0, 0>(lo, mid, hi); rb[0] = wrd<SL1, 0>(lo, mid, hi); ra[1] = wrd<SL0, 1>(lo, mid, hi); rb[1] = wrd<SL1, 1>(lo, mid, hi);
  ra[2] = wrd<SL0, 2>(lo, mid, hi); rb[2] = wrd<SL1, 2>(lo, mid, hi);
#define PH0(S) { constexpr int s = (S);                                                                            \
      waitv_pin<(s < 16 ? 15 : 31 - s)>(xf[s]);                                                                    \
      if (s == 0) mf0(acc[0], ra[0], xf[0]); else mfa(acc[0], ra[s], xf[s]);                                       \
      if (s + 16 < 32) XLD((s + 16) & 31);                                                                         \
      if (s + 3 < 32) { ra[(s + 3) & 31] = wrd<SL0, (s + 3) & 31>(lo, mid, hi); rb[(s + 3) & 31] = wrd<SL1, (s + 3) & 31>(lo, mid, hi); } \
      if (s == 0) mf0(acc[1], rb[0], xf[0]); else mfa(acc[1], rb[s], xf[s]); }
 PH0(0) PH0(1) PH0(2) PH0(3) PH0(4) PH0(5) PH0(6) PH0(7) \
 PH0(8) PH0(9) PH0(10) PH0(11) PH0(12) PH0(13) PH0(14) PH0(15) \
 PH0(16) PH0(17) PH0(18) PH0(19) PH0(20) PH0(21) PH0(22) PH0(23) \
 PH0(24) PH0(25) PH0(26) PH0(27) PH0(28) PH0(29) PH0(30) PH0(31)
#undef PH0
  ra[0] = wrd<SL2, 0>(lo, mid, hi); ra[1] = wrd<SL2, 1>(lo, mid, hi); ra[2] = wrd<SL2, 2>(lo, mid, hi); ra[3] = wrd<SL2, 3>(lo, mid, hi);
  STAMP(11)
  asm volatile("s_barrier" ::: "memory");
  xaug = lds128(GAPA + w * 512 + (l & 31) * 16);
  rs = *(const LAS float*)(uintptr_t)(GAPA + 2048 + w * 128 + (l & 31) * 4);
  f32x4 gv, uv, zz; u32x4 q;
#define E4(F) { _Pragma("unroll") for (int e = 0; e < 4; ++e) { F; } }
#define E2(B, F) { _Pragma("unroll") for (int e = B; e < B + 2; ++e) { F; } }
#define UNIT(T, U) { constexpr int u = (U) % 13, GO = ((U) / 13) * 8; const f32x16& k_ = acc[(T) % 3];              \
      if (u == 0) E4(gv[e] = vmul(k_[GO + e], rs))                                                                  \
      if (u == 1) E4(zz[e] = vmul(gv[e], gv[e]))                                                                    \
      if (u == 2) E4(zz[e] = vfma_k(zz[e], GC1, gc0))                                                               \
      if (u == 3) E4(zz[e] = vmul(zz[e], gv[e]))                                                                    \
      if (u == 4) E2(0, zz[e] = vexp(zz[e]))                                                                        \
      if (u == 5) E2(2, zz[e] = vexp(zz[e]))                                                                        \
      if (u == 6) E4(uv[e] = vmul(k_[GO + 4 + e], rs))                                                              \
      if (u == 7) E4(zz[e] = vadd1(zz[e]))                                                                          \
      if (u == 8) E2(0, zz[e] = vrcp(zz[e]))                                                                        \
      if (u == 9) E2(2, zz[e] = vrcp(zz[e]))                                                                        \
      if (u == 10) E4(uv[e] = vmul(uv[e], gv[e]))                                                                   \
      if (u == 11) E4(uv[e] = vmul(uv[e], zz[e]))                                                                   \
      if (u == 12) { q[GO / 4] = vcvt(uv[0], uv[1]); q[GO / 4 + 1] = vcvt(uv[2], uv[3]); if (GO) gst16_sc1(adst + (T) * 1024, vo, q); } }
#define E0(T) ((T) == 0 ? 9 : (T) == 1 ? 35 : (T) == 2 ? 61 : (T) == 3 ? 87 : (T) == 4 ? 113 : 139)
#define GAP1(T, G) { if ((G) >= E0(T) && (G) < E0(T) + 26) UNIT(T, ((G) - E0(T) + 26) % 26) }
#define GAP(G) { GAP1(0, G) GAP1(1, G) GAP1(2, G) GAP1(3, G) GAP1(4, G) GAP1(5, G) }
#define PH(S) { constexpr int s = (S);                                                                             \
      if (s == 0) mf0(acc[TT % 3], ra[0], xf[0]); else if (s < 32) mfa(acc[TT % 3], ra[s & 31], xf[s & 31]); else mfa(acc[TT % 3], aug[TT], xaug); \
      if (s < 32) { if (s + 4 < 32) ra[(s + 4) & 31] = wrd<SA, (s + 4) & 31>(lo, mid, hi); else if (TT < 6) ra[(s + 4) & 31] = wrd<SN, (s + 4) & 3>(lo, mid, hi); } \
      if (TT == 2 && s == 3) { mfa(acc[0], aug[0], xaug); mfa(acc[1], aug[1], xaug); }                             \
      if (s == 1 && TT < 6) aug[TT < 6 ? TT + 1 : 0] = gld_s<1, 0>(asrc + (TT + 1) * 1024, vo);                    \
      GAP((TT - 2) * 33 + s) }
#define PHASE  PH(0) PH(1) PH(2) PH(3) PH(4) PH(5) PH(6) PH(7) \
 PH(8) PH(9) PH(10) PH(11) PH(12) PH(13) PH(14) PH(15) \
 PH(16) PH(17) PH(18) PH(19) PH(20) PH(21) PH(22) PH(23) \
 PH(24) PH(25) PH(26) PH(27) PH(28) PH(29) PH(30) PH(31) \
 PH(32)
  { constexpr int TT = 2; constexpr u32 SA = SL2, SN = SL3; PHASE }
  asm volatile("s_waitcnt vmcnt(0)\n\ts_barrier" : "+v"(aug[3])::"memory");
  STAMP(12)
  { constexpr int TT = 3; constexpr u32 SA = SL3, SN = SL4; PHASE }
  asm volatile("s_waitcnt vmcnt(1)\n\ts_barrier" : "+v"(aug[4])::"memory");
  STAMP(13)
  { constexpr int TT = 4; constexpr u32 SA = SL4, SN = SL0; PHASE }
  asm volatile("s_waitcnt vmcnt(1)\n\ts_barrier" : "+v"(aug[5])::"memory");
  STAMP(14)
  { constexpr int TT = 5; constexpr u32 SA = SL0, SN = SL1; PHASE }
  asm volatile("s_waitcnt vmcnt(1)\n\ts_barrier" : "+v"(aug[6])::"memory");
  STAMP(15)
  { constexpr int TT = 6; constexpr u32 SA = SL1, SN = SL1; PHASE }
  STAMP(16)
  MFMA_SETTLE();
 UNIT(6, 0) UNIT(6, 1) UNIT(6, 2) UNIT(6, 3) UNIT(6, 4) UNIT(6, 5) UNIT(6, 6) UNIT(6, 7) UNIT(6, 8)
 UNIT(6, 9) UNIT(6, 10) UNIT(6, 11) UNIT(6, 12) UNIT(6, 13) UNIT(6, 14) UNIT(6, 15) UNIT(6, 16) UNIT(6, 17)
 UNIT(6, 18) UNIT(6, 19) UNIT(6, 20) UNIT(6, 21) UNIT(6, 22) UNIT(6, 23) UNIT(6, 24) UNIT(6, 25)
  STAMP(17)
  asm volatile("s_barrier" ::: "memory");
  {
    const u32 b = w * 8192;
#define WDR(F) (wd[F] = (F) < 8 ? lds128(lo + SL2 + b + ((F) & 7) * 1024) : (F) < 16 ? lds128(mid + (SL3 - 65536) + b + ((F) & 7) * 1024) : (F) < 22 ? lds128(hi + (TOPA - 131072) + w * 6144 + (((F) - 16) & 7) * 1024) : lds128(lo + SL0 + w * 6144 + (((F) - 22) & 7) * 1024))
 WDR(0); WDR(1); WDR(2); WDR(3); WDR(4); WDR(5); WDR(6); WDR(7); WDR(8); WDR(9);
 WDR(10); WDR(11); WDR(12); WDR(13); WDR(14); WDR(15); WDR(16); WDR(17); WDR(18); WDR(19);
 WDR(20); WDR(21); WDR(22); WDR(23); WDR(24); WDR(25); WDR(26); WDR(27);
#undef WDR
  }
#undef PHASE
#undef PH
#undef GAP
#undef GAP1
#undef UNIT
#undef XLD
}

DI void p4_helper(const Params& p, const Ids& id, int l, int w, u32 tag, u32x4 (&wd)[28] STAMP_ARG) {
  const u32 vo = l * 16;
  const int hw = w - 4;
  const char* wsrc = p.ws + WS_WGU + (size_t)id.c * 7 * 32768 + hw * 8192;
  const char* dme = p.ws + WS_WD + ((size_t)(2 * id.j) * 112 + 14 * w) * 1024;
  const char* dcw = p.ws + WS_WD + ((size_t)(2 * id.j) * 112 + 14 * hw) * 1024;
#define D8(SRC, DST) { dma4s(SRC, vo, DST); dma4s((SRC) + 4096, vo, (DST) + 4096); }
#define D6(SRC, DST) { dma4s(SRC, vo, DST); set_m0((DST) + 4096); dma1<0>((SRC) + 4096, vo); dma1<1024>((SRC) + 4096, vo); }
#define WDG(F) (wd[F] = gld_s<1, 0>(dme + ((F) < 14 ? (F) : 98 + (F)) * 1024, vo))
  D8(wsrc + 3 * 32768, SL3 + hw * 8192)
  WDG(16); WDG(17); WDG(18); WDG(19); WDG(20); WDG(21); WDG(22); WDG(23); WDG(24); WDG(25); WDG(26); WDG(27);
  WAITV_BAR(20);
  {
    __builtin_amdgcn_s_sleep(16);
    poll_flags<16, 8, 8>(p.ws + WS_FS + id.G * 512, tag, l);
    u32x4 st0, st1;
    asm volatile("global_load_dwordx4 %0, %2, %3 sc1\n\tglobal_load_dwordx4 %1, %2, %3 offset:16 sc1\n\ts_waitcnt vmcnt(0)" : "=&v"(st0), "=&v"(st1)
                 : "v"((l & 31) * 64 + (l >> 5) * 32), "s"(p.ws + WS_STAT + (size_t)(128 * id.G + 32 * hw) * 64) : "memory");
    const float s1 = swap32_sum((__uint_as_float(st0[0]) + __uint_as_float(st0[2])) + (__uint_as_float(st1[0]) + __uint_as_float(st1[2])));
    const float s2 = swap32_sum((__uint_as_float(st0[1]) + __uint_as_float(st0[3])) + (__uint_as_float(st1[1]) + __uint_as_float(st1[3])));
    const float nmu = -s1 * (1.f / DM), var = fmaxf(s2 * (1.f / DM) - nmu * nmu, 0.f) + EPS;
    const float rs = __builtin_amdgcn_rsqf(var), sg = var * rs, mh = rbf(nmu), sh = rbf(sg);
    if (l < 32) { sts128(GAPA + hw * 512 + vo, u32x4{pk(mh, mh), pk(nmu - mh, sh), pk(sh, sg - sh), 0u}); *(LAS float*)(uintptr_t)(GAPA + 2048 + hw * 128 + l * 4) = rs; }
  }
  dma4s(wsrc + 4 * 32768, vo, TOPA + hw * 8192); if (hw < 3) dma4s(wsrc + 4 * 32768 + 4096, vo, TOPA + hw * 8192 + 4096);
  BAR();
  D8(wsrc + 5 * 32768, SL0 + hw * 8192) WDG(0); WDG(1); WDG(2); WDG(3);
  WAITV_BAR(12);
  if (hw == 3) dma4s(wsrc + 4 * 32768 + 4096, vo, GAPA);
  D8(wsrc + 6 * 32768, SL1 + hw * 8192) dma4s(dcw, vo, SL2 + hw * 8192);
  WAITV_BAR(12);
  dma4s(dcw + 4096, vo, SL2 + hw * 8192 + 4096); D6(dcw + 8192, SL3 + hw * 8192)
  set_m0(SL3 + hw * 8192 + 6144); dma1<0>(dcw + 114688, vo); dma1<1024>(dcw + 114688, vo);
  WAITV_BAR(16);
  D6(dcw + 114688 + 2048, TOPA + hw * 6144) WDG(4); WDG(5); WDG(6); WDG(7); WDG(8); WDG(9);
  if (w == 4 && l == 0) *(LAS u32*)(uintptr_t)GAPA = 0u;
  BAR();
  if (w == 5 && l == 0) put_flag(p.ws + WS_FE + id.G * 512 + fofs<8>(id.c), tag);
  D6(dcw + 114688 + 8192, SL0 + hw * 6144) WDG(10); WDG(11); WDG(12); WDG(13); WDG(14); WDG(15);
  WAITV_BAR(6);
  poll_flags<16, 8, 4>(p.ws + WS_FE + (id.g >> 1) * 512, tag, l);
  asm volatile("s_waitcnt vmcnt(0)" : "+v"(wd[0]), "+v"(wd[1]), "+v"(wd[2]), "+v"(wd[3]), "+v"(wd[4]), "+v"(wd[5]), "+v"(wd[6]), "+v"(wd[7]), "+v"(wd[8]), "+v"(wd[9]), "+v"(wd[10]),
               "+v"(wd[11]), "+v"(wd[12]), "+v"(wd[13])::"memory");
  asm volatile("" : "+v"(wd[14]), "+v"(wd[15]), "+v"(wd[16]), "+v"(wd[17]), "+v"(wd[18]), "+v"(wd[19]), "+v"(wd[20]), "+v"(wd[21]), "+v"(wd[22]), "+v"(wd[23]), "+v"(wd[24]),
               "+v"(wd[25]), "+v"(wd[26]), "+v"(wd[27])::"memory");
#undef WDG
#undef D8
#undef D6
}

__global__ __launch_bounds__(512, 2) void k_main(Params p) {
  extern __shared__ __attribute__((aligned(16))) char smem[];
  const int tid = threadIdx.x, l = tid & 63, w = rfl(tid >> 6);
  const Ids id = ids();
  const int g = id.g, j = id.j, h = l >> 5;
  const u32 vo = l * 16;
  const u32 epoch = *(const u32*)(p.ws + WS_EPOCH);
  const u32 tag = epoch * 0x9E3779B1u + 0x7F4A7C15u;
  const int tt = w & 1, nt = (w >> 1) & 1, kh = w >> 2, kq = w >> 1;
  const char* modr = p.ws + WS_MOD + (blockIdx.x & 7) * 12288;
  STAMP_DECL STAMP(0)
  u32x4 q0, q1, fo_, fd_, fg[2];
  char *pdo, *pdd, *pdg;
  {
    const int nh = (l >> 4) & 1, q = (l & 15) >> 2, pp = l & 3;
    const int uid = g * 7 + j - 1, uT = uid >> 1, ukh = uid & 1;
    const int rk = (blockIdx.x >> 6) * 7 + j - 1;
    const bool gemv = j != 0 && rk < 24;
    const int mu = 4 * ((blockIdx.x & 7) + 8 * (rk >> 2)) + (rk & 3);
    const u32 rawo = (l >> 3) * 1024 + (((l & 7) ^ (((l >> 4) & 1) << 2)) * 16);
#define WQ_DMA() { _Pragma("unroll") for (int i = 0; i < 8; ++i) { set_m0(F_A + (8 * w + i) * 1024); dma<0>(p.wq + (size_t)(64 * w + 8 * i) * DM + 64 * j, rawo); } }
#define LATE_DMA() {                                                                                                                              \
      if (j != 0) {                                                                                                                               \
        const u32 go = (l >> 1) * (FF * 2) + (l & 1) * 16;                                                                                        \
        set_m0(F_B + w * 1024); dma<0>(p.wg + (size_t)(256 * ukh + 32 * w) * FF + 16 * uT, go);                                                   \
        set_m0(F_B + 8192 + 128 + w * 1024); dma<0>(p.wu + (size_t)(256 * ukh + 32 * w) * FF + 16 * uT, go);                                      \
        set_m0(F_D + w * 1024); dma<0>(p.wd + (size_t)(64 * (uid >> 3) + 8 * w) * DM + 64 * (uid & 7), rawo);                                     \
      }                                                                                                                                           \
      if (w >= 6) { set_m0(F_D + 8192 + (w - 6) * 1024); dma<0>(p.wo + (size_t)(16 * g + 8 * (w - 6)) * DM + 64 * j, rawo); } }
    u32x4 w0, w1; u32 c0, c1;
    if (gemv) {
      const u32 mo = (tid >> 1) * 6144 + (tid & 1) * 16;
      w0 = gld_s<0, 0>(p.wm + 16 * mu, mo); w1 = gld_s<0, 0>(p.wm + 16 * mu + 256 * 3072, mo);
      c0 = gld2_s<0>(p.c, (tid >> 1) * 2); c1 = gld2_s<512>(p.c, (tid >> 1) * 2);
    }
    u32x4 xv[8];
    {
      const char* xs = (const char*)(p.x + (size_t)(64 * g + 8 * w) * DM);
      xv[0] = gld_s<0, 0>(xs, vo); xv[1] = gld_s<0, 1024>(xs, vo); xv[2] = gld_s<0, 2048>(xs, vo); xv[3] = gld_s<0, 3072>(xs, vo);
      xv[4] = gld_s<0, 0>(xs + 4096, vo); xv[5] = gld_s<0, 1024>(xs + 4096, vo); xv[6] = gld_s<0, 2048>(xs + 4096, vo); xv[7] = gld_s<0, 3072>(xs + 4096, vo);
    }
    if (j == 0) {
      set_m0(F_H + w * 2048); dma<0>(p.wv + w * 1024, vo); dma<1024>(p.wv + w * 1024, vo);
      set_m0(F_H + 16384 + 128 + w * 2048); dma<0>(p.wk + w * 1024, vo); dma<1024>(p.wk + w * 1024, vo);
      WQ_DMA() LATE_DMA()
    }
    if (gemv) {
      asm volatile("s_waitcnt vmcnt(8)" : "+v"(w0), "+v"(w1), "+v"(c0), "+v"(c1)::"memory");
      const float f0 = bflo(c0), f1 = bflo(c1);
      const float s0 = rbf(f0 * __builtin_amdgcn_rcpf(1.f + EXP2(-1.4426950408889634f * f0))), s1 = rbf(f1 * __builtin_amdgcn_rcpf(1.f + EXP2(-1.4426950408889634f * f1)));
      float a[8];
#pragma unroll
      for (int i = 0; i < 4; ++i) { a[2 * i] = s0 * bflo(w0[i]) + s1 * bflo(w1[i]); a[2 * i + 1] = s0 * bfhi(w0[i]) + s1 * bfhi(w1[i]); }
#pragma unroll
      for (int i = 0; i < 4; ++i) a[i] = halve<0x4E>(l & 2, a[i], a[i + 4]);
#pragma unroll
      for (int i = 0; i < 2; ++i) a[i] = halve<0x124>(l & 4, a[i], a[i + 2]);
      a[0] = halve<0x128>(l & 8, a[0], a[1]);
      a[0] = swap32_sum(swap16_sum(a[0]));
      if (l < 16) *(LAS float*)(uintptr_t)(F_M + w * 64 + ((l & 1) * 8 + (l & 2) * 2 + ((l >> 1) & 2) + (l >> 3)) * 4) = a[0];
      BAR();
      if (w == 7) {
        f32x2 s = {0.f, 0.f};
#pragma unroll
        for (int i = 0; i < 8; ++i) s += ldsf2(F_M + i * 64 + (l & 7) * 8);
        gst8_sc1(p.ws + WS_MOD + mu * 64, (l >> 3) * 12288 + (l & 7) * 8, u32x2{pk(s[0], s[1]), tag});
        if (rk < 8) { STAMP(10) }
      }
    }
    STAMP(22)
    if (j == 0) WAITV(12); else if (gemv && w == 7) WAITV(1); else WAITV(0);
    asm volatile("" : "+v"(xv[0]), "+v"(xv[1]), "+v"(xv[2]), "+v"(xv[3]), "+v"(xv[4]), "+v"(xv[5]), "+v"(xv[6]), "+v"(xv[7]));
    float xh[8][8];
    {
      float s1[8], s2[8];
#pragma unroll
      for (int i = 0; i < 8; ++i) {
        s1[i] = 0.f; s2[i] = 0.f;
#pragma unroll
        for (int e = 0; e < 4; ++e) { const u32 v = xv[i][e]; s1[i] = dot2(v, 0x3f803f80u, s1[i]); s2[i] = dot2(v, v, s2[i]); }
      }
#pragma unroll
      for (int i = 0; i < 4; ++i) { s1[i] = halve<0xB1>(l & 1, s1[i], s1[i + 4]); s2[i] = halve<0xB1>(l & 1, s2[i], s2[i + 4]); }
#pragma unroll
      for (int i = 0; i < 2; ++i) { s1[i] = halve<0x4E>(l & 2, s1[i], s1[i + 2]); s2[i] = halve<0x4E>(l & 2, s2[i], s2[i + 2]); }
      float t1 = halve<0x124>(l & 4, s1[0], s1[1]), t2 = halve<0x124>(l & 4, s2[0], s2[1]);
      t1 += dpp<0x128>(t1); t2 += dpp<0x128>(t2);
      t1 = swap32_sum(swap16_sum(t1)); t2 = swap32_sum(swap16_sum(t2));
      const float m = t1 * (1.f / DM), r = __builtin_amdgcn_rsqf(fmaxf(t2 * (1.f / DM) - m * m, 0.f) + EPS), nm = -m * r;
#define LNROW(I, LN) { const float ri = dpp<0x150 + (LN)>(r), ni = dpp<0x150 + (LN)>(nm); _Pragma("unroll") for (int e = 0; e < 4; ++e) { xh[I][2 * e] = fmaf(bflo(xv[I][e]), ri, ni); xh[I][2 * e + 1] = fmaf(bfhi(xv[I][e]), ri, ni); } }
      LNROW(0, 0) LNROW(1, 4) LNROW(2, 2) LNROW(3, 6) LNROW(4, 1) LNROW(5, 5) LNROW(6, 3) LNROW(7, 7)
#undef LNROW
#pragma unroll
      for (int i = 0; i < 8; ++i) asm volatile("" : "+v"(xh[i][0]), "+v"(xh[i][1]), "+v"(xh[i][2]), "+v"(xh[i][3]), "+v"(xh[i][4]), "+v"(xh[i][5]), "+v"(xh[i][6]), "+v"(xh[i][7]));
    }
    STAMP(23)
    if (w > 0 && j != 0) WQ_DMA()
    u32x4 wkvf[16];
    if (j == 0) {
      WAITV_BAR(8);
      const u32 a0 = F_H + nh * (16384 + 128) + (256 * kh + 8 * h + q) * 32 + pp * 8;
#pragma unroll
      for (int s = 0; s < 16; ++s) wkvf[s] = ldstr2(a0 + s * 512, a0 + s * 512 + 128);
    }
    STAMP(24)
    if (w == 0) {
      u32x4 a0, a1, b0, b1;
#pragma unroll 1
      for (int it = 0; it < SPIN_CAP; ++it) {
        asm volatile("global_load_dwordx4 %0, %4, %5 sc1\n\tglobal_load_dwordx4 %1, %4, %5 offset:16 sc1\n\tglobal_load_dwordx4 %2, %4, %5 offset:2048 sc1\n\t"
                     "global_load_dwordx4 %3, %4, %5 offset:2064 sc1\n\ts_waitcnt vmcnt(0)" : "=&v"(b0), "=&v"(b1), "=&v"(a0), "=&v"(a1) : "v"(l * 32), "s"(modr) : "memory");
        if (__all(a0[1] == tag && a0[3] == tag && a1[1] == tag && a1[3] == tag && b0[1] == tag && b0[3] == tag && b1[1] == tag && b1[3] == tag)) break;
      }
      sts128(F_M + 512 + vo, u32x4{pk(1.f + bflo(a0[0]), 1.f + bfhi(a0[0])), pk(1.f + bflo(a0[2]), 1.f + bfhi(a0[2])), pk(1.f + bflo(a1[0]), 1.f + bfhi(a1[0])), pk(1.f + bflo(a1[2]), 1.f + bfhi(a1[2]))});
      sts128(F_M + 1536 + vo, u32x4{b0[0], b0[2], b1[0], b1[2]});
    }
    BAR();
    STAMP(25)
    if (w == 0 && j != 0) WQ_DMA()
    {
      const u32x4 ca = lds128(F_M + 512 + vo), sa = lds128(F_M + 1536 + vo);
      float A[8], B[8];
#pragma unroll
      for (int e = 0; e < 4; ++e) { A[2 * e] = bflo(ca[e]); A[2 * e + 1] = bfhi(ca[e]); B[2 * e] = bflo(sa[e]); B[2 * e + 1] = bfhi(sa[e]); }
#pragma unroll
      for (int i = 0; i < 8; ++i) {
        u32x4 o;
#pragma unroll
        for (int e = 0; e < 4; ++e) o[e] = pk(fmaf(xh[i][2 * e], A[2 * e], B[2 * e]), fmaf(xh[i][2 * e + 1], A[2 * e + 1], B[2 * e + 1]));
        const int tk = 8 * w + i;
        sts128(F_H + ((tk >> 5) * 32 + (l >> 1)) * 1024 + (l & 1) * 512 + (((tk & 31) ^ (l & 7)) * 16), o);
      }
    }
    if (j != 0) LATE_DMA()
    BAR();
    STAMP(26)
    u32x4 xf[16];
#pragma unroll
    for (int s = 0; s < 16; ++s) xf[s] = lds128(F_H + (tt * 32 + 16 * kh + s) * 1024 + h * 512 + (((l & 31) ^ ((2 * s + h) & 7)) * 16));
    if (j == 0) {
      const u32 xs = F_B + w * 2048 + vo, xr = F_B + (w ^ 4) * 2048 + vo;
      f32x16 acc = {};
      if (nt == 0) {
#pragma unroll
        for (int s = 0; s < 16; ++s) acc = mfma(wkvf[s], xf[s], acc);
      } else {
#pragma unroll
        for (int s = 0; s < 16; ++s) acc = mfma(xf[s], wkvf[s], acc);
      }
      float keep[8];
      if (kh == 0) { stsf4(xs, f32x4{acc[8], acc[9], acc[10], acc[11]}); stsf4(xs + 1024, f32x4{acc[12], acc[13], acc[14], acc[15]});
#pragma unroll
        for (int e = 0; e < 8; ++e) keep[e] = acc[e];
      } else { stsf4(xs, f32x4{acc[0], acc[1], acc[2], acc[3]}); stsf4(xs + 1024, f32x4{acc[4], acc[5], acc[6], acc[7]});
#pragma unroll
        for (int e = 0; e < 8; ++e) keep[e] = acc[8 + e];
      }
      BAR();
      const f32x4 r0 = ldsf4(xr), r1 = ldsf4(xr + 1024);
      u32x4 o = {pk(keep[0] + r0[0], keep[1] + r0[1]), pk(keep[2] + r0[2], keep[3] + r0[3]), pk(keep[4] + r1[0], keep[5] + r1[1]), pk(keep[6] + r1[2], keep[7] + r1[3])};
      char* img = p.ws + WS_KV + (size_t)(2 * g + tt) * TS;
      if (nt == 0) { if (kh == 1) gst16_sc1(img, vo, o); }
      else if ((l & 31) <= 17) {
        if ((l & 31) == 16) o = u32x4{0x3f803f80u, 0x3f803f80u, 0x3f803f80u, 0x3f803f80u};
        if ((l & 31) == 17) o = u32x4{0u, 0u, 0u, 0u};
        gst16_sc1(img + 1024 + kh * 576, h * 288 + (l & 31) * 16, o);
      }
      WAITV_BAR(0);
      if (w == 0 && l < 8) put_flag(p.ws + WS_FKV + l * 1024 + fofs<8>(g), tag);
    }
    STAMP(21)
    if (j != 0) { if (w >= 6) WAITV(4); else WAITV(3); }
    BAR();
    const u32 rawr = (((nh * 2 + (pp >> 1)) ^ (((q >> 1) & 1) << 2)) * 16) + (pp & 1) * 8;
    f32x16 acc = {};
    {
      const u32 a0 = F_A + (256 * kh + 8 * h + q) * 128 + (rawr ^ (nt * 64));
#pragma unroll
      for (int s = 0; s < 16; ++s) acc = mfma(ldstr2(a0 + s * 2048, a0 + s * 2048 + 512), xf[s], acc);
    }
    const u32 xs = F_H + w * 4096 + vo, xr = F_H + (w ^ 4) * 4096 + vo;
    stsf4(xs, f32x4{acc[0], acc[1], acc[2], acc[3]}); stsf4(xs + 1024, f32x4{acc[4], acc[5], acc[6], acc[7]});
    stsf4(xs + 2048, f32x4{acc[8], acc[9], acc[10], acc[11]}); stsf4(xs + 3072, f32x4{acc[12], acc[13], acc[14], acc[15]});
    STAMP(27)
    if (w == 1 && j != 0) {
      {
        u32x4 a0, b0;
#pragma unroll 1
        for (int it = 0; it < SPIN_CAP; ++it) {
          asm volatile("global_load_dwordx4 %0, %2, %3 sc1\n\tglobal_load_dwordx4 %1, %2, %3 offset:2048 sc1\n\ts_waitcnt vmcnt(0)" : "=&v"(b0), "=&v"(a0) : "v"(vo), "s"(modr + 6144 + ukh * 1024) : "memory");
          if (__all(a0[1] == tag && a0[3] == tag && b0[1] == tag && b0[3] == tag)) break;
          __builtin_amdgcn_s_sleep(2);
        }
        stsf4(F_M + 2560 + vo, f32x4{rbf(1.f + bflo(a0[0])), rbf(1.f + bfhi(a0[0])), rbf(1.f + bflo(a0[2])), rbf(1.f + bfhi(a0[2]))});
        stsf4(F_M + 3584 + vo, f32x4{bflo(b0[0]), bfhi(b0[0]), bflo(b0[2]), bfhi(b0[2])});
      }
    }
    WAITV_BAR(0);
    f32x4 cs[2][4];
    if (w >= 6) { const u32 a0 = F_D + 8192 + (4 * h + q) * 128 + (rawr ^ (tt * 64)); fo_ = ldstr2(a0, a0 + 1024); }
    if (j != 0) {
      const u32 a0 = F_D + (16 * kq + 4 * h + q) * 128 + (rawr ^ (tt * 64));
      fd_ = ldstr2(a0, a0 + 1024);
#pragma unroll
      for (int i = 0; i < 2; ++i) {
        const int s = 2 * w + i;
        const u32 a1 = F_B + (pp >> 1) * (8192 + 128) + (16 * s + 4 * h + q) * 32 + nh * 16 + (pp & 1) * 8;
        fg[i] = ldstr2(a1, a1 + 256);
        cs[i][0] = ldsf4(F_M + 2560 + (16 * s + 4 * h) * 4); cs[i][1] = ldsf4(F_M + 2560 + (16 * s + 8 + 4 * h) * 4);
        cs[i][2] = ldsf4(F_M + 3584 + (16 * s + 4 * h) * 4); cs[i][3] = ldsf4(F_M + 3584 + (16 * s + 8 + 4 * h) * 4);
      }
    }
    const f32x4 r0 = ldsf4(xr), r1 = ldsf4(xr + 1024), r2 = ldsf4(xr + 2048), r3 = ldsf4(xr + 3072);
    q0 = u32x4{pk((acc[0] + r0[0]) * C2, (acc[1] + r0[1]) * C2), pk((acc[2] + r0[2]) * C2, (acc[3] + r0[3]) * C2), pk((acc[4] + r1[0]) * C2, (acc[5] + r1[1]) * C2), pk((acc[6] + r1[2]) * C2, (acc[7] + r1[3]) * C2)};
    q1 = u32x4{pk((acc[8] + r2[0]) * C2, (acc[9] + r2[1]) * C2), pk((acc[10] + r2[2]) * C2, (acc[11] + r2[3]) * C2), pk((acc[12] + r3[0]) * C2, (acc[13] + r3[1]) * C2), pk((acc[14] + r3[2]) * C2, (acc[15] + r3[3]) * C2)};
    PIN(q0); PIN(q1);
    if (w == 2) poll_flags<32, 8>(p.ws + WS_FKV + (blockIdx.x & 7) * 1024, tag, l);
    if (w == 3) {
#pragma unroll 1
      for (int it = 0; it < SPIN_CAP; ++it) {
        const u32 v = __hip_atomic_load((const GAS u32*)(modr + 4096 + j * 256 + h * 6144 + (l & 31) * 8 + 4), RLX_AGENT);
        if (__all(v == tag)) break;
        __builtin_amdgcn_s_sleep(2);
      }
    }
    BAR();
    STAMP(28)
    STAMP(29)
    { const u32 o = (w & 1) * 69632 + (w >> 1) * 6144;
      dma4s(p.ws + WS_KV + o, vo, o); set_m0(o + 4096); dma1<0>(p.ws + WS_KV + o + 4096, vo); dma1<1024>(p.ws + WS_KV + o + 4096, vo); }
    pdo = p.ws + WS_WO + ((size_t)(2 * j + tt) * 32 + g) * 1024;
    pdd = p.ws + WS_WD + ((size_t)(2 * (uid & 7) + tt) * 112 + 4 * (uid >> 3) + kq) * 1024;
    pdg = p.ws + WS_WGU + ((size_t)uT * 32 + 16 * ukh + 2 * w) * 1024;
    if (j != 0) {
      float us = 0.f, wsum = 0.f;
#pragma unroll
      for (int i = 0; i < 2; ++i) {
        const u32x4 f = fg[i];
        const f32x4 c0 = cs[i][0], c1 = cs[i][1], s0 = cs[i][2], s1 = cs[i][3];
        u32x4 o;
        o[0] = pk(bflo(f[0]) * c0[0], bfhi(f[0]) * c0[1]); o[1] = pk(bflo(f[1]) * c0[2], bfhi(f[1]) * c0[3]);
        o[2] = pk(bflo(f[2]) * c1[0], bfhi(f[2]) * c1[1]); o[3] = pk(bflo(f[3]) * c1[2], bfhi(f[3]) * c1[3]);
#pragma unroll
        for (int e = 0; e < 4; ++e) { const u32 v = o[e]; us = dot2(v, 0x3f803f80u, us); }
        wsum += bflo(f[0]) * s0[0] + bfhi(f[0]) * s0[1] + bflo(f[1]) * s0[2] + bfhi(f[1]) * s0[3] + bflo(f[2]) * s1[0] + bfhi(f[2]) * s1[1] + bflo(f[3]) * s1[2] + bfhi(f[3]) * s1[3];
        fg[i] = o;
      }
      us = swap32_sum(us); wsum = swap32_sum(wsum);
      if (l < 32) *(LAS f32x2*)(uintptr_t)(OEX + w * 256 + l * 8) = f32x2{us, wsum};
      WAITV_BAR(0);
      if (w == 7) {
        f32x2 s = {0.f, 0.f};
#pragma unroll
        for (int i = 0; i < 8; ++i) s += ldsf2(OEX + i * 256 + (l & 31) * 8);
        const float uh = rbf(s[0]), wh = rbf(s[1]);
        if (l < 32) gst16_sc1(p.ws + WS_AUG + (size_t)uT * 1024 + ukh * 512, vo, u32x4{pk(uh, s[0] - uh), pk(uh, wh), pk(s[1] - wh, wh), 0u});
      }
    } else {
      WAITV_BAR(0);
    }
  }
  const size_t fo = ((size_t)(2 * g + tt) * 32 + 4 * j + 2 * nt) * 1024;
  STAMP(1)
  f32x16 nm0, nm1;
  {
    const u32x4 k0 = lds128(vo);
    const float m0 = rowmax16(mfma(k0, q0, f32x16{})), m1 = rowmax16(mfma(k0, q1, f32x16{}));
#pragma unroll
    for (int r = 0; r < 16; ++r) { nm0[r] = -m0; nm1[r] = -m1; }
    PIN(nm0); PIN(nm1);
  }
  const u32 vrow = 1024 + h * 288 + min(l & 31, 17) * 16;
  u32 ka = kh * 32 * TS + vo, va = kh * 32 * TS + vrow;
  f32x16 o0 = {}, o1 = {};
  u32x4 kA = lds128(ka), kB = lds128(ka + TS);
  f32x16 s0 = mfma(kA, q0, nm0), s1;
  u32x4 vA0 = lds128(va), vA1 = lds128(va + 576), vB0 = {}, vB1 = {};
  u32x4 pa0, pa1, pb0 = {}, pb1 = {};
  PIN(s0);
  if (kh) {
    const u32 o = nt * 69632 + 24576 + tt * 22528;
#pragma unroll
    for (int i = 0; i < 5; ++i) dma4s(p.ws + WS_KV + o + i * 4096, vo, o + i * 4096);
    set_m0(o + 20480); dma1<0>(p.ws + WS_KV + o + 20480, vo); dma1<1024>(p.ws + WS_KV + o + 20480, vo);
  }
#define PREP_ST() { if (j != 0) { gst16_sc1(pdd, vo, fd_); gst16_sc1(pdg, vo, fg[0]); gst16_sc1(pdg + 1024, vo, fg[1]); } }
  if (kh) { PREP_ST() if (w >= 6) gst16_sc1(pdo, vo, fo_); }
  u32x4 wf[16];
#define HALF(SC, N0, N1, MF, OP, VP0, P0, VP1, P1, LD1, LD2)                                              \
  {                                                                                                       \
    MF; LD1; SB();                                                                                        \
    float e0 = EXP2(SC[0]), e1 = EXP2(SC[1]), e2 = EXP2(SC[2]), e3 = EXP2(SC[3]), e4 = EXP2(SC[4]), e5 = EXP2(SC[5]), e6 = EXP2(SC[6]), e7 = EXP2(SC[7]); SB(); \
    OP = mfma(VP0, P0, OP); SB();                                                                         \
    float e8 = EXP2(SC[8]), e9 = EXP2(SC[9]), e10 = EXP2(SC[10]), e11 = EXP2(SC[11]), e12 = EXP2(SC[12]), e13 = EXP2(SC[13]), e14 = EXP2(SC[14]), e15 = EXP2(SC[15]); SB(); \
    OP = mfma(VP1, P1, OP); LD2; SB();                                                                    \
    N0[0] = pk(e0, e1); N0[1] = pk(e2, e3); N0[2] = pk(e4, e5); N0[3] = pk(e6, e7); N1[0] = pk(e8, e9);   \
    N1[1] = pk(e10, e11); N1[2] = pk(e12, e13); N1[3] = pk(e14, e15); SB();                               \
  }
#define TILE(T, KC, KN, VC0, VC1, VP0, VP1)                                                               \
    HALF(s0, pa0, pa1, s1 = mfma(KC, q1, nm1), o1, VP0, pb0, VP1, pb1, KC = lds128(ka + ((T) + 2) * TS),  \
         (VP0 = lds128(va + ((T) + 1) * TS), VP1 = lds128(va + ((T) + 1) * TS + 576)))                    \
    HALF(s1, pb0, pb1, s0 = mfma(KN, q0, nm0), o0, VC0, pa0, VC1, pa1, , )
  const char* wgu = p.ws + WS_WGU + (size_t)id.c * 7 * 32768;
  const char* wob = p.ws + WS_WO + ((size_t)(2 * j) * 32 + 8 * kq) * 1024;
#define LDWF(SEL)                                                                                                     \
  asm volatile("s_cmp_lg_u32 %21, 0\n\ts_cbranch_scc1 .Lwf%=\n\t"                                                     \
               "global_load_dwordx4 %0, %16, %17 sc1\n\tglobal_load_dwordx4 %1, %16, %17 offset:1024 sc1\n\tglobal_load_dwordx4 %2, %16, %17 offset:2048 sc1\n\tglobal_load_dwordx4 %3, %16, %17 offset:3072 sc1\n\t"     \
               "global_load_dwordx4 %4, %16, %18 sc1\n\tglobal_load_dwordx4 %5, %16, %18 offset:1024 sc1\n\tglobal_load_dwordx4 %6, %16, %18 offset:2048 sc1\n\tglobal_load_dwordx4 %7, %16, %18 offset:3072 sc1\n\t"     \
               "global_load_dwordx4 %8, %16, %19 sc1\n\tglobal_load_dwordx4 %9, %16, %19 offset:1024 sc1\n\tglobal_load_dwordx4 %10, %16, %19 offset:2048 sc1\n\tglobal_load_dwordx4 %11, %16, %19 offset:3072 sc1\n\t"   \
               "global_load_dwordx4 %12, %16, %20 sc1\n\tglobal_load_dwordx4 %13, %16, %20 offset:1024 sc1\n\tglobal_load_dwordx4 %14, %16, %20 offset:2048 sc1\n\tglobal_load_dwordx4 %15, %16, %20 offset:3072 sc1\n"   \
               ".Lwf%=:"                                                                                              \
               : "+v"(wf[0]), "+v"(wf[1]), "+v"(wf[2]), "+v"(wf[3]), "+v"(wf[4]), "+v"(wf[5]), "+v"(wf[6]), "+v"(wf[7]), "+v"(wf[8]), "+v"(wf[9]), "+v"(wf[10]), \
                 "+v"(wf[11]), "+v"(wf[12]), "+v"(wf[13]), "+v"(wf[14]), "+v"(wf[15])                                  \
               : "v"(vo), "s"(wob), "s"(wob + 4096), "s"(wob + 32768), "s"(wob + 36864), "s"(SEL) : "memory", "scc")
#pragma unroll
  for (int s4 = 0; s4 < 16; ++s4) asm volatile("" : "=v"(wf[s4]));
  const int qt = blockIdx.x >> 6;
#pragma unroll 1
  for (int seg = 0; seg < 4; ++seg) {
    if (seg == 1) {
      if (!kh) PREP_ST()
      ASTAMP(10)
      WAITV_BAR(0);
      ASTAMP(24)
      if (w == 0 && l < 8) put_flag(p.ws + WS_FP + l * 1024 + blockIdx.x * 4, tag);
    } else if (seg == 2) {
      ASTAMP(21)
      if (w == 0) {
        u32x4 f;
#pragma unroll 1
        for (int it = 0; it < SPIN_CAP; ++it) {
          asm volatile("global_load_dwordx4 %0, %1, %2 sc1\n\ts_waitcnt vmcnt(0)" : "=&v"(f) : "v"(vo), "s"(p.ws + WS_FP + (blockIdx.x & 7) * 1024) : "memory");
          if (__all(f[0] == tag && f[1] == tag && f[2] == tag && f[3] == tag)) break;
          __builtin_amdgcn_s_sleep(1);
        }
        if (blockIdx.x == 0 && l == 0) put_flag(p.ws + WS_EPOCH, epoch + 1u);
      }
      BAR();
      ASTAMP(23)
    } else if (seg == 3) {
      ASTAMP(22)
      if (!kh) {
        dma4s(wgu + w * 8192, vo, SL0 + w * 8192); dma4s(wgu + w * 8192 + 4096, vo, SL0 + w * 8192 + 4096);
        dma4s(wgu + 32768 + w * 8192, vo, SL1 + w * 8192); dma4s(wgu + 32768 + w * 8192 + 4096, vo, SL1 + w * 8192 + 4096);
        set_m0(JUNK);
        touch8k(wgu + 65536 + qt * 40960 + w * 8192, l * 128);
        if (w < 2) touch8k(w == 0 ? wgu + 65536 + qt * 40960 + 32768 : p.ws + WS_AUG + (size_t)id.c * 7168, l * 128);
      }
    }
    LDWF((seg ^ 2) | (kh ^ 1));
    TILE(0, kA, kB, vA0, vA1, vB0, vB1) TILE(1, kB, kA, vB0, vB1, vA0, vA1) TILE(2, kA, kB, vA0, vA1, vB0, vB1) TILE(3, kB, kA, vB0, vB1, vA0, vA1)
    TILE(4, kA, kB, vA0, vA1, vB0, vB1) TILE(5, kB, kA, vB0, vB1, vA0, vA1) TILE(6, kA, kB, vA0, vA1, vB0, vB1) TILE(7, kB, kA, vB0, vB1, vA0, vA1)
    ka += 8 * TS; va += 8 * TS; PIN(ka); PIN(va);
  }
  o1 = mfma(vB0, pb0, o1); o1 = mfma(vB1, pb1, o1);
  LDWF(kh);
  if (!kh) {
    set_m0(JUNK);
    touch8k(p.ws + WS_WD + (size_t)(2 * j) * 114688 + qt * 57344 + w * 8192, l * 128);
    if (w < 3) touch8k(p.ws + WS_WD + (size_t)(2 * j) * 114688 + qt * 57344 + (w + 4) * 8192, l * 128);
  }
#undef HALF
#undef TILE
  STAMP(2)
  const u32 xo = (u32)((l & 31) * DM + 4 * h) * 2;
  const char* xres = (const char*)(p.x + (size_t)(64 * g + 32 * tt) * DM + 64 * j + 32 * nt + 16 * kh);
  u32x2 xa = gld8_s<0>(xres, xo), xb = gld8_s<16>(xres, xo);
  u32x4 ga = gld_s<1, 0>(modr + 4096 + (64 * j + 32 * nt + 16 * kh) * 4, h * 16), gb = gld_s<1, 32>(modr + 4096 + (64 * j + 32 * nt + 16 * kh) * 4, h * 16);
  {
    const u32 xs = OEX + w * 2304, xr = OEX + (w ^ 4) * 2304;
    float o[9];
    if (kh) { stsf4(xs + vo, f32x4{o0[0], o0[1], o0[2], o0[3]}); stsf4(xs + 1024 + vo, f32x4{o0[4], o0[5], o0[6], o0[7]}); *(LAS float*)(uintptr_t)(xs + 2048 + l * 4) = o0[8];
#pragma unroll
      for (int e = 0; e < 9; ++e) o[e] = o1[e];
    } else { stsf4(xs + vo, f32x4{o1[0], o1[1], o1[2], o1[3]}); stsf4(xs + 1024 + vo, f32x4{o1[4], o1[5], o1[6], o1[7]}); *(LAS float*)(uintptr_t)(xs + 2048 + l * 4) = o1[8];
#pragma unroll
      for (int e = 0; e < 9; ++e) o[e] = o0[e];
    }
    BAR();
    const f32x4 r0 = ldsf4(xr + vo), r1 = ldsf4(xr + 1024 + vo);
#pragma unroll
    for (int e = 0; e < 4; ++e) { o[e] += r0[e]; o[4 + e] += r1[e]; }
    float ls = lowhalf(o[8] + *(const LAS float*)(uintptr_t)(xr + 2048 + l * 4));
    bool bad = !(ls < 1.2676506e30f);
#pragma unroll
    for (int e = 0; e < 8; ++e) bad |= !(fabsf(o[e]) < 3.0e38f);
    if (__builtin_expect(__any(bad), 0)) {
      const u32x4 qf = kh ? q1 : q0;
      float m = -INFINITY;
      f32x16 oo = {};
#pragma unroll 1
      for (int t = 0; t < 64; ++t) {
        u32x4 kf, v0, v1;
        asm volatile("global_load_dwordx4 %0, %3, %5 sc1\n\tglobal_load_dwordx4 %1, %4, %5 sc1\n\tglobal_load_dwordx4 %2, %4, %5 offset:576 sc1\n\ts_waitcnt vmcnt(0)"
                     : "=&v"(kf), "=&v"(v0), "=&v"(v1) : "v"(vo), "v"(vrow), "s"(p.ws + WS_KV + t * TS) : "memory");
        const f32x16 s = mfma(kf, qf, f32x16{});
        const float mn = fmaxf(m, rowmax16(s)), al = EXP2(m - mn);
        m = mn;
        u32x4 pa, pb;
#pragma unroll
        for (int r = 0; r < 4; ++r) { pa[r] = pk(EXP2(s[2 * r] - mn), EXP2(s[2 * r + 1] - mn)); pb[r] = pk(EXP2(s[8 + 2 * r] - mn), EXP2(s[9 + 2 * r] - mn)); }
#pragma unroll
        for (int r = 0; r < 16; ++r) oo[r] *= al;
        oo = mfma(v0, pa, oo); oo = mfma(v1, pb, oo);
      }
#pragma unroll
      for (int e = 0; e < 8; ++e) o[e] = oo[e];
      ls = lowhalf(oo[8]);
    }
    const float rl = __builtin_amdgcn_rcpf(ls);
    gst16_sc1(p.ws + WS_O + fo + kh * 1024, vo, u32x4{pk(o[0] * rl, o[1] * rl), pk(o[2] * rl, o[3] * rl), pk(o[4] * rl, o[5] * rl), pk(o[6] * rl, o[7] * rl)});
  }
  asm volatile("s_waitcnt vmcnt(0)\n\ts_barrier"
               : "+v"(wf[0]), "+v"(wf[1]), "+v"(wf[2]), "+v"(wf[3]), "+v"(wf[4]), "+v"(wf[5]), "+v"(wf[6]), "+v"(wf[7]), "+v"(wf[8]), "+v"(wf[9]),
                 "+v"(wf[10]), "+v"(wf[11]), "+v"(wf[12]), "+v"(wf[13]), "+v"(wf[14]), "+v"(wf[15]), "+v"(xa), "+v"(xb), "+v"(ga), "+v"(gb)::"memory");
  STAMP(3)
  if (w == 1 && l == 0) put_flag(p.ws + WS_FO + g * 512 + fofs<4>(j), tag);
  if (w == 0) poll_flags<8, 4>(p.ws + WS_FO + g * 512, tag, l);
  BAR();
  STAMP(4)
  u32x4 x1;
  {
    const char* osrc = p.ws + WS_O + ((size_t)(2 * g + tt) * 32 + 8 * kq) * 1024;
    u32x4 xf[8];
    xf[0] = gld_s<1, 0>(osrc, vo); xf[1] = gld_s<1, 1024>(osrc, vo); xf[2] = gld_s<1, 2048>(osrc, vo); xf[3] = gld_s<1, 3072>(osrc, vo);
    xf[4] = gld_s<1, 0>(osrc + 4096, vo); xf[5] = gld_s<1, 1024>(osrc + 4096, vo); xf[6] = gld_s<1, 2048>(osrc + 4096, vo); xf[7] = gld_s<1, 3072>(osrc + 4096, vo);
    f32x16 acc0 = {}, acc1 = {};
#define P3_STEP(S) { waitv_pin<7 - (S)>(xf[S]); acc0 = mfma(wf[S], xf[S], acc0); acc1 = mfma(wf[8 + (S)], xf[S], acc1); }
    P3_STEP(0) P3_STEP(1) P3_STEP(2) P3_STEP(3) P3_STEP(4) P3_STEP(5) P3_STEP(6) P3_STEP(7)
#undef P3_STEP
    STAMP(5)
    float r[8];
    {
      const u32 d = SL3 + tt * 4096 + kq * 1024 + vo;
      sts128(d, pack8(acc0, 0)); sts128(d + 8192, pack8(acc1, 0)); sts128(d + 16384, pack8(acc0, 8)); sts128(d + 24576, pack8(acc1, 8));
      BAR();
      sum_pieces<4>(SL3 + w * 4096 + vo, r);
    }
    STAMP(6)
    x1 = u32x4{pk(fmaf(bflo(ga[0]), r[0], bflo(xa[0])), fmaf(bfhi(ga[0]), r[1], bfhi(xa[0]))), pk(fmaf(bflo(ga[2]), r[2], bflo(xa[1])), fmaf(bfhi(ga[2]), r[3], bfhi(xa[1]))),
               pk(fmaf(bflo(gb[0]), r[4], bflo(xb[0])), fmaf(bfhi(gb[0]), r[5], bfhi(xb[0]))), pk(fmaf(bflo(gb[2]), r[6], bflo(xb[1])), fmaf(bfhi(gb[2]), r[7], bfhi(xb[1])))};
    gst16_sc1(p.ws + WS_X1 + fo + kh * 1024, vo, x1);
    dma4s(wgu + 2 * 32768 + w * 4096, vo, SL2 + w * 4096);
    float s1 = 0.f, s2 = 0.f;
#pragma unroll
    for (int e = 0; e < 4; ++e) { const u32 v = x1[e]; s1 = dot2(v, 0x3f803f80u, s1); s2 = dot2(v, v, s2); }
    s1 = swap32_sum(s1); s2 = swap32_sum(s2);
    if (l < 32) *(LAS f32x2*)(uintptr_t)(GAPA + kq * 512 + tt * 256 + l * 8) = f32x2{s1, s2};
    WAITV_BAR(4);
    STAMP(8)
    if (w == 0 && l == 0) put_flag(p.ws + WS_FX + (g >> 1) * 512 + fofs<8>((g & 1) * 8 + j), tag);
    if (w == 7) {
      f32x2 s = {0.f, 0.f};
#pragma unroll
      for (int i = 0; i < 4; ++i) s += ldsf2(GAPA + i * 512 + l * 8);
      gst8_sc1(p.ws + WS_STAT + ((size_t)64 * g * 8 + j) * 8, l * 64, u32x2{__float_as_uint(s[0]), __float_as_uint(s[1])});
      WAITV(0);
      if (l == 0) put_flag(p.ws + WS_FS + (g >> 1) * 512 + fofs<8>((g & 1) * 8 + j), tag);
    }
  }
  u32x4 wd[28];
  if (kh == 0) p4_compute(p, id, l, w, tag, wd STAMP_PASS); else p4_helper(p, id, l, w, tag, wd STAMP_PASS);
  {
    int ly = l; PIN(ly);
    const char* xsrc = p.ws + WS_ACT + ((size_t)(2 * g) * 112 + 14 * w) * 1024;
    u32x4 xf[16];
#define XL(N) (xf[(N) % 16] = gld_s<1, 0>(xsrc + dks((N) >> 1) * 1024 + ((N) & 1) * 114688, vo))
    if (!kh) {
      WAITV(0);
      PSTAMP(19)
      if (ly == 0 && __hip_atomic_fetch_add((LAS u32*)(uintptr_t)GAPA, 1u, __ATOMIC_RELAXED, __HIP_MEMORY_SCOPE_WORKGROUP) == 3u)
        put_flag(p.ws + WS_FA + id.G * 512 + fofs<8>(id.c), tag);
      BAR();
    }
    STAMP(18)
    XL(0); XL(1); XL(2); XL(3); XL(4); XL(5); XL(6); XL(7); XL(8); XL(9); XL(10); XL(11);
    u32x4 g0 = gld_s<1, 0>(modr + 10240 + (64 * j + 32 * nt + 16 * kh) * 4, (ly >> 5) * 16), g1 = gld_s<1, 32>(modr + 10240 + (64 * j + 32 * nt + 16 * kh) * 4, (ly >> 5) * 16);
    if (kh) asm volatile("s_barrier" ::: "memory");
    f32x16 acc[4];
#define K5_X(N, CNT)                                                                                 \
  {                                                                                                  \
    waitv_pin<CNT>(xf[(N) % 16]);                                                                    \
    if ((N) < 2) { mf0(acc[(N) & 1], wd[0], xf[(N) & 1]); mf0(acc[2 + ((N) & 1)], wd[14], xf[(N) & 1]); } \
    else { mfa(acc[(N) & 1], wd[dks((N) >> 1)], xf[(N) % 16]); mfa(acc[2 + ((N) & 1)], wd[14 + dks((N) >> 1)], xf[(N) % 16]); } \
  }
    K5_X(0, 13) K5_X(1, 12) K5_X(2, 11) K5_X(3, 10) K5_X(4, 9) K5_X(5, 8) K5_X(6, 7) K5_X(7, 6) K5_X(8, 5) K5_X(9, 4) K5_X(10, 3) K5_X(11, 2)
    PSTAMP(24)
    if (w == 4) poll_flags<16, 8>(p.ws + WS_FA + (g >> 1) * 512, tag, ly);
    asm volatile("s_barrier" ::: "memory");
    PSTAMP(25)
    XL(12); XL(13); XL(14); XL(15); XL(16); XL(17); XL(18); XL(19); XL(20); XL(21); XL(22); XL(23); XL(24); XL(25); XL(26); XL(27);
    K5_X(12, 15) K5_X(13, 14) K5_X(14, 13) K5_X(15, 12) K5_X(16, 11) K5_X(17, 10) K5_X(18, 9) K5_X(19, 8) K5_X(20, 7) K5_X(21, 6) K5_X(22, 5) K5_X(23, 4)
    K5_X(24, 3) K5_X(25, 2) K5_X(26, 1) K5_X(27, 0)
#undef K5_X
    MFMA_SETTLE();
    PIN(g0); PIN(g1); PIN(acc[0]); PIN(acc[1]); PIN(acc[2]); PIN(acc[3]);
    STAMP(20)
    float r[8];
    {
      const u32 d = w * 1024 + vo;
#pragma unroll
      for (int pc = 0; pc < 8; ++pc) sts128(d + pc * 8192, pack8(acc[pc & 3], (pc >> 2) * 8));
      PSTAMP(26)
      BAR();
      PSTAMP(27)
      sum_pieces<8>(w * 8192 + vo, r);
    }
    const u32 a0 = pk(fmaf(bflo(g0[0]), r[0], bflo(x1[0])), fmaf(bfhi(g0[0]), r[1], bfhi(x1[0]))), a1 = pk(fmaf(bflo(g0[2]), r[2], bflo(x1[1])), fmaf(bfhi(g0[2]), r[3], bfhi(x1[1])));
    const u32 b0 = pk(fmaf(bflo(g1[0]), r[4], bflo(x1[2])), fmaf(bfhi(g1[0]), r[5], bfhi(x1[2]))), b1 = pk(fmaf(bflo(g1[2]), r[6], bflo(x1[3])), fmaf(bfhi(g1[2]), r[7], bfhi(x1[3])));
    const auto s0 = __builtin_amdgcn_permlane32_swap(a0, b0, false, false);
    const auto s1 = __builtin_amdgcn_permlane32_swap(a1, b1, false, false);
    int lz = l; PIN(lz);
    *(u32x4*)(p.out + (size_t)(64 * g + 32 * tt + (lz & 31)) * DM + 64 * j + 32 * nt + 16 * kh + 8 * (lz >> 5)) = u32x4{s0[0], s1[0], s0[1], s1[1]};
  }
  STAMP_FLUSH(0)
}

extern "C" void kernel_launch(void* const* d_in, const int* in_sizes, int n_in, void* d_out, int out_size, void* d_ws, size_t ws_size, hipStream_t stream) {
  Params p;
  p.x = (const u16*)d_in[0]; p.wq = (const u16*)d_in[1]; p.wk = (const u16*)d_in[2]; p.wv = (const u16*)d_in[3]; p.wo = (const u16*)d_in[4];
  p.wg = (const u16*)d_in[6]; p.wu = (const u16*)d_in[7]; p.wd = (const u16*)d_in[8]; p.c = (const u16*)d_in[9]; p.wm = (const u16*)d_in[10];
  p.out = (u16*)d_out; p.ws = (char*)d_ws;
  (void)hipFuncSetAttribute((const void*)k_main, hipFuncAttributeMaxDynamicSharedMemorySize, M_LDS);
  k_main<<<256, 512, M_LDS, stream>>>(p);
}
```

```cpp
#include <hip/hip_runtime.h>
#include <stdint.h>

typedef unsigned short u16;
typedef unsigned int u32;
typedef __attribute__((ext_vector_type(8))) __bf16 bf16x8;
typedef __attribute__((ext_vector_type(2))) __bf16 bf16x2;
typedef __attribute__((ext_vector_type(2))) float f32x2;
typedef __attribute__((ext_vector_type(4))) float f32x4;
typedef __attribute__((ext_vector_type(16))) float f32x16;
typedef __attribute__((ext_vector_type(4))) u32 u32x4;
typedef __attribute__((ext_vector_type(2))) u32 u32x2;
typedef __attribute__((ext_vector_type(4))) short s16x4;
#define LAS __attribute__((address_space(3)))
#define GAS __attribute__((address_space(1)))
#define DI __device__ __forceinline__

constexpr int DM = 512, FF = 1792;
constexpr float EPS = 1e-5f;
constexpr float C2 = 0.25f * 1.4426950408889634f;

constexpr size_t WS_EPOCH = 0;
constexpr size_t WS_FO = 4096;
constexpr size_t WS_FX = WS_FO + 16384;
constexpr size_t WS_FS = WS_FX + 8192;
constexpr size_t WS_FE = WS_FS + 8192;
constexpr size_t WS_FA = WS_FE + 8192;
constexpr size_t WS_FKV = WS_FA + 8192;
constexpr size_t WS_FP = WS_FKV + 8192;
constexpr size_t WS_MOD = 81920;
constexpr size_t WS_KV = WS_MOD + 8 * 12288;
constexpr size_t WS_STAT = WS_KV + 139264;
constexpr size_t WS_AUG = WS_STAT + 131072;
constexpr size_t WS_WO = 1u << 20;
constexpr size_t WS_WD = WS_WO + (512u << 10);
constexpr size_t WS_WGU = WS_WD + (1792u << 10);
constexpr size_t WS_O = WS_WGU + (3584u << 10);
constexpr size_t WS_X1 = WS_O + (2u << 20);
constexpr size_t WS_ACT = WS_X1 + (2u << 20);
static_assert(WS_AUG + 229376 <= WS_WO, "workspace map");
constexpr int TS = 2176;

struct Params {
  const u16 *x, *wq, *wk, *wv, *wo, *wg, *wu, *wd, *c, *wm;
  u16* out;
  char* ws;
};

DI f32x16 mfma(u32x4 a, u32x4 b, f32x16 c) {
  return __builtin_amdgcn_mfma_f32_32x32x16_bf16(__builtin_bit_cast(bf16x8, a), __builtin_bit_cast(bf16x8, b), c, 0, 0, 0);
}
DI u32 pk(float lo, float hi) {
  f32x2 v = {lo, hi};
  return __builtin_bit_cast(u32, __builtin_convertvector(v, bf16x2));
}
DI float bflo(u32 u) { return __uint_as_float(u << 16); }
DI float bfhi(u32 u) { return __uint_as_float(u & 0xffff0000u); }
DI float rbf(float f) { return bflo(pk(f, 0.f)); }
DI float dot2(u32 a, u32 b, float c) { return __builtin_amdgcn_fdot2_f32_bf16(__builtin_bit_cast(bf16x2, a), __builtin_bit_cast(bf16x2, b), c, false); }
DI void set_m0(u32 lds) { asm volatile("s_mov_b32 m0, %0\n\ts_nop 0" ::"s"(lds) : "memory"); }
template <int OFF> DI void dma(const void* base, u32 vo) { asm volatile("global_load_lds_dwordx4 %0, %1 offset:%2" ::"v"(vo), "s"(base), "n"(OFF) : "memory"); }
template <int OFF> DI void dma1(const void* base, u32 vo) { asm volatile("global_load_lds_dwordx4 %0, %1 offset:%2 sc1" ::"v"(vo), "s"(base), "n"(OFF) : "memory"); }
DI void dma4(const void* base, u32 vo, u32 lds) { set_m0(lds); dma<0>(base, vo); dma<1024>(base, vo); dma<2048>(base, vo); dma<3072>(base, vo); }
DI void dma4s(const void* base, u32 vo, u32 lds) { set_m0(lds); dma1<0>(base, vo); dma1<1024>(base, vo); dma1<2048>(base, vo); dma1<3072>(base, vo); }
template <int SC, int OFF> DI u32x4 gld_s(const void* base, u32 vo) {
  u32x4 r;
  if (SC) asm volatile("global_load_dwordx4 %0, %1, %2 offset:%3 sc1" : "=v"(r) : "v"(vo), "s"(base), "n"(OFF) : "memory");
  else asm volatile("global_load_dwordx4 %0, %1, %2 offset:%3" : "=v"(r) : "v"(vo), "s"(base), "n"(OFF) : "memory");
  return r;
}
template <int OFF> DI u32x2 gld8_s(const void* base, u32 vo) { u32x2 r; asm volatile("global_load_dwordx2 %0, %1, %2 offset:%3" : "=v"(r) : "v"(vo), "s"(base), "n"(OFF) : "memory"); return r; }
template <int OFF> DI u32 gld2_s(const void* base, u32 vo) { u32 r; asm volatile("global_load_ushort %0, %1, %2 offset:%3" : "=v"(r) : "v"(vo), "s"(base), "n"(OFF) : "memory"); return r; }
DI void touch8k(const char* base, u32 l128) { asm volatile("global_load_lds_dword %0, %1 sc1" ::"v"(l128), "s"(base) : "memory"); }
DI void gst16_sc1(char* base, u32 vo, u32x4 v) { asm volatile("global_store_dwordx4 %0, %1, %2 sc1\n\ts_nop 1" ::"v"(vo), "v"(v), "s"(base) : "memory"); }
DI void gst8_sc1(char* base, u32 vo, u32x2 v) { asm volatile("global_store_dwordx2 %0, %1, %2 sc1\n\ts_nop 1" ::"v"(vo), "v"(v), "s"(base) : "memory"); }
#define RLX_AGENT __ATOMIC_RELAXED, __HIP_MEMORY_SCOPE_AGENT
DI void put_flag(char* f, u32 tag) { __hip_atomic_store((GAS u32*)f, tag, RLX_AGENT); }
#define SLP 16
constexpr int SPIN_CAP = 1 << 20;
template <int PER> DI u32 fofs(int i) { return (u32)((i / PER) * 256 + (i % PER) * 4); }
template <int N, int PER, int NAP = 0> DI void poll_flags(const char* f, u32 tag, int l) {
  const char* a = f + fofs<PER>(l < N ? l : 0);
#pragma unroll 1
  for (int it = 0; it < SPIN_CAP; ++it) {
    const u32 v = __hip_atomic_load((const GAS u32*)a, RLX_AGENT);
    if (__all(v == tag)) break;
    if (NAP) __builtin_amdgcn_s_sleep(NAP);
  }
  __builtin_amdgcn_fence(__ATOMIC_ACQUIRE, "wavefront");
}
#define SB() __builtin_amdgcn_sched_barrier(0)
#define PIN(x) asm volatile("" : "+v"(x))
#define WAITV(N) asm volatile("s_waitcnt vmcnt(" #N ")" ::: "memory")
#define WAITV_BAR(N) asm volatile("s_waitcnt vmcnt(" #N ") lgkmcnt(0)\n\ts_barrier" ::: "memory")
#define BAR() asm volatile("s_waitcnt lgkmcnt(0)\n\ts_barrier" ::: "memory")
template <int N> DI void waitv_pin(u32x4& v) { asm volatile("s_waitcnt vmcnt(%1)" : "+v"(v) : "n"(N) : "memory"); }
DI u32x4 lds128(u32 a) { return *(const LAS u32x4*)(uintptr_t)a; }
DI f32x4 ldsf4(u32 a) { return *(const LAS f32x4*)(uintptr_t)a; }
DI f32x2 ldsf2(u32 a) { return *(const LAS f32x2*)(uintptr_t)a; }
DI void sts128(u32 a, u32x4 v) { *(LAS u32x4*)(uintptr_t)a = v; }
DI void stsf4(u32 a, f32x4 v) { *(LAS f32x4*)(uintptr_t)a = v; }
DI u32x2 ldstr(u32 a) { return __builtin_bit_cast(u32x2, __builtin_amdgcn_ds_read_tr16_b64_v4i16((LAS s16x4*)(uintptr_t)a)); }
DI u32x4 ldstr2(u32 a0, u32 a1) { u32x2 p = ldstr(a0), q = ldstr(a1); return u32x4{p[0], p[1], q[0], q[1]}; }
template <int CTRL> DI float dpp(float v) { return __int_as_float(__builtin_amdgcn_mov_dpp(__float_as_int(v), CTRL, 0xf, 0xf, true)); }
DI float swap32_sum(float v) { auto r = __builtin_amdgcn_permlane32_swap(__float_as_uint(v), __float_as_uint(v), false, false); return __uint_as_float(r[0]) + __uint_as_float(r[1]); }
DI float swap32_max(float v) { auto r = __builtin_amdgcn_permlane32_swap(__float_as_uint(v), __float_as_uint(v), false, false); return fmaxf(__uint_as_float(r[0]), __uint_as_float(r[1])); }
DI float swap16_sum(float v) { auto r = __builtin_amdgcn_permlane16_swap(__float_as_uint(v), __float_as_uint(v), false, false); return __uint_as_float(r[0]) + __uint_as_float(r[1]); }
DI float lowhalf(float v) { auto r = __builtin_amdgcn_permlane32_swap(__float_as_uint(v), __float_as_uint(v), false, false); return __uint_as_float(r[0]); }
template <int CTRL> DI float halve(bool bit, float a, float b) { return (bit ? b : a) + dpp<CTRL>(bit ? a : b); }
DI int rfl(int v) { return __builtin_amdgcn_readfirstlane(v); }
#define EXP2(v) __builtin_amdgcn_exp2f(v)

struct Ids { int G, c, g, j; };
DI Ids ids() { const int b = blockIdx.x, x = b & 7, i = b >> 3; Ids r; r.G = (x >> 1) * 4 + (i >> 3); r.c = (x & 1) * 8 + (i & 7); r.g = 4 * x + (i >> 3); r.j = i & 7; return r; }

#define STAMP_DECL
#define STAMP(I)
#define STAMP_FLUSH(K)
#define STAMP_ARG
#define STAMP_PASS

#define PSTAMP(I)
#define ASTAMP(I)
constexpr u32 M_LDS = 163840;
constexpr u32 F_A = 0;
constexpr u32 F_H = 65536;
constexpr u32 F_B = 131072;
constexpr u32 F_D = F_B + 16640;
constexpr u32 F_M = F_D + 10240;
static_assert(F_M + 4608 <= M_LDS, "front-end LDS map");
constexpr u32 SL0 = 0, SL1 = 69632, SL2 = 32768, SL3 = 102400, TOPA = 135168, GAPA = 65536, OEX = 143616, JUNK = M_LDS - 256;

DI void mfa(f32x16& c, u32x4 a, u32x4 b) { asm volatile("v_mfma_f32_32x32x16_bf16 %0, %1, %2, %0" : "+v"(c) : "v"(a), "v"(b) : "memory"); }
DI void mf0(f32x16& c, u32x4 a, u32x4 b) { asm volatile("v_mfma_f32_32x32x16_bf16 %0, %1, %2, 0" : "=&v"(c) : "v"(a), "v"(b) : "memory"); }
DI float vexp(float a) { float r; asm volatile("v_exp_f32 %0, %1" : "=v"(r) : "v"(a)); return r; }
DI float vrcp(float a) { float r; asm volatile("v_rcp_f32 %0, %1" : "=v"(r) : "v"(a)); return r; }
DI u32 vcvt(float a, float b) { u32 r; asm volatile("v_cvt_pk_bf16_f32 %0, %1, %2" : "=v"(r) : "v"(a), "v"(b)); return r; }
DI float vadd(float a, float b) { float r; asm volatile("v_add_f32 %0, %1, %2" : "=v"(r) : "v"(a), "v"(b)); return r; }
DI float vmul(float a, float b) { float r; asm volatile("v_mul_f32 %0, %1, %2" : "=v"(r) : "v"(a), "v"(b)); return r; }
DI float vfma_k(float a, float k, float c) { float r; asm volatile("v_fma_f32 %0, %1, %2, %3" : "=v"(r) : "v"(a), "s"(k), "v"(c)); return r; }
DI float vadd1(float a) { float r; asm volatile("v_add_f32 %0, 1.0, %1" : "=v"(r) : "v"(a)); return r; }
DI void vdotk(float& c, u32 a, u32 k) { asm volatile("v_dot2c_f32_bf16 %0, %2, %1" : "+v"(c) : "v"(a), "s"(k)); }
DI void vdot(float& c, u32 a) { asm volatile("v_dot2c_f32_bf16 %0, %1, %1" : "+v"(c) : "v"(a)); }
#define MFMA_SETTLE() asm volatile("s_nop 15\n\ts_nop 3" ::: "memory")
DI float rowmax16(const f32x16& s) {
  float m = fmaxf(fmaxf(s[0], s[1]), s[2]);
#pragma unroll
  for (int r = 3; r < 15; r += 2) m = fmaxf(fmaxf(m, s[r]), s[r + 1]);
  return swap32_max(fmaxf(m, s[15]));
}
constexpr float GC0 = -2.f * 0.7978845608028654f * 1.4426950408889634f, GC1 = GC0 * 0.044715f;
template <u32 A> DI u32x4 ldsr(u32 lo, u32 mid) { return A < 65536 ? lds128(lo + A) : lds128(mid + (A - 65536)); }

DI u32x4 pack8(const f32x16& a, int o8) { return u32x4{pk(a[o8], a[o8 + 1]), pk(a[o8 + 2], a[o8 + 3]), pk(a[o8 + 4], a[o8 + 5]), pk(a[o8 + 6], a[o8 + 7])}; }
template <int N> DI void sum_pieces(u32 a, float (&r)[8]) {
  u32 klo = 0x00003f80u, khi = 0x3f800000u;
  asm volatile("" : "+s"(klo), "+s"(khi));
  u32x4 u[N];
#pragma unroll
  for (int i = 0; i < N; ++i) u[i] = lds128(a + i * 1024);
#pragma unroll
  for (int e = 0; e < 4; ++e) { r[2 * e] = bflo(u[0][e]); r[2 * e + 1] = bfhi(u[0][e]); }
#pragma unroll
  for (int i = 1; i < N; ++i)
#pragma unroll
    for (int e = 0; e < 4; ++e) { const u32 v = u[i][e]; r[2 * e] = dot2(v, klo, r[2 * e]); r[2 * e + 1] = dot2(v, khi, r[2 * e + 1]); }
}

constexpr u32 SL4 = 0xffffffffu;
template <u32 SL, int S> DI u32x4 wrd(u32 lo, u32 mid, u32 hi) {
  constexpr u32 A = SL == SL4 ? (S < 28 ? TOPA + S * 1024 : GAPA + (S - 28) * 1024) : SL + S * 1024;
  return A < 65536 ? lds128(lo + A) : (A < 131072 ? lds128(mid + (A - 65536)) : lds128(hi + (A - 131072)));
}
DI constexpr int dks(int i) { return i < 6 ? 7 * (i / 3) + i % 3 : 7 * ((i - 6) >> 2) + 3 + ((i - 6) & 3); }

DI void p4_compute(const Params& p, const Ids& id, int l, int w, u32 tag, u32x4 (&wd)[28] STAMP_ARG) {
  const u32 vo = l * 16;
  char* adst = p.ws + WS_ACT + ((size_t)(4 * id.G + w) * 112 + 7 * id.c) * 1024;
  const char* asrc = p.ws + WS_AUG + (size_t)id.c * 7168;
  u32x4 aug[7];
  aug[0] = gld_s<1, 0>(asrc, vo); aug[1] = gld_s<1, 1024>(asrc, vo); aug[2] = gld_s<1, 2048>(asrc, vo);
  if (w == 0) poll_flags<16, 8>(p.ws + WS_FX + id.G * 512, tag, l);
  asm volatile("s_waitcnt vmcnt(0)\n\ts_barrier" : "+v"(aug[0]), "+v"(aug[1]), "+v"(aug[2])::"memory");
  STAMP(9)
  const char* xsrc = p.ws + WS_X1 + (size_t)(4 * id.G + w) * 32768;
  u32x4 xf[32];
#define XLD(S) (xf[S] = gld_s<1, ((S) & 3) * 1024>(xsrc + ((S) >> 2) * 4096, vo))
  XLD(0); XLD(1); XLD(2); XLD(3); XLD(4); XLD(5); XLD(6); XLD(7); XLD(8); XLD(9); XLD(10); XLD(11); XLD(12); XLD(13); XLD(14); XLD(15);
  float rs, gc0 = GC0; u32x4 xaug;
  u32 lo = vo, mid = vo + 65536, hi = vo + 131072;
  PIN(lo); PIN(mid); PIN(hi); PIN(gc0);
  f32x16 acc[3]; u32x4 ra[32], rb[32];
  ra[0] = wrd<SL0, 0>(lo, mid, hi); rb[0] = wrd<SL1, 0>(lo, mid, hi); ra[1] = wrd<SL0, 1>(lo, mid, hi); rb[1] = wrd<SL1, 1>(lo, mid, hi);
  ra[2] = wrd<SL0, 2>(lo, mid, hi); rb[2] = wrd<SL1, 2>(lo, mid, hi);
#define PH0(S) { constexpr int s = (S);                                                                            \
      waitv_pin<(s < 16 ? 15 : 31 - s)>(xf[s]);                                                                    \
      if (s == 0) mf0(acc[0], ra[0], xf[0]); else mfa(acc[0], ra[s], xf[s]);                                       \
      if (s + 16 < 32) XLD((s + 16) & 31);                                                                         \
      if (s + 3 < 32) { ra[(s + 3) & 31] = wrd<SL0, (s + 3) & 31>(lo, mid, hi); rb[(s + 3) & 31] = wrd<SL1, (s + 3) & 31>(lo, mid, hi); } \
      if (s == 0) mf0(acc[1], rb[0], xf[0]); else mfa(acc[1], rb[s], xf[s]); }
 PH0(0) PH0(1) PH0(2) PH0(3) PH0(4) PH0(5) PH0(6) PH0(7) \
 PH0(8) PH0(9) PH0(10) PH0(11) PH0(12) PH0(13) PH0(14) PH0(15) \
 PH0(16) PH0(17) PH0(18) PH0(19) PH0(20) PH0(21) PH0(22) PH0(23) \
 PH0(24) PH0(25) PH0(26) PH0(27) PH0(28) PH0(29) PH0(30) PH0(31)
#undef PH0
  ra[0] = wrd<SL2, 0>(lo, mid, hi); ra[1] = wrd<SL2, 1>(lo, mid, hi); ra[2] = wrd<SL2, 2>(lo, mid, hi); ra[3] = wrd<SL2, 3>(lo, mid, hi);
  STAMP(11)
  asm volatile("s_barrier" ::: "memory");
  xaug = lds128(GAPA + w * 512 + (l & 31) * 16);
  rs = *(const LAS float*)(uintptr_t)(GAPA + 2048 + w * 128 + (l & 31) * 4);
  f32x4 gv, uv, zz; u32x4 q;
#define E4(F) { _Pragma("unroll") for (int e = 0; e < 4; ++e) { F; } }
#define E2(B, F) { _Pragma("unroll") for (int e = B; e < B + 2; ++e) { F; } }
#define UNIT(T, U) { constexpr int u = (U) % 13, GO = ((U) / 13) * 8; const f32x16& k_ = acc[(T) % 3];              \
      if (u == 0) E4(gv[e] = vmul(k_[GO + e], rs))                                                                  \
      if (u == 1) E4(zz[e] = vmul(gv[e], gv[e]))                                                                    \
      if (u == 2) E4(zz[e] = vfma_k(zz[e], GC1, gc0))                                                               \
      if (u == 3) E4(zz[e] = vmul(zz[e], gv[e]))                                                                    \
      if (u == 4) E2(0, zz[e] = vexp(zz[e]))                                                                        \
      if (u == 5) E2(2, zz[e] = vexp(zz[e]))                                                                        \
      if (u == 6) E4(uv[e] = vmul(k_[GO + 4 + e], rs))                                                              \
      if (u == 7) E4(zz[e] = vadd1(zz[e]))                                                                          \
      if (u == 8) E2(0, zz[e] = vrcp(zz[e]))                                                                        \
      if (u == 9) E2(2, zz[e] = vrcp(zz[e]))                                                                        \
      if (u == 10) E4(uv[e] = vmul(uv[e], gv[e]))                                                                   \
      if (u == 11) E4(uv[e] = vmul(uv[e], zz[e]))                                                                   \
      if (u == 12) { q[GO / 4] = vcvt(uv[0], uv[1]); q[GO / 4 + 1] = vcvt(uv[2], uv[3]); if (GO) gst16_sc1(adst + (T) * 1024, vo, q); } }
#define E0(T) ((T) == 0 ? 9 : (T) == 1 ? 35 : (T) == 2 ? 61 : (T) == 3 ? 87 : (T) == 4 ? 113 : 139)
#define GAP1(T, G) { if ((G) >= E0(T) && (G) < E0(T) + 26) UNIT(T, ((G) - E0(T) + 26) % 26) }
#define GAP(G) { GAP1(0, G) GAP1(1, G) GAP1(2, G) GAP1(3, G) GAP1(4, G) GAP1(5, G) }
#define PH(S) { constexpr int s = (S);                                                                             \
      if (s == 0) mf0(acc[TT % 3], ra[0], xf[0]); else if (s < 32) mfa(acc[TT % 3], ra[s & 31], xf[s & 31]); else mfa(acc[TT % 3], aug[TT], xaug); \
      if (s < 32) { if (s + 4 < 32) ra[(s + 4) & 31] = wrd<SA, (s + 4) & 31>(lo, mid, hi); else if (TT < 6) ra[(s + 4) & 31] = wrd<SN, (s + 4) & 3>(lo, mid, hi); } \
      if (TT == 2 && s == 3) { mfa(acc[0], aug[0], xaug); mfa(acc[1], aug[1], xaug); }                             \
      if (s == 1 && TT < 6) aug[TT < 6 ? TT + 1 : 0] = gld_s<1, 0>(asrc + (TT + 1) * 1024, vo);                    \
      GAP((TT - 2) * 33 + s) }
#define PHASE  PH(0) PH(1) PH(2) PH(3) PH(4) PH(5) PH(6) PH(7) \
 PH(8) PH(9) PH(10) PH(11) PH(12) PH(13) PH(14) PH(15) \
 PH(16) PH(17) PH(18) PH(19) PH(20) PH(21) PH(22) PH(23) \
 PH(24) PH(25) PH(26) PH(27) PH(28) PH(29) PH(30) PH(31) \
 PH(32)
  { constexpr int TT = 2; constexpr u32 SA = SL2, SN = SL3; PHASE }
  asm volatile("s_waitcnt vmcnt(0)\n\ts_barrier" : "+v"(aug[3])::"memory");
  STAMP(12)
  { constexpr int TT = 3; constexpr u32 SA = SL3, SN = SL4; PHASE }
  asm volatile("s_waitcnt vmcnt(1)\n\ts_barrier" : "+v"(aug[4])::"memory");
  STAMP(13)
  { constexpr int TT = 4; constexpr u32 SA = SL4, SN = SL0; PHASE }
  asm volatile("s_waitcnt vmcnt(1)\n\ts_barrier" : "+v"(aug[5])::"memory");
  STAMP(14)
  { constexpr int TT = 5; constexpr u32 SA = SL0, SN = SL1; PHASE }
  asm volatile("s_waitcnt vmcnt(1)\n\ts_barrier" : "+v"(aug[6])::"memory");
  STAMP(15)
  { constexpr int TT = 6; constexpr u32 SA = SL1, SN = SL1; PHASE }
  STAMP(16)
  MFMA_SETTLE();
 UNIT(6, 0) UNIT(6, 1) UNIT(6, 2) UNIT(6, 3) UNIT(6, 4) UNIT(6, 5) UNIT(6, 6) UNIT(6, 7) UNIT(6, 8)
 UNIT(6, 9) UNIT(6, 10) UNIT(6, 11) UNIT(6, 12) UNIT(6, 13) UNIT(6, 14) UNIT(6, 15) UNIT(6, 16) UNIT(6, 17)
 UNIT(6, 18) UNIT(6, 19) UNIT(6, 20) UNIT(6, 21) UNIT(6, 22) UNIT(6, 23) UNIT(6, 24) UNIT(6, 25)
  STAMP(17)
  asm volatile("s_barrier" ::: "memory");
  {
    const u32 b = w * 8192;
#define WDR(F) (wd[F] = (F) < 8 ? lds128(lo + SL2 + b + ((F) & 7) * 1024) : (F) < 16 ? lds128(mid + (SL3 - 65536) + b + ((F) & 7) * 1024) : (F) < 22 ? lds128(hi + (TOPA - 131072) + w * 6144 + (((F) - 16) & 7) * 1024) : lds128(lo + SL0 + w * 6144 + (((F) - 22) & 7) * 1024))
 WDR(0); WDR(1); WDR(2); WDR(3); WDR(4); WDR(5); WDR(6); WDR(7); WDR(8); WDR(9);
 WDR(10); WDR(11); WDR(12); WDR(13); WDR(14); WDR(15); WDR(16); WDR(17); WDR(18); WDR(19);
 WDR(20); WDR(21); WDR(22); WDR(23); WDR(24); WDR(25); WDR(26); WDR(27);
#undef WDR
  }
#undef PHASE
#undef PH
#undef GAP
#undef GAP1
#undef UNIT
#undef XLD
}

DI void p4_helper(const Params& p, const Ids& id, int l, int w, u32 tag, u32x4 (&wd)[28] STAMP_ARG) {
  const u32 vo = l * 16;
  const int hw = w - 4;
  const char* wsrc = p.ws + WS_WGU + (size_t)id.c * 7 * 32768 + hw * 8192;
  const char* dme = p.ws + WS_WD + ((size_t)(2 * id.j) * 112 + 14 * w) * 1024;
  const char* dcw = p.ws + WS_WD + ((size_t)(2 * id.j) * 112 + 14 * hw) * 1024;
#define D8(SRC, DST) { dma4s(SRC, vo, DST); dma4s((SRC) + 4096, vo, (DST) + 4096); }
#define D6(SRC, DST) { dma4s(SRC, vo, DST); set_m0((DST) + 4096); dma1<0>((SRC) + 4096, vo); dma1<1024>((SRC) + 4096, vo); }
#define WDG(F) (wd[F] = gld_s<1, 0>(dme + ((F) < 14 ? (F) : 98 + (F)) * 1024, vo))
  D8(wsrc + 3 * 32768, SL3 + hw * 8192)
  WDG(16); WDG(17); WDG(18); WDG(19); WDG(20); WDG(21); WDG(22); WDG(23); WDG(24); WDG(25); WDG(26); WDG(27);
  WAITV_BAR(20);
  {
    __builtin_amdgcn_s_sleep(16);
    poll_flags<16, 8, 8>(p.ws + WS_FS + id.G * 512, tag, l);
    u32x4 st0, st1;
    asm volatile("global_load_dwordx4 %0, %2, %3 sc1\n\tglobal_load_dwordx4 %1, %2, %3 offset:16 sc1\n\ts_waitcnt vmcnt(0)" : "=&v"(st0), "=&v"(st1)
                 : "v"((l & 31) * 64 + (l >> 5) * 32), "s"(p.ws + WS_STAT + (size_t)(128 * id.G + 32 * hw) * 64) : "memory");
    const float s1 = swap32_sum((__uint_as_float(st0[0]) + __uint_as_float(st0[2])) + (__uint_as_float(st1[0]) + __uint_as_float(st1[2])));
    const float s2 = swap32_sum((__uint_as_float(st0[1]) + __uint_as_float(st0[3])) + (__uint_as_float(st1[1]) + __uint_as_float(st1[3])));
    const float nmu = -s1 * (1.f / DM), var = fmaxf(s2 * (1.f / DM) - nmu * nmu, 0.f) + EPS;
    const float rs = __builtin_amdgcn_rsqf(var), sg = var * rs, mh = rbf(nmu), sh = rbf(sg);
    if (l < 32) { sts128(GAPA + hw * 512 + vo, u32x4{pk(mh, mh), pk(nmu - mh, sh), pk(sh, sg - sh), 0u}); *(LAS float*)(uintptr_t)(GAPA + 2048 + hw * 128 + l * 4) = rs; }
  }
  dma4s(wsrc + 4 * 32768, vo, TOPA + hw * 8192); if (hw < 3) dma4s(wsrc + 4 * 32768 + 4096, vo, TOPA + hw * 8192 + 4096);
  BAR();
  D8(wsrc + 5 * 32768, SL0 + hw * 8192) WDG(0); WDG(1); WDG(2); WDG(3);
  WAITV_BAR(12);
  if (hw == 3) dma4s(wsrc + 4 * 32768 + 4096, vo, GAPA);
  D8(wsrc + 6 * 32768, SL1 + hw * 8192) dma4s(dcw, vo, SL2 + hw * 8192);
  WAITV_BAR(12);
  dma4s(dcw + 4096, vo, SL2 + hw * 8192 + 4096); D6(dcw + 8192, SL3 + hw * 8192)
  set_m0(SL3 + hw * 8192 + 6144); dma1<0>(dcw + 114688, vo); dma1<1024>(dcw + 114688, vo);
  WAITV_BAR(16);
  D6(dcw + 114688 + 2048, TOPA + hw * 6144) WDG(4); WDG(5); WDG(6); WDG(7); WDG(8); WDG(9);
  if (w == 4 && l == 0) *(LAS u32*)(uintptr_t)GAPA = 0u;
  BAR();
  if (w == 5 && l == 0) put_flag(p.ws + WS_FE + id.G * 512 + fofs<8>(id.c), tag);
  D6(dcw + 114688 + 8192, SL0 + hw * 6144) WDG(10); WDG(11); WDG(12); WDG(13); WDG(14); WDG(15);
  WAITV_BAR(6);
  poll_flags<16, 8, 4>(p.ws + WS_FE + (id.g >> 1) * 512, tag, l);
  asm volatile("s_waitcnt vmcnt(0)" : "+v"(wd[0]), "+v"(wd[1]), "+v"(wd[2]), "+v"(wd[3]), "+v"(wd[4]), "+v"(wd[5]), "+v"(wd[6]), "+v"(wd[7]), "+v"(wd[8]), "+v"(wd[9]), "+v"(wd[10]),
               "+v"(wd[11]), "+v"(wd[12]), "+v"(wd[13])::"memory");
  asm volatile("" : "+v"(wd[14]), "+v"(wd[15]), "+v"(wd[16]), "+v"(wd[17]), "+v"(wd[18]), "+v"(wd[19]), "+v"(wd[20]), "+v"(wd[21]), "+v"(wd[22]), "+v"(wd[23]), "+v"(wd[24]),
               "+v"(wd[25]), "+v"(wd[26]), "+v"(wd[27])::"memory");
#undef WDG
#undef D8
#undef D6
}

__global__ __launch_bounds__(512, 2) void k_main(Params p) {
  extern __shared__ __attribute__((aligned(16))) char smem[];
  const int tid = threadIdx.x, l = tid & 63, w = rfl(tid >> 6);
  const Ids id = ids();
  const int g = id.g, j = id.j, h = l >> 5;
  const u32 vo = l * 16;
  const u32 epoch = *(const u32*)(p.ws + WS_EPOCH);
  const u32 tag = epoch * 0x9E3779B1u + 0x7F4A7C15u;
  const int tt = w & 1, nt = (w >> 1) & 1, kh = w >> 2, kq = w >> 1;
  const char* modr = p.ws + WS_MOD + (blockIdx.x & 7) * 12288;
  STAMP_DECL STAMP(0)
  const char* wgu = p.ws + WS_WGU + (size_t)id.c * 7 * 32768;
  const char* wob = p.ws + WS_WO + ((size_t)(2 * j) * 32 + 8 * kq) * 1024;
  const char *wob1 = wob + 4096, *wob2 = wob + 32768, *wob3 = wob + 36864;
  u32x4 q0, q1, fo_, fd_, fg[2];
  char *pdo, *pdd, *pdg;
  f32x16 o0, o1;
  u32x4 vB0, vB1, pb0, pb1;
  {
    const int nh = (l >> 4) & 1, q = (l & 15) >> 2, pp = l & 3;
    const int uid = g * 7 + j - 1, uT = uid >> 1, ukh = uid & 1;
    const int rk = (blockIdx.x >> 6) * 7 + j - 1;
    const bool gemv = j != 0 && rk < 24;
    const int mu = 4 * ((blockIdx.x & 7) + 8 * (rk >> 2)) + (rk & 3);
    const u32 rawo = (l >> 3) * 1024 + (((l & 7) ^ (((l >> 4) & 1) << 2)) * 16);
#define WQ_DMA() { _Pragma("unroll") for (int i = 0; i < 8; ++i) { set_m0(F_A + (8 * w + i) * 1024); dma<0>(p.wq + (size_t)(64 * w + 8 * i) * DM + 64 * j, rawo); } }
#define LATE_DMA() {                                                                                                                              \
      if (j != 0) {                                                                                                                               \
        const u32 go = (l >> 1) * (FF * 2) + (l & 1) * 16;                                                                                        \
        set_m0(F_B + w * 1024); dma<0>(p.wg + (size_t)(256 * ukh + 32 * w) * FF + 16 * uT, go);                                                   \
        set_m0(F_B + 8192 + 128 + w * 1024); dma<0>(p.wu + (size_t)(256 * ukh + 32 * w) * FF + 16 * uT, go);                                      \
        set_m0(F_D + w * 1024); dma<0>(p.wd + (size_t)(64 * (uid >> 3) + 8 * w) * DM + 64 * (uid & 7), rawo);                                     \
      }                                                                                                                                           \
      if (w >= 6) { set_m0(F_D + 8192 + (w - 6) * 1024); dma<0>(p.wo + (size_t)(16 * g + 8 * (w - 6)) * DM + 64 * j, rawo); } }
    u32x4 w0, w1; u32 c0, c1;
    if (gemv) {
      const u32 mo = (tid >> 1) * 6144 + (tid & 1) * 16;
      w0 = gld_s<0, 0>(p.wm + 16 * mu, mo); w1 = gld_s<0, 0>(p.wm + 16 * mu + 256 * 3072, mo);
      c0 = gld2_s<0>(p.c, (tid >> 1) * 2); c1 = gld2_s<512>(p.c, (tid >> 1) * 2);
    }
    u32x4 xv[8];
    {
      const char* xs = (const char*)(p.x + (size_t)(64 * g + 8 * w) * DM);
      xv[0] = gld_s<0, 0>(xs, vo); xv[1] = gld_s<0, 1024>(xs, vo); xv[2] = gld_s<0, 2048>(xs, vo); xv[3] = gld_s<0, 3072>(xs, vo);
      xv[4] = gld_s<0, 0>(xs + 4096, vo); xv[5] = gld_s<0, 1024>(xs + 4096, vo); xv[6] = gld_s<0, 2048>(xs + 4096, vo); xv[7] = gld_s<0, 3072>(xs + 4096, vo);
    }
    if (j == 0) {
      set_m0(F_H + w * 2048); dma<0>(p.wv + w * 1024, vo); dma<1024>(p.wv + w * 1024, vo);
      set_m0(F_H + 16384 + 128 + w * 2048); dma<0>(p.wk + w * 1024, vo); dma<1024>(p.wk + w * 1024, vo);
      WQ_DMA() LATE_DMA()
    }
    if (gemv) {
      asm volatile("s_waitcnt vmcnt(8)" : "+v"(w0), "+v"(w1), "+v"(c0), "+v"(c1)::"memory");
      const float f0 = bflo(c0), f1 = bflo(c1);
      const float s0 = rbf(f0 * __builtin_amdgcn_rcpf(1.f + EXP2(-1.4426950408889634f * f0))), s1 = rbf(f1 * __builtin_amdgcn_rcpf(1.f + EXP2(-1.4426950408889634f * f1)));
      float a[8];
#pragma unroll
      for (int i = 0; i < 4; ++i) { a[2 * i] = s0 * bflo(w0[i]) + s1 * bflo(w1[i]); a[2 * i + 1] = s0 * bfhi(w0[i]) + s1 * bfhi(w1[i]); }
#pragma unroll
      for (int i = 0; i < 4; ++i) a[i] = halve<0x4E>(l & 2, a[i], a[i + 4]);
#pragma unroll
      for (int i = 0; i < 2; ++i) a[i] = halve<0x124>(l & 4, a[i], a[i + 2]);
      a[0] = halve<0x128>(l & 8, a[0], a[1]);
      a[0] = swap32_sum(swap16_sum(a[0]));
      if (l < 16) *(LAS float*)(uintptr_t)(F_M + w * 64 + ((l & 1) * 8 + (l & 2) * 2 + ((l >> 1) & 2) + (l >> 3)) * 4) = a[0];
      BAR();
      if (w == 7) {
        f32x2 s = {0.f, 0.f};
#pragma unroll
        for (int i = 0; i < 8; ++i) s += ldsf2(F_M + i * 64 + (l & 7) * 8);
        gst8_sc1(p.ws + WS_MOD + mu * 64, (l >> 3) * 12288 + (l & 7) * 8, u32x2{pk(s[0], s[1]), tag});
        if (rk < 8) { STAMP(10) }
      }
    }
    STAMP(22)
    if (j == 0) WAITV(12); else if (gemv && w == 7) WAITV(1); else WAITV(0);
    asm volatile("" : "+v"(xv[0]), "+v"(xv[1]), "+v"(xv[2]), "+v"(xv[3]), "+v"(xv[4]), "+v"(xv[5]), "+v"(xv[6]), "+v"(xv[7]));
    float xh[8][8];
    {
      float s1[8], s2[8];
#pragma unroll
      for (int i = 0; i < 8; ++i) {
        s1[i] = 0.f; s2[i] = 0.f;
#pragma unroll
        for (int e = 0; e < 4; ++e) { const u32 v = xv[i][e]; s1[i] = dot2(v, 0x3f803f80u, s1[i]); s2[i] = dot2(v, v, s2[i]); }
      }
#pragma unroll
      for (int i = 0; i < 4; ++i) { s1[i] = halve<0xB1>(l & 1, s1[i], s1[i + 4]); s2[i] = halve<0xB1>(l & 1, s2[i], s2[i + 4]); }
#pragma unroll
      for (int i = 0; i < 2; ++i) { s1[i] = halve<0x4E>(l & 2, s1[i], s1[i + 2]); s2[i] = halve<0x4E>(l & 2, s2[i], s2[i + 2]); }
      float t1 = halve<0x124>(l & 4, s1[0], s1[1]), t2 = halve<0x124>(l & 4, s2[0], s2[1]);
      t1 += dpp<0x128>(t1); t2 += dpp<0x128>(t2);
      t1 = swap32_sum(swap16_sum(t1)); t2 = swap32_sum(swap16_sum(t2));
      const float m = t1 * (1.f / DM), r = __builtin_amdgcn_rsqf(fmaxf(t2 * (1.f / DM) - m * m, 0.f) + EPS), nm = -m * r;
#define LNROW(I, LN) { const float ri = dpp<0x150 + (LN)>(r), ni = dpp<0x150 + (LN)>(nm); _Pragma("unroll") for (int e = 0; e < 4; ++e) { xh[I][2 * e] = fmaf(bflo(xv[I][e]), ri, ni); xh[I][2 * e + 1] = fmaf(bfhi(xv[I][e]), ri, ni); } }
      LNROW(0, 0) LNROW(1, 4) LNROW(2, 2) LNROW(3, 6) LNROW(4, 1) LNROW(5, 5) LNROW(6, 3) LNROW(7, 7)
#undef LNROW
#pragma unroll
      for (int i = 0; i < 8; ++i) asm volatile("" : "+v"(xh[i][0]), "+v"(xh[i][1]), "+v"(xh[i][2]), "+v"(xh[i][3]), "+v"(xh[i][4]), "+v"(xh[i][5]), "+v"(xh[i][6]), "+v"(xh[i][7]));
    }
    STAMP(23)
    if (w > 0 && j != 0) WQ_DMA()
    u32x4 wkvf[16];
    if (j == 0) {
      WAITV_BAR(8);
      const u32 a0 = F_H + nh * (16384 + 128) + (256 * kh + 8 * h + q) * 32 + pp * 8;
#pragma unroll
      for (int s = 0; s < 16; ++s) wkvf[s] = ldstr2(a0 + s * 512, a0 + s * 512 + 128);
    }
    STAMP(24)
    if (w == 0) {
      u32x4 a0, a1, b0, b1;
#pragma unroll 1
      for (int it = 0; it < SPIN_CAP; ++it) {
        asm volatile("global_load_dwordx4 %0, %4, %5 sc1\n\tglobal_load_dwordx4 %1, %4, %5 offset:16 sc1\n\tglobal_load_dwordx4 %2, %4, %5 offset:2048 sc1\n\t"
                     "global_load_dwordx4 %3, %4, %5 offset:2064 sc1\n\ts_waitcnt vmcnt(0)" : "=&v"(b0), "=&v"(b1), "=&v"(a0), "=&v"(a1) : "v"(l * 32), "s"(modr) : "memory");
        if (__all(a0[1] == tag && a0[3] == tag && a1[1] == tag && a1[3] == tag && b0[1] == tag && b0[3] == tag && b1[1] == tag && b1[3] == tag)) break;
      }
      sts128(F_M + 512 + vo, u32x4{pk(1.f + bflo(a0[0]), 1.f + bfhi(a0[0])), pk(1.f + bflo(a0[2]), 1.f + bfhi(a0[2])), pk(1.f + bflo(a1[0]), 1.f + bfhi(a1[0])), pk(1.f + bflo(a1[2]), 1.f + bfhi(a1[2]))});
      sts128(F_M + 1536 + vo, u32x4{b0[0], b0[2], b1[0], b1[2]});
    }
    BAR();
    STAMP(25)
    if (w == 0 && j != 0) WQ_DMA()
    {
      const u32x4 ca = lds128(F_M + 512 + vo), sa = lds128(F_M + 1536 + vo);
      float A[8], B[8];
#pragma unroll
      for (int e = 0; e < 4; ++e) { A[2 * e] = bflo(ca[e]); A[2 * e + 1] = bfhi(ca[e]); B[2 * e] = bflo(sa[e]); B[2 * e + 1] = bfhi(sa[e]); }
#pragma unroll
      for (int i = 0; i < 8; ++i) {
        u32x4 o;
#pragma unroll
        for (int e = 0; e < 4; ++e) o[e] = pk(fmaf(xh[i][2 * e], A[2 * e], B[2 * e]), fmaf(xh[i][2 * e + 1], A[2 * e + 1], B[2 * e + 1]));
        const int tk = 8 * w + i;
        sts128(F_H + ((tk >> 5) * 32 + (l >> 1)) * 1024 + (l & 1) * 512 + (((tk & 31) ^ (l & 7)) * 16), o);
      }
    }
    if (j != 0) LATE_DMA()
    BAR();
    STAMP(26)
    u32x4 xf[16];
#pragma unroll
    for (int s = 0; s < 16; ++s) xf[s] = lds128(F_H + (tt * 32 + 16 * kh + s) * 1024 + h * 512 + (((l & 31) ^ ((2 * s + h) & 7)) * 16));
    if (j == 0) {
      const u32 xs = F_B + w * 2048 + vo, xr = F_B + (w ^ 4) * 2048 + vo;
      f32x16 acc = {};
      if (nt == 0) {
#pragma unroll
        for (int s = 0; s < 16; ++s) acc = mfma(wkvf[s], xf[s], acc);
      } else {
#pragma unroll
        for (int s = 0; s < 16; ++s) acc = mfma(xf[s], wkvf[s], acc);
      }
      float keep[8];
      if (kh == 0) { stsf4(xs, f32x4{acc[8], acc[9], acc[10], acc[11]}); stsf4(xs + 1024, f32x4{acc[12], acc[13], acc[14], acc[15]});
#pragma unroll
        for (int e = 0; e < 8; ++e) keep[e] = acc[e];
      } else { stsf4(xs, f32x4{acc[0], acc[1], acc[2], acc[3]}); stsf4(xs + 1024, f32x4{acc[4], acc[5], acc[6], acc[7]});
#pragma unroll
        for (int e = 0; e < 8; ++e) keep[e] = acc[8 + e];
      }
      BAR();
      const f32x4 r0 = ldsf4(xr), r1 = ldsf4(xr + 1024);
      u32x4 o = {pk(keep[0] + r0[0], keep[1] + r0[1]), pk(keep[2] + r0[2], keep[3] + r0[3]), pk(keep[4] + r1[0], keep[5] + r1[1]), pk(keep[6] + r1[2], keep[7] + r1[3])};
      char* img = p.ws + WS_KV + (size_t)(2 * g + tt) * TS;
      if (nt == 0) { if (kh == 1) gst16_sc1(img, vo, o); }
      else if ((l & 31) <= 17) {
        if ((l & 31) == 16) o = u32x4{0x3f803f80u, 0x3f803f80u, 0x3f803f80u, 0x3f803f80u};
        if ((l & 31) == 17) o = u32x4{0u, 0u, 0u, 0u};
        gst16_sc1(img + 1024 + kh * 576, h * 288 + (l & 31) * 16, o);
      }
      WAITV_BAR(0);
      if (w == 0 && l < 8) put_flag(p.ws + WS_FKV + l * 1024 + fofs<8>(g), tag);
    }
    STAMP(21)
    if (j != 0) { if (w >= 6) WAITV(4); else WAITV(3); }
    BAR();
    const u32 rawr = (((nh * 2 + (pp >> 1)) ^ (((q >> 1) & 1) << 2)) * 16) + (pp & 1) * 8;
    f32x16 acc = {};
    {
      const u32 a0 = F_A + (256 * kh + 8 * h + q) * 128 + (rawr ^ (nt * 64));
#pragma unroll
      for (int s = 0; s < 16; ++s) acc = mfma(ldstr2(a0 + s * 2048, a0 + s * 2048 + 512), xf[s], acc);
    }
    const u32 xs = F_H + w * 4096 + vo, xr = F_H + (w ^ 4) * 4096 + vo;
    stsf4(xs, f32x4{acc[0], acc[1], acc[2], acc[3]}); stsf4(xs + 1024, f32x4{acc[4], acc[5], acc[6], acc[7]});
    stsf4(xs + 2048, f32x4{acc[8], acc[9], acc[10], acc[11]}); stsf4(xs + 3072, f32x4{acc[12], acc[13], acc[14], acc[15]});
    STAMP(27)
    if (w == 1 && j != 0) {
      {
        u32x4 a0, b0;
#pragma unroll 1
        for (int it = 0; it < SPIN_CAP; ++it) {
          asm volatile("global_load_dwordx4 %0, %2, %3 sc1\n\tglobal_load_dwordx4 %1, %2, %3 offset:2048 sc1\n\ts_waitcnt vmcnt(0)" : "=&v"(b0), "=&v"(a0) : "v"(vo), "s"(modr + 6144 + ukh * 1024) : "memory");
          if (__all(a0[1] == tag && a0[3] == tag && b0[1] == tag && b0[3] == tag)) break;
          __builtin_amdgcn_s_sleep(2);
        }
        stsf4(F_M + 2560 + vo, f32x4{rbf(1.f + bflo(a0[0])), rbf(1.f + bfhi(a0[0])), rbf(1.f + bflo(a0[2])), rbf(1.f + bfhi(a0[2]))});
        stsf4(F_M + 3584 + vo, f32x4{bflo(b0[0]), bfhi(b0[0]), bflo(b0[2]), bfhi(b0[2])});
      }
    }
    WAITV_BAR(0);
    f32x4 cs[2][4];
    if (w >= 6) { const u32 a0 = F_D + 8192 + (4 * h + q) * 128 + (rawr ^ (tt * 64)); fo_ = ldstr2(a0, a0 + 1024); }
    if (j != 0) {
      const u32 a0 = F_D + (16 * kq + 4 * h + q) * 128 + (rawr ^ (tt * 64));
      fd_ = ldstr2(a0, a0 + 1024);
#pragma unroll
      for (int i = 0; i < 2; ++i) {
        const int s = 2 * w + i;
        const u32 a1 = F_B + (pp >> 1) * (8192 + 128) + (16 * s + 4 * h + q) * 32 + nh * 16 + (pp & 1) * 8;
        fg[i] = ldstr2(a1, a1 + 256);
        cs[i][0] = ldsf4(F_M + 2560 + (16 * s + 4 * h) * 4); cs[i][1] = ldsf4(F_M + 2560 + (16 * s + 8 + 4 * h) * 4);
        cs[i][2] = ldsf4(F_M + 3584 + (16 * s + 4 * h) * 4); cs[i][3] = ldsf4(F_M + 3584 + (16 * s + 8 + 4 * h) * 4);
      }
    }
    const f32x4 r0 = ldsf4(xr), r1 = ldsf4(xr + 1024), r2 = ldsf4(xr + 2048), r3 = ldsf4(xr + 3072);
    q0 = u32x4{pk((acc[0] + r0[0]) * C2, (acc[1] + r0[1]) * C2), pk((acc[2] + r0[2]) * C2, (acc[3] + r0[3]) * C2), pk((acc[4] + r1[0]) * C2, (acc[5] + r1[1]) * C2), pk((acc[6] + r1[2]) * C2, (acc[7] + r1[3]) * C2)};
    q1 = u32x4{pk((acc[8] + r2[0]) * C2, (acc[9] + r2[1]) * C2), pk((acc[10] + r2[2]) * C2, (acc[11] + r2[3]) * C2), pk((acc[12] + r3[0]) * C2, (acc[13] + r3[1]) * C2), pk((acc[14] + r3[2]) * C2, (acc[15] + r3[3]) * C2)};
    PIN(q0); PIN(q1);
    pdo = p.ws + WS_WO + ((size_t)(2 * j + tt) * 32 + g) * 1024;
    pdd = p.ws + WS_WD + ((size_t)(2 * (uid & 7) + tt) * 112 + 4 * (uid >> 3) + kq) * 1024;
    pdg = p.ws + WS_WGU + ((size_t)uT * 32 + 16 * ukh + 2 * w) * 1024;
    asm volatile("" : "+s"(pdo), "+s"(pdd), "+s"(pdg), "+s"(wob), "+s"(wob1), "+s"(wob2), "+s"(wob3));
    if (w == 2) poll_flags<32, 8>(p.ws + WS_FKV + (blockIdx.x & 7) * 1024, tag, l);
    if (w == 3) {
#pragma unroll 1
      for (int it = 0; it < SPIN_CAP; ++it) {
        const u32 v = __hip_atomic_load((const GAS u32*)(modr + 4096 + j * 256 + h * 6144 + (l & 31) * 8 + 4), RLX_AGENT);
        if (__all(v == tag)) break;
        __builtin_amdgcn_s_sleep(2);
      }
    }
    BAR();
    STAMP(28)
    STAMP(29)
    { const u32 o = (w & 1) * 69632 + (w >> 1) * 6144;
      dma4s(p.ws + WS_KV + o, vo, o); set_m0(o + 4096); dma1<0>(p.ws + WS_KV + o + 4096, vo); dma1<1024>(p.ws + WS_KV + o + 4096, vo); }
    o0 = f32x16{}; o1 = f32x16{}; vB0 = u32x4{}; vB1 = u32x4{}; pb0 = u32x4{}; pb1 = u32x4{};
    PIN(o0); PIN(o1); PIN(vB0); PIN(vB1); PIN(pb0); PIN(pb1);
    if (j != 0) {
      float us = 0.f, wsum = 0.f;
#pragma unroll
      for (int i = 0; i < 2; ++i) {
        const u32x4 f = fg[i];
        const f32x4 c0 = cs[i][0], c1 = cs[i][1], s0 = cs[i][2], s1 = cs[i][3];
        u32x4 o;
        o[0] = pk(bflo(f[0]) * c0[0], bfhi(f[0]) * c0[1]); o[1] = pk(bflo(f[1]) * c0[2], bfhi(f[1]) * c0[3]);
        o[2] = pk(bflo(f[2]) * c1[0], bfhi(f[2]) * c1[1]); o[3] = pk(bflo(f[3]) * c1[2], bfhi(f[3]) * c1[3]);
#pragma unroll
        for (int e = 0; e < 4; ++e) { const u32 v = o[e]; us = dot2(v, 0x3f803f80u, us); }
        wsum += bflo(f[0]) * s0[0] + bfhi(f[0]) * s0[1] + bflo(f[1]) * s0[2] + bfhi(f[1]) * s0[3] + bflo(f[2]) * s1[0] + bfhi(f[2]) * s1[1] + bflo(f[3]) * s1[2] + bfhi(f[3]) * s1[3];
        fg[i] = o;
      }
      us = swap32_sum(us); wsum = swap32_sum(wsum);
      if (l < 32) *(LAS f32x2*)(uintptr_t)(OEX + w * 256 + l * 8) = f32x2{us, wsum};
      WAITV_BAR(0);
      if (w == 7) {
        f32x2 s = {0.f, 0.f};
#pragma unroll
        for (int i = 0; i < 8; ++i) s += ldsf2(OEX + i * 256 + (l & 31) * 8);
        const float uh = rbf(s[0]), wh = rbf(s[1]);
        if (l < 32) gst16_sc1(p.ws + WS_AUG + (size_t)uT * 1024 + ukh * 512, vo, u32x4{pk(uh, s[0] - uh), pk(uh, wh), pk(s[1] - wh, wh), 0u});
      }
    } else {
      WAITV_BAR(0);
    }
  }
  const size_t fo = ((size_t)(2 * g + tt) * 32 + 4 * j + 2 * nt) * 1024;
  STAMP(1)
  const u32 vrow = 1024 + h * 288 + min(l & 31, 17) * 16;
  u32 ka = kh * 32 * TS + vo, va = kh * 32 * TS + vrow;
  f32x16 nm0, nm1, s0, s1;
  const u32x4 k0 = lds128(vo);
  u32x4 kA = lds128(ka), kB = lds128(ka + TS);
  u32x4 vA0 = lds128(va), vA1 = lds128(va + 576);
  u32x4 pa0, pa1;
  {
    mf0(s0, k0, q0); mf0(s1, k0, q1);
    MFMA_SETTLE();
    PIN(s0); PIN(s1);
#define MX3(a, b, c) fmaxf(fmaxf(a, b), c)
    const float m0 = swap32_max(MX3(MX3(MX3(s0[0], s0[1], s0[2]), MX3(s0[3], s0[4], s0[5]), MX3(s0[6], s0[7], s0[8])), MX3(MX3(s0[9], s0[10], s0[11]), MX3(s0[12], s0[13], s0[14]), s0[15]), -3.0e38f));
    const float m1 = swap32_max(MX3(MX3(MX3(s1[0], s1[1], s1[2]), MX3(s1[3], s1[4], s1[5]), MX3(s1[6], s1[7], s1[8])), MX3(MX3(s1[9], s1[10], s1[11]), MX3(s1[12], s1[13], s1[14]), s1[15]), -3.0e38f));
#undef MX3
#pragma unroll
    for (int r = 0; r < 16; ++r) nm0[r] = -m0;
    PIN(nm0);
    s0 = mfma(kA, q0, nm0);
#pragma unroll
    for (int r = 0; r < 16; ++r) nm1[r] = -m1;
    PIN(nm1);
  }
  ASTAMP(25)
  if (!kh) {
    const u32 o = nt * 69632 + 24576 + tt * 8192;
    dma4s(p.ws + WS_KV + o, vo, o); dma4s(p.ws + WS_KV + o + 4096, vo, o + 4096);
  }
#define PREP_ST() { if (j != 0) { gst16_sc1(pdd, vo, fd_); gst16_sc1(pdg, vo, fg[0]); gst16_sc1(pdg + 1024, vo, fg[1]); } }
  if (kh) { PREP_ST() if (w >= 6) gst16_sc1(pdo, vo, fo_); }
  ASTAMP(26)
  u32x4 wf[16];
#define HALF(SC, N0, N1, MF, OP, VP0, P0, VP1, P1, LD1, LD2)                                              \
  {                                                                                                       \
    MF; LD1; SB();                                                                                        \
    float e0 = EXP2(SC[0]), e1 = EXP2(SC[1]), e2 = EXP2(SC[2]), e3 = EXP2(SC[3]), e4 = EXP2(SC[4]), e5 = EXP2(SC[5]), e6 = EXP2(SC[6]), e7 = EXP2(SC[7]); SB(); \
    OP = mfma(VP0, P0, OP); SB();                                                                         \
    float e8 = EXP2(SC[8]), e9 = EXP2(SC[9]), e10 = EXP2(SC[10]), e11 = EXP2(SC[11]), e12 = EXP2(SC[12]), e13 = EXP2(SC[13]), e14 = EXP2(SC[14]), e15 = EXP2(SC[15]); SB(); \
    OP = mfma(VP1, P1, OP); LD2; SB();                                                                    \
    N0[0] = pk(e0, e1); N0[1] = pk(e2, e3); N0[2] = pk(e4, e5); N0[3] = pk(e6, e7); N1[0] = pk(e8, e9);   \
    N1[1] = pk(e10, e11); N1[2] = pk(e12, e13); N1[3] = pk(e14, e15); SB();                               \
  }
#define TILE(T, KC, KN, VC0, VC1, VP0, VP1)                                                               \
    HALF(s0, pa0, pa1, s1 = mfma(KC, q1, nm1), o1, VP0, pb0, VP1, pb1, KC = lds128(ka + ((T) + 2) * TS),  \
         (VP0 = lds128(va + ((T) + 1) * TS), VP1 = lds128(va + ((T) + 1) * TS + 576)))                    \
    HALF(s1, pb0, pb1, s0 = mfma(KN, q0, nm0), o0, VC0, pa0, VC1, pa1, , )
#define LDWF(SEL)                                                                                                     \
  asm volatile("s_cmp_lg_u32 %21, 0\n\ts_cbranch_scc1 .Lwf%=\n\t"                                                     \
               "global_load_dwordx4 %0, %16, %17 sc1\n\tglobal_load_dwordx4 %1, %16, %17 offset:1024 sc1\n\tglobal_load_dwordx4 %2, %16, %17 offset:2048 sc1\n\tglobal_load_dwordx4 %3, %16, %17 offset:3072 sc1\n\t"     \
               "global_load_dwordx4 %4, %16, %18 sc1\n\tglobal_load_dwordx4 %5, %16, %18 offset:1024 sc1\n\tglobal_load_dwordx4 %6, %16, %18 offset:2048 sc1\n\tglobal_load_dwordx4 %7, %16, %18 offset:3072 sc1\n\t"     \
               "global_load_dwordx4 %8, %16, %19 sc1\n\tglobal_load_dwordx4 %9, %16, %19 offset:1024 sc1\n\tglobal_load_dwordx4 %10, %16, %19 offset:2048 sc1\n\tglobal_load_dwordx4 %11, %16, %19 offset:3072 sc1\n\t"   \
               "global_load_dwordx4 %12, %16, %20 sc1\n\tglobal_load_dwordx4 %13, %16, %20 offset:1024 sc1\n\tglobal_load_dwordx4 %14, %16, %20 offset:2048 sc1\n\tglobal_load_dwordx4 %15, %16, %20 offset:3072 sc1\n"   \
               ".Lwf%=:"                                                                                              \
               : "+v"(wf[0]), "+v"(wf[1]), "+v"(wf[2]), "+v"(wf[3]), "+v"(wf[4]), "+v"(wf[5]), "+v"(wf[6]), "+v"(wf[7]), "+v"(wf[8]), "+v"(wf[9]), "+v"(wf[10]), \
                 "+v"(wf[11]), "+v"(wf[12]), "+v"(wf[13]), "+v"(wf[14]), "+v"(wf[15])                                  \
               : "v"(vo), "s"(wob), "s"(wob1), "s"(wob2), "s"(wob3), "s"(SEL) : "memory", "scc")
#pragma unroll
  for (int s4 = 0; s4 < 16; ++s4) asm volatile("" : "=v"(wf[s4]));
  const int qt = blockIdx.x >> 6;
#pragma unroll 1
  for (int seg = 0; seg < 4; ++seg) {
    char* wsl = p.ws; int wl = w; u32 bl = blockIdx.x;
    if (seg != 0) asm volatile("" : "+s"(wsl), "+s"(wl), "+s"(bl));
    if (seg == 1) {
      ASTAMP(27)
      if (!kh) {
        PREP_ST()
        const u32 o = ((wl >> 1) & 1) * 69632 + 40960 + (wl & 1) * 14336;
        dma4s(wsl + WS_KV + o, vo, o); dma4s(wsl + WS_KV + o + 4096, vo, o + 4096); dma4s(wsl + WS_KV + o + 8192, vo, o + 8192);
        set_m0(o + 12288); dma1<0>(wsl + WS_KV + o + 12288, vo); dma1<1024>(wsl + WS_KV + o + 12288, vo);
        WAITV(14);
      } else {
        WAITV(0);
      }
      ASTAMP(10)
      BAR();
      ASTAMP(24)
      if (w == 0 && l < 8) put_flag(wsl + WS_FP + l * 1024 + bl * 4, tag);
    } else if (seg == 2) {
      ASTAMP(21)
      if (w == 0) {
        u32x4 f;
#pragma unroll 1
        for (int it = 0; it < SPIN_CAP; ++it) {
          asm volatile("global_load_dwordx4 %0, %1, %2 sc1\n\ts_waitcnt vmcnt(0)" : "=&v"(f) : "v"(vo), "s"(wsl + WS_FP + (bl & 7) * 1024) : "memory");
          if (__all(f[0] == tag && f[1] == tag && f[2] == tag && f[3] == tag)) break;
          __builtin_amdgcn_s_sleep(1);
        }
        if (bl == 0 && l == 0) put_flag(wsl + WS_EPOCH, epoch + 1u);
      }
      WAITV_BAR(0);
      ASTAMP(23)
    } else if (seg == 3) {
      ASTAMP(22)
      if (!kh) {
        dma4s((wsl + WS_WGU + (size_t)(((bl & 1) * 8 + ((bl >> 3) & 7)) * 7) * 32768) + wl * 8192, vo, SL0 + wl * 8192); dma4s((wsl + WS_WGU + (size_t)(((bl & 1) * 8 + ((bl >> 3) & 7)) * 7) * 32768) + wl * 8192 + 4096, vo, SL0 + wl * 8192 + 4096);
        dma4s((wsl + WS_WGU + (size_t)(((bl & 1) * 8 + ((bl >> 3) & 7)) * 7) * 32768) + 32768 + wl * 8192, vo, SL1 + wl * 8192); dma4s((wsl + WS_WGU + (size_t)(((bl & 1) * 8 + ((bl >> 3) & 7)) * 7) * 32768) + 32768 + wl * 8192 + 4096, vo, SL1 + wl * 8192 + 4096);
        set_m0(JUNK);
        touch8k((wsl + WS_WGU + (size_t)(((bl & 1) * 8 + ((bl >> 3) & 7)) * 7) * 32768) + 65536 + (bl >> 6) * 40960 + wl * 8192, l * 128);
        if (w < 2) touch8k(w == 0 ? (wsl + WS_WGU + (size_t)(((bl & 1) * 8 + ((bl >> 3) & 7)) * 7) * 32768) + 65536 + (bl >> 6) * 40960 + 32768 : wsl + WS_AUG + (size_t)((bl & 1) * 8 + ((bl >> 3) & 7)) * 7168, l * 128);
      }
    }
    LDWF((seg ^ 2) | (kh ^ 1));
    TILE(0, kA, kB, vA0, vA1, vB0, vB1) TILE(1, kB, kA, vB0, vB1, vA0, vA1) TILE(2, kA, kB, vA0, vA1, vB0, vB1) TILE(3, kB, kA, vB0, vB1, vA0, vA1)
    TILE(4, kA, kB, vA0, vA1, vB0, vB1) TILE(5, kB, kA, vB0, vB1, vA0, vA1) TILE(6, kA, kB, vA0, vA1, vB0, vB1) TILE(7, kB, kA, vB0, vB1, vA0, vA1)
    ka += 8 * TS; va += 8 * TS; PIN(ka); PIN(va);
  }
  o1 = mfma(vB0, pb0, o1); o1 = mfma(vB1, pb1, o1);
  LDWF(kh);
  if (!kh) {
    set_m0(JUNK);
    touch8k(p.ws + WS_WD + (size_t)(2 * j) * 114688 + qt * 57344 + w * 8192, l * 128);
    if (w < 3) touch8k(p.ws + WS_WD + (size_t)(2 * j) * 114688 + qt * 57344 + (w + 4) * 8192, l * 128);
  }
#undef HALF
#undef TILE
  STAMP(2)
  const u32 xo = (u32)((l & 31) * DM + 4 * h) * 2;
  const char* xres = (const char*)(p.x + (size_t)(64 * g + 32 * tt) * DM + 64 * j + 32 * nt + 16 * kh);
  u32x2 xa = gld8_s<0>(xres, xo), xb = gld8_s<16>(xres, xo);
  u32x4 ga = gld_s<1, 0>(modr + 4096 + (64 * j + 32 * nt + 16 * kh) * 4, h * 16), gb = gld_s<1, 32>(modr + 4096 + (64 * j + 32 * nt + 16 * kh) * 4, h * 16);
  {
    const u32 xs = OEX + w * 2304, xr = OEX + (w ^ 4) * 2304;
    float o[9];
    if (kh) { stsf4(xs + vo, f32x4{o0[0], o0[1], o0[2], o0[3]}); stsf4(xs + 1024 + vo, f32x4{o0[4], o0[5], o0[6], o0[7]}); *(LAS float*)(uintptr_t)(xs + 2048 + l * 4) = o0[8];
#pragma unroll
      for (int e = 0; e < 9; ++e) o[e] = o1[e];
    } else { stsf4(xs + vo, f32x4{o1[0], o1[1], o1[2], o1[3]}); stsf4(xs + 1024 + vo, f32x4{o1[4], o1[5], o1[6], o1[7]}); *(LAS float*)(uintptr_t)(xs + 2048 + l * 4) = o1[8];
#pragma unroll
      for (int e = 0; e < 9; ++e) o[e] = o0[e];
    }
    BAR();
    const f32x4 r0 = ldsf4(xr + vo), r1 = ldsf4(xr + 1024 + vo);
#pragma unroll
    for (int e = 0; e < 4; ++e) { o[e] += r0[e]; o[4 + e] += r1[e]; }
    float ls = lowhalf(o[8] + *(const LAS float*)(uintptr_t)(xr + 2048 + l * 4));
    bool bad = !(ls < 1.2676506e30f);
#pragma unroll
    for (int e = 0; e < 8; ++e) bad |= !(fabsf(o[e]) < 3.0e38f);
    if (__builtin_expect(__any(bad), 0)) {
      const u32x4 qf = kh ? q1 : q0;
      float m = -INFINITY;
      f32x16 oo = {};
#pragma unroll 1
      for (int t = 0; t < 64; ++t) {
        u32x4 kf, v0, v1;
        asm volatile("global_load_dwordx4 %0, %3, %5 sc1\n\tglobal_load_dwordx4 %1, %4, %5 sc1\n\tglobal_load_dwordx4 %2, %4, %5 offset:576 sc1\n\ts_waitcnt vmcnt(0)"
                     : "=&v"(kf), "=&v"(v0), "=&v"(v1) : "v"(vo), "v"(vrow), "s"(p.ws + WS_KV + t * TS) : "memory");
        const f32x16 s = mfma(kf, qf, f32x16{});
        const float mn = fmaxf(m, rowmax16(s)), al = EXP2(m - mn);
        m = mn;
        u32x4 pa, pb;
#pragma unroll
        for (int r = 0; r < 4; ++r) { pa[r] = pk(EXP2(s[2 * r] - mn), EXP2(s[2 * r + 1] - mn)); pb[r] = pk(EXP2(s[8 + 2 * r] - mn), EXP2(s[9 + 2 * r] - mn)); }
#pragma unroll
        for (int r = 0; r < 16; ++r) oo[r] *= al;
        oo = mfma(v0, pa, oo); oo = mfma(v1, pb, oo);
      }
#pragma unroll
      for (int e = 0; e < 8; ++e) o[e] = oo[e];
      ls = lowhalf(oo[8]);
    }
    const float rl = __builtin_amdgcn_rcpf(ls);
    gst16_sc1(p.ws + WS_O + fo + kh * 1024, vo, u32x4{pk(o[0] * rl, o[1] * rl), pk(o[2] * rl, o[3] * rl), pk(o[4] * rl, o[5] * rl), pk(o[6] * rl, o[7] * rl)});
  }
  asm volatile("s_waitcnt vmcnt(0)\n\ts_barrier"
               : "+v"(wf[0]), "+v"(wf[1]), "+v"(wf[2]), "+v"(wf[3]), "+v"(wf[4]), "+v"(wf[5]), "+v"(wf[6]), "+v"(wf[7]), "+v"(wf[8]), "+v"(wf[9]),
                 "+v"(wf[10]), "+v"(wf[11]), "+v"(wf[12]), "+v"(wf[13]), "+v"(wf[14]), "+v"(wf[15]), "+v"(xa), "+v"(xb), "+v"(ga), "+v"(gb)::"memory");
  STAMP(3)
  if (w == 1 && l == 0) put_flag(p.ws + WS_FO + g * 512 + fofs<4>(j), tag);
  if (w == 0) poll_flags<8, 4>(p.ws + WS_FO + g * 512, tag, l);
  BAR();
  STAMP(4)
  u32x4 x1;
  {
    const char* osrc = p.ws + WS_O + ((size_t)(2 * g + tt) * 32 + 8 * kq) * 1024;
    u32x4 xf[8];
    xf[0] = gld_s<1, 0>(osrc, vo); xf[1] = gld_s<1, 1024>(osrc, vo); xf[2] = gld_s<1, 2048>(osrc, vo); xf[3] = gld_s<1, 3072>(osrc, vo);
    xf[4] = gld_s<1, 0>(osrc + 4096, vo); xf[5] = gld_s<1, 1024>(osrc + 4096, vo); xf[6] = gld_s<1, 2048>(osrc + 4096, vo); xf[7] = gld_s<1, 3072>(osrc + 4096, vo);
    f32x16 acc0 = {}, acc1 = {};
#define P3_STEP(S) { waitv_pin<7 - (S)>(xf[S]); acc0 = mfma(wf[S], xf[S], acc0); acc1 = mfma(wf[8 + (S)], xf[S], acc1); }
    P3_STEP(0) P3_STEP(1) P3_STEP(2) P3_STEP(3) P3_STEP(4) P3_STEP(5) P3_STEP(6) P3_STEP(7)
#undef P3_STEP
    STAMP(5)
    float r[8];
    {
      const u32 d = SL3 + tt * 4096 + kq * 1024 + vo;
      sts128(d, pack8(acc0, 0)); sts128(d + 8192, pack8(acc1, 0)); sts128(d + 16384, pack8(acc0, 8)); sts128(d + 24576, pack8(acc1, 8));
      BAR();
      sum_pieces<4>(SL3 + w * 4096 + vo, r);
    }
    STAMP(6)
    x1 = u32x4{pk(fmaf(bflo(ga[0]), r[0], bflo(xa[0])), fmaf(bfhi(ga[0]), r[1], bfhi(xa[0]))), pk(fmaf(bflo(ga[2]), r[2], bflo(xa[1])), fmaf(bfhi(ga[2]), r[3], bfhi(xa[1]))),
               pk(fmaf(bflo(gb[0]), r[4], bflo(xb[0])), fmaf(bfhi(gb[0]), r[5], bfhi(xb[0]))), pk(fmaf(bflo(gb[2]), r[6], bflo(xb[1])), fmaf(bfhi(gb[2]), r[7], bfhi(xb[1])))};
    gst16_sc1(p.ws + WS_X1 + fo + kh * 1024, vo, x1);
    dma4s(wgu + 2 * 32768 + w * 4096, vo, SL2 + w * 4096);
    float s1 = 0.f, s2 = 0.f;
#pragma unroll
    for (int e = 0; e < 4; ++e) { const u32 v = x1[e]; s1 = dot2(v, 0x3f803f80u, s1); s2 = dot2(v, v, s2); }
    s1 = swap32_sum(s1); s2 = swap32_sum(s2);
    if (l < 32) *(LAS f32x2*)(uintptr_t)(GAPA + kq * 512 + tt * 256 + l * 8) = f32x2{s1, s2};
    WAITV_BAR(4);
    STAMP(8)
    if (w == 0 && l == 0) put_flag(p.ws + WS_FX + (g >> 1) * 512 + fofs<8>((g & 1) * 8 + j), tag);
    if (w == 7) {
      f32x2 s = {0.f, 0.f};
#pragma unroll
      for (int i = 0; i < 4; ++i) s += ldsf2(GAPA + i * 512 + l * 8);
      gst8_sc1(p.ws + WS_STAT + ((size_t)64 * g * 8 + j) * 8, l * 64, u32x2{__float_as_uint(s[0]), __float_as_uint(s[1])});
      WAITV(0);
      if (l == 0) put_flag(p.ws + WS_FS + (g >> 1) * 512 + fofs<8>((g & 1) * 8 + j), tag);
    }
  }
  u32x4 wd[28];
  if (kh == 0) p4_compute(p, id, l, w, tag, wd STAMP_PASS); else p4_helper(p, id, l, w, tag, wd STAMP_PASS);
  {
    int ly = l; PIN(ly);
    const char* xsrc = p.ws + WS_ACT + ((size_t)(2 * g) * 112 + 14 * w) * 1024;
    u32x4 xf[16];
#define XL(N) (xf[(N) % 16] = gld_s<1, 0>(xsrc + dks((N) >> 1) * 1024 + ((N) & 1) * 114688, vo))
    if (!kh) {
      WAITV(0);
      PSTAMP(19)
      if (ly == 0 && __hip_atomic_fetch_add((LAS u32*)(uintptr_t)GAPA, 1u, __ATOMIC_RELAXED, __HIP_MEMORY_SCOPE_WORKGROUP) == 3u)
        put_flag(p.ws + WS_FA + id.G * 512 + fofs<8>(id.c), tag);
      BAR();
    }
    STAMP(18)
    XL(0); XL(1); XL(2); XL(3); XL(4); XL(5); XL(6); XL(7); XL(8); XL(9); XL(10); XL(11);
    u32x4 g0 = gld_s<1, 0>(modr + 10240 + (64 * j + 32 * nt + 16 * kh) * 4, (ly >> 5) * 16), g1 = gld_s<1, 32>(modr + 10240 + (64 * j + 32 * nt + 16 * kh) * 4, (ly >> 5) * 16);
    if (kh) asm volatile("s_barrier" ::: "memory");
    f32x16 acc[4];
#define K5_X(N, CNT)                                                                                 \
  {                                                                                                  \
    waitv_pin<CNT>(xf[(N) % 16]);                                                                    \
    if ((N) < 2) { mf0(acc[(N) & 1], wd[0], xf[(N) & 1]); mf0(acc[2 + ((N) & 1)], wd[14], xf[(N) & 1]); } \
    else { mfa(acc[(N) & 1], wd[dks((N) >> 1)], xf[(N) % 16]); mfa(acc[2 + ((N) & 1)], wd[14 + dks((N) >> 1)], xf[(N) % 16]); } \
  }
    K5_X(0, 13) K5_X(1, 12) K5_X(2, 11) K5_X(3, 10) K5_X(4, 9) K5_X(5, 8) K5_X(6, 7) K5_X(7, 6) K5_X(8, 5) K5_X(9, 4) K5_X(10, 3) K5_X(11, 2)
    PSTAMP(24)
    if (w == 4) poll_flags<16, 8>(p.ws + WS_FA + (g >> 1) * 512, tag, ly);
    asm volatile("s_barrier" ::: "memory");
    PSTAMP(25)
    XL(12); XL(13); XL(14); XL(15); XL(16); XL(17); XL(18); XL(19); XL(20); XL(21); XL(22); XL(23); XL(24); XL(25); XL(26); XL(27);
    K5_X(12, 15) K5_X(13, 14) K5_X(14, 13) K5_X(15, 12) K5_X(16, 11) K5_X(17, 10) K5_X(18, 9) K5_X(19, 8) K5_X(20, 7) K5_X(21, 6) K5_X(22, 5) K5_X(23, 4)
    K5_X(24, 3) K5_X(25, 2) K5_X(26, 1) K5_X(27, 0)
#undef K5_X
    MFMA_SETTLE();
    PIN(g0); PIN(g1); PIN(acc[0]); PIN(acc[1]); PIN(acc[2]); PIN(acc[3]);
    STAMP(20)
    float r[8];
    {
      const u32 d = w * 1024 + vo;
#pragma unroll
      for (int pc = 0; pc < 8; ++pc) sts128(d + pc * 8192, pack8(acc[pc & 3], (pc >> 2) * 8));
      PSTAMP(26)
      BAR();
      PSTAMP(27)
      sum_pieces<8>(w * 8192 + vo, r);
    }
    const u32 a0 = pk(fmaf(bflo(g0[0]), r[0], bflo(x1[0])), fmaf(bfhi(g0[0]), r[1], bfhi(x1[0]))), a1 = pk(fmaf(bflo(g0[2]), r[2], bflo(x1[1])), fmaf(bfhi(g0[2]), r[3], bfhi(x1[1])));
    const u32 b0 = pk(fmaf(bflo(g1[0]), r[4], bflo(x1[2])), fmaf(bfhi(g1[0]), r[5], bfhi(x1[2]))), b1 = pk(fmaf(bflo(g1[2]), r[6], bflo(x1[3])), fmaf(bfhi(g1[2]), r[7], bfhi(x1[3])));
    const auto s0 = __builtin_amdgcn_permlane32_swap(a0, b0, false, false);
    const auto s1 = __builtin_amdgcn_permlane32_swap(a1, b1, false, false);
    int lz = l; PIN(lz);
    *(u32x4*)(p.out + (size_t)(64 * g + 32 * tt + (lz & 31)) * DM + 64 * j + 32 * nt + 16 * kh + 8 * (lz >> 5)) = u32x4{s0[0], s1[0], s0[1], s1[1]};
  }
  STAMP_FLUSH(0)
}

extern "C" void kernel_launch(void* const* d_in, const int* in_sizes, int n_in, void* d_out, int out_size, void* d_ws, size_t ws_size, hipStream_t stream) {
  Params p;
  p.x = (const u16*)d_in[0]; p.wq = (const u16*)d_in[1]; p.wk = (const u16*)d_in[2]; p.wv = (const u16*)d_in[3]; p.wo = (const u16*)d_in[4];
  p.wg = (const u16*)d_in[6]; p.wu = (const u16*)d_in[7]; p.wd = (const u16*)d_in[8]; p.c = (const u16*)d_in[9]; p.wm = (const u16*)d_in[10];
  p.out = (u16*)d_out; p.ws = (char*)d_ws;
  (void)hipFuncSetAttribute((const void*)k_main, hipFuncAttributeMaxDynamicSharedMemorySize, M_LDS);
  k_main<<<256, 512, M_LDS, stream>>>(p);
}
```

```cpp
#include <hip/hip_runtime.h>
#include <stdint.h>

typedef unsigned short u16;
typedef unsigned int u32;
typedef __attribute__((ext_vector_type(8))) __bf16 bf16x8;
typedef __attribute__((ext_vector_type(2))) __bf16 bf16x2;
typedef __attribute__((ext_vector_type(2))) float f32x2;
typedef __attribute__((ext_vector_type(4))) float f32x4;
typedef __attribute__((ext_vector_type(16))) float f32x16;
typedef __attribute__((ext_vector_type(4))) u32 u32x4;
typedef __attribute__((ext_vector_type(2))) u32 u32x2;
typedef __attribute__((ext_vector_type(4))) short s16x4;
#define LAS __attribute__((address_space(3)))
#define GAS __attribute__((address_space(1)))
#define DI __device__ __forceinline__

constexpr int DM = 512, FF = 1792;
constexpr float EPS = 1e-5f;
constexpr float C2 = 0.25f * 1.4426950408889634f;

constexpr size_t WS_EPOCH = 0;
constexpr size_t WS_FO = 4096;
constexpr size_t WS_FX = WS_FO + 16384;
constexpr size_t WS_FS = WS_FX + 8192;
constexpr size_t WS_FE = WS_FS + 8192;
constexpr size_t WS_FA = WS_FE + 8192;
constexpr size_t WS_FKV = WS_FA + 8192;
constexpr size_t WS_FP = WS_FKV + 8192;
constexpr size_t WS_MOD = 81920;
constexpr size_t WS_KV = WS_MOD + 8 * 12288;
constexpr size_t WS_STAT = WS_KV + 139264;
constexpr size_t WS_AUG = WS_STAT + 131072;
constexpr size_t WS_WO = 1u << 20;
constexpr size_t WS_WD = WS_WO + (512u << 10);
constexpr size_t WS_WGU = WS_WD + (1792u << 10);
constexpr size_t WS_O = WS_WGU + (3584u << 10);
constexpr size_t WS_X1 = WS_O + (2u << 20);
constexpr size_t WS_ACT = WS_X1 + (2u << 20);
static_assert(WS_AUG + 229376 <= WS_WO, "workspace map");
constexpr int TS = 2176;

struct Params {
  const u16 *x, *wq, *wk, *wv, *wo, *wg, *wu, *wd, *c, *wm;
  u16* out;
  char* ws;
};

DI f32x16 mfma(u32x4 a, u32x4 b, f32x16 c) {
  return __builtin_amdgcn_mfma_f32_32x32x16_bf16(__builtin_bit_cast(bf16x8, a), __builtin_bit_cast(bf16x8, b), c, 0, 0, 0);
}
DI u32 pk(float lo, float hi) {
  f32x2 v = {lo, hi};
  return __builtin_bit_cast(u32, __builtin_convertvector(v, bf16x2));
}
DI float bflo(u32 u) { return __uint_as_float(u << 16); }
DI float bfhi(u32 u) { return __uint_as_float(u & 0xffff0000u); }
DI float rbf(float f) { return bflo(pk(f, 0.f)); }
DI float dot2(u32 a, u32 b, float c) { return __builtin_amdgcn_fdot2_f32_bf16(__builtin_bit_cast(bf16x2, a), __builtin_bit_cast(bf16x2, b), c, false); }
DI void set_m0(u32 lds) { asm volatile("s_mov_b32 m0, %0\n\ts_nop 0" ::"s"(lds) : "memory"); }
template <int OFF> DI void dma(const void* base, u32 vo) { asm volatile("global_load_lds_dwordx4 %0, %1 offset:%2" ::"v"(vo), "s"(base), "n"(OFF) : "memory"); }
template <int OFF> DI void dma1(const void* base, u32 vo) { asm volatile("global_load_lds_dwordx4 %0, %1 offset:%2 sc1" ::"v"(vo), "s"(base), "n"(OFF) : "memory"); }
DI void dma4(const void* base, u32 vo, u32 lds) { set_m0(lds); dma<0>(base, vo); dma<1024>(base, vo); dma<2048>(base, vo); dma<3072>(base, vo); }
DI void dma4s(const void* base, u32 vo, u32 lds) { set_m0(lds); dma1<0>(base, vo); dma1<1024>(base, vo); dma1<2048>(base, vo); dma1<3072>(base, vo); }
template <int SC, int OFF> DI u32x4 gld_s(const void* base, u32 vo) {
  u32x4 r;
  if (SC) asm volatile("global_load_dwordx4 %0, %1, %2 offset:%3 sc1" : "=v"(r) : "v"(vo), "s"(base), "n"(OFF) : "memory");
  else asm volatile("global_load_dwordx4 %0, %1, %2 offset:%3" : "=v"(r) : "v"(vo), "s"(base), "n"(OFF) : "memory");
  return r;
}
template <int OFF> DI u32x2 gld8_s(const void* base, u32 vo) { u32x2 r; asm volatile("global_load_dwordx2 %0, %1, %2 offset:%3" : "=v"(r) : "v"(vo), "s"(base), "n"(OFF) : "memory"); return r; }
template <int OFF> DI u32 gld2_s(const void* base, u32 vo) { u32 r; asm volatile("global_load_ushort %0, %1, %2 offset:%3" : "=v"(r) : "v"(vo), "s"(base), "n"(OFF) : "memory"); return r; }
DI void touch8k(const char* base, u32 l128) { asm volatile("global_load_lds_dword %0, %1 sc1" ::"v"(l128), "s"(base) : "memory"); }
DI void gst16_sc1(char* base, u32 vo, u32x4 v) { asm volatile("global_store_dwordx4 %0, %1, %2 sc1\n\ts_nop 1" ::"v"(vo), "v"(v), "s"(base) : "memory"); }
DI void gst8_sc1(char* base, u32 vo, u32x2 v) { asm volatile("global_store_dwordx2 %0, %1, %2 sc1\n\ts_nop 1" ::"v"(vo), "v"(v), "s"(base) : "memory"); }
#define RLX_AGENT __ATOMIC_RELAXED, __HIP_MEMORY_SCOPE_AGENT
DI void put_flag(char* f, u32 tag) { __hip_atomic_store((GAS u32*)f, tag, RLX_AGENT); }
#define SLP 16
constexpr int SPIN_CAP = 1 << 20;
template <int PER> DI u32 fofs(int i) { return (u32)((i / PER) * 256 + (i % PER) * 4); }
template <int N, int PER, int NAP = 0> DI void poll_flags(const char* f, u32 tag, int l) {
  const char* a = f + fofs<PER>(l < N ? l : 0);
#pragma unroll 1
  for (int it = 0; it < SPIN_CAP; ++it) {
    const u32 v = __hip_atomic_load((const GAS u32*)a, RLX_AGENT);
    if (__all(v == tag)) break;
    if (NAP) __builtin_amdgcn_s_sleep(NAP);
  }
  __builtin_amdgcn_fence(__ATOMIC_ACQUIRE, "wavefront");
}
#define SB() __builtin_amdgcn_sched_barrier(0)
#define PIN(x) asm volatile("" : "+v"(x))
#define WAITV(N) asm volatile("s_waitcnt vmcnt(" #N ")" ::: "memory")
#define WAITV_BAR(N) asm volatile("s_waitcnt vmcnt(" #N ") lgkmcnt(0)\n\ts_barrier" ::: "memory")
#define BAR() asm volatile("s_waitcnt lgkmcnt(0)\n\ts_barrier" ::: "memory")
template <int N> DI void waitv_pin(u32x4& v) { asm volatile("s_waitcnt vmcnt(%1)" : "+v"(v) : "n"(N) : "memory"); }
DI u32x4 lds128(u32 a) { return *(const LAS u32x4*)(uintptr_t)a; }
DI f32x4 ldsf4(u32 a) { return *(const LAS f32x4*)(uintptr_t)a; }
DI f32x2 ldsf2(u32 a) { return *(const LAS f32x2*)(uintptr_t)a; }
DI void sts128(u32 a, u32x4 v) { *(LAS u32x4*)(uintptr_t)a = v; }
DI void stsf4(u32 a, f32x4 v) { *(LAS f32x4*)(uintptr_t)a = v; }
DI u32x2 ldstr(u32 a) { return __builtin_bit_cast(u32x2, __builtin_amdgcn_ds_read_tr16_b64_v4i16((LAS s16x4*)(uintptr_t)a)); }
DI u32x4 ldstr2(u32 a0, u32 a1) { u32x2 p = ldstr(a0), q = ldstr(a1); return u32x4{p[0], p[1], q[0], q[1]}; }
template <int CTRL> DI float dpp(float v) { return __int_as_float(__builtin_amdgcn_mov_dpp(__float_as_int(v), CTRL, 0xf, 0xf, true)); }
DI float swap32_sum(float v) { auto r = __builtin_amdgcn_permlane32_swap(__float_as_uint(v), __float_as_uint(v), false, false); return __uint_as_float(r[0]) + __uint_as_float(r[1]); }
DI float swap32_max(float v) { auto r = __builtin_amdgcn_permlane32_swap(__float_as_uint(v), __float_as_uint(v), false, false); return fmaxf(__uint_as_float(r[0]), __uint_as_float(r[1])); }
DI float swap16_sum(float v) { auto r = __builtin_amdgcn_permlane16_swap(__float_as_uint(v), __float_as_uint(v), false, false); return __uint_as_float(r[0]) + __uint_as_float(r[1]); }
DI float lowhalf(float v) { auto r = __builtin_amdgcn_permlane32_swap(__float_as_uint(v), __float_as_uint(v), false, false); return __uint_as_float(r[0]); }
template <int CTRL> DI float halve(bool bit, float a, float b) { return (bit ? b : a) + dpp<CTRL>(bit ? a : b); }
DI int rfl(int v) { return __builtin_amdgcn_readfirstlane(v); }
#define EXP2(v) __builtin_amdgcn_exp2f(v)

struct Ids { int G, c, g, j; };
DI Ids ids() { const int b = blockIdx.x, x = b & 7, i = b >> 3; Ids r; r.G = (x >> 1) * 4 + (i >> 3); r.c = (x & 1) * 8 + (i & 7); r.g = 4 * x + (i >> 3); r.j = i & 7; return r; }

#define STAMP_DECL
#define STAMP(I)
#define STAMP_FLUSH(K)
#define STAMP_ARG
#define STAMP_PASS

#define PSTAMP(I)
#define ASTAMP(I)
constexpr u32 M_LDS = 163840;
constexpr u32 F_A = 0;
constexpr u32 F_H = 65536;
constexpr u32 F_B = 131072;
constexpr u32 F_D = F_B + 16640;
constexpr u32 F_M = F_D + 10240;
static_assert(F_M + 4608 <= M_LDS, "front-end LDS map");
constexpr u32 SL0 = 0, SL1 = 69632, SL2 = 32768, SL3 = 102400, TOPA = 135168, GAPA = 65536, OEX = 143616, JUNK = M_LDS - 256;

DI void mfa(f32x16& c, u32x4 a, u32x4 b) { asm volatile("v_mfma_f32_32x32x16_bf16 %0, %1, %2, %0" : "+v"(c) : "v"(a), "v"(b) : "memory"); }
DI void mf0(f32x16& c, u32x4 a, u32x4 b) { asm volatile("v_mfma_f32_32x32x16_bf16 %0, %1, %2, 0" : "=&v"(c) : "v"(a), "v"(b) : "memory"); }
DI float vexp(float a) { float r; asm volatile("v_exp_f32 %0, %1" : "=v"(r) : "v"(a)); return r; }
DI float vrcp(float a) { float r; asm volatile("v_rcp_f32 %0, %1" : "=v"(r) : "v"(a)); return r; }
DI u32 vcvt(float a, float b) { u32 r; asm volatile("v_cvt_pk_bf16_f32 %0, %1, %2" : "=v"(r) : "v"(a), "v"(b)); return r; }
DI float vadd(float a, float b) { float r; asm volatile("v_add_f32 %0, %1, %2" : "=v"(r) : "v"(a), "v"(b)); return r; }
DI float vmul(float a, float b) { float r; asm volatile("v_mul_f32 %0, %1, %2" : "=v"(r) : "v"(a), "v"(b)); return r; }
DI float vfma_k(float a, float k, float c) { float r; asm volatile("v_fma_f32 %0, %1, %2, %3" : "=v"(r) : "v"(a), "s"(k), "v"(c)); return r; }
DI float vadd1(float a) { float r; asm volatile("v_add_f32 %0, 1.0, %1" : "=v"(r) : "v"(a)); return r; }
DI void vdotk(float& c, u32 a, u32 k) { asm volatile("v_dot2c_f32_bf16 %0, %2, %1" : "+v"(c) : "v"(a), "s"(k)); }
DI void vdot(float& c, u32 a) { asm volatile("v_dot2c_f32_bf16 %0, %1, %1" : "+v"(c) : "v"(a)); }
#define MFMA_SETTLE() asm volatile("s_nop 15\n\ts_nop 3" ::: "memory")
DI float rowmax16(const f32x16& s) {
  float m = fmaxf(fmaxf(s[0], s[1]), s[2]);
#pragma unroll
  for (int r = 3; r < 15; r += 2) m = fmaxf(fmaxf(m, s[r]), s[r + 1]);
  return swap32_max(fmaxf(m, s[15]));
}
constexpr float GC0 = -2.f * 0.7978845608028654f * 1.4426950408889634f, GC1 = GC0 * 0.044715f;
template <u32 A> DI u32x4 ldsr(u32 lo, u32 mid) { return A < 65536 ? lds128(lo + A) : lds128(mid + (A - 65536)); }

DI u32x4 pack8(const f32x16& a, int o8) { return u32x4{pk(a[o8], a[o8 + 1]), pk(a[o8 + 2], a[o8 + 3]), pk(a[o8 + 4], a[o8 + 5]), pk(a[o8 + 6], a[o8 + 7])}; }
template <int N> DI void sum_pieces(u32 a, float (&r)[8]) {
  u32 klo = 0x00003f80u, khi = 0x3f800000u;
  asm volatile("" : "+s"(klo), "+s"(khi));
  u32x4 u[N];
#pragma unroll
  for (int i = 0; i < N; ++i) u[i] = lds128(a + i * 1024);
#pragma unroll
  for (int e = 0; e < 4; ++e) { r[2 * e] = bflo(u[0][e]); r[2 * e + 1] = bfhi(u[0][e]); }
#pragma unroll
  for (int i = 1; i < N; ++i)
#pragma unroll
    for (int e = 0; e < 4; ++e) { const u32 v = u[i][e]; r[2 * e] = dot2(v, klo, r[2 * e]); r[2 * e + 1] = dot2(v, khi, r[2 * e + 1]); }
}

constexpr u32 SL4 = 0xffffffffu;
template <u32 SL, int S> DI u32x4 wrd(u32 lo, u32 mid, u32 hi) {
  constexpr u32 A = SL == SL4 ? (S < 28 ? TOPA + S * 1024 : GAPA + (S - 28) * 1024) : SL + S * 1024;
  return A < 65536 ? lds128(lo + A) : (A < 131072 ? lds128(mid + (A - 65536)) : lds128(hi + (A - 131072)));
}
DI constexpr int dks(int i) { return i < 6 ? 7 * (i / 3) + i % 3 : 7 * ((i - 6) >> 2) + 3 + ((i - 6) & 3); }

DI void p4_compute(const Params& p, const Ids& id, int l, int w, u32 tag, u32x4 (&wd)[28] STAMP_ARG) {
  const u32 vo = l * 16;
  char* adst = p.ws + WS_ACT + ((size_t)(4 * id.G + w) * 112 + 7 * id.c) * 1024;
  const char* asrc = p.ws + WS_AUG + (size_t)id.c * 7168;
  u32x4 aug[7];
  aug[0] = gld_s<1, 0>(asrc, vo); aug[1] = gld_s<1, 1024>(asrc, vo); aug[2] = gld_s<1, 2048>(asrc, vo);
  if (w == 0) poll_flags<16, 8>(p.ws + WS_FX + id.G * 512, tag, l);
  asm volatile("s_waitcnt vmcnt(0)\n\ts_barrier" : "+v"(aug[0]), "+v"(aug[1]), "+v"(aug[2])::"memory");
  STAMP(9)
  const char* xsrc = p.ws + WS_X1 + (size_t)(4 * id.G + w) * 32768;
  u32x4 xf[32];
#define XLD(S) (xf[S] = gld_s<1, ((S) & 3) * 1024>(xsrc + ((S) >> 2) * 4096, vo))
  XLD(0); XLD(1); XLD(2); XLD(3); XLD(4); XLD(5); XLD(6); XLD(7); XLD(8); XLD(9); XLD(10); XLD(11); XLD(12); XLD(13); XLD(14); XLD(15);
  float rs, gc0 = GC0; u32x4 xaug;
  u32 lo = vo, mid = vo + 65536, hi = vo + 131072;
  PIN(lo); PIN(mid); PIN(hi); PIN(gc0);
  f32x16 acc[3]; u32x4 ra[32], rb[32];
  ra[0] = wrd<SL0, 0>(lo, mid, hi); rb[0] = wrd<SL1, 0>(lo, mid, hi); ra[1] = wrd<SL0, 1>(lo, mid, hi); rb[1] = wrd<SL1, 1>(lo, mid, hi);
  ra[2] = wrd<SL0, 2>(lo, mid, hi); rb[2] = wrd<SL1, 2>(lo, mid, hi);
#define PH0(S) { constexpr int s = (S);                                                                            \
      waitv_pin<(s < 16 ? 15 : 31 - s)>(xf[s]);                                                                    \
      if (s == 0) mf0(acc[0], ra[0], xf[0]); else mfa(acc[0], ra[s], xf[s]);                                       \
      if (s + 16 < 32) XLD((s + 16) & 31);                                                                         \
      if (s + 3 < 32) { ra[(s + 3) & 31] = wrd<SL0, (s + 3) & 31>(lo, mid, hi); rb[(s + 3) & 31] = wrd<SL1, (s + 3) & 31>(lo, mid, hi); } \
      if (s == 0) mf0(acc[1], rb[0], xf[0]); else mfa(acc[1], rb[s], xf[s]); }
 PH0(0) PH0(1) PH0(2) PH0(3) PH0(4) PH0(5) PH0(6) PH0(7) \
 PH0(8) PH0(9) PH0(10) PH0(11) PH0(12) PH0(13) PH0(14) PH0(15) \
 PH0(16) PH0(17) PH0(18) PH0(19) PH0(20) PH0(21) PH0(22) PH0(23) \
 PH0(24) PH0(25) PH0(26) PH0(27) PH0(28) PH0(29) PH0(30) PH0(31)
#undef PH0
  ra[0] = wrd<SL2, 0>(lo, mid, hi); ra[1] = wrd<SL2, 1>(lo, mid, hi); ra[2] = wrd<SL2, 2>(lo, mid, hi); ra[3] = wrd<SL2, 3>(lo, mid, hi);
  STAMP(11)
  asm volatile("s_barrier" ::: "memory");
  xaug = lds128(GAPA + w * 512 + (l & 31) * 16);
  rs = *(const LAS float*)(uintptr_t)(GAPA + 2048 + w * 128 + (l & 31) * 4);
  f32x4 gv, uv, zz; u32x4 q;
#define E4(F) { _Pragma("unroll") for (int e = 0; e < 4; ++e) { F; } }
#define E2(B, F) { _Pragma("unroll") for (int e = B; e < B + 2; ++e) { F; } }
#define UNIT(T, U) { constexpr int u = (U) % 13, GO = ((U) / 13) * 8; const f32x16& k_ = acc[(T) % 3];              \
      if (u == 0) E4(gv[e] = vmul(k_[GO + e], rs))                                                                  \
      if (u == 1) E4(zz[e] = vmul(gv[e], gv[e]))                                                                    \
      if (u == 2) E4(zz[e] = vfma_k(zz[e], GC1, gc0))                                                               \
      if (u == 3) E4(zz[e] = vmul(zz[e], gv[e]))                                                                    \
      if (u == 4) E2(0, zz[e] = vexp(zz[e]))                                                                        \
      if (u == 5) E2(2, zz[e] = vexp(zz[e]))                                                                        \
      if (u == 6) E4(uv[e] = vmul(k_[GO + 4 + e], rs))                                                              \
      if (u == 7) E4(zz[e] = vadd1(zz[e]))                                                                          \
      if (u == 8) E2(0, zz[e] = vrcp(zz[e]))                                                                        \
      if (u == 9) E2(2, zz[e] = vrcp(zz[e]))                                                                        \
      if (u == 10) E4(uv[e] = vmul(uv[e], gv[e]))                                                                   \
      if (u == 11) E4(uv[e] = vmul(uv[e], zz[e]))                                                                   \
      if (u == 12) { q[GO / 4] = vcvt(uv[0], uv[1]); q[GO / 4 + 1] = vcvt(uv[2], uv[3]); if (GO) gst16_sc1(adst + (T) * 1024, vo, q); } }
#define E0(T) ((T) == 0 ? 9 : (T) == 1 ? 35 : (T) == 2 ? 61 : (T) == 3 ? 87 : (T) == 4 ? 113 : 139)
#define GAP1(T, G) { if ((G) >= E0(T) && (G) < E0(T) + 26) UNIT(T, ((G) - E0(T) + 26) % 26) }
#define GAP(G) { GAP1(0, G) GAP1(1, G) GAP1(2, G) GAP1(3, G) GAP1(4, G) GAP1(5, G) }
#define PH(S) { constexpr int s = (S);                                                                             \
      if (s == 0) mf0(acc[TT % 3], ra[0], xf[0]); else if (s < 32) mfa(acc[TT % 3], ra[s & 31], xf[s & 31]); else mfa(acc[TT % 3], aug[TT], xaug); \
      if (s < 32) { if (s + 4 < 32) ra[(s + 4) & 31] = wrd<SA, (s + 4) & 31>(lo, mid, hi); else if (TT < 6) ra[(s + 4) & 31] = wrd<SN, (s + 4) & 3>(lo, mid, hi); } \
      if (TT == 2 && s == 3) { mfa(acc[0], aug[0], xaug); mfa(acc[1], aug[1], xaug); }                             \
      if (s == 1 && TT < 6) aug[TT < 6 ? TT + 1 : 0] = gld_s<1, 0>(asrc + (TT + 1) * 1024, vo);                    \
      GAP((TT - 2) * 33 + s) }
#define PHASE  PH(0) PH(1) PH(2) PH(3) PH(4) PH(5) PH(6) PH(7) \
 PH(8) PH(9) PH(10) PH(11) PH(12) PH(13) PH(14) PH(15) \
 PH(16) PH(17) PH(18) PH(19) PH(20) PH(21) PH(22) PH(23) \
 PH(24) PH(25) PH(26) PH(27) PH(28) PH(29) PH(30) PH(31) \
 PH(32)
  { constexpr int TT = 2; constexpr u32 SA = SL2, SN = SL3; PHASE }
  asm volatile("s_waitcnt vmcnt(0)\n\ts_barrier" : "+v"(aug[3])::"memory");
  STAMP(12)
  { constexpr int TT = 3; constexpr u32 SA = SL3, SN = SL4; PHASE }
  asm volatile("s_waitcnt vmcnt(1)\n\ts_barrier" : "+v"(aug[4])::"memory");
  STAMP(13)
  { constexpr int TT = 4; constexpr u32 SA = SL4, SN = SL0; PHASE }
  asm volatile("s_waitcnt vmcnt(1)\n\ts_barrier" : "+v"(aug[5])::"memory");
  STAMP(14)
  { constexpr int TT = 5; constexpr u32 SA = SL0, SN = SL1; PHASE }
  asm volatile("s_waitcnt vmcnt(1)\n\ts_barrier" : "+v"(aug[6])::"memory");
  STAMP(15)
  { constexpr int TT = 6; constexpr u32 SA = SL1, SN = SL1; PHASE }
  STAMP(16)
  MFMA_SETTLE();
 UNIT(6, 0) UNIT(6, 1) UNIT(6, 2) UNIT(6, 3) UNIT(6, 4) UNIT(6, 5) UNIT(6, 6) UNIT(6, 7) UNIT(6, 8)
 UNIT(6, 9) UNIT(6, 10) UNIT(6, 11) UNIT(6, 12) UNIT(6, 13) UNIT(6, 14) UNIT(6, 15) UNIT(6, 16) UNIT(6, 17)
 UNIT(6, 18) UNIT(6, 19) UNIT(6, 20) UNIT(6, 21) UNIT(6, 22) UNIT(6, 23) UNIT(6, 24) UNIT(6, 25)
  STAMP(17)
  asm volatile("s_barrier" ::: "memory");
  {
    const u32 b = w * 8192;
#define WDR(F) (wd[F] = (F) < 8 ? lds128(lo + SL2 + b + ((F) & 7) * 1024) : (F) < 16 ? lds128(mid + (SL3 - 65536) + b + ((F) & 7) * 1024) : (F) < 22 ? lds128(hi + (TOPA - 131072) + w * 6144 + (((F) - 16) & 7) * 1024) : lds128(lo + SL0 + w * 6144 + (((F) - 22) & 7) * 1024))
 WDR(0); WDR(1); WDR(2); WDR(3); WDR(4); WDR(5); WDR(6); WDR(7); WDR(8); WDR(9);
 WDR(10); WDR(11); WDR(12); WDR(13); WDR(14); WDR(15); WDR(16); WDR(17); WDR(18); WDR(19);
 WDR(20); WDR(21); WDR(22); WDR(23); WDR(24); WDR(25); WDR(26); WDR(27);
#undef WDR
  }
#undef PHASE
#undef PH
#undef GAP
#undef GAP1
#undef UNIT
#undef XLD
}

DI void p4_helper(const Params& p, const Ids& id, int l, int w, u32 tag, u32x4 (&wd)[28] STAMP_ARG) {
  const u32 vo = l * 16;
  const int hw = w - 4;
  const char* wsrc = p.ws + WS_WGU + (size_t)id.c * 7 * 32768 + hw * 8192;
  const char* dme = p.ws + WS_WD + ((size_t)(2 * id.j) * 112 + 14 * w) * 1024;
  const char* dcw = p.ws + WS_WD + ((size_t)(2 * id.j) * 112 + 14 * hw) * 1024;
#define D8(SRC, DST) { dma4s(SRC, vo, DST); dma4s((SRC) + 4096, vo, (DST) + 4096); }
#define D6(SRC, DST) { dma4s(SRC, vo, DST); set_m0((DST) + 4096); dma1<0>((SRC) + 4096, vo); dma1<1024>((SRC) + 4096, vo); }
#define WDG(F) (wd[F] = gld_s<1, 0>(dme + ((F) < 14 ? (F) : 98 + (F)) * 1024, vo))
  D8(wsrc + 3 * 32768, SL3 + hw * 8192)
  WDG(16); WDG(17); WDG(18); WDG(19); WDG(20); WDG(21); WDG(22); WDG(23); WDG(24); WDG(25); WDG(26); WDG(27);
  WAITV_BAR(20);
  {
    __builtin_amdgcn_s_sleep(8);
    poll_flags<16, 8, 3>(p.ws + WS_FS + id.G * 512, tag, l);
    u32x4 st0, st1;
    asm volatile("global_load_dwordx4 %0, %2, %3 sc1\n\tglobal_load_dwordx4 %1, %2, %3 offset:16 sc1\n\ts_waitcnt vmcnt(0)" : "=&v"(st0), "=&v"(st1)
                 : "v"((l & 31) * 64 + (l >> 5) * 32), "s"(p.ws + WS_STAT + (size_t)(128 * id.G + 32 * hw) * 64) : "memory");
    const float s1 = swap32_sum((__uint_as_float(st0[0]) + __uint_as_float(st0[2])) + (__uint_as_float(st1[0]) + __uint_as_float(st1[2])));
    const float s2 = swap32_sum((__uint_as_float(st0[1]) + __uint_as_float(st0[3])) + (__uint_as_float(st1[1]) + __uint_as_float(st1[3])));
    const float nmu = -s1 * (1.f / DM), var = fmaxf(s2 * (1.f / DM) - nmu * nmu, 0.f) + EPS;
    const float rs = __builtin_amdgcn_rsqf(var), sg = var * rs, mh = rbf(nmu), sh = rbf(sg);
    if (l < 32) { sts128(GAPA + hw * 512 + vo, u32x4{pk(mh, mh), pk(nmu - mh, sh), pk(sh, sg - sh), 0u}); *(LAS float*)(uintptr_t)(GAPA + 2048 + hw * 128 + l * 4) = rs; }
  }
  dma4s(wsrc + 4 * 32768, vo, TOPA + hw * 8192); if (hw < 3) dma4s(wsrc + 4 * 32768 + 4096, vo, TOPA + hw * 8192 + 4096);
  BAR();
  D8(wsrc + 5 * 32768, SL0 + hw * 8192) WDG(0); WDG(1); WDG(2); WDG(3);
  WAITV_BAR(12);
  if (hw == 3) dma4s(wsrc + 4 * 32768 + 4096, vo, GAPA);
  D8(wsrc + 6 * 32768, SL1 + hw * 8192) dma4s(dcw, vo, SL2 + hw * 8192);
  WAITV_BAR(12);
  dma4s(dcw + 4096, vo, SL2 + hw * 8192 + 4096); D6(dcw + 8192, SL3 + hw * 8192)
  set_m0(SL3 + hw * 8192 + 6144); dma1<0>(dcw + 114688, vo); dma1<1024>(dcw + 114688, vo);
  WAITV_BAR(16);
  D6(dcw + 114688 + 2048, TOPA + hw * 6144) WDG(4); WDG(5); WDG(6); WDG(7); WDG(8); WDG(9);
  if (w == 4 && l == 0) *(LAS u32*)(uintptr_t)GAPA = 0u;
  BAR();
  if (w == 5 && l == 0) put_flag(p.ws + WS_FE + id.G * 512 + fofs<8>(id.c), tag);
  D6(dcw + 114688 + 8192, SL0 + hw * 6144) WDG(10); WDG(11); WDG(12); WDG(13); WDG(14); WDG(15);
  WAITV_BAR(6);
  poll_flags<16, 8, 4>(p.ws + WS_FE + (id.g >> 1) * 512, tag, l);
  asm volatile("s_waitcnt vmcnt(0)" : "+v"(wd[0]), "+v"(wd[1]), "+v"(wd[2]), "+v"(wd[3]), "+v"(wd[4]), "+v"(wd[5]), "+v"(wd[6]), "+v"(wd[7]), "+v"(wd[8]), "+v"(wd[9]), "+v"(wd[10]),
               "+v"(wd[11]), "+v"(wd[12]), "+v"(wd[13])::"memory");
  asm volatile("" : "+v"(wd[14]), "+v"(wd[15]), "+v"(wd[16]), "+v"(wd[17]), "+v"(wd[18]), "+v"(wd[19]), "+v"(wd[20]), "+v"(wd[21]), "+v"(wd[22]), "+v"(wd[23]), "+v"(wd[24]),
               "+v"(wd[25]), "+v"(wd[26]), "+v"(wd[27])::"memory");
#undef WDG
#undef D8
#undef D6
}

__global__ __launch_bounds__(512, 2) void k_main(Params p) {
  extern __shared__ __attribute__((aligned(16))) char smem[];
  const int tid = threadIdx.x, l = tid & 63, w = rfl(tid >> 6);
  const Ids id = ids();
  const int g = id.g, j = id.j, h = l >> 5;
  const u32 vo = l * 16;
  const u32 epoch = *(const u32*)(p.ws + WS_EPOCH);
  const u32 tag = epoch * 0x9E3779B1u + 0x7F4A7C15u;
  const int tt = w & 1, nt = (w >> 1) & 1, kh = w >> 2, kq = w >> 1;
  const char* modr = p.ws + WS_MOD + (blockIdx.x & 7) * 12288;
  STAMP_DECL STAMP(0)
  const char* wgu = p.ws + WS_WGU + (size_t)id.c * 7 * 32768;
  const char* wob = p.ws + WS_WO + ((size_t)(2 * j) * 32 + 8 * kq) * 1024;
  const char *wob1 = wob + 4096, *wob2 = wob + 32768, *wob3 = wob + 36864;
  u32x4 q0, q1, fo_, fd_, fg[2];
  char *pdo, *pdd, *pdg;
  f32x16 o0, o1;
  u32x4 vB0, vB1, pb0, pb1;
  {
    const int nh = (l >> 4) & 1, q = (l & 15) >> 2, pp = l & 3;
    const int uid = g * 7 + j - 1, uT = uid >> 1, ukh = uid & 1;
    const int rk = (blockIdx.x >> 6) * 7 + j - 1;
    const bool gemv = j != 0 && rk < 24;
    const int mu = 4 * ((blockIdx.x & 7) + 8 * (rk >> 2)) + (rk & 3);
    const u32 rawo = (l >> 3) * 1024 + (((l & 7) ^ (((l >> 4) & 1) << 2)) * 16);
#define WQ_DMA() { _Pragma("unroll") for (int i = 0; i < 8; ++i) { set_m0(F_A + (8 * w + i) * 1024); dma<0>(p.wq + (size_t)(64 * w + 8 * i) * DM + 64 * j, rawo); } }
#define LATE_DMA() {                                                                                                                              \
      if (j != 0) {                                                                                                                               \
        const u32 go = (l >> 1) * (FF * 2) + (l & 1) * 16;                                                                                        \
        set_m0(F_B + w * 1024); dma<0>(p.wg + (size_t)(256 * ukh + 32 * w) * FF + 16 * uT, go);                                                   \
        set_m0(F_B + 8192 + 128 + w * 1024); dma<0>(p.wu + (size_t)(256 * ukh + 32 * w) * FF + 16 * uT, go);                                      \
        set_m0(F_D + w * 1024); dma<0>(p.wd + (size_t)(64 * (uid >> 3) + 8 * w) * DM + 64 * (uid & 7), rawo);                                     \
      }                                                                                                                                           \
      if (w >= 6) { set_m0(F_D + 8192 + (w - 6) * 1024); dma<0>(p.wo + (size_t)(16 * g + 8 * (w - 6)) * DM + 64 * j, rawo); } }
    u32x4 w0, w1; u32 c0, c1;
    if (gemv) {
      const u32 mo = (tid >> 1) * 6144 + (tid & 1) * 16;
      w0 = gld_s<0, 0>(p.wm + 16 * mu, mo); w1 = gld_s<0, 0>(p.wm + 16 * mu + 256 * 3072, mo);
      c0 = gld2_s<0>(p.c, (tid >> 1) * 2); c1 = gld2_s<512>(p.c, (tid >> 1) * 2);
    }
    u32x4 xv[8];
    {
      const char* xs = (const char*)(p.x + (size_t)(64 * g + 8 * w) * DM);
      xv[0] = gld_s<0, 0>(xs, vo); xv[1] = gld_s<0, 1024>(xs, vo); xv[2] = gld_s<0, 2048>(xs, vo); xv[3] = gld_s<0, 3072>(xs, vo);
      xv[4] = gld_s<0, 0>(xs + 4096, vo); xv[5] = gld_s<0, 1024>(xs + 4096, vo); xv[6] = gld_s<0, 2048>(xs + 4096, vo); xv[7] = gld_s<0, 3072>(xs + 4096, vo);
    }
    if (j == 0) {
      set_m0(F_H + w * 2048); dma<0>(p.wv + w * 1024, vo); dma<1024>(p.wv + w * 1024, vo);
      set_m0(F_H + 16384 + 128 + w * 2048); dma<0>(p.wk + w * 1024, vo); dma<1024>(p.wk + w * 1024, vo);
      WQ_DMA() LATE_DMA()
    }
    if (gemv) {
      asm volatile("s_waitcnt vmcnt(8)" : "+v"(w0), "+v"(w1), "+v"(c0), "+v"(c1)::"memory");
      const float f0 = bflo(c0), f1 = bflo(c1);
      const float s0 = rbf(f0 * __builtin_amdgcn_rcpf(1.f + EXP2(-1.4426950408889634f * f0))), s1 = rbf(f1 * __builtin_amdgcn_rcpf(1.f + EXP2(-1.4426950408889634f * f1)));
      float a[8];
#pragma unroll
      for (int i = 0; i < 4; ++i) { a[2 * i] = s0 * bflo(w0[i]) + s1 * bflo(w1[i]); a[2 * i + 1] = s0 * bfhi(w0[i]) + s1 * bfhi(w1[i]); }
#pragma unroll
      for (int i = 0; i < 4; ++i) a[i] = halve<0x4E>(l & 2, a[i], a[i + 4]);
#pragma unroll
      for (int i = 0; i < 2; ++i) a[i] = halve<0x124>(l & 4, a[i], a[i + 2]);
      a[0] = halve<0x128>(l & 8, a[0], a[1]);
      a[0] = swap32_sum(swap16_sum(a[0]));
      if (l < 16) *(LAS float*)(uintptr_t)(F_M + w * 64 + ((l & 1) * 8 + (l & 2) * 2 + ((l >> 1) & 2) + (l >> 3)) * 4) = a[0];
      BAR();
      if (w == 7) {
        f32x2 s = {0.f, 0.f};
#pragma unroll
        for (int i = 0; i < 8; ++i) s += ldsf2(F_M + i * 64 + (l & 7) * 8);
        gst8_sc1(p.ws + WS_MOD + mu * 64, (l >> 3) * 12288 + (l & 7) * 8, u32x2{pk(s[0], s[1]), tag});
        if (rk < 8) { STAMP(10) }
      }
    }
    STAMP(22)
    if (j == 0) WAITV(12); else if (gemv && w == 7) WAITV(1); else WAITV(0);
    asm volatile("" : "+v"(xv[0]), "+v"(xv[1]), "+v"(xv[2]), "+v"(xv[3]), "+v"(xv[4]), "+v"(xv[5]), "+v"(xv[6]), "+v"(xv[7]));
    float xh[8][8];
    {
      float s1[8], s2[8];
#pragma unroll
      for (int i = 0; i < 8; ++i) {
        s1[i] = 0.f; s2[i] = 0.f;
#pragma unroll
        for (int e = 0; e < 4; ++e) { const u32 v = xv[i][e]; s1[i] = dot2(v, 0x3f803f80u, s1[i]); s2[i] = dot2(v, v, s2[i]); }
      }
#pragma unroll
      for (int i = 0; i < 4; ++i) { s1[i] = halve<0xB1>(l & 1, s1[i], s1[i + 4]); s2[i] = halve<0xB1>(l & 1, s2[i], s2[i + 4]); }
#pragma unroll
      for (int i = 0; i < 2; ++i) { s1[i] = halve<0x4E>(l & 2, s1[i], s1[i + 2]); s2[i] = halve<0x4E>(l & 2, s2[i], s2[i + 2]); }
      float t1 = halve<0x124>(l & 4, s1[0], s1[1]), t2 = halve<0x124>(l & 4, s2[0], s2[1]);
      t1 += dpp<0x128>(t1); t2 += dpp<0x128>(t2);
      t1 = swap32_sum(swap16_sum(t1)); t2 = swap32_sum(swap16_sum(t2));
      const float m = t1 * (1.f / DM), r = __builtin_amdgcn_rsqf(fmaxf(t2 * (1.f / DM) - m * m, 0.f) + EPS), nm = -m * r;
#define LNROW(I, LN) { const float ri = dpp<0x150 + (LN)>(r), ni = dpp<0x150 + (LN)>(nm); _Pragma("unroll") for (int e = 0; e < 4; ++e) { xh[I][2 * e] = fmaf(bflo(xv[I][e]), ri, ni); xh[I][2 * e + 1] = fmaf(bfhi(xv[I][e]), ri, ni); } }
      LNROW(0, 0) LNROW(1, 4) LNROW(2, 2) LNROW(3, 6) LNROW(4, 1) LNROW(5, 5) LNROW(6, 3) LNROW(7, 7)
#undef LNROW
#pragma unroll
      for (int i = 0; i < 8; ++i) asm volatile("" : "+v"(xh[i][0]), "+v"(xh[i][1]), "+v"(xh[i][2]), "+v"(xh[i][3]), "+v"(xh[i][4]), "+v"(xh[i][5]), "+v"(xh[i][6]), "+v"(xh[i][7]));
    }
    STAMP(23)
    if (w > 0 && j != 0) WQ_DMA()
    u32x4 wkvf[16];
    if (j == 0) {
      WAITV_BAR(8);
      const u32 a0 = F_H + nh * (16384 + 128) + (256 * kh + 8 * h + q) * 32 + pp * 8;
#pragma unroll
      for (int s = 0; s < 16; ++s) wkvf[s] = ldstr2(a0 + s * 512, a0 + s * 512 + 128);
    }
    STAMP(24)
    if (w == 0) {
      u32x4 a0, a1, b0, b1;
#pragma unroll 1
      for (int it = 0; it < SPIN_CAP; ++it) {
        asm volatile("global_load_dwordx4 %0, %4, %5 sc1\n\tglobal_load_dwordx4 %1, %4, %5 offset:16 sc1\n\tglobal_load_dwordx4 %2, %4, %5 offset:2048 sc1\n\t"
                     "global_load_dwordx4 %3, %4, %5 offset:2064 sc1\n\ts_waitcnt vmcnt(0)" : "=&v"(b0), "=&v"(b1), "=&v"(a0), "=&v"(a1) : "v"(l * 32), "s"(modr) : "memory");
        if (__all(a0[1] == tag && a0[3] == tag && a1[1] == tag && a1[3] == tag && b0[1] == tag && b0[3] == tag && b1[1] == tag && b1[3] == tag)) break;
      }
      sts128(F_M + 512 + vo, u32x4{pk(1.f + bflo(a0[0]), 1.f + bfhi(a0[0])), pk(1.f + bflo(a0[2]), 1.f + bfhi(a0[2])), pk(1.f + bflo(a1[0]), 1.f + bfhi(a1[0])), pk(1.f + bflo(a1[2]), 1.f + bfhi(a1[2]))});
      sts128(F_M + 1536 + vo, u32x4{b0[0], b0[2], b1[0], b1[2]});
    }
    BAR();
    STAMP(25)
    if (w == 0 && j != 0) WQ_DMA()
    if (j != 0) LATE_DMA()
    {
      const u32x4 ca = lds128(F_M + 512 + vo), sa = lds128(F_M + 1536 + vo);
      float A[8], B[8];
#pragma unroll
      for (int e = 0; e < 4; ++e) { A[2 * e] = bflo(ca[e]); A[2 * e + 1] = bfhi(ca[e]); B[2 * e] = bflo(sa[e]); B[2 * e + 1] = bfhi(sa[e]); }
#pragma unroll
      for (int i = 0; i < 8; ++i) {
        u32x4 o;
#pragma unroll
        for (int e = 0; e < 4; ++e) o[e] = pk(fmaf(xh[i][2 * e], A[2 * e], B[2 * e]), fmaf(xh[i][2 * e + 1], A[2 * e + 1], B[2 * e + 1]));
        const int tk = 8 * w + i;
        sts128(F_H + ((tk >> 5) * 32 + (l >> 1)) * 1024 + (l & 1) * 512 + (((tk & 31) ^ (l & 7)) * 16), o);
      }
    }
    BAR();
    STAMP(26)
    u32x4 xf[16];
#pragma unroll
    for (int s = 0; s < 16; ++s) xf[s] = lds128(F_H + (tt * 32 + 16 * kh + s) * 1024 + h * 512 + (((l & 31) ^ ((2 * s + h) & 7)) * 16));
    if (j == 0) {
      const u32 xs = F_B + w * 2048 + vo, xr = F_B + (w ^ 4) * 2048 + vo;
      f32x16 acc = {};
      if (nt == 0) {
#pragma unroll
        for (int s = 0; s < 16; ++s) acc = mfma(wkvf[s], xf[s], acc);
      } else {
#pragma unroll
        for (int s = 0; s < 16; ++s) acc = mfma(xf[s], wkvf[s], acc);
      }
      float keep[8];
      if (kh == 0) { stsf4(xs, f32x4{acc[8], acc[9], acc[10], acc[11]}); stsf4(xs + 1024, f32x4{acc[12], acc[13], acc[14], acc[15]});
#pragma unroll
        for (int e = 0; e < 8; ++e) keep[e] = acc[e];
      } else { stsf4(xs, f32x4{acc[0], acc[1], acc[2], acc[3]}); stsf4(xs + 1024, f32x4{acc[4], acc[5], acc[6], acc[7]});
#pragma unroll
        for (int e = 0; e < 8; ++e) keep[e] = acc[8 + e];
      }
      BAR();
      const f32x4 r0 = ldsf4(xr), r1 = ldsf4(xr + 1024);
      u32x4 o = {pk(keep[0] + r0[0], keep[1] + r0[1]), pk(keep[2] + r0[2], keep[3] + r0[3]), pk(keep[4] + r1[0], keep[5] + r1[1]), pk(keep[6] + r1[2], keep[7] + r1[3])};
      char* img = p.ws + WS_KV + (size_t)(2 * g + tt) * TS;
      if (nt == 0) { if (kh == 1) gst16_sc1(img, vo, o); }
      else if ((l & 31) <= 17) {
        if ((l & 31) == 16) o = u32x4{0x3f803f80u, 0x3f803f80u, 0x3f803f80u, 0x3f803f80u};
        if ((l & 31) == 17) o = u32x4{0u, 0u, 0u, 0u};
        gst16_sc1(img + 1024 + kh * 576, h * 288 + (l & 31) * 16, o);
      }
      WAITV_BAR(0);
      if (w == 0 && l < 8) put_flag(p.ws + WS_FKV + l * 1024 + fofs<8>(g), tag);
    }
    STAMP(21)
    if (j != 0) { if (w >= 6) WAITV(4); else WAITV(3); }
    BAR();
    const u32 rawr = (((nh * 2 + (pp >> 1)) ^ (((q >> 1) & 1) << 2)) * 16) + (pp & 1) * 8;
    f32x16 acc = {};
    {
      const u32 a0 = F_A + (256 * kh + 8 * h + q) * 128 + (rawr ^ (nt * 64));
#pragma unroll
      for (int s = 0; s < 16; ++s) acc = mfma(ldstr2(a0 + s * 2048, a0 + s * 2048 + 512), xf[s], acc);
    }
    const u32 xs = F_H + w * 4096 + vo, xr = F_H + (w ^ 4) * 4096 + vo;
    stsf4(xs, f32x4{acc[0], acc[1], acc[2], acc[3]}); stsf4(xs + 1024, f32x4{acc[4], acc[5], acc[6], acc[7]});
    stsf4(xs + 2048, f32x4{acc[8], acc[9], acc[10], acc[11]}); stsf4(xs + 3072, f32x4{acc[12], acc[13], acc[14], acc[15]});
    STAMP(27)
    if (w == 1 && j != 0) {
      {
        u32x4 a0, b0;
#pragma unroll 1
        for (int it = 0; it < SPIN_CAP; ++it) {
          asm volatile("global_load_dwordx4 %0, %2, %3 sc1\n\tglobal_load_dwordx4 %1, %2, %3 offset:2048 sc1\n\ts_waitcnt vmcnt(0)" : "=&v"(b0), "=&v"(a0) : "v"(vo), "s"(modr + 6144 + ukh * 1024) : "memory");
          if (__all(a0[1] == tag && a0[3] == tag && b0[1] == tag && b0[3] == tag)) break;
          __builtin_amdgcn_s_sleep(2);
        }
        stsf4(F_M + 2560 + vo, f32x4{rbf(1.f + bflo(a0[0])), rbf(1.f + bfhi(a0[0])), rbf(1.f + bflo(a0[2])), rbf(1.f + bfhi(a0[2]))});
        stsf4(F_M + 3584 + vo, f32x4{bflo(b0[0]), bfhi(b0[0]), bflo(b0[2]), bfhi(b0[2])});
      }
    }
    WAITV_BAR(0);
    f32x4 cs[2][4];
    if (w >= 6) { const u32 a0 = F_D + 8192 + (4 * h + q) * 128 + (rawr ^ (tt * 64)); fo_ = ldstr2(a0, a0 + 1024); }
    if (j != 0) {
      const u32 a0 = F_D + (16 * kq + 4 * h + q) * 128 + (rawr ^ (tt * 64));
      fd_ = ldstr2(a0, a0 + 1024);
#pragma unroll
      for (int i = 0; i < 2; ++i) {
        const int s = 2 * w + i;
        const u32 a1 = F_B + (pp >> 1) * (8192 + 128) + (16 * s + 4 * h + q) * 32 + nh * 16 + (pp & 1) * 8;
        fg[i] = ldstr2(a1, a1 + 256);
        cs[i][0] = ldsf4(F_M + 2560 + (16 * s + 4 * h) * 4); cs[i][1] = ldsf4(F_M + 2560 + (16 * s + 8 + 4 * h) * 4);
        cs[i][2] = ldsf4(F_M + 3584 + (16 * s + 4 * h) * 4); cs[i][3] = ldsf4(F_M + 3584 + (16 * s + 8 + 4 * h) * 4);
      }
    }
    const f32x4 r0 = ldsf4(xr), r1 = ldsf4(xr + 1024), r2 = ldsf4(xr + 2048), r3 = ldsf4(xr + 3072);
    q0 = u32x4{pk((acc[0] + r0[0]) * C2, (acc[1] + r0[1]) * C2), pk((acc[2] + r0[2]) * C2, (acc[3] + r0[3]) * C2), pk((acc[4] + r1[0]) * C2, (acc[5] + r1[1]) * C2), pk((acc[6] + r1[2]) * C2, (acc[7] + r1[3]) * C2)};
    q1 = u32x4{pk((acc[8] + r2[0]) * C2, (acc[9] + r2[1]) * C2), pk((acc[10] + r2[2]) * C2, (acc[11] + r2[3]) * C2), pk((acc[12] + r3[0]) * C2, (acc[13] + r3[1]) * C2), pk((acc[14] + r3[2]) * C2, (acc[15] + r3[3]) * C2)};
    PIN(q0); PIN(q1);
    pdo = p.ws + WS_WO + ((size_t)(2 * j + tt) * 32 + g) * 1024;
    pdd = p.ws + WS_WD + ((size_t)(2 * (uid & 7) + tt) * 112 + 4 * (uid >> 3) + kq) * 1024;
    pdg = p.ws + WS_WGU + ((size_t)uT * 32 + 16 * ukh + 2 * w) * 1024;
    asm volatile("" : "+s"(pdo), "+s"(pdd), "+s"(pdg), "+s"(wob), "+s"(wob1), "+s"(wob2), "+s"(wob3));
    if (w == 2) poll_flags<32, 8>(p.ws + WS_FKV + (blockIdx.x & 7) * 1024, tag, l);
    if (w == 3) {
#pragma unroll 1
      for (int it = 0; it < SPIN_CAP; ++it) {
        const u32 v = __hip_atomic_load((const GAS u32*)(modr + 4096 + j * 256 + h * 6144 + (l & 31) * 8 + 4), RLX_AGENT);
        if (__all(v == tag)) break;
        __builtin_amdgcn_s_sleep(2);
      }
    }
    BAR();
    STAMP(28)
    STAMP(29)
    { const u32 o = (w & 1) * 69632 + (w >> 1) * 6144;
      dma4s(p.ws + WS_KV + o, vo, o); set_m0(o + 4096); dma1<0>(p.ws + WS_KV + o + 4096, vo); dma1<1024>(p.ws + WS_KV + o + 4096, vo); }
    o0 = f32x16{}; o1 = f32x16{}; vB0 = u32x4{}; vB1 = u32x4{}; pb0 = u32x4{}; pb1 = u32x4{};
    PIN(o0); PIN(o1); PIN(vB0); PIN(vB1); PIN(pb0); PIN(pb1);
    if (j != 0) {
      float us = 0.f, wsum = 0.f;
#pragma unroll
      for (int i = 0; i < 2; ++i) {
        const u32x4 f = fg[i];
        const f32x4 c0 = cs[i][0], c1 = cs[i][1], s0 = cs[i][2], s1 = cs[i][3];
        u32x4 o;
        o[0] = pk(bflo(f[0]) * c0[0], bfhi(f[0]) * c0[1]); o[1] = pk(bflo(f[1]) * c0[2], bfhi(f[1]) * c0[3]);
        o[2] = pk(bflo(f[2]) * c1[0], bfhi(f[2]) * c1[1]); o[3] = pk(bflo(f[3]) * c1[2], bfhi(f[3]) * c1[3]);
#pragma unroll
        for (int e = 0; e < 4; ++e) { const u32 v = o[e]; us = dot2(v, 0x3f803f80u, us); }
        wsum += bflo(f[0]) * s0[0] + bfhi(f[0]) * s0[1] + bflo(f[1]) * s0[2] + bfhi(f[1]) * s0[3] + bflo(f[2]) * s1[0] + bfhi(f[2]) * s1[1] + bflo(f[3]) * s1[2] + bfhi(f[3]) * s1[3];
        fg[i] = o;
      }
      us = swap32_sum(us); wsum = swap32_sum(wsum);
      if (l < 32) *(LAS f32x2*)(uintptr_t)(OEX + w * 256 + l * 8) = f32x2{us, wsum};
      WAITV_BAR(0);
      if (w == 7) {
        f32x2 s = {0.f, 0.f};
#pragma unroll
        for (int i = 0; i < 8; ++i) s += ldsf2(OEX + i * 256 + (l & 31) * 8);
        const float uh = rbf(s[0]), wh = rbf(s[1]);
        if (l < 32) gst16_sc1(p.ws + WS_AUG + (size_t)uT * 1024 + ukh * 512, vo, u32x4{pk(uh, s[0] - uh), pk(uh, wh), pk(s[1] - wh, wh), 0u});
      }
    } else {
      WAITV_BAR(0);
    }
  }
  const size_t fo = ((size_t)(2 * g + tt) * 32 + 4 * j + 2 * nt) * 1024;
  STAMP(1)
  const u32 vrow = 1024 + h * 288 + min(l & 31, 17) * 16;
  u32 ka = kh * 32 * TS + vo, va = kh * 32 * TS + vrow;
  f32x16 nm0, nm1, s0, s1;
  const u32x4 k0 = lds128(vo);
  u32x4 kA = lds128(ka), kB = lds128(ka + TS);
  u32x4 vA0 = lds128(va), vA1 = lds128(va + 576);
  u32x4 pa0, pa1;
  {
    mf0(s0, k0, q0); mf0(s1, k0, q1);
    MFMA_SETTLE();
    PIN(s0); PIN(s1);
#define MX3(a, b, c) fmaxf(fmaxf(a, b), c)
    const float m0 = swap32_max(MX3(MX3(MX3(s0[0], s0[1], s0[2]), MX3(s0[3], s0[4], s0[5]), MX3(s0[6], s0[7], s0[8])), MX3(MX3(s0[9], s0[10], s0[11]), MX3(s0[12], s0[13], s0[14]), s0[15]), -3.0e38f));
    const float m1 = swap32_max(MX3(MX3(MX3(s1[0], s1[1], s1[2]), MX3(s1[3], s1[4], s1[5]), MX3(s1[6], s1[7], s1[8])), MX3(MX3(s1[9], s1[10], s1[11]), MX3(s1[12], s1[13], s1[14]), s1[15]), -3.0e38f));
#undef MX3
#pragma unroll
    for (int r = 0; r < 16; ++r) nm0[r] = -m0;
    PIN(nm0);
    s0 = mfma(kA, q0, nm0);
#pragma unroll
    for (int r = 0; r < 16; ++r) nm1[r] = -m1;
    PIN(nm1);
  }
  ASTAMP(25)
  if (!kh) {
    const u32 o = nt * 69632 + 24576 + tt * 8192;
    dma4s(p.ws + WS_KV + o, vo, o); dma4s(p.ws + WS_KV + o + 4096, vo, o + 4096);
  }
#define PREP_ST() { if (j != 0) { gst16_sc1(pdd, vo, fd_); gst16_sc1(pdg, vo, fg[0]); gst16_sc1(pdg + 1024, vo, fg[1]); } }
  if (kh) { PREP_ST() if (w >= 6) gst16_sc1(pdo, vo, fo_); }
  ASTAMP(26)
  u32x4 wf[16];
#define HALF(SC, N0, N1, MF, OP, VP0, P0, VP1, P1, LD1, LD2)                                              \
  {                                                                                                       \
    MF; LD1; SB();                                                                                        \
    float e0 = EXP2(SC[0]), e1 = EXP2(SC[1]), e2 = EXP2(SC[2]), e3 = EXP2(SC[3]), e4 = EXP2(SC[4]), e5 = EXP2(SC[5]), e6 = EXP2(SC[6]), e7 = EXP2(SC[7]); SB(); \
    OP = mfma(VP0, P0, OP); SB();                                                                         \
    float e8 = EXP2(SC[8]), e9 = EXP2(SC[9]), e10 = EXP2(SC[10]), e11 = EXP2(SC[11]), e12 = EXP2(SC[12]), e13 = EXP2(SC[13]), e14 = EXP2(SC[14]), e15 = EXP2(SC[15]); SB(); \
    OP = mfma(VP1, P1, OP); LD2; SB();                                                                    \
    N0[0] = pk(e0, e1); N0[1] = pk(e2, e3); N0[2] = pk(e4, e5); N0[3] = pk(e6, e7); N1[0] = pk(e8, e9);   \
    N1[1] = pk(e10, e11); N1[2] = pk(e12, e13); N1[3] = pk(e14, e15); SB();                               \
  }
#define TILE(T, KC, KN, VC0, VC1, VP0, VP1)                                                               \
    HALF(s0, pa0, pa1, s1 = mfma(KC, q1, nm1), o1, VP0, pb0, VP1, pb1, KC = lds128(ka + ((T) + 2) * TS),  \
         (VP0 = lds128(va + ((T) + 1) * TS), VP1 = lds128(va + ((T) + 1) * TS + 576)))                    \
    HALF(s1, pb0, pb1, s0 = mfma(KN, q0, nm0), o0, VC0, pa0, VC1, pa1, , )
#define LDWF(SEL)                                                                                                     \
  asm volatile("s_cmp_lg_u32 %21, 0\n\ts_cbranch_scc1 .Lwf%=\n\t"                                                     \
               "global_load_dwordx4 %0, %16, %17 sc1\n\tglobal_load_dwordx4 %1, %16, %17 offset:1024 sc1\n\tglobal_load_dwordx4 %2, %16, %17 offset:2048 sc1\n\tglobal_load_dwordx4 %3, %16, %17 offset:3072 sc1\n\t"     \
               "global_load_dwordx4 %4, %16, %18 sc1\n\tglobal_load_dwordx4 %5, %16, %18 offset:1024 sc1\n\tglobal_load_dwordx4 %6, %16, %18 offset:2048 sc1\n\tglobal_load_dwordx4 %7, %16, %18 offset:3072 sc1\n\t"     \
               "global_load_dwordx4 %8, %16, %19 sc1\n\tglobal_load_dwordx4 %9, %16, %19 offset:1024 sc1\n\tglobal_load_dwordx4 %10, %16, %19 offset:2048 sc1\n\tglobal_load_dwordx4 %11, %16, %19 offset:3072 sc1\n\t"   \
               "global_load_dwordx4 %12, %16, %20 sc1\n\tglobal_load_dwordx4 %13, %16, %20 offset:1024 sc1\n\tglobal_load_dwordx4 %14, %16, %20 offset:2048 sc1\n\tglobal_load_dwordx4 %15, %16, %20 offset:3072 sc1\n"   \
               ".Lwf%=:"                                                                                              \
               : "+v"(wf[0]), "+v"(wf[1]), "+v"(wf[2]), "+v"(wf[3]), "+v"(wf[4]), "+v"(wf[5]), "+v"(wf[6]), "+v"(wf[7]), "+v"(wf[8]), "+v"(wf[9]), "+v"(wf[10]), \
                 "+v"(wf[11]), "+v"(wf[12]), "+v"(wf[13]), "+v"(wf[14]), "+v"(wf[15])                                  \
               : "v"(vo), "s"(wob), "s"(wob1), "s"(wob2), "s"(wob3), "s"(SEL) : "memory", "scc")
#pragma unroll
  for (int s4 = 0; s4 < 16; ++s4) asm volatile("" : "=v"(wf[s4]));
  const int cb = rfl(64 * j + 32 * nt + 16 * kh);
  const u32 xo = (u32)((l & 31) * DM + 4 * h) * 2, go = h * 16;
  const char* xres = (const char*)(p.x + (size_t)(64 * g + 32 * tt) * DM + cb);
  u32x2 xa, xb; u32x4 ga, gb;
  asm volatile("" : "=v"(xa), "=v"(xb), "=v"(ga), "=v"(gb));
#define LDXR(SEL)                                                                                                     \
    asm volatile("s_cmp_lg_u32 %8, 0\n\ts_cbranch_scc1 .Lxr%=\n\t"                                                   \
                 "global_load_dwordx2 %0, %4, %5\n\tglobal_load_dwordx2 %1, %4, %5 offset:16\n\tglobal_load_dwordx4 %2, %6, %7 sc1\n\tglobal_load_dwordx4 %3, %6, %7 offset:32 sc1\n" \
                 ".Lxr%=:"                                                                                            \
                 : "+v"(xa), "+v"(xb), "+v"(ga), "+v"(gb) : "v"(xo), "s"(xres), "v"(go), "s"(modr + 4096 + cb * 4), "s"(SEL) : "memory", "scc")
  const int qt = blockIdx.x >> 6;
#pragma unroll 1
  for (int seg = 0; seg < 4; ++seg) {
    char* wsl = p.ws; int wl = w; u32 bl = blockIdx.x;
    if (seg != 0) asm volatile("" : "+s"(wsl), "+s"(wl), "+s"(bl));
    if (seg == 1) {
      ASTAMP(27)
      if (!kh) {
        PREP_ST()
        const u32 o = ((wl >> 1) & 1) * 69632 + 40960 + (wl & 1) * 14336;
        dma4s(wsl + WS_KV + o, vo, o); dma4s(wsl + WS_KV + o + 4096, vo, o + 4096); dma4s(wsl + WS_KV + o + 8192, vo, o + 8192);
        set_m0(o + 12288); dma1<0>(wsl + WS_KV + o + 12288, vo); dma1<1024>(wsl + WS_KV + o + 12288, vo);
        WAITV(14);
      } else {
        WAITV(0);
      }
      ASTAMP(10)
      BAR();
      ASTAMP(24)
      if (w == 0 && l < 8) put_flag(wsl + WS_FP + l * 1024 + bl * 4, tag);
    } else if (seg == 2) {
      ASTAMP(21)
      if (w == 0) {
        u32x4 f;
#pragma unroll 1
        for (int it = 0; it < SPIN_CAP; ++it) {
          asm volatile("global_load_dwordx4 %0, %1, %2 sc1\n\ts_waitcnt vmcnt(0)" : "=&v"(f) : "v"(vo), "s"(wsl + WS_FP + (bl & 7) * 1024) : "memory");
          if (__all(f[0] == tag && f[1] == tag && f[2] == tag && f[3] == tag)) break;
          __builtin_amdgcn_s_sleep(1);
        }
        if (bl == 0 && l == 0) put_flag(wsl + WS_EPOCH, epoch + 1u);
      }
      WAITV_BAR(0);
      ASTAMP(23)
    } else if (seg == 3) {
      ASTAMP(22)
      if (!kh) {
        dma4s((wsl + WS_WGU + (size_t)(((bl & 1) * 8 + ((bl >> 3) & 7)) * 7) * 32768) + wl * 8192, vo, SL0 + wl * 8192); dma4s((wsl + WS_WGU + (size_t)(((bl & 1) * 8 + ((bl >> 3) & 7)) * 7) * 32768) + wl * 8192 + 4096, vo, SL0 + wl * 8192 + 4096);
        dma4s((wsl + WS_WGU + (size_t)(((bl & 1) * 8 + ((bl >> 3) & 7)) * 7) * 32768) + 32768 + wl * 8192, vo, SL1 + wl * 8192); dma4s((wsl + WS_WGU + (size_t)(((bl & 1) * 8 + ((bl >> 3) & 7)) * 7) * 32768) + 32768 + wl * 8192 + 4096, vo, SL1 + wl * 8192 + 4096);
        set_m0(JUNK);
        touch8k((wsl + WS_WGU + (size_t)(((bl & 1) * 8 + ((bl >> 3) & 7)) * 7) * 32768) + 65536 + (bl >> 6) * 40960 + wl * 8192, l * 128);
        if (w < 2) touch8k(w == 0 ? (wsl + WS_WGU + (size_t)(((bl & 1) * 8 + ((bl >> 3) & 7)) * 7) * 32768) + 65536 + (bl >> 6) * 40960 + 32768 : wsl + WS_AUG + (size_t)((bl & 1) * 8 + ((bl >> 3) & 7)) * 7168, l * 128);
      }
    }
    LDWF((seg ^ 2) | (kh ^ 1));
    LDXR((seg ^ 2) | (kh ^ 1));
    TILE(0, kA, kB, vA0, vA1, vB0, vB1) TILE(1, kB, kA, vB0, vB1, vA0, vA1) TILE(2, kA, kB, vA0, vA1, vB0, vB1) TILE(3, kB, kA, vB0, vB1, vA0, vA1)
    TILE(4, kA, kB, vA0, vA1, vB0, vB1) TILE(5, kB, kA, vB0, vB1, vA0, vA1) TILE(6, kA, kB, vA0, vA1, vB0, vB1) TILE(7, kB, kA, vB0, vB1, vA0, vA1)
    ka += 8 * TS; va += 8 * TS; PIN(ka); PIN(va);
  }
  o1 = mfma(vB0, pb0, o1); o1 = mfma(vB1, pb1, o1);
  LDWF(kh);
  LDXR(kh);
  if (!kh) {
    set_m0(JUNK);
    touch8k(p.ws + WS_WD + (size_t)(2 * j) * 114688 + qt * 57344 + w * 8192, l * 128);
    if (w < 3) touch8k(p.ws + WS_WD + (size_t)(2 * j) * 114688 + qt * 57344 + (w + 4) * 8192, l * 128);
  }
#undef HALF
#undef TILE
  STAMP(2)
  {
    const u32 xs = OEX + w * 2304, xr = OEX + (w ^ 4) * 2304;
    float o[9];
    if (kh) { stsf4(xs + vo, f32x4{o0[0], o0[1], o0[2], o0[3]}); stsf4(xs + 1024 + vo, f32x4{o0[4], o0[5], o0[6], o0[7]}); *(LAS float*)(uintptr_t)(xs + 2048 + l * 4) = o0[8];
#pragma unroll
      for (int e = 0; e < 9; ++e) o[e] = o1[e];
    } else { stsf4(xs + vo, f32x4{o1[0], o1[1], o1[2], o1[3]}); stsf4(xs + 1024 + vo, f32x4{o1[4], o1[5], o1[6], o1[7]}); *(LAS float*)(uintptr_t)(xs + 2048 + l * 4) = o1[8];
#pragma unroll
      for (int e = 0; e < 9; ++e) o[e] = o0[e];
    }
    BAR();
    const f32x4 r0 = ldsf4(xr + vo), r1 = ldsf4(xr + 1024 + vo);
#pragma unroll
    for (int e = 0; e < 4; ++e) { o[e] += r0[e]; o[4 + e] += r1[e]; }
    float ls = lowhalf(o[8] + *(const LAS float*)(uintptr_t)(xr + 2048 + l * 4));
    bool bad = !(ls < 1.2676506e30f);
#pragma unroll
    for (int e = 0; e < 8; ++e) bad |= !(fabsf(o[e]) < 3.0e38f);
    if (__builtin_expect(__any(bad), 0)) {
      const u32x4 qf = kh ? q1 : q0;
      float m = -INFINITY;
      f32x16 oo = {};
#pragma unroll 1
      for (int t = 0; t < 64; ++t) {
        u32x4 kf, v0, v1;
        asm volatile("global_load_dwordx4 %0, %3, %5 sc1\n\tglobal_load_dwordx4 %1, %4, %5 sc1\n\tglobal_load_dwordx4 %2, %4, %5 offset:576 sc1\n\ts_waitcnt vmcnt(0)"
                     : "=&v"(kf), "=&v"(v0), "=&v"(v1) : "v"(vo), "v"(vrow), "s"(p.ws + WS_KV + t * TS) : "memory");
        const f32x16 s = mfma(kf, qf, f32x16{});
        const float mn = fmaxf(m, rowmax16(s)), al = EXP2(m - mn);
        m = mn;
        u32x4 pa, pb;
#pragma unroll
        for (int r = 0; r < 4; ++r) { pa[r] = pk(EXP2(s[2 * r] - mn), EXP2(s[2 * r + 1] - mn)); pb[r] = pk(EXP2(s[8 + 2 * r] - mn), EXP2(s[9 + 2 * r] - mn)); }
#pragma unroll
        for (int r = 0; r < 16; ++r) oo[r] *= al;
        oo = mfma(v0, pa, oo); oo = mfma(v1, pb, oo);
      }
#pragma unroll
      for (int e = 0; e < 8; ++e) o[e] = oo[e];
      ls = lowhalf(oo[8]);
    }
    const float rl = __builtin_amdgcn_rcpf(ls);
    gst16_sc1(p.ws + WS_O + fo + kh * 1024, vo, u32x4{pk(o[0] * rl, o[1] * rl), pk(o[2] * rl, o[3] * rl), pk(o[4] * rl, o[5] * rl), pk(o[6] * rl, o[7] * rl)});
  }
  asm volatile("s_waitcnt vmcnt(0)\n\ts_barrier"
               : "+v"(wf[0]), "+v"(wf[1]), "+v"(wf[2]), "+v"(wf[3]), "+v"(wf[4]), "+v"(wf[5]), "+v"(wf[6]), "+v"(wf[7]), "+v"(wf[8]), "+v"(wf[9]),
                 "+v"(wf[10]), "+v"(wf[11]), "+v"(wf[12]), "+v"(wf[13]), "+v"(wf[14]), "+v"(wf[15]), "+v"(xa), "+v"(xb), "+v"(ga), "+v"(gb)::"memory");
  STAMP(3)
  if (w == 1 && l == 0) put_flag(p.ws + WS_FO + g * 512 + fofs<4>(j), tag);
  if (w == 0) poll_flags<8, 4>(p.ws + WS_FO + g * 512, tag, l);
  BAR();
  STAMP(4)
  u32x4 x1;
  {
    const char* osrc = p.ws + WS_O + ((size_t)(2 * g + tt) * 32 + 8 * kq) * 1024;
    u32x4 xf[8];
    xf[0] = gld_s<1, 0>(osrc, vo); xf[1] = gld_s<1, 1024>(osrc, vo); xf[2] = gld_s<1, 2048>(osrc, vo); xf[3] = gld_s<1, 3072>(osrc, vo);
    xf[4] = gld_s<1, 0>(osrc + 4096, vo); xf[5] = gld_s<1, 1024>(osrc + 4096, vo); xf[6] = gld_s<1, 2048>(osrc + 4096, vo); xf[7] = gld_s<1, 3072>(osrc + 4096, vo);
    f32x16 acc0 = {}, acc1 = {};
#define P3_STEP(S) { waitv_pin<7 - (S)>(xf[S]); acc0 = mfma(wf[S], xf[S], acc0); acc1 = mfma(wf[8 + (S)], xf[S], acc1); }
    P3_STEP(0) P3_STEP(1) P3_STEP(2) P3_STEP(3) P3_STEP(4) P3_STEP(5) P3_STEP(6) P3_STEP(7)
#undef P3_STEP
    STAMP(5)
    float r[8];
    {
      const u32 d = SL3 + tt * 4096 + kq * 1024 + vo;
      sts128(d, pack8(acc0, 0)); sts128(d + 8192, pack8(acc1, 0)); sts128(d + 16384, pack8(acc0, 8)); sts128(d + 24576, pack8(acc1, 8));
      BAR();
      sum_pieces<4>(SL3 + w * 4096 + vo, r);
    }
    STAMP(6)
    x1 = u32x4{pk(fmaf(bflo(ga[0]), r[0], bflo(xa[0])), fmaf(bfhi(ga[0]), r[1], bfhi(xa[0]))), pk(fmaf(bflo(ga[2]), r[2], bflo(xa[1])), fmaf(bfhi(ga[2]), r[3], bfhi(xa[1]))),
               pk(fmaf(bflo(gb[0]), r[4], bflo(xb[0])), fmaf(bfhi(gb[0]), r[5], bfhi(xb[0]))), pk(fmaf(bflo(gb[2]), r[6], bflo(xb[1])), fmaf(bfhi(gb[2]), r[7], bfhi(xb[1])))};
    gst16_sc1(p.ws + WS_X1 + fo + kh * 1024, vo, x1);
    dma4s(wgu + 2 * 32768 + w * 4096, vo, SL2 + w * 4096);
    float s1 = 0.f, s2 = 0.f;
#pragma unroll
    for (int e = 0; e < 4; ++e) { const u32 v = x1[e]; s1 = dot2(v, 0x3f803f80u, s1); s2 = dot2(v, v, s2); }
    s1 = swap32_sum(s1); s2 = swap32_sum(s2);
    if (l < 32) *(LAS f32x2*)(uintptr_t)(GAPA + kq * 512 + tt * 256 + l * 8) = f32x2{s1, s2};
    BAR();
    if (w == 7) {
      f32x2 s = {0.f, 0.f};
#pragma unroll
      for (int i = 0; i < 4; ++i) s += ldsf2(GAPA + i * 512 + l * 8);
      gst8_sc1(p.ws + WS_STAT + ((size_t)64 * g * 8 + j) * 8, l * 64, u32x2{__float_as_uint(s[0]), __float_as_uint(s[1])});
      WAITV(5);
    } else {
      WAITV(4);
    }
    asm volatile("s_barrier" ::: "memory");
    STAMP(8)
    if (w == 0 && l == 0) put_flag(p.ws + WS_FX + (g >> 1) * 512 + fofs<8>((g & 1) * 8 + j), tag);
    if (w == 7) {
      WAITV(0);
      if (l == 0) put_flag(p.ws + WS_FS + (g >> 1) * 512 + fofs<8>((g & 1) * 8 + j), tag);
    }
  }
  u32x4 wd[28];
  if (kh == 0) p4_compute(p, id, l, w, tag, wd STAMP_PASS); else p4_helper(p, id, l, w, tag, wd STAMP_PASS);
  {
    int ly = l; PIN(ly);
    const char* xsrc = p.ws + WS_ACT + ((size_t)(2 * g) * 112 + 14 * w) * 1024;
    u32x4 xf[16];
#define XL(N) (xf[(N) % 16] = gld_s<1, 0>(xsrc + dks((N) >> 1) * 1024 + ((N) & 1) * 114688, vo))
    if (!kh) {
      WAITV(0);
      PSTAMP(19)
      if (ly == 0 && __hip_atomic_fetch_add((LAS u32*)(uintptr_t)GAPA, 1u, __ATOMIC_RELAXED, __HIP_MEMORY_SCOPE_WORKGROUP) == 3u)
        put_flag(p.ws + WS_FA + id.G * 512 + fofs<8>(id.c), tag);
      BAR();
    }
    STAMP(18)
    XL(0); XL(1); XL(2); XL(3); XL(4); XL(5); XL(6); XL(7); XL(8); XL(9); XL(10); XL(11);
    u32x4 g0 = gld_s<1, 0>(modr + 10240 + (64 * j + 32 * nt + 16 * kh) * 4, (ly >> 5) * 16), g1 = gld_s<1, 32>(modr + 10240 + (64 * j + 32 * nt + 16 * kh) * 4, (ly >> 5) * 16);
    if (kh) asm volatile("s_barrier" ::: "memory");
    f32x16 acc[4];
#define K5_X(N, CNT)                                                                                 \
  {                                                                                                  \
    waitv_pin<CNT>(xf[(N) % 16]);                                                                    \
    if ((N) < 2) { mf0(acc[(N) & 1], wd[0], xf[(N) & 1]); mf0(acc[2 + ((N) & 1)], wd[14], xf[(N) & 1]); } \
    else { mfa(acc[(N) & 1], wd[dks((N) >> 1)], xf[(N) % 16]); mfa(acc[2 + ((N) & 1)], wd[14 + dks((N) >> 1)], xf[(N) % 16]); } \
  }
    K5_X(0, 13) K5_X(1, 12) K5_X(2, 11) K5_X(3, 10) K5_X(4, 9) K5_X(5, 8) K5_X(6, 7) K5_X(7, 6) K5_X(8, 5) K5_X(9, 4) K5_X(10, 3) K5_X(11, 2)
    PSTAMP(24)
    if (w == 4) poll_flags<16, 8>(p.ws + WS_FA + (g >> 1) * 512, tag, ly);
    asm volatile("s_barrier" ::: "memory");
    PSTAMP(25)
    XL(12); XL(13); XL(14); XL(15); XL(16); XL(17); XL(18); XL(19); XL(20); XL(21); XL(22); XL(23); XL(24); XL(25); XL(26); XL(27);
    K5_X(12, 15) K5_X(13, 14) K5_X(14, 13) K5_X(15, 12) K5_X(16, 11) K5_X(17, 10) K5_X(18, 9) K5_X(19, 8) K5_X(20, 7) K5_X(21, 6) K5_X(22, 5) K5_X(23, 4)
    K5_X(24, 3) K5_X(25, 2) K5_X(26, 1) K5_X(27, 0)
#undef K5_X
    MFMA_SETTLE();
    PIN(g0); PIN(g1); PIN(acc[0]); PIN(acc[1]); PIN(acc[2]); PIN(acc[3]);
    STAMP(20)
    float r[8];
    {
      const u32 d = w * 1024 + vo;
#pragma unroll
      for (int pc = 0; pc < 8; ++pc) sts128(d + pc * 8192, pack8(acc[pc & 3], (pc >> 2) * 8));
      PSTAMP(26)
      BAR();
      PSTAMP(27)
      sum_pieces<8>(w * 8192 + vo, r);
    }
    const u32 a0 = pk(fmaf(bflo(g0[0]), r[0], bflo(x1[0])), fmaf(bfhi(g0[0]), r[1], bfhi(x1[0]))), a1 = pk(fmaf(bflo(g0[2]), r[2], bflo(x1[1])), fmaf(bfhi(g0[2]), r[3], bfhi(x1[1])));
    const u32 b0 = pk(fmaf(bflo(g1[0]), r[4], bflo(x1[2])), fmaf(bfhi(g1[0]), r[5], bfhi(x1[2]))), b1 = pk(fmaf(bflo(g1[2]), r[6], bflo(x1[3])), fmaf(bfhi(g1[2]), r[7], bfhi(x1[3])));
    const auto s0 = __builtin_amdgcn_permlane32_swap(a0, b0, false, false);
    const auto s1 = __builtin_amdgcn_permlane32_swap(a1, b1, false, false);
    int lz = l; PIN(lz);
    *(u32x4*)(p.out + (size_t)(64 * g + 32 * tt + (lz & 31)) * DM + 64 * j + 32 * nt + 16 * kh + 8 * (lz >> 5)) = u32x4{s0[0], s1[0], s0[1], s1[1]};
  }
  STAMP_FLUSH(0)
}

extern "C" void kernel_launch(void* const* d_in, const int* in_sizes, int n_in, void* d_out, int out_size, void* d_ws, size_t ws_size, hipStream_t stream) {
  Params p;
  p.x = (const u16*)d_in[0]; p.wq = (const u16*)d_in[1]; p.wk = (const u16*)d_in[2]; p.wv = (const u16*)d_in[3]; p.wo = (const u16*)d_in[4];
  p.wg = (const u16*)d_in[6]; p.wu = (const u16*)d_in[7]; p.wd = (const u16*)d_in[8]; p.c = (const u16*)d_in[9]; p.wm = (const u16*)d_in[10];
  p.out = (u16*)d_out; p.ws = (char*)d_ws;
  (void)hipFuncSetAttribute((const void*)k_main, hipFuncAttributeMaxDynamicSharedMemorySize, M_LDS);
  k_main<<<256, 512, M_LDS, stream>>>(p);
}
```

```cpp
#include <hip/hip_runtime.h>
#include <stdint.h>

typedef unsigned short u16;
typedef unsigned int u32;
typedef __attribute__((ext_vector_type(8))) __bf16 bf16x8;
typedef __attribute__((ext_vector_type(2))) __bf16 bf16x2;
typedef __attribute__((ext_vector_type(2))) float f32x2;
typedef __attribute__((ext_vector_type(4))) float f32x4;
typedef __attribute__((ext_vector_type(16))) float f32x16;
typedef __attribute__((ext_vector_type(4))) u32 u32x4;
typedef __attribute__((ext_vector_type(2))) u32 u32x2;
typedef __attribute__((ext_vector_type(4))) short s16x4;
#define LAS __attribute__((address_space(3)))
#define GAS __attribute__((address_space(1)))
#define DI __device__ __forceinline__

constexpr int DM = 512, FF = 1792;
constexpr float EPS = 1e-5f;
constexpr float C2 = 0.25f * 1.4426950408889634f;

constexpr size_t WS_EPOCH = 0;
constexpr size_t WS_FO = 4096;
constexpr size_t WS_FX = WS_FO + 16384;
constexpr size_t WS_FS = WS_FX + 8192;
constexpr size_t WS_FE = WS_FS + 8192;
constexpr size_t WS_FA = WS_FE + 8192;
constexpr size_t WS_FKV = WS_FA + 8192;
constexpr size_t WS_FP = WS_FKV + 8192;
constexpr size_t WS_MOD = 81920;
constexpr size_t WS_KV = WS_MOD + 8 * 12288;
constexpr size_t WS_STAT = WS_KV + 139264;
constexpr size_t WS_AUG = WS_STAT + 131072;
constexpr size_t WS_WO = 1u << 20;
constexpr size_t WS_WD = WS_WO + (512u << 10);
constexpr size_t WS_WGU = WS_WD + (1792u << 10);
constexpr size_t WS_O = WS_WGU + (3584u << 10);
constexpr size_t WS_X1 = WS_O + (2u << 20);
constexpr size_t WS_ACT = WS_X1 + (2u << 20);
static_assert(WS_FP + 8192 <= WS_MOD && WS_AUG + 114688 + 8192 <= WS_WO, "workspace map");
constexpr int TS = 2176;

struct Params {
  const u16 *x, *wq, *wk, *wv, *wo, *wg, *wu, *wd, *c, *wm;
  u16* out;
  char* ws;
};

DI f32x16 mfma(u32x4 a, u32x4 b, f32x16 c) {
  return __builtin_amdgcn_mfma_f32_32x32x16_bf16(__builtin_bit_cast(bf16x8, a), __builtin_bit_cast(bf16x8, b), c, 0, 0, 0);
}
DI u32 pk(float lo, float hi) {
  f32x2 v = {lo, hi};
  return __builtin_bit_cast(u32, __builtin_convertvector(v, bf16x2));
}
DI float bflo(u32 u) { return __uint_as_float(u << 16); }
DI float bfhi(u32 u) { return __uint_as_float(u & 0xffff0000u); }
DI float rbf(float f) { return bflo(pk(f, 0.f)); }
DI float dot2(u32 a, u32 b, float c) { return __builtin_amdgcn_fdot2_f32_bf16(__builtin_bit_cast(bf16x2, a), __builtin_bit_cast(bf16x2, b), c, false); }
template <int N, int SC> DI void dma(const void* base, u32 vo, u32 lds) {
#define DMA_HEAD "s_mov_b32 m0, %2\n\ts_nop 0\n\t"
#define DMA_OPS ::"v"(vo), "s"(base), "s"(lds) : "memory"
  if (N == 1 && !SC) asm volatile(DMA_HEAD "global_load_lds_dwordx4 %0, %1" DMA_OPS);
  if (N == 2 && !SC) asm volatile(DMA_HEAD "global_load_lds_dwordx4 %0, %1\n\tglobal_load_lds_dwordx4 %0, %1 offset:1024" DMA_OPS);
  if (N == 2 && SC) asm volatile(DMA_HEAD "global_load_lds_dwordx4 %0, %1 sc1\n\tglobal_load_lds_dwordx4 %0, %1 offset:1024 sc1" DMA_OPS);
  if (N == 4 && SC) asm volatile(DMA_HEAD "global_load_lds_dwordx4 %0, %1 sc1\n\tglobal_load_lds_dwordx4 %0, %1 offset:1024 sc1\n\t"
                                 "global_load_lds_dwordx4 %0, %1 offset:2048 sc1\n\tglobal_load_lds_dwordx4 %0, %1 offset:3072 sc1" DMA_OPS);
}
DI void dma4s(const void* base, u32 vo, u32 lds) { dma<4, 1>(base, vo, lds); }
template <int SC, int OFF> DI u32x4 gld_s(const void* base, u32 vo) {
  u32x4 r;
  if (SC) asm volatile("global_load_dwordx4 %0, %1, %2 offset:%3 sc1" : "=v"(r) : "v"(vo), "s"(base), "n"(OFF) : "memory");
  else asm volatile("global_load_dwordx4 %0, %1, %2 offset:%3" : "=v"(r) : "v"(vo), "s"(base), "n"(OFF) : "memory");
  return r;
}
template <int OFF> DI u32 gld2_s(const void* base, u32 vo) { u32 r; asm volatile("global_load_ushort %0, %1, %2 offset:%3" : "=v"(r) : "v"(vo), "s"(base), "n"(OFF) : "memory"); return r; }
DI void touch8k(const char* base, u32 vo) { const u32 lds = 163840 - 256; asm volatile(DMA_HEAD "global_load_lds_dword %0, %1 sc1" DMA_OPS); }
DI void gst16_sc1(char* base, u32 vo, u32x4 v) { asm volatile("global_store_dwordx4 %0, %1, %2 sc1\n\ts_nop 1" ::"v"(vo), "v"(v), "s"(base) : "memory"); }
DI void gst8_sc1(char* base, u32 vo, u32x2 v) { asm volatile("global_store_dwordx2 %0, %1, %2 sc1\n\ts_nop 1" ::"v"(vo), "v"(v), "s"(base) : "memory"); }
#define RLX_AGENT __ATOMIC_RELAXED, __HIP_MEMORY_SCOPE_AGENT
DI void put_flag(char* f, u32 tag) { __hip_atomic_store((GAS u32*)f, tag, RLX_AGENT); }
constexpr int SPIN_CAP = 1 << 20;
template <int PER> DI u32 fofs(int i) { return (u32)((i / PER) * 256 + (i % PER) * 4); }
template <int N, int PER, int NAP = 0> DI void poll_flags(const char* f, u32 tag, int l) {
  const char* a = f + fofs<PER>(l < N ? l : 0);
#pragma unroll 1
  for (int it = 0; it < SPIN_CAP; ++it) {
    const u32 v = __hip_atomic_load((const GAS u32*)a, RLX_AGENT);
    if (__all(v == tag)) break;
    if (NAP) __builtin_amdgcn_s_sleep(NAP);
  }
  __builtin_amdgcn_fence(__ATOMIC_ACQUIRE, "wavefront");
}
#define SB() __builtin_amdgcn_sched_barrier(0)
#define PIN(x) asm volatile("" : "+v"(x))
#define WAITV(N) asm volatile("s_waitcnt vmcnt(" #N ")" ::: "memory")
#define WAITV_BAR(N) asm volatile("s_waitcnt vmcnt(" #N ") lgkmcnt(0)\n\ts_barrier" ::: "memory")
#define BAR() asm volatile("s_waitcnt lgkmcnt(0)\n\ts_barrier" ::: "memory")
template <int N> DI void waitv_pin(u32x4& v) { asm volatile("s_waitcnt vmcnt(%1)" : "+v"(v) : "n"(N) : "memory"); }
DI u32x4 lds128(u32 a) { return *(const LAS u32x4*)(uintptr_t)a; }
DI f32x4 ldsf4(u32 a) { return *(const LAS f32x4*)(uintptr_t)a; }
DI f32x2 ldsf2(u32 a) { return *(const LAS f32x2*)(uintptr_t)a; }
DI void sts128(u32 a, u32x4 v) { *(LAS u32x4*)(uintptr_t)a = v; }
DI void stsf4(u32 a, f32x4 v) { *(LAS f32x4*)(uintptr_t)a = v; }
DI u32x2 ldstr(u32 a) { return __builtin_bit_cast(u32x2, __builtin_amdgcn_ds_read_tr16_b64_v4i16((LAS s16x4*)(uintptr_t)a)); }
DI u32x4 ldstr2(u32 a0, u32 a1) { u32x2 p = ldstr(a0), q = ldstr(a1); return u32x4{p[0], p[1], q[0], q[1]}; }
template <int CTRL> DI float dpp(float v) { return __int_as_float(__builtin_amdgcn_mov_dpp(__float_as_int(v), CTRL, 0xf, 0xf, true)); }
DI float swap32_sum(float v) { auto r = __builtin_amdgcn_permlane32_swap(__float_as_uint(v), __float_as_uint(v), false, false); return __uint_as_float(r[0]) + __uint_as_float(r[1]); }
DI float swap32_max(float v) { auto r = __builtin_amdgcn_permlane32_swap(__float_as_uint(v), __float_as_uint(v), false, false); return fmaxf(__uint_as_float(r[0]), __uint_as_float(r[1])); }
DI float swap16_sum(float v) { auto r = __builtin_amdgcn_permlane16_swap(__float_as_uint(v), __float_as_uint(v), false, false); return __uint_as_float(r[0]) + __uint_as_float(r[1]); }
DI float lowhalf(float v) { auto r = __builtin_amdgcn_permlane32_swap(__float_as_uint(v), __float_as_uint(v), false, false); return __uint_as_float(r[0]); }
template <int CTRL> DI float halve(bool bit, float a, float b) { return (bit ? b : a) + dpp<CTRL>(bit ? a : b); }
DI int rfl(int v) { return __builtin_amdgcn_readfirstlane(v); }
#define EXP2(v) __builtin_amdgcn_exp2f(v)

struct Ids { int G, c, g, j; };
DI Ids ids() { const int b = blockIdx.x, x = b & 7, i = b >> 3; Ids r; r.G = (x >> 1) * 4 + (i >> 3); r.c = (x & 1) * 8 + (i & 7); r.g = 4 * x + (i >> 3); r.j = i & 7; return r; }

constexpr u32 M_LDS = 163840;
constexpr u32 F_A = 0;
constexpr u32 F_H = 65536;
constexpr u32 F_B = 131072;
constexpr u32 F_D = F_B + 16640;
constexpr u32 F_M = F_D + 10240;
static_assert(F_M + 4608 <= M_LDS, "front-end LDS map");
constexpr u32 SL0 = 0, SL1 = 69632, SL2 = 32768, SL3 = 102400, TOPA = 135168, GAPA = 65536, OEX = 143616;

DI void mfa(f32x16& c, u32x4 a, u32x4 b) { asm volatile("v_mfma_f32_32x32x16_bf16 %0, %1, %2, %0" : "+v"(c) : "v"(a), "v"(b) : "memory"); }
DI void mf0(f32x16& c, u32x4 a, u32x4 b) { asm volatile("v_mfma_f32_32x32x16_bf16 %0, %1, %2, 0" : "=&v"(c) : "v"(a), "v"(b) : "memory"); }
DI float vexp(float a) { float r; asm volatile("v_exp_f32 %0, %1" : "=v"(r) : "v"(a)); return r; }
DI float vrcp(float a) { float r; asm volatile("v_rcp_f32 %0, %1" : "=v"(r) : "v"(a)); return r; }
DI u32 vcvt(float a, float b) { u32 r; asm volatile("v_cvt_pk_bf16_f32 %0, %1, %2" : "=v"(r) : "v"(a), "v"(b)); return r; }
DI float vmul(float a, float b) { float r; asm volatile("v_mul_f32 %0, %1, %2" : "=v"(r) : "v"(a), "v"(b)); return r; }
DI float vfma_k(float a, float k, float c) { float r; asm volatile("v_fma_f32 %0, %1, %2, %3" : "=v"(r) : "v"(a), "s"(k), "v"(c)); return r; }
DI float vadd1(float a) { float r; asm volatile("v_add_f32 %0, 1.0, %1" : "=v"(r) : "v"(a)); return r; }
#define MFMA_SETTLE() asm volatile("s_nop 15\n\ts_nop 3" ::: "memory")
DI float rowmax16(const f32x16& s) {
  float m = fmaxf(fmaxf(s[0], s[1]), s[2]);
#pragma unroll
  for (int r = 3; r < 15; r += 2) m = fmaxf(fmaxf(m, s[r]), s[r + 1]);
  return swap32_max(fmaxf(m, s[15]));
}
constexpr float GC0 = -2.f * 0.7978845608028654f * 1.4426950408889634f, GC1 = GC0 * 0.044715f;

DI u32x4 pack8(const f32x16& a, int o8) { return u32x4{pk(a[o8], a[o8 + 1]), pk(a[o8 + 2], a[o8 + 3]), pk(a[o8 + 4], a[o8 + 5]), pk(a[o8 + 6], a[o8 + 7])}; }
template <int N> DI void sum_pieces(u32 a, float (&r)[8]) {
  u32 klo = 0x00003f80u, khi = 0x3f800000u;
  asm volatile("" : "+s"(klo), "+s"(khi));
  u32x4 u[N];
#pragma unroll
  for (int i = 0; i < N; ++i) u[i] = lds128(a + i * 1024);
#pragma unroll
  for (int e = 0; e < 4; ++e) { r[2 * e] = bflo(u[0][e]); r[2 * e + 1] = bfhi(u[0][e]); }
#pragma unroll
  for (int i = 1; i < N; ++i)
#pragma unroll
    for (int e = 0; e < 4; ++e) { const u32 v = u[i][e]; r[2 * e] = dot2(v, klo, r[2 * e]); r[2 * e + 1] = dot2(v, khi, r[2 * e + 1]); }
}

constexpr u32 SL4 = 0xffffffffu;
template <u32 SL, int S> DI u32x4 wrd(u32 lo, u32 mid, u32 hi) {
  constexpr u32 A = SL == SL4 ? (S < 28 ? TOPA + S * 1024 : GAPA + (S - 28) * 1024) : SL + S * 1024;
  return A < 65536 ? lds128(lo + A) : (A < 131072 ? lds128(mid + (A - 65536)) : lds128(hi + (A - 131072)));
}
DI constexpr int dks(int i) { return i < 6 ? 7 * (i / 3) + i % 3 : 7 * ((i - 6) >> 2) + 3 + ((i - 6) & 3); }

DI void p4_compute(const Params& p, const Ids& id, int l, int w, u32 tag, u32x4 (&wd)[28]) {
  const u32 vo = l * 16;
  char* adst = p.ws + WS_ACT + ((size_t)(4 * id.G + w) * 112 + 7 * id.c) * 1024;
  const char* asrc = p.ws + WS_AUG + (size_t)id.c * 7168;
  u32x4 aug[7];
  aug[0] = gld_s<1, 0>(asrc, vo); aug[1] = gld_s<1, 1024>(asrc, vo); aug[2] = gld_s<1, 2048>(asrc, vo);
  if (w == 0) poll_flags<16, 8>(p.ws + WS_FX + id.G * 512, tag, l);
  asm volatile("s_waitcnt vmcnt(0)\n\ts_barrier" : "+v"(aug[0]), "+v"(aug[1]), "+v"(aug[2])::"memory");
  const char* xsrc = p.ws + WS_X1 + (size_t)(4 * id.G + w) * 32768;
  u32x4 xf[32];
#define XLD(S) (xf[S] = gld_s<1, ((S) & 3) * 1024>(xsrc + ((S) >> 2) * 4096, vo))
  XLD(0); XLD(1); XLD(2); XLD(3); XLD(4); XLD(5); XLD(6); XLD(7); XLD(8); XLD(9); XLD(10); XLD(11); XLD(12); XLD(13); XLD(14); XLD(15);
  float rs, gc0 = GC0; u32x4 xaug;
  u32 lo = vo, mid = vo + 65536, hi = vo + 131072;
  PIN(lo); PIN(mid); PIN(hi); PIN(gc0);
  f32x16 acc[3]; u32x4 ra[32], rb[32];
  ra[0] = wrd<SL0, 0>(lo, mid, hi); rb[0] = wrd<SL1, 0>(lo, mid, hi); ra[1] = wrd<SL0, 1>(lo, mid, hi); rb[1] = wrd<SL1, 1>(lo, mid, hi);
  ra[2] = wrd<SL0, 2>(lo, mid, hi); rb[2] = wrd<SL1, 2>(lo, mid, hi);
#define PH0(S) { constexpr int s = (S);                                                                            \
      waitv_pin<(s < 16 ? 15 : 31 - s)>(xf[s]);                                                                    \
      if (s == 0) mf0(acc[0], ra[0], xf[0]); else mfa(acc[0], ra[s], xf[s]);                                       \
      if (s + 16 < 32) XLD((s + 16) & 31);                                                                         \
      if (s + 3 < 32) { ra[(s + 3) & 31] = wrd<SL0, (s + 3) & 31>(lo, mid, hi); rb[(s + 3) & 31] = wrd<SL1, (s + 3) & 31>(lo, mid, hi); } \
      if (s == 0) mf0(acc[1], rb[0], xf[0]); else mfa(acc[1], rb[s], xf[s]); }
 PH0(0) PH0(1) PH0(2) PH0(3) PH0(4) PH0(5) PH0(6) PH0(7) \
 PH0(8) PH0(9) PH0(10) PH0(11) PH0(12) PH0(13) PH0(14) PH0(15) \
 PH0(16) PH0(17) PH0(18) PH0(19) PH0(20) PH0(21) PH0(22) PH0(23) \
 PH0(24) PH0(25) PH0(26) PH0(27) PH0(28) PH0(29) PH0(30) PH0(31)
#undef PH0
  ra[0] = wrd<SL2, 0>(lo, mid, hi); ra[1] = wrd<SL2, 1>(lo, mid, hi); ra[2] = wrd<SL2, 2>(lo, mid, hi); ra[3] = wrd<SL2, 3>(lo, mid, hi);
  asm volatile("s_barrier" ::: "memory");
  xaug = lds128(GAPA + w * 512 + (l & 31) * 16);
  rs = *(const LAS float*)(uintptr_t)(GAPA + 2048 + w * 128 + (l & 31) * 4);
  f32x4 gv, uv, zz; u32x4 q;
#define E4(F) { _Pragma("unroll") for (int e = 0; e < 4; ++e) { F; } }
#define E2(B, F) { _Pragma("unroll") for (int e = B; e < B + 2; ++e) { F; } }
#define UNIT(T, U) { constexpr int u = (U) % 13, GO = ((U) / 13) * 8; const f32x16& k_ = acc[(T) % 3];              \
      if (u == 0) E4(gv[e] = vmul(k_[GO + e], rs))                                                                  \
      if (u == 1) E4(zz[e] = vmul(gv[e], gv[e]))                                                                    \
      if (u == 2) E4(zz[e] = vfma_k(zz[e], GC1, gc0))                                                               \
      if (u == 3) E4(zz[e] = vmul(zz[e], gv[e]))                                                                    \
      if (u == 4) E2(0, zz[e] = vexp(zz[e]))                                                                        \
      if (u == 5) E2(2, zz[e] = vexp(zz[e]))                                                                        \
      if (u == 6) E4(uv[e] = vmul(k_[GO + 4 + e], rs))                                                              \
      if (u == 7) E4(zz[e] = vadd1(zz[e]))                                                                          \
      if (u == 8) E2(0, zz[e] = vrcp(zz[e]))                                                                        \
      if (u == 9) E2(2, zz[e] = vrcp(zz[e]))                                                                        \
      if (u == 10) E4(uv[e] = vmul(uv[e], gv[e]))                                                                   \
      if (u == 11) E4(uv[e] = vmul(uv[e], zz[e]))                                                                   \
      if (u == 12) { q[GO / 4] = vcvt(uv[0], uv[1]); q[GO / 4 + 1] = vcvt(uv[2], uv[3]); if (GO) gst16_sc1(adst + (T) * 1024, vo, q); } }
#define E0(T) ((T) == 0 ? 9 : (T) == 1 ? 35 : (T) == 2 ? 61 : (T) == 3 ? 87 : (T) == 4 ? 113 : 139)
#define GAP1(T, G) { if ((G) >= E0(T) && (G) < E0(T) + 26) UNIT(T, ((G) - E0(T) + 26) % 26) }
#define GAP(G) { GAP1(0, G) GAP1(1, G) GAP1(2, G) GAP1(3, G) GAP1(4, G) GAP1(5, G) }
#define PH(S) { constexpr int s = (S);                                                                             \
      if (s == 0) mf0(acc[TT % 3], ra[0], xf[0]); else if (s < 32) mfa(acc[TT % 3], ra[s & 31], xf[s & 31]); else mfa(acc[TT % 3], aug[TT], xaug); \
      if (s < 32) { if (s + 4 < 32) ra[(s + 4) & 31] = wrd<SA, (s + 4) & 31>(lo, mid, hi); else if (TT < 6) ra[(s + 4) & 31] = wrd<SN, (s + 4) & 3>(lo, mid, hi); } \
      if (TT == 2 && s == 3) { mfa(acc[0], aug[0], xaug); mfa(acc[1], aug[1], xaug); }                             \
      if (s == 1 && TT < 6) aug[TT < 6 ? TT + 1 : 0] = gld_s<1, 0>(asrc + (TT + 1) * 1024, vo);                    \
      GAP((TT - 2) * 33 + s) }
#define PHASE  PH(0) PH(1) PH(2) PH(3) PH(4) PH(5) PH(6) PH(7) \
 PH(8) PH(9) PH(10) PH(11) PH(12) PH(13) PH(14) PH(15) \
 PH(16) PH(17) PH(18) PH(19) PH(20) PH(21) PH(22) PH(23) \
 PH(24) PH(25) PH(26) PH(27) PH(28) PH(29) PH(30) PH(31) \
 PH(32)
  { constexpr int TT = 2; constexpr u32 SA = SL2, SN = SL3; PHASE }
  asm volatile("s_waitcnt vmcnt(0)\n\ts_barrier" : "+v"(aug[3])::"memory");
  { constexpr int TT = 3; constexpr u32 SA = SL3, SN = SL4; PHASE }
  asm volatile("s_waitcnt vmcnt(1)\n\ts_barrier" : "+v"(aug[4])::"memory");
  { constexpr int TT = 4; constexpr u32 SA = SL4, SN = SL0; PHASE }
  asm volatile("s_waitcnt vmcnt(1)\n\ts_barrier" : "+v"(aug[5])::"memory");
  { constexpr int TT = 5; constexpr u32 SA = SL0, SN = SL1; PHASE }
  asm volatile("s_waitcnt vmcnt(1)\n\ts_barrier" : "+v"(aug[6])::"memory");
  { constexpr int TT = 6; constexpr u32 SA = SL1, SN = SL1; PHASE }
  MFMA_SETTLE();
 UNIT(6, 0) UNIT(6, 1) UNIT(6, 2) UNIT(6, 3) UNIT(6, 4) UNIT(6, 5) UNIT(6, 6) UNIT(6, 7) UNIT(6, 8)
 UNIT(6, 9) UNIT(6, 10) UNIT(6, 11) UNIT(6, 12) UNIT(6, 13) UNIT(6, 14) UNIT(6, 15) UNIT(6, 16) UNIT(6, 17)
 UNIT(6, 18) UNIT(6, 19) UNIT(6, 20) UNIT(6, 21) UNIT(6, 22) UNIT(6, 23) UNIT(6, 24) UNIT(6, 25)
  asm volatile("s_barrier" ::: "memory");
  {
    const u32 b = w * 8192;
#define WDR(F) (wd[F] = (F) < 8 ? lds128(lo + SL2 + b + ((F) & 7) * 1024) : (F) < 16 ? lds128(mid + (SL3 - 65536) + b + ((F) & 7) * 1024) : (F) < 22 ? lds128(hi + (TOPA - 131072) + w * 6144 + (((F) - 16) & 7) * 1024) : lds128(lo + SL0 + w * 6144 + (((F) - 22) & 7) * 1024))
 WDR(0); WDR(1); WDR(2); WDR(3); WDR(4); WDR(5); WDR(6); WDR(7); WDR(8); WDR(9);
 WDR(10); WDR(11); WDR(12); WDR(13); WDR(14); WDR(15); WDR(16); WDR(17); WDR(18); WDR(19);
 WDR(20); WDR(21); WDR(22); WDR(23); WDR(24); WDR(25); WDR(26); WDR(27);
#undef WDR
  }
#undef PHASE
#undef PH
#undef GAP
#undef GAP1
#undef UNIT
#undef XLD
}

DI void p4_helper(const Params& p, const Ids& id, int l, int w, u32 tag, u32x4 (&wd)[28]) {
  const u32 vo = l * 16;
  const int hw = w - 4;
  const char* wsrc = p.ws + WS_WGU + (size_t)id.c * 7 * 32768 + hw * 8192;
  const char* dme = p.ws + WS_WD + ((size_t)(2 * id.j) * 112 + 14 * w) * 1024;
  const char* dcw = p.ws + WS_WD + ((size_t)(2 * id.j) * 112 + 14 * hw) * 1024;
#define D8(SRC, DST) { dma4s(SRC, vo, DST); dma4s((SRC) + 4096, vo, (DST) + 4096); }
#define D6(SRC, DST) { dma4s(SRC, vo, DST); dma<2, 1>((SRC) + 4096, vo, (DST) + 4096); }
#define WDG(F) (wd[F] = gld_s<1, 0>(dme + ((F) < 14 ? (F) : 98 + (F)) * 1024, vo))
  D8(wsrc + 3 * 32768, SL3 + hw * 8192)
  WDG(16); WDG(17); WDG(18); WDG(19); WDG(20); WDG(21); WDG(22); WDG(23); WDG(24); WDG(25); WDG(26); WDG(27);
  WAITV_BAR(20);
  {
    __builtin_amdgcn_s_sleep(8);
    poll_flags<16, 8, 3>(p.ws + WS_FS + id.G * 512, tag, l);
    u32x4 st0, st1;
    asm volatile("global_load_dwordx4 %0, %2, %3 sc1\n\tglobal_load_dwordx4 %1, %2, %3 offset:16 sc1\n\ts_waitcnt vmcnt(0)" : "=&v"(st0), "=&v"(st1)
                 : "v"((l & 31) * 64 + (l >> 5) * 32), "s"(p.ws + WS_STAT + (size_t)(128 * id.G + 32 * hw) * 64) : "memory");
    const float s1 = swap32_sum((__uint_as_float(st0[0]) + __uint_as_float(st0[2])) + (__uint_as_float(st1[0]) + __uint_as_float(st1[2])));
    const float s2 = swap32_sum((__uint_as_float(st0[1]) + __uint_as_float(st0[3])) + (__uint_as_float(st1[1]) + __uint_as_float(st1[3])));
    const float nmu = -s1 * (1.f / DM), var = fmaxf(s2 * (1.f / DM) - nmu * nmu, 0.f) + EPS;
    const float rs = __builtin_amdgcn_rsqf(var), sg = var * rs, mh = rbf(nmu), sh = rbf(sg);
    if (l < 32) { sts128(GAPA + hw * 512 + vo, u32x4{pk(mh, mh), pk(nmu - mh, sh), pk(sh, sg - sh), 0u}); *(LAS float*)(uintptr_t)(GAPA + 2048 + hw * 128 + l * 4) = rs; }
  }
  dma4s(wsrc + 4 * 32768, vo, TOPA + hw * 8192); if (hw < 3) dma4s(wsrc + 4 * 32768 + 4096, vo, TOPA + hw * 8192 + 4096);
  BAR();
  D8(wsrc + 5 * 32768, SL0 + hw * 8192) WDG(0); WDG(1); WDG(2); WDG(3);
  WAITV_BAR(12);
  if (hw == 3) dma4s(wsrc + 4 * 32768 + 4096, vo, GAPA);
  D8(wsrc + 6 * 32768, SL1 + hw * 8192) dma4s(dcw, vo, SL2 + hw * 8192);
  WAITV_BAR(12);
  dma4s(dcw + 4096, vo, SL2 + hw * 8192 + 4096); D6(dcw + 8192, SL3 + hw * 8192)
  dma<2, 1>(dcw + 114688, vo, SL3 + hw * 8192 + 6144);
  WAITV_BAR(16);
  D6(dcw + 114688 + 2048, TOPA + hw * 6144) WDG(4); WDG(5); WDG(6); WDG(7); WDG(8); WDG(9);
  if (w == 4 && l == 0) *(LAS u32*)(uintptr_t)GAPA = 0u;
  BAR();
  if (w == 5 && l == 0) put_flag(p.ws + WS_FE + id.G * 512 + fofs<8>(id.c), tag);
  D6(dcw + 114688 + 8192, SL0 + hw * 6144) WDG(10); WDG(11); WDG(12); WDG(13); WDG(14); WDG(15);
  WAITV_BAR(6);
  poll_flags<16, 8, 4>(p.ws + WS_FE + (id.g >> 1) * 512, tag, l);
  asm volatile("s_waitcnt vmcnt(0)" : "+v"(wd[0]), "+v"(wd[1]), "+v"(wd[2]), "+v"(wd[3]), "+v"(wd[4]), "+v"(wd[5]), "+v"(wd[6]), "+v"(wd[7]), "+v"(wd[8]), "+v"(wd[9]), "+v"(wd[10]),
               "+v"(wd[11]), "+v"(wd[12]), "+v"(wd[13])::"memory");
  asm volatile("" : "+v"(wd[14]), "+v"(wd[15]), "+v"(wd[16]), "+v"(wd[17]), "+v"(wd[18]), "+v"(wd[19]), "+v"(wd[20]), "+v"(wd[21]), "+v"(wd[22]), "+v"(wd[23]), "+v"(wd[24]),
               "+v"(wd[25]), "+v"(wd[26]), "+v"(wd[27])::"memory");
#undef WDG
#undef D8
#undef D6
}

__global__ __launch_bounds__(512, 2) void k_main(Params p) {
  extern __shared__ __attribute__((aligned(16))) char smem[];
  const int tid = threadIdx.x, l = tid & 63, w = rfl(tid >> 6);
  const Ids id = ids();
  const int g = id.g, j = id.j, h = l >> 5;
  const u32 vo = l * 16;
  const u32 epoch = *(const u32*)(p.ws + WS_EPOCH);
  const u32 tag = epoch * 0x9E3779B1u + 0x7F4A7C15u;
  const int tt = w & 1, nt = (w >> 1) & 1, kh = w >> 2, kq = w >> 1;
  const char* modr = p.ws + WS_MOD + (blockIdx.x & 7) * 12288;
  const char* wgu = p.ws + WS_WGU + (size_t)id.c * 7 * 32768;
  const char* wob = p.ws + WS_WO + ((size_t)(2 * j) * 32 + 8 * kq) * 1024;
  const char *wob1 = wob + 4096, *wob2 = wob + 32768, *wob3 = wob + 36864;
  u32x4 q0, q1, fo_, fd_, fg[2];
  char *pdo, *pdd, *pdg;
  f32x16 o0, o1;
  u32x4 vB0, vB1, pb0, pb1;
  {
    const int nh = (l >> 4) & 1, q = (l & 15) >> 2, pp = l & 3;
    const int uid = g * 7 + j - 1, uT = uid >> 1, ukh = uid & 1;
    const int rk = (blockIdx.x >> 6) * 7 + j - 1;
    const bool gemv = j != 0 && rk < 24;
    const int mu = 4 * ((blockIdx.x & 7) + 8 * (rk >> 2)) + (rk & 3);
    const u32 rawo = (l >> 3) * 1024 + (((l & 7) ^ (((l >> 4) & 1) << 2)) * 16);
#define WQ_DMA() { _Pragma("unroll") for (int i = 0; i < 8; ++i) { dma<1, 0>(p.wq + (size_t)(64 * w + 8 * i) * DM + 64 * j, rawo, F_A + (8 * w + i) * 1024); } }
#define LATE_DMA() {                                                                                                                              \
      if (j != 0) {                                                                                                                               \
        const u32 go = (l >> 1) * (FF * 2) + (l & 1) * 16;                                                                                        \
        dma<1, 0>(p.wg + (size_t)(256 * ukh + 32 * w) * FF + 16 * uT, go, F_B + w * 1024);                                                   \
        dma<1, 0>(p.wu + (size_t)(256 * ukh + 32 * w) * FF + 16 * uT, go, F_B + 8192 + 128 + w * 1024);                                      \
        dma<1, 0>(p.wd + (size_t)(64 * (uid >> 3) + 8 * w) * DM + 64 * (uid & 7), rawo, F_D + w * 1024);                                     \
      }                                                                                                                                           \
      if (w >= 6) { dma<1, 0>(p.wo + (size_t)(16 * g + 8 * (w - 6)) * DM + 64 * j, rawo, F_D + 8192 + (w - 6) * 1024); } }
    u32x4 w0, w1; u32 c0, c1;
    if (gemv) {
      const u32 mo = (tid >> 1) * 6144 + (tid & 1) * 16;
      w0 = gld_s<0, 0>(p.wm + 16 * mu, mo); w1 = gld_s<0, 0>(p.wm + 16 * mu + 256 * 3072, mo);
      c0 = gld2_s<0>(p.c, (tid >> 1) * 2); c1 = gld2_s<512>(p.c, (tid >> 1) * 2);
    }
    u32x4 xv[8];
    {
      const char* xs = (const char*)(p.x + (size_t)(64 * g + 8 * w) * DM);
      xv[0] = gld_s<0, 0>(xs, vo); xv[1] = gld_s<0, 1024>(xs, vo); xv[2] = gld_s<0, 2048>(xs, vo); xv[3] = gld_s<0, 3072>(xs, vo);
      xv[4] = gld_s<0, 0>(xs + 4096, vo); xv[5] = gld_s<0, 1024>(xs + 4096, vo); xv[6] = gld_s<0, 2048>(xs + 4096, vo); xv[7] = gld_s<0, 3072>(xs + 4096, vo);
    }
    if (j == 0) {
      dma<2, 0>(p.wv + w * 1024, vo, F_H + w * 2048);
      dma<2, 0>(p.wk + w * 1024, vo, F_H + 16384 + 128 + w * 2048);
      WQ_DMA() LATE_DMA()
    }
    if (gemv) {
      asm volatile("s_waitcnt vmcnt(8)" : "+v"(w0), "+v"(w1), "+v"(c0), "+v"(c1)::"memory");
      const float f0 = bflo(c0), f1 = bflo(c1);
      const float s0 = rbf(f0 * __builtin_amdgcn_rcpf(1.f + EXP2(-1.4426950408889634f * f0))), s1 = rbf(f1 * __builtin_amdgcn_rcpf(1.f + EXP2(-1.4426950408889634f * f1)));
      float a[8];
#pragma unroll
      for (int i = 0; i < 4; ++i) { a[2 * i] = s0 * bflo(w0[i]) + s1 * bflo(w1[i]); a[2 * i + 1] = s0 * bfhi(w0[i]) + s1 * bfhi(w1[i]); }
#pragma unroll
      for (int i = 0; i < 4; ++i) a[i] = halve<0x4E>(l & 2, a[i], a[i + 4]);
#pragma unroll
      for (int i = 0; i < 2; ++i) a[i] = halve<0x124>(l & 4, a[i], a[i + 2]);
      a[0] = halve<0x128>(l & 8, a[0], a[1]);
      a[0] = swap32_sum(swap16_sum(a[0]));
      if (l < 16) *(LAS float*)(uintptr_t)(F_M + w * 64 + ((l & 1) * 8 + (l & 2) * 2 + ((l >> 1) & 2) + (l >> 3)) * 4) = a[0];
      BAR();
      if (w == 7) {
        f32x2 s = {0.f, 0.f};
#pragma unroll
        for (int i = 0; i < 8; ++i) s += ldsf2(F_M + i * 64 + (l & 7) * 8);
        gst8_sc1(p.ws + WS_MOD + mu * 64, (l >> 3) * 12288 + (l & 7) * 8, u32x2{pk(s[0], s[1]), tag});
      }
    }
    if (j == 0) WAITV(12); else if (gemv && w == 7) WAITV(1); else WAITV(0);
    asm volatile("" : "+v"(xv[0]), "+v"(xv[1]), "+v"(xv[2]), "+v"(xv[3]), "+v"(xv[4]), "+v"(xv[5]), "+v"(xv[6]), "+v"(xv[7]));
    float xh[8][8];
    {
      float s1[8], s2[8];
#pragma unroll
      for (int i = 0; i < 8; ++i) {
        s1[i] = 0.f; s2[i] = 0.f;
#pragma unroll
        for (int e = 0; e < 4; ++e) { const u32 v = xv[i][e]; s1[i] = dot2(v, 0x3f803f80u, s1[i]); s2[i] = dot2(v, v, s2[i]); }
      }
#pragma unroll
      for (int i = 0; i < 4; ++i) { s1[i] = halve<0xB1>(l & 1, s1[i], s1[i + 4]); s2[i] = halve<0xB1>(l & 1, s2[i], s2[i + 4]); }
#pragma unroll
      for (int i = 0; i < 2; ++i) { s1[i] = halve<0x4E>(l & 2, s1[i], s1[i + 2]); s2[i] = halve<0x4E>(l & 2, s2[i], s2[i + 2]); }
      float t1 = halve<0x124>(l & 4, s1[0], s1[1]), t2 = halve<0x124>(l & 4, s2[0], s2[1]);
      t1 += dpp<0x128>(t1); t2 += dpp<0x128>(t2);
      t1 = swap32_sum(swap16_sum(t1)); t2 = swap32_sum(swap16_sum(t2));
      const float m = t1 * (1.f / DM), r = __builtin_amdgcn_rsqf(fmaxf(t2 * (1.f / DM) - m * m, 0.f) + EPS), nm = -m * r;
#define LNROW(I, LN) { const float ri = dpp<0x150 + (LN)>(r), ni = dpp<0x150 + (LN)>(nm); _Pragma("unroll") for (int e = 0; e < 4; ++e) { xh[I][2 * e] = fmaf(bflo(xv[I][e]), ri, ni); xh[I][2 * e + 1] = fmaf(bfhi(xv[I][e]), ri, ni); } }
      LNROW(0, 0) LNROW(1, 4) LNROW(2, 2) LNROW(3, 6) LNROW(4, 1) LNROW(5, 5) LNROW(6, 3) LNROW(7, 7)
#undef LNROW
#pragma unroll
      for (int i = 0; i < 8; ++i) asm volatile("" : "+v"(xh[i][0]), "+v"(xh[i][1]), "+v"(xh[i][2]), "+v"(xh[i][3]), "+v"(xh[i][4]), "+v"(xh[i][5]), "+v"(xh[i][6]), "+v"(xh[i][7]));
    }
    if (w > 0 && j != 0) WQ_DMA()
    u32x4 wkvf[16];
    if (j == 0) {
      WAITV_BAR(8);
      const u32 a0 = F_H + nh * (16384 + 128) + (256 * kh + 8 * h + q) * 32 + pp * 8;
#pragma unroll
      for (int s = 0; s < 16; ++s) wkvf[s] = ldstr2(a0 + s * 512, a0 + s * 512 + 128);
    }
    if (w == 0) {
      u32x4 a0, a1, b0, b1;
#pragma unroll 1
      for (int it = 0; it < SPIN_CAP; ++it) {
        asm volatile("global_load_dwordx4 %0, %4, %5 sc1\n\tglobal_load_dwordx4 %1, %4, %5 offset:16 sc1\n\tglobal_load_dwordx4 %2, %4, %5 offset:2048 sc1\n\t"
                     "global_load_dwordx4 %3, %4, %5 offset:2064 sc1\n\ts_waitcnt vmcnt(0)" : "=&v"(b0), "=&v"(b1), "=&v"(a0), "=&v"(a1) : "v"(l * 32), "s"(modr) : "memory");
        if (__all(a0[1] == tag && a0[3] == tag && a1[1] == tag && a1[3] == tag && b0[1] == tag && b0[3] == tag && b1[1] == tag && b1[3] == tag)) break;
      }
      sts128(F_M + 512 + vo, u32x4{pk(1.f + bflo(a0[0]), 1.f + bfhi(a0[0])), pk(1.f + bflo(a0[2]), 1.f + bfhi(a0[2])), pk(1.f + bflo(a1[0]), 1.f + bfhi(a1[0])), pk(1.f + bflo(a1[2]), 1.f + bfhi(a1[2]))});
      sts128(F_M + 1536 + vo, u32x4{b0[0], b0[2], b1[0], b1[2]});
    }
    BAR();
    if (w == 0 && j != 0) WQ_DMA()
    if (j != 0) LATE_DMA()
    {
      const u32x4 ca = lds128(F_M + 512 + vo), sa = lds128(F_M + 1536 + vo);
      float A[8], B[8];
#pragma unroll
      for (int e = 0; e < 4; ++e) { A[2 * e] = bflo(ca[e]); A[2 * e + 1] = bfhi(ca[e]); B[2 * e] = bflo(sa[e]); B[2 * e + 1] = bfhi(sa[e]); }
#pragma unroll
      for (int i = 0; i < 8; ++i) {
        u32x4 o;
#pragma unroll
        for (int e = 0; e < 4; ++e) o[e] = pk(fmaf(xh[i][2 * e], A[2 * e], B[2 * e]), fmaf(xh[i][2 * e + 1], A[2 * e + 1], B[2 * e + 1]));
        const int tk = 8 * w + i;
        sts128(F_H + ((tk >> 5) * 32 + (l >> 1)) * 1024 + (l & 1) * 512 + (((tk & 31) ^ (l & 7)) * 16), o);
      }
    }
    BAR();
    u32x4 xf[16];
#pragma unroll
    for (int s = 0; s < 16; ++s) xf[s] = lds128(F_H + (tt * 32 + 16 * kh + s) * 1024 + h * 512 + (((l & 31) ^ ((2 * s + h) & 7)) * 16));
    if (j == 0) {
      const u32 xs = F_B + w * 2048 + vo, xr = F_B + (w ^ 4) * 2048 + vo;
      f32x16 acc = {};
      if (nt == 0) {
#pragma unroll
        for (int s = 0; s < 16; ++s) acc = mfma(wkvf[s], xf[s], acc);
      } else {
#pragma unroll
        for (int s = 0; s < 16; ++s) acc = mfma(xf[s], wkvf[s], acc);
      }
      float keep[8], send[8];
      int khv = kh; PIN(khv);
#pragma unroll
      for (int e = 0; e < 8; ++e) { send[e] = khv ? acc[e] : acc[8 + e]; keep[e] = khv ? acc[8 + e] : acc[e]; }
      stsf4(xs, f32x4{send[0], send[1], send[2], send[3]}); stsf4(xs + 1024, f32x4{send[4], send[5], send[6], send[7]});
      BAR();
      const f32x4 r0 = ldsf4(xr), r1 = ldsf4(xr + 1024);
      u32x4 o = {pk(keep[0] + r0[0], keep[1] + r0[1]), pk(keep[2] + r0[2], keep[3] + r0[3]), pk(keep[4] + r1[0], keep[5] + r1[1]), pk(keep[6] + r1[2], keep[7] + r1[3])};
      char* img = p.ws + WS_KV + (size_t)(2 * g + tt) * TS;
      if (nt == 0) { if (kh == 1) gst16_sc1(img, vo, o); }
      else if ((l & 31) <= 17) {
        if ((l & 31) == 16) o = u32x4{0x3f803f80u, 0x3f803f80u, 0x3f803f80u, 0x3f803f80u};
        if ((l & 31) == 17) o = u32x4{0u, 0u, 0u, 0u};
        gst16_sc1(img + 1024 + kh * 576, h * 288 + (l & 31) * 16, o);
      }
      WAITV_BAR(0);
      if (w == 0 && l < 8) put_flag(p.ws + WS_FKV + l * 1024 + fofs<8>(g), tag);
    }
    if (j != 0) { if (w >= 6) WAITV(4); else WAITV(3); }
    BAR();
    const u32 rawr = (((nh * 2 + (pp >> 1)) ^ (((q >> 1) & 1) << 2)) * 16) + (pp & 1) * 8;
    f32x16 acc = {};
    {
      const u32 a0 = F_A + (256 * kh + 8 * h + q) * 128 + (rawr ^ (nt * 64));
#pragma unroll
      for (int s = 0; s < 16; ++s) acc = mfma(ldstr2(a0 + s * 2048, a0 + s * 2048 + 512), xf[s], acc);
    }
    const u32 xs = F_H + w * 4096 + vo, xr = F_H + (w ^ 4) * 4096 + vo;
    stsf4(xs, f32x4{acc[0], acc[1], acc[2], acc[3]}); stsf4(xs + 1024, f32x4{acc[4], acc[5], acc[6], acc[7]});
    stsf4(xs + 2048, f32x4{acc[8], acc[9], acc[10], acc[11]}); stsf4(xs + 3072, f32x4{acc[12], acc[13], acc[14], acc[15]});
    if (w == 1 && j != 0) {
      {
        u32x4 a0, b0;
#pragma unroll 1
        for (int it = 0; it < SPIN_CAP; ++it) {
          asm volatile("global_load_dwordx4 %0, %2, %3 sc1\n\tglobal_load_dwordx4 %1, %2, %3 offset:2048 sc1\n\ts_waitcnt vmcnt(0)" : "=&v"(b0), "=&v"(a0) : "v"(vo), "s"(modr + 6144 + ukh * 1024) : "memory");
          if (__all(a0[1] == tag && a0[3] == tag && b0[1] == tag && b0[3] == tag)) break;
          __builtin_amdgcn_s_sleep(2);
        }
        stsf4(F_M + 2560 + vo, f32x4{rbf(1.f + bflo(a0[0])), rbf(1.f + bfhi(a0[0])), rbf(1.f + bflo(a0[2])), rbf(1.f + bfhi(a0[2]))});
        stsf4(F_M + 3584 + vo, f32x4{bflo(b0[0]), bfhi(b0[0]), bflo(b0[2]), bfhi(b0[2])});
      }
    }
    WAITV_BAR(0);
    f32x4 cs[2][4];
    if (w >= 6) { const u32 a0 = F_D + 8192 + (4 * h + q) * 128 + (rawr ^ (tt * 64)); fo_ = ldstr2(a0, a0 + 1024); }
    if (j != 0) {
      const u32 a0 = F_D + (16 * kq + 4 * h + q) * 128 + (rawr ^ (tt * 64));
      fd_ = ldstr2(a0, a0 + 1024);
#pragma unroll
      for (int i = 0; i < 2; ++i) {
        const int s = 2 * w + i;
        const u32 a1 = F_B + (pp >> 1) * (8192 + 128) + (16 * s + 4 * h + q) * 32 + nh * 16 + (pp & 1) * 8;
        fg[i] = ldstr2(a1, a1 + 256);
        cs[i][0] = ldsf4(F_M + 2560 + (16 * s + 4 * h) * 4); cs[i][1] = ldsf4(F_M + 2560 + (16 * s + 8 + 4 * h) * 4);
        cs[i][2] = ldsf4(F_M + 3584 + (16 * s + 4 * h) * 4); cs[i][3] = ldsf4(F_M + 3584 + (16 * s + 8 + 4 * h) * 4);
      }
    }
    const f32x4 r0 = ldsf4(xr), r1 = ldsf4(xr + 1024), r2 = ldsf4(xr + 2048), r3 = ldsf4(xr + 3072);
    q0 = u32x4{pk((acc[0] + r0[0]) * C2, (acc[1] + r0[1]) * C2), pk((acc[2] + r0[2]) * C2, (acc[3] + r0[3]) * C2), pk((acc[4] + r1[0]) * C2, (acc[5] + r1[1]) * C2), pk((acc[6] + r1[2]) * C2, (acc[7] + r1[3]) * C2)};
    q1 = u32x4{pk((acc[8] + r2[0]) * C2, (acc[9] + r2[1]) * C2), pk((acc[10] + r2[2]) * C2, (acc[11] + r2[3]) * C2), pk((acc[12] + r3[0]) * C2, (acc[13] + r3[1]) * C2), pk((acc[14] + r3[2]) * C2, (acc[15] + r3[3]) * C2)};
    PIN(q0); PIN(q1);
    pdo = p.ws + WS_WO + ((size_t)(2 * j + tt) * 32 + g) * 1024;
    pdd = p.ws + WS_WD + ((size_t)(2 * (uid & 7) + tt) * 112 + 4 * (uid >> 3) + kq) * 1024;
    pdg = p.ws + WS_WGU + ((size_t)uT * 32 + 16 * ukh + 2 * w) * 1024;
    asm volatile("" : "+s"(pdo), "+s"(pdd), "+s"(pdg), "+s"(wob), "+s"(wob1), "+s"(wob2), "+s"(wob3));
    if (w == 2) poll_flags<32, 8>(p.ws + WS_FKV + (blockIdx.x & 7) * 1024, tag, l);
    if (w == 3) {
#pragma unroll 1
      for (int it = 0; it < SPIN_CAP; ++it) {
        const u32 v = __hip_atomic_load((const GAS u32*)(modr + 4096 + j * 256 + h * 6144 + (l & 31) * 8 + 4), RLX_AGENT);
        if (__all(v == tag)) break;
        __builtin_amdgcn_s_sleep(2);
      }
    }
    BAR();
    { const u32 o = (w & 1) * 69632 + (w >> 1) * 6144;
      dma4s(p.ws + WS_KV + o, vo, o); dma<2, 1>(p.ws + WS_KV + o + 4096, vo, o + 4096); }
    o0 = f32x16{}; o1 = f32x16{}; vB0 = u32x4{}; vB1 = u32x4{}; pb0 = u32x4{}; pb1 = u32x4{};
    PIN(o0); PIN(o1); PIN(vB0); PIN(vB1); PIN(pb0); PIN(pb1);
    if (j != 0) {
      float us = 0.f, wsum = 0.f;
#pragma unroll
      for (int i = 0; i < 2; ++i) {
        const u32x4 f = fg[i];
        const f32x4 c0 = cs[i][0], c1 = cs[i][1], s0 = cs[i][2], s1 = cs[i][3];
        u32x4 o;
        o[0] = pk(bflo(f[0]) * c0[0], bfhi(f[0]) * c0[1]); o[1] = pk(bflo(f[1]) * c0[2], bfhi(f[1]) * c0[3]);
        o[2] = pk(bflo(f[2]) * c1[0], bfhi(f[2]) * c1[1]); o[3] = pk(bflo(f[3]) * c1[2], bfhi(f[3]) * c1[3]);
#pragma unroll
        for (int e = 0; e < 4; ++e) { const u32 v = o[e]; us = dot2(v, 0x3f803f80u, us); }
        wsum += bflo(f[0]) * s0[0] + bfhi(f[0]) * s0[1] + bflo(f[1]) * s0[2] + bfhi(f[1]) * s0[3] + bflo(f[2]) * s1[0] + bfhi(f[2]) * s1[1] + bflo(f[3]) * s1[2] + bfhi(f[3]) * s1[3];
        fg[i] = o;
      }
      us = swap32_sum(us); wsum = swap32_sum(wsum);
      if (l < 32) *(LAS f32x2*)(uintptr_t)(OEX + w * 256 + l * 8) = f32x2{us, wsum};
      WAITV_BAR(0);
      if (w == 7) {
        f32x2 s = {0.f, 0.f};
#pragma unroll
        for (int i = 0; i < 8; ++i) s += ldsf2(OEX + i * 256 + (l & 31) * 8);
        const float uh = rbf(s[0]), wh = rbf(s[1]);
        if (l < 32) gst16_sc1(p.ws + WS_AUG + (size_t)uT * 1024 + ukh * 512, vo, u32x4{pk(uh, s[0] - uh), pk(uh, wh), pk(s[1] - wh, wh), 0u});
      }
    } else {
      WAITV_BAR(0);
    }
  }
  const size_t fo = ((size_t)(2 * g + tt) * 32 + 4 * j + 2 * nt) * 1024;
  const u32 vrow = 1024 + h * 288 + min(l & 31, 17) * 16;
  u32 ka = kh * 32 * TS + vo, va = kh * 32 * TS + vrow;
  f32x16 nm0, nm1, s0, s1;
  const u32x4 k0 = lds128(vo);
  u32x4 kA = lds128(ka), kB = lds128(ka + TS);
  u32x4 vA0 = lds128(va), vA1 = lds128(va + 576);
  u32x4 pa0, pa1;
  {
    mf0(s0, k0, q0); mf0(s1, k0, q1);
    MFMA_SETTLE();
    PIN(s0); PIN(s1);
#define MX3(a, b, c) fmaxf(fmaxf(a, b), c)
    const float m0 = swap32_max(MX3(MX3(MX3(s0[0], s0[1], s0[2]), MX3(s0[3], s0[4], s0[5]), MX3(s0[6], s0[7], s0[8])), MX3(MX3(s0[9], s0[10], s0[11]), MX3(s0[12], s0[13], s0[14]), s0[15]), -3.0e38f));
    const float m1 = swap32_max(MX3(MX3(MX3(s1[0], s1[1], s1[2]), MX3(s1[3], s1[4], s1[5]), MX3(s1[6], s1[7], s1[8])), MX3(MX3(s1[9], s1[10], s1[11]), MX3(s1[12], s1[13], s1[14]), s1[15]), -3.0e38f));
#undef MX3
#pragma unroll
    for (int r = 0; r < 16; ++r) nm0[r] = -m0;
    PIN(nm0);
    s0 = mfma(kA, q0, nm0);
#pragma unroll
    for (int r = 0; r < 16; ++r) nm1[r] = -m1;
    PIN(nm1);
  }
  if (!kh) {
    const u32 o = nt * 69632 + 24576 + tt * 8192;
    dma4s(p.ws + WS_KV + o, vo, o); dma4s(p.ws + WS_KV + o + 4096, vo, o + 4096);
  }
#define PREP_ST() { if (j != 0) { gst16_sc1(pdd, vo, fd_); gst16_sc1(pdg, vo, fg[0]); gst16_sc1(pdg + 1024, vo, fg[1]); } }
  if (kh) { PREP_ST() if (w >= 6) gst16_sc1(pdo, vo, fo_); }
  u32x4 wf[16];
#define HALF(SC, N0, N1, MF, OP, VP0, P0, VP1, P1, LD1, LD2)                                              \
  {                                                                                                       \
    MF; LD1; SB();                                                                                        \
    float e0 = EXP2(SC[0]), e1 = EXP2(SC[1]), e2 = EXP2(SC[2]), e3 = EXP2(SC[3]), e4 = EXP2(SC[4]), e5 = EXP2(SC[5]), e6 = EXP2(SC[6]), e7 = EXP2(SC[7]); SB(); \
    OP = mfma(VP0, P0, OP); SB();                                                                         \
    float e8 = EXP2(SC[8]), e9 = EXP2(SC[9]), e10 = EXP2(SC[10]), e11 = EXP2(SC[11]), e12 = EXP2(SC[12]), e13 = EXP2(SC[13]), e14 = EXP2(SC[14]), e15 = EXP2(SC[15]); SB(); \
    OP = mfma(VP1, P1, OP); LD2; SB();                                                                    \
    N0[0] = pk(e0, e1); N0[1] = pk(e2, e3); N0[2] = pk(e4, e5); N0[3] = pk(e6, e7); N1[0] = pk(e8, e9);   \
    N1[1] = pk(e10, e11); N1[2] = pk(e12, e13); N1[3] = pk(e14, e15); SB();                               \
  }
#define TILE(T, KC, KN, VC0, VC1, VP0, VP1)                                                               \
    HALF(s0, pa0, pa1, s1 = mfma(KC, q1, nm1), o1, VP0, pb0, VP1, pb1, KC = lds128(ka + ((T) + 2) * TS),  \
         (VP0 = lds128(va + ((T) + 1) * TS), VP1 = lds128(va + ((T) + 1) * TS + 576)))                    \
    HALF(s1, pb0, pb1, s0 = mfma(KN, q0, nm0), o0, VC0, pa0, VC1, pa1, , )
#define LDWF(SEL)                                                                                                     \
  asm volatile("s_cmp_lg_u32 %21, 0\n\ts_cbranch_scc1 .Lwf%=\n\t"                                                     \
               "global_load_dwordx4 %0, %16, %17 sc1\n\tglobal_load_dwordx4 %1, %16, %17 offset:1024 sc1\n\tglobal_load_dwordx4 %2, %16, %17 offset:2048 sc1\n\tglobal_load_dwordx4 %3, %16, %17 offset:3072 sc1\n\t"     \
               "global_load_dwordx4 %4, %16, %18 sc1\n\tglobal_load_dwordx4 %5, %16, %18 offset:1024 sc1\n\tglobal_load_dwordx4 %6, %16, %18 offset:2048 sc1\n\tglobal_load_dwordx4 %7, %16, %18 offset:3072 sc1\n\t"     \
               "global_load_dwordx4 %8, %16, %19 sc1\n\tglobal_load_dwordx4 %9, %16, %19 offset:1024 sc1\n\tglobal_load_dwordx4 %10, %16, %19 offset:2048 sc1\n\tglobal_load_dwordx4 %11, %16, %19 offset:3072 sc1\n\t"   \
               "global_load_dwordx4 %12, %16, %20 sc1\n\tglobal_load_dwordx4 %13, %16, %20 offset:1024 sc1\n\tglobal_load_dwordx4 %14, %16, %20 offset:2048 sc1\n\tglobal_load_dwordx4 %15, %16, %20 offset:3072 sc1\n"   \
               ".Lwf%=:"                                                                                              \
               : "+v"(wf[0]), "+v"(wf[1]), "+v"(wf[2]), "+v"(wf[3]), "+v"(wf[4]), "+v"(wf[5]), "+v"(wf[6]), "+v"(wf[7]), "+v"(wf[8]), "+v"(wf[9]), "+v"(wf[10]), \
                 "+v"(wf[11]), "+v"(wf[12]), "+v"(wf[13]), "+v"(wf[14]), "+v"(wf[15])                                  \
               : "v"(vo), "s"(wob), "s"(wob1), "s"(wob2), "s"(wob3), "s"(SEL) : "memory", "scc")
#pragma unroll
  for (int s4 = 0; s4 < 16; ++s4) asm volatile("" : "=v"(wf[s4]));
  const int cb = rfl(64 * j + 32 * nt + 16 * kh);
  const u32 xo = (u32)((l & 31) * DM + 4 * h) * 2, go = h * 16;
  const char* xres = (const char*)(p.x + (size_t)(64 * g + 32 * tt) * DM + cb);
  u32x2 xa, xb; u32x4 ga, gb;
  asm volatile("" : "=v"(xa), "=v"(xb), "=v"(ga), "=v"(gb));
#define LDXR(SEL)                                                                                                     \
    asm volatile("s_cmp_lg_u32 %8, 0\n\ts_cbranch_scc1 .Lxr%=\n\t"                                                   \
                 "global_load_dwordx2 %0, %4, %5\n\tglobal_load_dwordx2 %1, %4, %5 offset:16\n\tglobal_load_dwordx4 %2, %6, %7 sc1\n\tglobal_load_dwordx4 %3, %6, %7 offset:32 sc1\n" \
                 ".Lxr%=:"                                                                                            \
                 : "+v"(xa), "+v"(xb), "+v"(ga), "+v"(gb) : "v"(xo), "s"(xres), "v"(go), "s"(modr + 4096 + cb * 4), "s"(SEL) : "memory", "scc")
  const int qt = blockIdx.x >> 6;
#pragma unroll 1
  for (int seg = 0; seg < 4; ++seg) {
    char* wsl = p.ws; int wl = w; u32 bl = blockIdx.x;
    if (seg != 0) asm volatile("" : "+s"(wsl), "+s"(wl), "+s"(bl));
    if (seg == 1) {
      if (!kh) {
        PREP_ST()
        const u32 o = ((wl >> 1) & 1) * 69632 + 40960 + (wl & 1) * 14336;
        dma4s(wsl + WS_KV + o, vo, o); dma4s(wsl + WS_KV + o + 4096, vo, o + 4096); dma4s(wsl + WS_KV + o + 8192, vo, o + 8192);
        dma<2, 1>(wsl + WS_KV + o + 12288, vo, o + 12288);
        WAITV(14);
      } else {
        WAITV(0);
      }
      BAR();
      if (w == 0 && l < 8) put_flag(wsl + WS_FP + l * 1024 + bl * 4, tag);
    } else if (seg == 2) {
      if (w == 0) {
        u32x4 f;
#pragma unroll 1
        for (int it = 0; it < SPIN_CAP; ++it) {
          asm volatile("global_load_dwordx4 %0, %1, %2 sc1\n\ts_waitcnt vmcnt(0)" : "=&v"(f) : "v"(vo), "s"(wsl + WS_FP + (bl & 7) * 1024) : "memory");
          if (__all(f[0] == tag && f[1] == tag && f[2] == tag && f[3] == tag)) break;
          __builtin_amdgcn_s_sleep(1);
        }
        if (bl == 0 && l == 0) put_flag(wsl + WS_EPOCH, epoch + 1u);
      }
      WAITV_BAR(0);
    } else if (seg == 3) {
      if (!kh) {
        dma4s((wsl + WS_WGU + (size_t)(((bl & 1) * 8 + ((bl >> 3) & 7)) * 7) * 32768) + wl * 8192, vo, SL0 + wl * 8192); dma4s((wsl + WS_WGU + (size_t)(((bl & 1) * 8 + ((bl >> 3) & 7)) * 7) * 32768) + wl * 8192 + 4096, vo, SL0 + wl * 8192 + 4096);
        dma4s((wsl + WS_WGU + (size_t)(((bl & 1) * 8 + ((bl >> 3) & 7)) * 7) * 32768) + 32768 + wl * 8192, vo, SL1 + wl * 8192); dma4s((wsl + WS_WGU + (size_t)(((bl & 1) * 8 + ((bl >> 3) & 7)) * 7) * 32768) + 32768 + wl * 8192 + 4096, vo, SL1 + wl * 8192 + 4096);
        touch8k((wsl + WS_WGU + (size_t)(((bl & 1) * 8 + ((bl >> 3) & 7)) * 7) * 32768) + 65536 + (bl >> 6) * 40960 + wl * 8192, l * 128);
        if (w < 2) touch8k(w == 0 ? (wsl + WS_WGU + (size_t)(((bl & 1) * 8 + ((bl >> 3) & 7)) * 7) * 32768) + 65536 + (bl >> 6) * 40960 + 32768 : wsl + WS_AUG + (size_t)((bl & 1) * 8 + ((bl >> 3) & 7)) * 7168, l * 128);
      }
    }
    LDWF((seg ^ 2) | (kh ^ 1));
    LDXR((seg ^ 2) | (kh ^ 1));
    TILE(0, kA, kB, vA0, vA1, vB0, vB1) TILE(1, kB, kA, vB0, vB1, vA0, vA1) TILE(2, kA, kB, vA0, vA1, vB0, vB1) TILE(3, kB, kA, vB0, vB1, vA0, vA1)
    TILE(4, kA, kB, vA0, vA1, vB0, vB1) TILE(5, kB, kA, vB0, vB1, vA0, vA1) TILE(6, kA, kB, vA0, vA1, vB0, vB1) TILE(7, kB, kA, vB0, vB1, vA0, vA1)
    ka += 8 * TS; va += 8 * TS; PIN(ka); PIN(va);
  }
  o1 = mfma(vB0, pb0, o1); o1 = mfma(vB1, pb1, o1);
  LDWF(kh);
  LDXR(kh);
  if (!kh) {
    touch8k(p.ws + WS_WD + (size_t)(2 * j) * 114688 + qt * 57344 + w * 8192, l * 128);
    if (w < 3) touch8k(p.ws + WS_WD + (size_t)(2 * j) * 114688 + qt * 57344 + (w + 4) * 8192, l * 128);
  }
#undef HALF
#undef TILE
  {
    const u32 xs = OEX + w * 2304, xr = OEX + (w ^ 4) * 2304;
    float o[9];
    if (kh) { stsf4(xs + vo, f32x4{o0[0], o0[1], o0[2], o0[3]}); stsf4(xs + 1024 + vo, f32x4{o0[4], o0[5], o0[6], o0[7]}); *(LAS float*)(uintptr_t)(xs + 2048 + l * 4) = o0[8];
#pragma unroll
      for (int e = 0; e < 9; ++e) o[e] = o1[e];
    } else { stsf4(xs + vo, f32x4{o1[0], o1[1], o1[2], o1[3]}); stsf4(xs + 1024 + vo, f32x4{o1[4], o1[5], o1[6], o1[7]}); *(LAS float*)(uintptr_t)(xs + 2048 + l * 4) = o1[8];
#pragma unroll
      for (int e = 0; e < 9; ++e) o[e] = o0[e];
    }
    BAR();
    const f32x4 r0 = ldsf4(xr + vo), r1 = ldsf4(xr + 1024 + vo);
#pragma unroll
    for (int e = 0; e < 4; ++e) { o[e] += r0[e]; o[4 + e] += r1[e]; }
    float ls = lowhalf(o[8] + *(const LAS float*)(uintptr_t)(xr + 2048 + l * 4));
    bool bad = !(ls < 1.2676506e30f);
#pragma unroll
    for (int e = 0; e < 8; ++e) bad |= !(fabsf(o[e]) < 3.0e38f);
    if (__builtin_expect(__any(bad), 0)) {
      const u32x4 qf = kh ? q1 : q0;
      float m = -INFINITY;
      f32x16 oo = {};
#pragma unroll 1
      for (int t = 0; t < 64; ++t) {
        u32x4 kf, v0, v1;
        asm volatile("global_load_dwordx4 %0, %3, %5 sc1\n\tglobal_load_dwordx4 %1, %4, %5 sc1\n\tglobal_load_dwordx4 %2, %4, %5 offset:576 sc1\n\ts_waitcnt vmcnt(0)"
                     : "=&v"(kf), "=&v"(v0), "=&v"(v1) : "v"(vo), "v"(vrow), "s"(p.ws + WS_KV + t * TS) : "memory");
        const f32x16 s = mfma(kf, qf, f32x16{});
        const float mn = fmaxf(m, rowmax16(s)), al = EXP2(m - mn);
        m = mn;
        u32x4 pa, pb;
#pragma unroll
        for (int r = 0; r < 4; ++r) { pa[r] = pk(EXP2(s[2 * r] - mn), EXP2(s[2 * r + 1] - mn)); pb[r] = pk(EXP2(s[8 + 2 * r] - mn), EXP2(s[9 + 2 * r] - mn)); }
#pragma unroll
        for (int r = 0; r < 16; ++r) oo[r] *= al;
        oo = mfma(v0, pa, oo); oo = mfma(v1, pb, oo);
      }
#pragma unroll
      for (int e = 0; e < 8; ++e) o[e] = oo[e];
      ls = lowhalf(oo[8]);
    }
    const float rl = __builtin_amdgcn_rcpf(ls);
    gst16_sc1(p.ws + WS_O + fo + kh * 1024, vo, u32x4{pk(o[0] * rl, o[1] * rl), pk(o[2] * rl, o[3] * rl), pk(o[4] * rl, o[5] * rl), pk(o[6] * rl, o[7] * rl)});
  }
  asm volatile("s_waitcnt vmcnt(0)\n\ts_barrier"
               : "+v"(wf[0]), "+v"(wf[1]), "+v"(wf[2]), "+v"(wf[3]), "+v"(wf[4]), "+v"(wf[5]), "+v"(wf[6]), "+v"(wf[7]), "+v"(wf[8]), "+v"(wf[9]),
                 "+v"(wf[10]), "+v"(wf[11]), "+v"(wf[12]), "+v"(wf[13]), "+v"(wf[14]), "+v"(wf[15]), "+v"(xa), "+v"(xb), "+v"(ga), "+v"(gb)::"memory");
  if (w == 1 && l == 0) put_flag(p.ws + WS_FO + g * 512 + fofs<4>(j), tag);
  if (w == 0) poll_flags<8, 4>(p.ws + WS_FO + g * 512, tag, l);
  BAR();
  u32x4 x1;
  {
    const char* osrc = p.ws + WS_O + ((size_t)(2 * g + tt) * 32 + 8 * kq) * 1024;
    u32x4 xf[8];
    xf[0] = gld_s<1, 0>(osrc, vo); xf[1] = gld_s<1, 1024>(osrc, vo); xf[2] = gld_s<1, 2048>(osrc, vo); xf[3] = gld_s<1, 3072>(osrc, vo);
    xf[4] = gld_s<1, 0>(osrc + 4096, vo); xf[5] = gld_s<1, 1024>(osrc + 4096, vo); xf[6] = gld_s<1, 2048>(osrc + 4096, vo); xf[7] = gld_s<1, 3072>(osrc + 4096, vo);
    f32x16 acc0 = {}, acc1 = {};
#define P3_STEP(S) { waitv_pin<7 - (S)>(xf[S]); acc0 = mfma(wf[S], xf[S], acc0); acc1 = mfma(wf[8 + (S)], xf[S], acc1); }
    P3_STEP(0) P3_STEP(1) P3_STEP(2) P3_STEP(3) P3_STEP(4) P3_STEP(5) P3_STEP(6) P3_STEP(7)
#undef P3_STEP
    float r[8];
    {
      const u32 d = SL3 + tt * 4096 + kq * 1024 + vo;
      sts128(d, pack8(acc0, 0)); sts128(d + 8192, pack8(acc1, 0)); sts128(d + 16384, pack8(acc0, 8)); sts128(d + 24576, pack8(acc1, 8));
      BAR();
      sum_pieces<4>(SL3 + w * 4096 + vo, r);
    }
    x1 = u32x4{pk(fmaf(bflo(ga[0]), r[0], bflo(xa[0])), fmaf(bfhi(ga[0]), r[1], bfhi(xa[0]))), pk(fmaf(bflo(ga[2]), r[2], bflo(xa[1])), fmaf(bfhi(ga[2]), r[3], bfhi(xa[1]))),
               pk(fmaf(bflo(gb[0]), r[4], bflo(xb[0])), fmaf(bfhi(gb[0]), r[5], bfhi(xb[0]))), pk(fmaf(bflo(gb[2]), r[6], bflo(xb[1])), fmaf(bfhi(gb[2]), r[7], bfhi(xb[1])))};
    gst16_sc1(p.ws + WS_X1 + fo + kh * 1024, vo, x1);
    dma4s(wgu + 2 * 32768 + w * 4096, vo, SL2 + w * 4096);
    float s1 = 0.f, s2 = 0.f;
#pragma unroll
    for (int e = 0; e < 4; ++e) { const u32 v = x1[e]; s1 = dot2(v, 0x3f803f80u, s1); s2 = dot2(v, v, s2); }
    s1 = swap32_sum(s1); s2 = swap32_sum(s2);
    if (l < 32) *(LAS f32x2*)(uintptr_t)(GAPA + kq * 512 + tt * 256 + l * 8) = f32x2{s1, s2};
    BAR();
    if (w == 7) {
      f32x2 s = {0.f, 0.f};
#pragma unroll
      for (int i = 0; i < 4; ++i) s += ldsf2(GAPA + i * 512 + l * 8);
      gst8_sc1(p.ws + WS_STAT + ((size_t)64 * g * 8 + j) * 8, l * 64, u32x2{__float_as_uint(s[0]), __float_as_uint(s[1])});
      WAITV(5);
    } else {
      WAITV(4);
    }
    asm volatile("s_barrier" ::: "memory");
    if (w == 0 && l == 0) put_flag(p.ws + WS_FX + (g >> 1) * 512 + fofs<8>((g & 1) * 8 + j), tag);
    if (w == 7) {
      WAITV(0);
      if (l == 0) put_flag(p.ws + WS_FS + (g >> 1) * 512 + fofs<8>((g & 1) * 8 + j), tag);
    }
  }
  u32x4 wd[28];
  if (kh == 0) p4_compute(p, id, l, w, tag, wd); else p4_helper(p, id, l, w, tag, wd);
  {
    int ly = l; PIN(ly);
    const char* xsrc = p.ws + WS_ACT + ((size_t)(2 * g) * 112 + 14 * w) * 1024;
    u32x4 xf[16];
#define XL(N) (xf[(N) % 16] = gld_s<1, 0>(xsrc + dks((N) >> 1) * 1024 + ((N) & 1) * 114688, vo))
    if (!kh) {
      WAITV(0);
      if (ly == 0 && __hip_atomic_fetch_add((LAS u32*)(uintptr_t)GAPA, 1u, __ATOMIC_RELAXED, __HIP_MEMORY_SCOPE_WORKGROUP) == 3u)
        put_flag(p.ws + WS_FA + id.G * 512 + fofs<8>(id.c), tag);
      BAR();
    }
    XL(0); XL(1); XL(2); XL(3); XL(4); XL(5); XL(6); XL(7); XL(8); XL(9); XL(10); XL(11);
    u32x4 g0 = gld_s<1, 0>(modr + 10240 + (64 * j + 32 * nt + 16 * kh) * 4, (ly >> 5) * 16), g1 = gld_s<1, 32>(modr + 10240 + (64 * j + 32 * nt + 16 * kh) * 4, (ly >> 5) * 16);
    if (kh) asm volatile("s_barrier" ::: "memory");
    f32x16 acc[4];
#define K5_X(N, CNT)                                                                                 \
  {                                                                                                  \
    waitv_pin<CNT>(xf[(N) % 16]);                                                                    \
    if ((N) < 2) { mf0(acc[(N) & 1], wd[0], xf[(N) & 1]); mf0(acc[2 + ((N) & 1)], wd[14], xf[(N) & 1]); } \
    else { mfa(acc[(N) & 1], wd[dks((N) >> 1)], xf[(N) % 16]); mfa(acc[2 + ((N) & 1)], wd[14 + dks((N) >> 1)], xf[(N) % 16]); } \
  }
    K5_X(0, 13) K5_X(1, 12) K5_X(2, 11) K5_X(3, 10) K5_X(4, 9) K5_X(5, 8) K5_X(6, 7) K5_X(7, 6) K5_X(8, 5) K5_X(9, 4) K5_X(10, 3) K5_X(11, 2)
    if (w == 4) poll_flags<16, 8>(p.ws + WS_FA + (g >> 1) * 512, tag, ly);
    asm volatile("s_barrier" ::: "memory");
    XL(12); XL(13); XL(14); XL(15); XL(16); XL(17); XL(18); XL(19); XL(20); XL(21); XL(22); XL(23); XL(24); XL(25); XL(26); XL(27);
    K5_X(12, 15) K5_X(13, 14) K5_X(14, 13) K5_X(15, 12) K5_X(16, 11) K5_X(17, 10) K5_X(18, 9) K5_X(19, 8) K5_X(20, 7) K5_X(21, 6) K5_X(22, 5) K5_X(23, 4)
    K5_X(24, 3) K5_X(25, 2) K5_X(26, 1) K5_X(27, 0)
#undef K5_X
    MFMA_SETTLE();
    PIN(g0); PIN(g1); PIN(acc[0]); PIN(acc[1]); PIN(acc[2]); PIN(acc[3]);
    float r[8];
    {
      const u32 d = w * 1024 + vo;
#pragma unroll
      for (int pc = 0; pc < 8; ++pc) sts128(d + pc * 8192, pack8(acc[pc & 3], (pc >> 2) * 8));
      BAR();
      sum_pieces<8>(w * 8192 + vo, r);
    }
    const u32 a0 = pk(fmaf(bflo(g0[0]), r[0], bflo(x1[0])), fmaf(bfhi(g0[0]), r[1], bfhi(x1[0]))), a1 = pk(fmaf(bflo(g0[2]), r[2], bflo(x1[1])), fmaf(bfhi(g0[2]), r[3], bfhi(x1[1])));
    const u32 b0 = pk(fmaf(bflo(g1[0]), r[4], bflo(x1[2])), fmaf(bfhi(g1[0]), r[5], bfhi(x1[2]))), b1 = pk(fmaf(bflo(g1[2]), r[6], bflo(x1[3])), fmaf(bfhi(g1[2]), r[7], bfhi(x1[3])));
    const auto s0 = __builtin_amdgcn_permlane32_swap(a0, b0, false, false);
    const auto s1 = __builtin_amdgcn_permlane32_swap(a1, b1, false, false);
    int lz = l; PIN(lz);
    *(u32x4*)(p.out + (size_t)(64 * g + 32 * tt + (lz & 31)) * DM + 64 * j + 32 * nt + 16 * kh + 8 * (lz >> 5)) = u32x4{s0[0], s1[0], s0[1], s1[1]};
  }
}

extern "C" void kernel_launch(void* const* d_in, const int* in_sizes, int n_in, void* d_out, int out_size, void* d_ws, size_t ws_size, hipStream_t stream) {
  Params p;
  p.x = (const u16*)d_in[0]; p.wq = (const u16*)d_in[1]; p.wk = (const u16*)d_in[2]; p.wv = (const u16*)d_in[3]; p.wo = (const u16*)d_in[4];
  p.wg = (const u16*)d_in[6]; p.wu = (const u16*)d_in[7]; p.wd = (const u16*)d_in[8]; p.c = (const u16*)d_in[9]; p.wm = (const u16*)d_in[10];
  p.out = (u16*)d_out; p.ws = (char*)d_ws;
  (void)hipFuncSetAttribute((const void*)k_main, hipFuncAttributeMaxDynamicSharedMemorySize, M_LDS);
  k_main<<<256, 512, M_LDS, stream>>>(p);
}
```

```cpp
#include <hip/hip_runtime.h>
#include <stdint.h>

typedef unsigned short u16;
typedef unsigned int u32;
typedef __attribute__((ext_vector_type(8))) __bf16 bf16x8;
typedef __attribute__((ext_vector_type(2))) __bf16 bf16x2;
typedef __attribute__((ext_vector_type(2))) float f32x2;
typedef __attribute__((ext_vector_type(4))) float f32x4;
typedef __attribute__((ext_vector_type(16))) float f32x16;
typedef __attribute__((ext_vector_type(4))) u32 u32x4;
typedef __attribute__((ext_vector_type(2))) u32 u32x2;
typedef __attribute__((ext_vector_type(4))) short s16x4;
#define LAS __attribute__((address_space(3)))
#define GAS __attribute__((address_space(1)))
#define DI __device__ __forceinline__

constexpr int DM = 512, FF = 1792;
constexpr float EPS = 1e-5f;
constexpr float C2 = 0.25f * 1.4426950408889634f;

constexpr size_t WS_EPOCH = 0;
constexpr size_t WS_FO = 4096;
constexpr size_t WS_FX = WS_FO + 16384;
constexpr size_t WS_FS = WS_FX + 8192;
constexpr size_t WS_FE = WS_FS + 8192;
constexpr size_t WS_FA = WS_FE + 8192;
constexpr size_t WS_FKV = WS_FA + 8192;
constexpr size_t WS_FP = WS_FKV + 8192;
constexpr size_t WS_MOD = 81920;
constexpr size_t WS_KV = WS_MOD + 8 * 12288;
constexpr size_t WS_STAT = WS_KV + 139264;
constexpr size_t WS_AUG = WS_STAT + 131072;
constexpr size_t WS_WO = 1u << 20;
constexpr size_t WS_WD = WS_WO + (512u << 10);
constexpr size_t WS_WGU = WS_WD + (1792u << 10);
constexpr size_t WS_O = WS_WGU + (3584u << 10);
constexpr size_t WS_X1 = WS_O + (2u << 20);
constexpr size_t WS_ACT = WS_X1 + (2u << 20);
static_assert(WS_FP + 8192 <= WS_MOD && WS_AUG + 114688 + 8192 <= WS_WO, "workspace map");
constexpr int TS = 2176;

struct Params {
  const u16 *x, *wq, *wk, *wv, *wo, *wg, *wu, *wd, *c, *wm;
  u16* out;
  char* ws;
};

DI f32x16 mfma(u32x4 a, u32x4 b, f32x16 c) {
  return __builtin_amdgcn_mfma_f32_32x32x16_bf16(__builtin_bit_cast(bf16x8, a), __builtin_bit_cast(bf16x8, b), c, 0, 0, 0);
}
DI u32 pk(float lo, float hi) {
  f32x2 v = {lo, hi};
  return __builtin_bit_cast(u32, __builtin_convertvector(v, bf16x2));
}
DI float bflo(u32 u) { return __uint_as_float(u << 16); }
DI float bfhi(u32 u) { return __uint_as_float(u & 0xffff0000u); }
DI float rbf(float f) { return bflo(pk(f, 0.f)); }
DI float dot2(u32 a, u32 b, float c) { return __builtin_amdgcn_fdot2_f32_bf16(__builtin_bit_cast(bf16x2, a), __builtin_bit_cast(bf16x2, b), c, false); }
template <int N, int SC> DI void dma(const void* base, u32 vo, u32 lds) {
#define DMA_HEAD "s_mov_b32 m0, %2\n\ts_nop 0\n\t"
#define DMA_OPS ::"v"(vo), "s"(base), "s"(lds) : "memory"
  if (N == 1 && !SC) asm volatile(DMA_HEAD "global_load_lds_dwordx4 %0, %1" DMA_OPS);
  if (N == 2 && !SC) asm volatile(DMA_HEAD "global_load_lds_dwordx4 %0, %1\n\tglobal_load_lds_dwordx4 %0, %1 offset:1024" DMA_OPS);
  if (N == 2 && SC) asm volatile(DMA_HEAD "global_load_lds_dwordx4 %0, %1 sc1\n\tglobal_load_lds_dwordx4 %0, %1 offset:1024 sc1" DMA_OPS);
  if (N == 4 && SC) asm volatile(DMA_HEAD "global_load_lds_dwordx4 %0, %1 sc1\n\tglobal_load_lds_dwordx4 %0, %1 offset:1024 sc1\n\t"
                                 "global_load_lds_dwordx4 %0, %1 offset:2048 sc1\n\tglobal_load_lds_dwordx4 %0, %1 offset:3072 sc1" DMA_OPS);
}
DI void dma4s(const void* base, u32 vo, u32 lds) { dma<4, 1>(base, vo, lds); }
template <int SC, int OFF> DI u32x4 gld_s(const void* base, u32 vo) {
  u32x4 r;
  if (SC) asm volatile("global_load_dwordx4 %0, %1, %2 offset:%3 sc1" : "=v"(r) : "v"(vo), "s"(base), "n"(OFF) : "memory");
  else asm volatile("global_load_dwordx4 %0, %1, %2 offset:%3" : "=v"(r) : "v"(vo), "s"(base), "n"(OFF) : "memory");
  return r;
}
template <int OFF> DI u32 gld2_s(const void* base, u32 vo) { u32 r; asm volatile("global_load_ushort %0, %1, %2 offset:%3" : "=v"(r) : "v"(vo), "s"(base), "n"(OFF) : "memory"); return r; }
DI void touch8k(const char* base, u32 vo) { const u32 lds = 163840 - 256; asm volatile(DMA_HEAD "global_load_lds_dword %0, %1 sc1" DMA_OPS); }
DI void gst16_sc1(char* base, u32 vo, u32x4 v) { asm volatile("global_store_dwordx4 %0, %1, %2 sc1\n\ts_nop 1" ::"v"(vo), "v"(v), "s"(base) : "memory"); }
DI void gst8_sc1(char* base, u32 vo, u32x2 v) { asm volatile("global_store_dwordx2 %0, %1, %2 sc1\n\ts_nop 1" ::"v"(vo), "v"(v), "s"(base) : "memory"); }
#define RLX_AGENT __ATOMIC_RELAXED, __HIP_MEMORY_SCOPE_AGENT
DI void put_flag(char* f, u32 tag) { __hip_atomic_store((GAS u32*)f, tag, RLX_AGENT); }
constexpr int SPIN_CAP = 1 << 20;
template <int PER> DI u32 fofs(int i) { return (u32)((i / PER) * 256 + (i % PER) * 4); }
template <int NAP> DI void poll_at(const char* a, u32 tag) {
#pragma unroll 1
  for (int it = 0; it < SPIN_CAP; ++it) {
    const u32 v = __hip_atomic_load((const GAS u32*)a, RLX_AGENT);
    if (__all(v == tag)) break;
    if (NAP) __builtin_amdgcn_s_sleep(NAP);
  }
  __builtin_amdgcn_fence(__ATOMIC_ACQUIRE, "wavefront");
}
template <int N, int PER, int NAP = 0> DI void poll_flags(const char* f, u32 tag, int l) { poll_at<NAP>(f + fofs<PER>(l < N ? l : 0), tag); }
#define SB() __builtin_amdgcn_sched_barrier(0)
#define PIN(x) asm volatile("" : "+v"(x))
#define WAITV(N) asm volatile("s_waitcnt vmcnt(" #N ")" ::: "memory")
#define WAITV_BAR(N) asm volatile("s_waitcnt vmcnt(" #N ") lgkmcnt(0)\n\ts_barrier" ::: "memory")
#define BAR() asm volatile("s_waitcnt lgkmcnt(0)\n\ts_barrier" ::: "memory")
template <int N> DI void waitv_pin(u32x4& v) { asm volatile("s_waitcnt vmcnt(%1)" : "+v"(v) : "n"(N) : "memory"); }
DI u32x4 lds128(u32 a) { return *(const LAS u32x4*)(uintptr_t)a; }
DI f32x4 ldsf4(u32 a) { return *(const LAS f32x4*)(uintptr_t)a; }
DI f32x2 ldsf2(u32 a) { return *(const LAS f32x2*)(uintptr_t)a; }
DI void sts128(u32 a, u32x4 v) { *(LAS u32x4*)(uintptr_t)a = v; }
DI void stsf4(u32 a, f32x4 v) { *(LAS f32x4*)(uintptr_t)a = v; }
DI u32x2 ldstr(u32 a) { return __builtin_bit_cast(u32x2, __builtin_amdgcn_ds_read_tr16_b64_v4i16((LAS s16x4*)(uintptr_t)a)); }
DI u32x4 ldstr2(u32 a0, u32 a1) { u32x2 p = ldstr(a0), q = ldstr(a1); return u32x4{p[0], p[1], q[0], q[1]}; }
template <int CTRL> DI float dpp(float v) { return __int_as_float(__builtin_amdgcn_mov_dpp(__float_as_int(v), CTRL, 0xf, 0xf, true)); }
DI float swap32_sum(float v) { auto r = __builtin_amdgcn_permlane32_swap(__float_as_uint(v), __float_as_uint(v), false, false); return __uint_as_float(r[0]) + __uint_as_float(r[1]); }
DI float swap32_max(float v) { auto r = __builtin_amdgcn_permlane32_swap(__float_as_uint(v), __float_as_uint(v), false, false); return fmaxf(__uint_as_float(r[0]), __uint_as_float(r[1])); }
DI float swap16_sum(float v) { auto r = __builtin_amdgcn_permlane16_swap(__float_as_uint(v), __float_as_uint(v), false, false); return __uint_as_float(r[0]) + __uint_as_float(r[1]); }
DI float lowhalf(float v) { auto r = __builtin_amdgcn_permlane32_swap(__float_as_uint(v), __float_as_uint(v), false, false); return __uint_as_float(r[0]); }
template <int CTRL> DI float halve(bool bit, float a, float b) { return (bit ? b : a) + dpp<CTRL>(bit ? a : b); }
DI int rfl(int v) { return __builtin_amdgcn_readfirstlane(v); }
#define EXP2(v) __builtin_amdgcn_exp2f(v)

struct Ids { int G, c, g, j; };
DI Ids ids() { const int b = blockIdx.x, x = b & 7, i = b >> 3; Ids r; r.G = (x >> 1) * 4 + (i >> 3); r.c = (x & 1) * 8 + (i & 7); r.g = 4 * x + (i >> 3); r.j = i & 7; return r; }

constexpr u32 M_LDS = 163840;
constexpr u32 F_A = 0;
constexpr u32 F_H = 65536;
constexpr u32 F_B = 131072;
constexpr u32 F_D = F_B + 16640;
constexpr u32 F_M = F_D + 10240;
static_assert(F_M + 4608 <= M_LDS, "front-end LDS map");
constexpr u32 SL0 = 0, SL1 = 69632, SL2 = 32768, SL3 = 102400, TOPA = 135168, GAPA = 65536, OEX = 143616;

DI void mfa(f32x16& c, u32x4 a, u32x4 b) { asm volatile("v_mfma_f32_32x32x16_bf16 %0, %1, %2, %0" : "+v"(c) : "v"(a), "v"(b) : "memory"); }
DI void mf0(f32x16& c, u32x4 a, u32x4 b) { asm volatile("v_mfma_f32_32x32x16_bf16 %0, %1, %2, 0" : "=&v"(c) : "v"(a), "v"(b) : "memory"); }
DI float vexp(float a) { float r; asm volatile("v_exp_f32 %0, %1" : "=v"(r) : "v"(a)); return r; }
DI float vrcp(float a) { float r; asm volatile("v_rcp_f32 %0, %1" : "=v"(r) : "v"(a)); return r; }
DI u32 vcvt(float a, float b) { u32 r; asm volatile("v_cvt_pk_bf16_f32 %0, %1, %2" : "=v"(r) : "v"(a), "v"(b)); return r; }
DI float vmul(float a, float b) { float r; asm volatile("v_mul_f32 %0, %1, %2" : "=v"(r) : "v"(a), "v"(b)); return r; }
DI float vfma_k(float a, float k, float c) { float r; asm volatile("v_fma_f32 %0, %1, %2, %3" : "=v"(r) : "v"(a), "s"(k), "v"(c)); return r; }
DI float vadd1(float a) { float r; asm volatile("v_add_f32 %0, 1.0, %1" : "=v"(r) : "v"(a)); return r; }
#define MFMA_SETTLE() asm volatile("s_nop 15\n\ts_nop 3" ::: "memory")
DI float rowmax16(const f32x16& s) {
  float m = fmaxf(fmaxf(s[0], s[1]), s[2]);
#pragma unroll
  for (int r = 3; r < 15; r += 2) m = fmaxf(fmaxf(m, s[r]), s[r + 1]);
  return swap32_max(fmaxf(m, s[15]));
}
constexpr float GC0 = -2.f * 0.7978845608028654f * 1.4426950408889634f, GC1 = GC0 * 0.044715f;

DI u32x4 pack8(const f32x16& a, int o8) { return u32x4{pk(a[o8], a[o8 + 1]), pk(a[o8 + 2], a[o8 + 3]), pk(a[o8 + 4], a[o8 + 5]), pk(a[o8 + 6], a[o8 + 7])}; }
template <int N> DI void sum_pieces(u32 a, float (&r)[8]) {
  u32 klo = 0x00003f80u, khi = 0x3f800000u;
  asm volatile("" : "+s"(klo), "+s"(khi));
  u32x4 u[N];
#pragma unroll
  for (int i = 0; i < N; ++i) u[i] = lds128(a + i * 1024);
#pragma unroll
  for (int e = 0; e < 4; ++e) { r[2 * e] = bflo(u[0][e]); r[2 * e + 1] = bfhi(u[0][e]); }
#pragma unroll
  for (int i = 1; i < N; ++i)
#pragma unroll
    for (int e = 0; e < 4; ++e) { const u32 v = u[i][e]; r[2 * e] = dot2(v, klo, r[2 * e]); r[2 * e + 1] = dot2(v, khi, r[2 * e + 1]); }
}

constexpr u32 SL4 = 0xffffffffu;
template <u32 SL, int S> DI u32x4 wrd(u32 lo, u32 mid, u32 hi) {
  constexpr u32 A = SL == SL4 ? (S < 28 ? TOPA + S * 1024 : GAPA + (S - 28) * 1024) : SL + S * 1024;
  return A < 65536 ? lds128(lo + A) : (A < 131072 ? lds128(mid + (A - 65536)) : lds128(hi + (A - 131072)));
}
DI constexpr int dks(int i) { return i < 6 ? 7 * (i / 3) + i % 3 : 7 * ((i - 6) >> 2) + 3 + ((i - 6) & 3); }

DI void p4_compute(const Params& p, const Ids& id, int l, int w, u32 tag, u32x4 (&wd)[28]) {
  const u32 vo = l * 16;
  char* adst = p.ws + WS_ACT + ((size_t)(4 * id.G + w) * 112 + 7 * id.c) * 1024;
  const char* asrc = p.ws + WS_AUG + (size_t)id.c * 7168;
  u32x4 aug[7];
  aug[0] = gld_s<1, 0>(asrc, vo); aug[1] = gld_s<1, 1024>(asrc, vo); aug[2] = gld_s<1, 2048>(asrc, vo);
  if (w == 0) poll_flags<16, 8>(p.ws + WS_FX + id.G * 512, tag, l);
  asm volatile("s_waitcnt vmcnt(0)\n\ts_barrier" : "+v"(aug[0]), "+v"(aug[1]), "+v"(aug[2])::"memory");
  const char* xsrc = p.ws + WS_X1 + (size_t)(4 * id.G + w) * 32768;
  u32x4 xf[32];
#define XLD(S) (xf[S] = gld_s<1, ((S) & 3) * 1024>(xsrc + ((S) >> 2) * 4096, vo))
  XLD(0); XLD(1); XLD(2); XLD(3); XLD(4); XLD(5); XLD(6); XLD(7); XLD(8); XLD(9); XLD(10); XLD(11); XLD(12); XLD(13); XLD(14); XLD(15);
  float rs, gc0 = GC0; u32x4 xaug;
  u32 lo = vo, mid = vo + 65536, hi = vo + 131072;
  PIN(lo); PIN(mid); PIN(hi); PIN(gc0);
  f32x16 acc[3]; u32x4 ra[32], rb[32];
  ra[0] = wrd<SL0, 0>(lo, mid, hi); rb[0] = wrd<SL1, 0>(lo, mid, hi); ra[1] = wrd<SL0, 1>(lo, mid, hi); rb[1] = wrd<SL1, 1>(lo, mid, hi);
  ra[2] = wrd<SL0, 2>(lo, mid, hi); rb[2] = wrd<SL1, 2>(lo, mid, hi);
#define PH0(S) { constexpr int s = (S);                                                                            \
      waitv_pin<(s < 16 ? 15 : 31 - s)>(xf[s]);                                                                    \
      if (s == 0) mf0(acc[0], ra[0], xf[0]); else mfa(acc[0], ra[s], xf[s]);                                       \
      if (s + 16 < 32) XLD((s + 16) & 31);                                                                         \
      if (s + 3 < 32) { ra[(s + 3) & 31] = wrd<SL0, (s + 3) & 31>(lo, mid, hi); rb[(s + 3) & 31] = wrd<SL1, (s + 3) & 31>(lo, mid, hi); } \
      if (s == 0) mf0(acc[1], rb[0], xf[0]); else mfa(acc[1], rb[s], xf[s]); }
 PH0(0) PH0(1) PH0(2) PH0(3) PH0(4) PH0(5) PH0(6) PH0(7) \
 PH0(8) PH0(9) PH0(10) PH0(11) PH0(12) PH0(13) PH0(14) PH0(15) \
 PH0(16) PH0(17) PH0(18) PH0(19) PH0(20) PH0(21) PH0(22) PH0(23) \
 PH0(24) PH0(25) PH0(26) PH0(27) PH0(28) PH0(29) PH0(30) PH0(31)
#undef PH0
  ra[0] = wrd<SL2, 0>(lo, mid, hi); ra[1] = wrd<SL2, 1>(lo, mid, hi); ra[2] = wrd<SL2, 2>(lo, mid, hi); ra[3] = wrd<SL2, 3>(lo, mid, hi);
  asm volatile("s_barrier" ::: "memory");
  xaug = lds128(GAPA + w * 512 + (l & 31) * 16);
  rs = *(const LAS float*)(uintptr_t)(GAPA + 2048 + w * 128 + (l & 31) * 4);
  f32x4 gv, uv, zz; u32x4 q;
#define E4(F) { _Pragma("unroll") for (int e = 0; e < 4; ++e) { F; } }
#define E2(B, F) { _Pragma("unroll") for (int e = B; e < B + 2; ++e) { F; } }
#define UNIT(T, U) { constexpr int u = (U) % 13, GO = ((U) / 13) * 8; const f32x16& k_ = acc[(T) % 3];              \
      if (u == 0) E4(gv[e] = vmul(k_[GO + e], rs))                                                                  \
      if (u == 1) E4(zz[e] = vmul(gv[e], gv[e]))                                                                    \
      if (u == 2) E4(zz[e] = vfma_k(zz[e], GC1, gc0))                                                               \
      if (u == 3) E4(zz[e] = vmul(zz[e], gv[e]))                                                                    \
      if (u == 4) E2(0, zz[e] = vexp(zz[e]))                                                                        \
      if (u == 5) E2(2, zz[e] = vexp(zz[e]))                                                                        \
      if (u == 6) E4(uv[e] = vmul(k_[GO + 4 + e], rs))                                                              \
      if (u == 7) E4(zz[e] = vadd1(zz[e]))                                                                          \
      if (u == 8) E2(0, zz[e] = vrcp(zz[e]))                                                                        \
      if (u == 9) E2(2, zz[e] = vrcp(zz[e]))                                                                        \
      if (u == 10) E4(uv[e] = vmul(uv[e], gv[e]))                                                                   \
      if (u == 11) E4(uv[e] = vmul(uv[e], zz[e]))                                                                   \
      if (u == 12) { q[GO / 4] = vcvt(uv[0], uv[1]); q[GO / 4 + 1] = vcvt(uv[2], uv[3]); if (GO) gst16_sc1(adst + (T) * 1024, vo, q); } }
#define E0(T) ((T) == 0 ? 9 : (T) == 1 ? 35 : (T) == 2 ? 61 : (T) == 3 ? 87 : (T) == 4 ? 113 : 139)
#define GAP1(T, G) { if ((G) >= E0(T) && (G) < E0(T) + 26) UNIT(T, ((G) - E0(T) + 26) % 26) }
#define GAP(G) { GAP1(0, G) GAP1(1, G) GAP1(2, G) GAP1(3, G) GAP1(4, G) GAP1(5, G) }
#define PH(S) { constexpr int s = (S);                                                                             \
      if (s == 0) mf0(acc[TT % 3], ra[0], xf[0]); else if (s < 32) mfa(acc[TT % 3], ra[s & 31], xf[s & 31]); else mfa(acc[TT % 3], aug[TT], xaug); \
      if (s < 32) { if (s + 4 < 32) ra[(s + 4) & 31] = wrd<SA, (s + 4) & 31>(lo, mid, hi); else if (TT < 6) ra[(s + 4) & 31] = wrd<SN, (s + 4) & 3>(lo, mid, hi); } \
      if (TT == 2 && s == 3) { mfa(acc[0], aug[0], xaug); mfa(acc[1], aug[1], xaug); }                             \
      if (s == 1 && TT < 6) aug[TT < 6 ? TT + 1 : 0] = gld_s<1, 0>(asrc + (TT + 1) * 1024, vo);                    \
      GAP((TT - 2) * 33 + s) }
#define PHASE  PH(0) PH(1) PH(2) PH(3) PH(4) PH(5) PH(6) PH(7) \
 PH(8) PH(9) PH(10) PH(11) PH(12) PH(13) PH(14) PH(15) \
 PH(16) PH(17) PH(18) PH(19) PH(20) PH(21) PH(22) PH(23) \
 PH(24) PH(25) PH(26) PH(27) PH(28) PH(29) PH(30) PH(31) \
 PH(32)
  { constexpr int TT = 2; constexpr u32 SA = SL2, SN = SL3; PHASE }
  asm volatile("s_waitcnt vmcnt(0)\n\ts_barrier" : "+v"(aug[3])::"memory");
  { constexpr int TT = 3; constexpr u32 SA = SL3, SN = SL4; PHASE }
  asm volatile("s_waitcnt vmcnt(1)\n\ts_barrier" : "+v"(aug[4])::"memory");
  { constexpr int TT = 4; constexpr u32 SA = SL4, SN = SL0; PHASE }
  asm volatile("s_waitcnt vmcnt(1)\n\ts_barrier" : "+v"(aug[5])::"memory");
  { constexpr int TT = 5; constexpr u32 SA = SL0, SN = SL1; PHASE }
  asm volatile("s_waitcnt vmcnt(1)\n\ts_barrier" : "+v"(aug[6])::"memory");
  { constexpr int TT = 6; constexpr u32 SA = SL1, SN = SL1; PHASE }
  MFMA_SETTLE();
 UNIT(6, 0) UNIT(6, 1) UNIT(6, 2) UNIT(6, 3) UNIT(6, 4) UNIT(6, 5) UNIT(6, 6) UNIT(6, 7) UNIT(6, 8)
 UNIT(6, 9) UNIT(6, 10) UNIT(6, 11) UNIT(6, 12) UNIT(6, 13) UNIT(6, 14) UNIT(6, 15) UNIT(6, 16) UNIT(6, 17)
 UNIT(6, 18) UNIT(6, 19) UNIT(6, 20) UNIT(6, 21) UNIT(6, 22) UNIT(6, 23) UNIT(6, 24) UNIT(6, 25)
  asm volatile("s_barrier" ::: "memory");
  {
    const u32 b = w * 8192;
#define WDR(F) (wd[F] = (F) < 8 ? lds128(lo + SL2 + b + ((F) & 7) * 1024) : (F) < 16 ? lds128(mid + (SL3 - 65536) + b + ((F) & 7) * 1024) : (F) < 22 ? lds128(hi + (TOPA - 131072) + w * 6144 + (((F) - 16) & 7) * 1024) : lds128(lo + SL0 + w * 6144 + (((F) - 22) & 7) * 1024))
 WDR(0); WDR(1); WDR(2); WDR(3); WDR(4); WDR(5); WDR(6); WDR(7); WDR(8); WDR(9);
 WDR(10); WDR(11); WDR(12); WDR(13); WDR(14); WDR(15); WDR(16); WDR(17); WDR(18); WDR(19);
 WDR(20); WDR(21); WDR(22); WDR(23); WDR(24); WDR(25); WDR(26); WDR(27);
#undef WDR
  }
#undef PHASE
#undef PH
#undef GAP
#undef GAP1
#undef UNIT
#undef XLD
}

DI void p4_helper(const Params& p, const Ids& id, int l, int w, u32 tag, u32x4 (&wd)[28]) {
  const u32 vo = l * 16;
  const int hw = w - 4;
  const char* wsrc = p.ws + WS_WGU + (size_t)id.c * 7 * 32768 + hw * 8192;
  const char* dme = p.ws + WS_WD + ((size_t)(2 * id.j) * 112 + 14 * w) * 1024;
  const char* dcw = p.ws + WS_WD + ((size_t)(2 * id.j) * 112 + 14 * hw) * 1024;
#define D8(SRC, DST) { dma4s(SRC, vo, DST); dma4s((SRC) + 4096, vo, (DST) + 4096); }
#define D6(SRC, DST) { dma4s(SRC, vo, DST); dma<2, 1>((SRC) + 4096, vo, (DST) + 4096); }
#define WDG(F) (wd[F] = gld_s<1, 0>(dme + ((F) < 14 ? (F) : 98 + (F)) * 1024, vo))
  D8(wsrc + 3 * 32768, SL3 + hw * 8192)
  WDG(16); WDG(17); WDG(18); WDG(19); WDG(20); WDG(21); WDG(22); WDG(23); WDG(24); WDG(25); WDG(26); WDG(27);
  WAITV_BAR(20);
  {
    __builtin_amdgcn_s_sleep(8);
    poll_flags<16, 8, 3>(p.ws + WS_FS + id.G * 512, tag, l);
    u32x4 st0, st1;
    asm volatile("global_load_dwordx4 %0, %2, %3 sc1\n\tglobal_load_dwordx4 %1, %2, %3 offset:16 sc1\n\ts_waitcnt vmcnt(0)" : "=&v"(st0), "=&v"(st1)
                 : "v"((l & 31) * 64 + (l >> 5) * 32), "s"(p.ws + WS_STAT + (size_t)(128 * id.G + 32 * hw) * 64) : "memory");
    const float s1 = swap32_sum((__uint_as_float(st0[0]) + __uint_as_float(st0[2])) + (__uint_as_float(st1[0]) + __uint_as_float(st1[2])));
    const float s2 = swap32_sum((__uint_as_float(st0[1]) + __uint_as_float(st0[3])) + (__uint_as_float(st1[1]) + __uint_as_float(st1[3])));
    const float nmu = -s1 * (1.f / DM), var = fmaxf(s2 * (1.f / DM) - nmu * nmu, 0.f) + EPS;
    const float rs = __builtin_amdgcn_rsqf(var), sg = var * rs, mh = rbf(nmu), sh = rbf(sg);
    if (l < 32) { sts128(GAPA + hw * 512 + vo, u32x4{pk(mh, mh), pk(nmu - mh, sh), pk(sh, sg - sh), 0u}); *(LAS float*)(uintptr_t)(GAPA + 2048 + hw * 128 + l * 4) = rs; }
  }
  dma4s(wsrc + 4 * 32768, vo, TOPA + hw * 8192); if (hw < 3) dma4s(wsrc + 4 * 32768 + 4096, vo, TOPA + hw * 8192 + 4096);
  BAR();
  D8(wsrc + 5 * 32768, SL0 + hw * 8192) WDG(0); WDG(1); WDG(2); WDG(3);
  WAITV_BAR(12);
  if (hw == 3) dma4s(wsrc + 4 * 32768 + 4096, vo, GAPA);
  D8(wsrc + 6 * 32768, SL1 + hw * 8192) dma4s(dcw, vo, SL2 + hw * 8192);
  WAITV_BAR(12);
  dma4s(dcw + 4096, vo, SL2 + hw * 8192 + 4096); D6(dcw + 8192, SL3 + hw * 8192)
  dma<2, 1>(dcw + 114688, vo, SL3 + hw * 8192 + 6144);
  WAITV_BAR(16);
  D6(dcw + 114688 + 2048, TOPA + hw * 6144) WDG(4); WDG(5); WDG(6); WDG(7); WDG(8); WDG(9);
  if (w == 4 && l == 0) *(LAS u32*)(uintptr_t)GAPA = 0u;
  BAR();
  if (w == 5 && l == 0) put_flag(p.ws + WS_FE + id.G * 512 + fofs<8>(id.c), tag);
  D6(dcw + 114688 + 8192, SL0 + hw * 6144) WDG(10); WDG(11); WDG(12); WDG(13); WDG(14); WDG(15);
  WAITV_BAR(6);
  poll_flags<16, 8, 4>(p.ws + WS_FE + (id.g >> 1) * 512, tag, l);
  asm volatile("s_waitcnt vmcnt(0)" : "+v"(wd[0]), "+v"(wd[1]), "+v"(wd[2]), "+v"(wd[3]), "+v"(wd[4]), "+v"(wd[5]), "+v"(wd[6]), "+v"(wd[7]), "+v"(wd[8]), "+v"(wd[9]), "+v"(wd[10]),
               "+v"(wd[11]), "+v"(wd[12]), "+v"(wd[13])::"memory");
  asm volatile("" : "+v"(wd[14]), "+v"(wd[15]), "+v"(wd[16]), "+v"(wd[17]), "+v"(wd[18]), "+v"(wd[19]), "+v"(wd[20]), "+v"(wd[21]), "+v"(wd[22]), "+v"(wd[23]), "+v"(wd[24]),
               "+v"(wd[25]), "+v"(wd[26]), "+v"(wd[27])::"memory");
#undef WDG
#undef D8
#undef D6
}

__global__ __launch_bounds__(512, 2) void k_main(Params p) {
  extern __shared__ __attribute__((aligned(16))) char smem[];
  const int tid = threadIdx.x, l = tid & 63, w = rfl(tid >> 6);
  const Ids id = ids();
  const int g = id.g, j = id.j, h = l >> 5;
  const u32 vo = l * 16;
  const u32 epoch = *(const u32*)(p.ws + WS_EPOCH);
  const u32 tag = epoch * 0x9E3779B1u + 0x7F4A7C15u;
  const int tt = w & 1, nt = (w >> 1) & 1, kh = w >> 2, kq = w >> 1;
  const char* modr = p.ws + WS_MOD + (blockIdx.x & 7) * 12288;
  const char* wgu = p.ws + WS_WGU + (size_t)id.c * 7 * 32768;
  const char* wob = p.ws + WS_WO + ((size_t)(2 * j) * 32 + 8 * kq) * 1024;
  const char *wob1 = wob + 4096, *wob2 = wob + 32768, *wob3 = wob + 36864;
  const int cb = rfl(64 * j + 32 * nt + 16 * kh);
  u32 xo = (u32)((l & 31) * DM + 4 * h) * 2, go = h * 16;
  const char* xres = (const char*)(p.x + (size_t)(64 * g + 32 * tt) * DM + cb);
  const char* gres = modr + 4096 + cb * 4;
  const u32 vrow = 1024 + h * 288 + min(l & 31, 17) * 16;
  u32 ka = kh * 32 * TS + vo, va = kh * 32 * TS + vrow;
  u32 kv2 = nt * 69632 + 24576 + tt * 8192;
  const char* kv2p = p.ws + WS_KV + kv2;
  u32x4 q0, q1, fo_, fd_, fg[2];
  char *pdo, *pdd, *pdg;
  f32x16 o0, o1;
  u32x4 vB0, vB1, pb0, pb1;
  {
    const int nh = (l >> 4) & 1, q = (l & 15) >> 2, pp = l & 3;
    const int uid = g * 7 + j - 1, uT = uid >> 1, ukh = uid & 1;
    const int rk = (blockIdx.x >> 6) * 7 + j - 1;
    const bool gemv = j != 0 && rk < 24;
    const int mu = 4 * ((blockIdx.x & 7) + 8 * (rk >> 2)) + (rk & 3);
    const u32 rawo = (l >> 3) * 1024 + (((l & 7) ^ (((l >> 4) & 1) << 2)) * 16);
#define WQ_DMA() { _Pragma("unroll") for (int i = 0; i < 8; ++i) { dma<1, 0>(p.wq + (size_t)(64 * w + 8 * i) * DM + 64 * j, rawo, F_A + (8 * w + i) * 1024); } }
#define LATE_DMA() {                                                                                                                              \
      if (j != 0) {                                                                                                                               \
        const u32 go = (l >> 1) * (FF * 2) + (l & 1) * 16;                                                                                        \
        dma<1, 0>(p.wg + (size_t)(256 * ukh + 32 * w) * FF + 16 * uT, go, F_B + w * 1024);                                                   \
        dma<1, 0>(p.wu + (size_t)(256 * ukh + 32 * w) * FF + 16 * uT, go, F_B + 8192 + 128 + w * 1024);                                      \
        dma<1, 0>(p.wd + (size_t)(64 * (uid >> 3) + 8 * w) * DM + 64 * (uid & 7), rawo, F_D + w * 1024);                                     \
      }                                                                                                                                           \
      if (w >= 6) { dma<1, 0>(p.wo + (size_t)(16 * g + 8 * (w - 6)) * DM + 64 * j, rawo, F_D + 8192 + (w - 6) * 1024); } }
    u32x4 w0, w1; u32 c0, c1;
    if (gemv) {
      const u32 mo = (tid >> 1) * 6144 + (tid & 1) * 16;
      c0 = gld2_s<0>(p.c, (tid >> 1) * 2); c1 = gld2_s<512>(p.c, (tid >> 1) * 2);
      w0 = gld_s<0, 0>(p.wm + 16 * mu, mo); w1 = gld_s<0, 0>(p.wm + 16 * mu + 256 * 3072, mo);
    }
    u32x4 xv[8];
    {
      const char* xs = (const char*)(p.x + (size_t)(64 * g + 8 * w) * DM);
      xv[0] = gld_s<0, 0>(xs, vo); xv[1] = gld_s<0, 1024>(xs, vo); xv[2] = gld_s<0, 2048>(xs, vo); xv[3] = gld_s<0, 3072>(xs, vo);
      xv[4] = gld_s<0, 0>(xs + 4096, vo); xv[5] = gld_s<0, 1024>(xs + 4096, vo); xv[6] = gld_s<0, 2048>(xs + 4096, vo); xv[7] = gld_s<0, 3072>(xs + 4096, vo);
    }
    if (w == 5) {
      unsigned long long pc; u32 len;
      asm volatile("s_getpc_b64 %0\n.Lcode0:\n\ts_mov_b32 %1, .Lcode1-.Lcode0" : "=s"(pc), "=s"(len));
      const u32 q = blockIdx.x >> 6;
#pragma unroll 1
      for (u32 o = q * 8192; o < len; o += 32768) { touch8k((const char*)pc, min(o + l * 128, len - 4) & ~3u); }
    }
    if (j == 0) {
      dma<2, 0>(p.wv + w * 1024, vo, F_H + w * 2048);
      dma<2, 0>(p.wk + w * 1024, vo, F_H + 16384 + 128 + w * 2048);
      WQ_DMA() LATE_DMA()
    }
    if (gemv) {
      asm volatile("s_waitcnt vmcnt(10)" : "+v"(c0), "+v"(c1)::"memory");
      const float f0 = bflo(c0), f1 = bflo(c1);
      u32 pa = F_M + w * 64 + ((l & 1) * 8 + (l & 2) * 2 + ((l >> 1) & 2) + ((l >> 3) & 1)) * 4, ma = (l >> 3) * 12288 + (l & 7) * 8;
      char* mdst = p.ws + WS_MOD + mu * 64;
      float s0 = rbf(f0 * __builtin_amdgcn_rcpf(1.f + EXP2(-1.4426950408889634f * f0))), s1 = rbf(f1 * __builtin_amdgcn_rcpf(1.f + EXP2(-1.4426950408889634f * f1)));
      PIN(s0); PIN(s1); PIN(pa); PIN(ma);
      asm volatile("" : "+s"(mdst));
      asm volatile("s_waitcnt vmcnt(8)" : "+v"(w0), "+v"(w1)::"memory");
      float a[8];
#pragma unroll
      for (int i = 0; i < 4; ++i) { a[2 * i] = s0 * bflo(w0[i]) + s1 * bflo(w1[i]); a[2 * i + 1] = s0 * bfhi(w0[i]) + s1 * bfhi(w1[i]); }
#pragma unroll
      for (int i = 0; i < 4; ++i) a[i] = halve<0x4E>(l & 2, a[i], a[i + 4]);
#pragma unroll
      for (int i = 0; i < 2; ++i) a[i] = halve<0x124>(l & 4, a[i], a[i + 2]);
      a[0] = halve<0x128>(l & 8, a[0], a[1]);
      a[0] = swap32_sum(swap16_sum(a[0]));
      if (l < 16) *(LAS float*)(uintptr_t)pa = a[0];
      BAR();
      if (w == 7) {
        const f32x2 s = ldsf2(F_M + l * 8);
        const float t0 = swap32_sum(swap16_sum(s[0] + dpp<0x128>(s[0]))), t1 = swap32_sum(swap16_sum(s[1] + dpp<0x128>(s[1])));
        gst8_sc1(mdst, ma, u32x2{pk(t0, t1), tag});
      }
    }
    if (j == 0) { if (w >= 6) WAITV(13); else WAITV(12); } else if (gemv && w == 7) WAITV(1); else WAITV(0);
    asm volatile("" : "+v"(xv[0]), "+v"(xv[1]), "+v"(xv[2]), "+v"(xv[3]), "+v"(xv[4]), "+v"(xv[5]), "+v"(xv[6]), "+v"(xv[7]));
    float xh[8][8];
    {
      float s1[8], s2[8];
#pragma unroll
      for (int i = 0; i < 8; ++i) {
        s1[i] = 0.f; s2[i] = 0.f;
#pragma unroll
        for (int e = 0; e < 4; ++e) { const u32 v = xv[i][e]; s1[i] = dot2(v, 0x3f803f80u, s1[i]); s2[i] = dot2(v, v, s2[i]); }
      }
#pragma unroll
      for (int i = 0; i < 4; ++i) { s1[i] = halve<0xB1>(l & 1, s1[i], s1[i + 4]); s2[i] = halve<0xB1>(l & 1, s2[i], s2[i + 4]); }
#pragma unroll
      for (int i = 0; i < 2; ++i) { s1[i] = halve<0x4E>(l & 2, s1[i], s1[i + 2]); s2[i] = halve<0x4E>(l & 2, s2[i], s2[i + 2]); }
      float t1 = halve<0x124>(l & 4, s1[0], s1[1]), t2 = halve<0x124>(l & 4, s2[0], s2[1]);
      t1 += dpp<0x128>(t1); t2 += dpp<0x128>(t2);
      t1 = swap32_sum(swap16_sum(t1)); t2 = swap32_sum(swap16_sum(t2));
      const float m = t1 * (1.f / DM), r = __builtin_amdgcn_rsqf(fmaxf(t2 * (1.f / DM) - m * m, 0.f) + EPS), nm = -m * r;
#define LNROW(I, LN) { const float ri = dpp<0x150 + (LN)>(r), ni = dpp<0x150 + (LN)>(nm); _Pragma("unroll") for (int e = 0; e < 4; ++e) { xh[I][2 * e] = fmaf(bflo(xv[I][e]), ri, ni); xh[I][2 * e + 1] = fmaf(bfhi(xv[I][e]), ri, ni); } }
      LNROW(0, 0) LNROW(1, 4) LNROW(2, 2) LNROW(3, 6) LNROW(4, 1) LNROW(5, 5) LNROW(6, 3) LNROW(7, 7)
#undef LNROW
#pragma unroll
      for (int i = 0; i < 8; ++i) asm volatile("" : "+v"(xh[i][0]), "+v"(xh[i][1]), "+v"(xh[i][2]), "+v"(xh[i][3]), "+v"(xh[i][4]), "+v"(xh[i][5]), "+v"(xh[i][6]), "+v"(xh[i][7]));
    }
    if (w > 0 && j != 0) WQ_DMA()
    u32x4 wkvf[16];
    if (j == 0) {
      if (w >= 6) WAITV(9); else WAITV(8);
      BAR();
      const u32 a0 = F_H + nh * (16384 + 128) + (256 * kh + 8 * h + q) * 32 + pp * 8;
#pragma unroll
      for (int s = 0; s < 16; ++s) wkvf[s] = ldstr2(a0 + s * 512, a0 + s * 512 + 128);
    }
    u32 ha[8], xa_[4];
#pragma unroll
    for (int i = 0; i < 8; ++i) { const int tk = 8 * w + i; ha[i] = F_H + ((tk >> 5) * 32 + (l >> 1)) * 1024 + (l & 1) * 512 + (((tk & 31) ^ (l & 7)) * 16); PIN(ha[i]); }
#pragma unroll
    for (int s = 0; s < 4; ++s) { xa_[s] = F_H + (tt * 32 + 16 * kh + s) * 1024 + h * 512 + (((l & 31) ^ ((2 * s + h) & 7)) * 16); PIN(xa_[s]); }
    if (w == 0) {
      u32x4 a0, a1, b0, b1;
#pragma unroll 1
      for (int it = 0; it < SPIN_CAP; ++it) {
        asm volatile("global_load_dwordx4 %0, %4, %5 sc1\n\tglobal_load_dwordx4 %1, %4, %5 offset:16 sc1\n\tglobal_load_dwordx4 %2, %4, %5 offset:2048 sc1\n\t"
                     "global_load_dwordx4 %3, %4, %5 offset:2064 sc1\n\ts_waitcnt vmcnt(0)" : "=&v"(b0), "=&v"(b1), "=&v"(a0), "=&v"(a1) : "v"(l * 32), "s"(modr) : "memory");
        if (__all(a0[1] == tag && a0[3] == tag && a1[1] == tag && a1[3] == tag && b0[1] == tag && b0[3] == tag && b1[1] == tag && b1[3] == tag)) break;
      }
      sts128(F_M + 512 + vo, u32x4{pk(1.f + bflo(a0[0]), 1.f + bfhi(a0[0])), pk(1.f + bflo(a0[2]), 1.f + bfhi(a0[2])), pk(1.f + bflo(a1[0]), 1.f + bfhi(a1[0])), pk(1.f + bflo(a1[2]), 1.f + bfhi(a1[2]))});
      sts128(F_M + 1536 + vo, u32x4{b0[0], b0[2], b1[0], b1[2]});
    }
    BAR();
    if (w == 0 && j != 0) WQ_DMA()
    if (j != 0) LATE_DMA()
    {
      const u32x4 ca = lds128(F_M + 512 + vo), sa = lds128(F_M + 1536 + vo);
      float A[8], B[8];
#pragma unroll
      for (int e = 0; e < 4; ++e) { A[2 * e] = bflo(ca[e]); A[2 * e + 1] = bfhi(ca[e]); B[2 * e] = bflo(sa[e]); B[2 * e + 1] = bfhi(sa[e]); }
#pragma unroll
      for (int i = 0; i < 8; ++i) {
        u32x4 o;
#pragma unroll
        for (int e = 0; e < 4; ++e) o[e] = pk(fmaf(xh[i][2 * e], A[2 * e], B[2 * e]), fmaf(xh[i][2 * e + 1], A[2 * e + 1], B[2 * e + 1]));
        sts128(ha[i], o);
      }
    }
    BAR();
    u32x4 xf[16];
#pragma unroll
    for (int s = 0; s < 16; ++s) xf[s] = lds128(xa_[s & 3] + (s & 12) * 1024);
    if (j == 0) {
      const u32 xs = F_B + w * 2048 + vo, xr = F_B + (w ^ 4) * 2048 + vo;
      f32x16 acc = {};
      if (nt == 0) {
#pragma unroll
        for (int s = 0; s < 16; ++s) acc = mfma(wkvf[s], xf[s], acc);
      } else {
#pragma unroll
        for (int s = 0; s < 16; ++s) acc = mfma(xf[s], wkvf[s], acc);
      }
      float keep[8], send[8];
#pragma unroll
      for (int e = 0; e < 8; ++e) {
        float lo = acc[e], hi = acc[8 + e];
        PIN(lo); PIN(hi);
        send[e] = kh ? lo : hi; keep[e] = kh ? hi : lo;
      }
      stsf4(xs, f32x4{send[0], send[1], send[2], send[3]}); stsf4(xs + 1024, f32x4{send[4], send[5], send[6], send[7]});
      BAR();
      const f32x4 r0 = ldsf4(xr), r1 = ldsf4(xr + 1024);
      u32x4 o = {pk(keep[0] + r0[0], keep[1] + r0[1]), pk(keep[2] + r0[2], keep[3] + r0[3]), pk(keep[4] + r1[0], keep[5] + r1[1]), pk(keep[6] + r1[2], keep[7] + r1[3])};
      char* img = p.ws + WS_KV + (size_t)(2 * g + tt) * TS;
      if (nt == 0) { if (kh == 1) gst16_sc1(img, vo, o); }
      else if ((l & 31) <= 17) {
        if ((l & 31) == 16) o = u32x4{0x3f803f80u, 0x3f803f80u, 0x3f803f80u, 0x3f803f80u};
        if ((l & 31) == 17) o = u32x4{0u, 0u, 0u, 0u};
        gst16_sc1(img + 1024 + kh * 576, h * 288 + (l & 31) * 16, o);
      }
      WAITV_BAR(0);
      if (w == 0 && l < 8) put_flag(p.ws + WS_FKV + l * 1024 + fofs<8>(g), tag);
    }
    if (j != 0) { if (w >= 6) WAITV(4); else WAITV(3); }
    BAR();
    const u32 rawr = (((nh * 2 + (pp >> 1)) ^ (((q >> 1) & 1) << 2)) * 16) + (pp & 1) * 8;
    f32x16 acc = {};
    {
      const u32 a0 = F_A + (256 * kh + 8 * h + q) * 128 + (rawr ^ (nt * 64));
#pragma unroll
      for (int s = 0; s < 16; ++s) acc = mfma(ldstr2(a0 + s * 2048, a0 + s * 2048 + 512), xf[s], acc);
    }
    const u32 xs = F_H + w * 4096 + vo, xr = F_H + (w ^ 4) * 4096 + vo;
    stsf4(xs, f32x4{acc[0], acc[1], acc[2], acc[3]}); stsf4(xs + 1024, f32x4{acc[4], acc[5], acc[6], acc[7]});
    stsf4(xs + 2048, f32x4{acc[8], acc[9], acc[10], acc[11]}); stsf4(xs + 3072, f32x4{acc[12], acc[13], acc[14], acc[15]});
    if (w == 1 && j != 0) {
      {
        u32x4 a0, b0;
#pragma unroll 1
        for (int it = 0; it < SPIN_CAP; ++it) {
          asm volatile("global_load_dwordx4 %0, %2, %3 sc1\n\tglobal_load_dwordx4 %1, %2, %3 offset:2048 sc1\n\ts_waitcnt vmcnt(0)" : "=&v"(b0), "=&v"(a0) : "v"(vo), "s"(modr + 6144 + ukh * 1024) : "memory");
          if (__all(a0[1] == tag && a0[3] == tag && b0[1] == tag && b0[3] == tag)) break;
          __builtin_amdgcn_s_sleep(2);
        }
        stsf4(F_M + 2560 + vo, f32x4{rbf(1.f + bflo(a0[0])), rbf(1.f + bfhi(a0[0])), rbf(1.f + bflo(a0[2])), rbf(1.f + bfhi(a0[2]))});
        stsf4(F_M + 3584 + vo, f32x4{bflo(b0[0]), bfhi(b0[0]), bflo(b0[2]), bfhi(b0[2])});
      }
    }
    WAITV_BAR(0);
    f32x4 cs[2][4];
    if (w >= 6) { const u32 a0 = F_D + 8192 + (4 * h + q) * 128 + (rawr ^ (tt * 64)); fo_ = ldstr2(a0, a0 + 1024); }
    if (j != 0) {
      const u32 a0 = F_D + (16 * kq + 4 * h + q) * 128 + (rawr ^ (tt * 64));
      fd_ = ldstr2(a0, a0 + 1024);
#pragma unroll
      for (int i = 0; i < 2; ++i) {
        const int s = 2 * w + i;
        const u32 a1 = F_B + (pp >> 1) * (8192 + 128) + (16 * s + 4 * h + q) * 32 + nh * 16 + (pp & 1) * 8;
        fg[i] = ldstr2(a1, a1 + 256);
        cs[i][0] = ldsf4(F_M + 2560 + (16 * s + 4 * h) * 4); cs[i][1] = ldsf4(F_M + 2560 + (16 * s + 8 + 4 * h) * 4);
        cs[i][2] = ldsf4(F_M + 3584 + (16 * s + 4 * h) * 4); cs[i][3] = ldsf4(F_M + 3584 + (16 * s + 8 + 4 * h) * 4);
      }
    }
    const f32x4 r0 = ldsf4(xr), r1 = ldsf4(xr + 1024), r2 = ldsf4(xr + 2048), r3 = ldsf4(xr + 3072);
    q0 = u32x4{pk((acc[0] + r0[0]) * C2, (acc[1] + r0[1]) * C2), pk((acc[2] + r0[2]) * C2, (acc[3] + r0[3]) * C2), pk((acc[4] + r1[0]) * C2, (acc[5] + r1[1]) * C2), pk((acc[6] + r1[2]) * C2, (acc[7] + r1[3]) * C2)};
    q1 = u32x4{pk((acc[8] + r2[0]) * C2, (acc[9] + r2[1]) * C2), pk((acc[10] + r2[2]) * C2, (acc[11] + r2[3]) * C2), pk((acc[12] + r3[0]) * C2, (acc[13] + r3[1]) * C2), pk((acc[14] + r3[2]) * C2, (acc[15] + r3[3]) * C2)};
    PIN(q0); PIN(q1);
    pdo = p.ws + WS_WO + ((size_t)(2 * j + tt) * 32 + g) * 1024;
    pdd = p.ws + WS_WD + ((size_t)(2 * (uid & 7) + tt) * 112 + 4 * (uid >> 3) + kq) * 1024;
    pdg = p.ws + WS_WGU + ((size_t)uT * 32 + 16 * ukh + 2 * w) * 1024;
    asm volatile("" : "+s"(pdo), "+s"(pdd), "+s"(pdg), "+s"(wob), "+s"(wob1), "+s"(wob2), "+s"(wob3), "+s"(xres), "+s"(gres), "+s"(kv2), "+s"(kv2p));
    asm volatile("" : "+v"(xo), "+v"(go), "+v"(ka), "+v"(va));
    if (w == 2) poll_flags<32, 8>(p.ws + WS_FKV + (blockIdx.x & 7) * 1024, tag, l);
    if (w == 3) {
#pragma unroll 1
      for (int it = 0; it < SPIN_CAP; ++it) {
        const u32 v = __hip_atomic_load((const GAS u32*)(modr + 4096 + j * 256 + h * 6144 + (l & 31) * 8 + 4), RLX_AGENT);
        if (__all(v == tag)) break;
        __builtin_amdgcn_s_sleep(2);
      }
    }
    BAR();
    { const u32 o = (w & 1) * 69632 + (w >> 1) * 6144;
      dma4s(p.ws + WS_KV + o, vo, o); dma<2, 1>(p.ws + WS_KV + o + 4096, vo, o + 4096); }
    o0 = f32x16{}; o1 = f32x16{}; vB0 = u32x4{}; vB1 = u32x4{}; pb0 = u32x4{}; pb1 = u32x4{};
    PIN(o0); PIN(o1); PIN(vB0); PIN(vB1); PIN(pb0); PIN(pb1);
    if (j != 0) {
      float us = 0.f, wsum = 0.f;
#pragma unroll
      for (int i = 0; i < 2; ++i) {
        const u32x4 f = fg[i];
        const f32x4 c0 = cs[i][0], c1 = cs[i][1], s0 = cs[i][2], s1 = cs[i][3];
        u32x4 o;
        o[0] = pk(bflo(f[0]) * c0[0], bfhi(f[0]) * c0[1]); o[1] = pk(bflo(f[1]) * c0[2], bfhi(f[1]) * c0[3]);
        o[2] = pk(bflo(f[2]) * c1[0], bfhi(f[2]) * c1[1]); o[3] = pk(bflo(f[3]) * c1[2], bfhi(f[3]) * c1[3]);
#pragma unroll
        for (int e = 0; e < 4; ++e) { const u32 v = o[e]; us = dot2(v, 0x3f803f80u, us); }
        wsum += bflo(f[0]) * s0[0] + bfhi(f[0]) * s0[1] + bflo(f[1]) * s0[2] + bfhi(f[1]) * s0[3] + bflo(f[2]) * s1[0] + bfhi(f[2]) * s1[1] + bflo(f[3]) * s1[2] + bfhi(f[3]) * s1[3];
        fg[i] = o;
      }
      us = swap32_sum(us); wsum = swap32_sum(wsum);
      if (l < 32) *(LAS f32x2*)(uintptr_t)(OEX + w * 256 + l * 8) = f32x2{us, wsum};
      WAITV_BAR(0);
      if (w == 7) {
        f32x2 s = {0.f, 0.f};
#pragma unroll
        for (int i = 0; i < 8; ++i) s += ldsf2(OEX + i * 256 + (l & 31) * 8);
        const float uh = rbf(s[0]), wh = rbf(s[1]);
        if (l < 32) gst16_sc1(p.ws + WS_AUG + (size_t)uT * 1024 + ukh * 512, vo, u32x4{pk(uh, s[0] - uh), pk(uh, wh), pk(s[1] - wh, wh), 0u});
      }
    } else {
      WAITV_BAR(0);
    }
  }
  const size_t fo = ((size_t)(2 * g + tt) * 32 + 4 * j + 2 * nt) * 1024;
  f32x16 nm0, nm1, s0, s1;
  const u32x4 k0 = lds128(vo);
  u32x4 kA = lds128(ka), kB = lds128(ka + TS);
  u32x4 vA0 = lds128(va), vA1 = lds128(va + 576);
  u32x4 pa0, pa1;
  {
    mf0(s0, k0, q0); mf0(s1, k0, q1);
    MFMA_SETTLE();
    PIN(s0); PIN(s1);
#define MX3(a, b, c) fmaxf(fmaxf(a, b), c)
    const float m0 = swap32_max(MX3(MX3(MX3(s0[0], s0[1], s0[2]), MX3(s0[3], s0[4], s0[5]), MX3(s0[6], s0[7], s0[8])), MX3(MX3(s0[9], s0[10], s0[11]), MX3(s0[12], s0[13], s0[14]), s0[15]), -3.0e38f));
    const float m1 = swap32_max(MX3(MX3(MX3(s1[0], s1[1], s1[2]), MX3(s1[3], s1[4], s1[5]), MX3(s1[6], s1[7], s1[8])), MX3(MX3(s1[9], s1[10], s1[11]), MX3(s1[12], s1[13], s1[14]), s1[15]), -3.0e38f));
#undef MX3
#pragma unroll
    for (int r = 0; r < 16; ++r) nm0[r] = -m0;
    PIN(nm0);
    s0 = mfma(kA, q0, nm0);
#pragma unroll
    for (int r = 0; r < 16; ++r) nm1[r] = -m1;
    PIN(nm1);
  }
  if (!kh) {
    dma4s(kv2p, vo, kv2); dma4s(kv2p + 4096, vo, kv2 + 4096);
  }
#define PREP_ST() { if (j != 0) { gst16_sc1(pdd, vo, fd_); gst16_sc1(pdg, vo, fg[0]); gst16_sc1(pdg + 1024, vo, fg[1]); } }
  if (kh) { PREP_ST() if (w >= 6) gst16_sc1(pdo, vo, fo_); }
  u32x4 wf[16];
#define HALF(SC, N0, N1, MF, OP, VP0, P0, VP1, P1, LD1, LD2)                                              \
  {                                                                                                       \
    MF; LD1; SB();                                                                                        \
    float e0 = EXP2(SC[0]), e1 = EXP2(SC[1]), e2 = EXP2(SC[2]), e3 = EXP2(SC[3]), e4 = EXP2(SC[4]), e5 = EXP2(SC[5]), e6 = EXP2(SC[6]), e7 = EXP2(SC[7]); SB(); \
    OP = mfma(VP0, P0, OP); SB();                                                                         \
    float e8 = EXP2(SC[8]), e9 = EXP2(SC[9]), e10 = EXP2(SC[10]), e11 = EXP2(SC[11]), e12 = EXP2(SC[12]), e13 = EXP2(SC[13]), e14 = EXP2(SC[14]), e15 = EXP2(SC[15]); SB(); \
    OP = mfma(VP1, P1, OP); LD2; SB();                                                                    \
    N0[0] = pk(e0, e1); N0[1] = pk(e2, e3); N0[2] = pk(e4, e5); N0[3] = pk(e6, e7); N1[0] = pk(e8, e9);   \
    N1[1] = pk(e10, e11); N1[2] = pk(e12, e13); N1[3] = pk(e14, e15); SB();                               \
  }
#define TILE(T, KC, KN, VC0, VC1, VP0, VP1)                                                               \
    HALF(s0, pa0, pa1, s1 = mfma(KC, q1, nm1), o1, VP0, pb0, VP1, pb1, KC = lds128(ka + ((T) + 2) * TS),  \
         (VP0 = lds128(va + ((T) + 1) * TS), VP1 = lds128(va + ((T) + 1) * TS + 576)))                    \
    HALF(s1, pb0, pb1, s0 = mfma(KN, q0, nm0), o0, VC0, pa0, VC1, pa1, , )
#define LDWF(SEL)                                                                                                     \
  asm volatile("s_cmp_lg_u32 %21, 0\n\ts_cbranch_scc1 .Lwf%=\n\t"                                                     \
               "global_load_dwordx4 %0, %16, %17 sc1\n\tglobal_load_dwordx4 %1, %16, %17 offset:1024 sc1\n\tglobal_load_dwordx4 %2, %16, %17 offset:2048 sc1\n\tglobal_load_dwordx4 %3, %16, %17 offset:3072 sc1\n\t"     \
               "global_load_dwordx4 %4, %16, %18 sc1\n\tglobal_load_dwordx4 %5, %16, %18 offset:1024 sc1\n\tglobal_load_dwordx4 %6, %16, %18 offset:2048 sc1\n\tglobal_load_dwordx4 %7, %16, %18 offset:3072 sc1\n\t"     \
               "global_load_dwordx4 %8, %16, %19 sc1\n\tglobal_load_dwordx4 %9, %16, %19 offset:1024 sc1\n\tglobal_load_dwordx4 %10, %16, %19 offset:2048 sc1\n\tglobal_load_dwordx4 %11, %16, %19 offset:3072 sc1\n\t"   \
               "global_load_dwordx4 %12, %16, %20 sc1\n\tglobal_load_dwordx4 %13, %16, %20 offset:1024 sc1\n\tglobal_load_dwordx4 %14, %16, %20 offset:2048 sc1\n\tglobal_load_dwordx4 %15, %16, %20 offset:3072 sc1\n"   \
               ".Lwf%=:"                                                                                              \
               : "+v"(wf[0]), "+v"(wf[1]), "+v"(wf[2]), "+v"(wf[3]), "+v"(wf[4]), "+v"(wf[5]), "+v"(wf[6]), "+v"(wf[7]), "+v"(wf[8]), "+v"(wf[9]), "+v"(wf[10]), \
                 "+v"(wf[11]), "+v"(wf[12]), "+v"(wf[13]), "+v"(wf[14]), "+v"(wf[15])                                  \
               : "v"(vo), "s"(wob), "s"(wob1), "s"(wob2), "s"(wob3), "s"(SEL) : "memory", "scc")
#pragma unroll
  for (int s4 = 0; s4 < 16; ++s4) asm volatile("" : "=v"(wf[s4]));
  u32x2 xa, xb; u32x4 ga, gb;
  asm volatile("" : "=v"(xa), "=v"(xb), "=v"(ga), "=v"(gb));
#define LDXR(SEL)                                                                                                     \
    asm volatile("s_cmp_lg_u32 %8, 0\n\ts_cbranch_scc1 .Lxr%=\n\t"                                                   \
                 "global_load_dwordx2 %0, %4, %5\n\tglobal_load_dwordx2 %1, %4, %5 offset:16\n\tglobal_load_dwordx4 %2, %6, %7 sc1\n\tglobal_load_dwordx4 %3, %6, %7 offset:32 sc1\n" \
                 ".Lxr%=:"                                                                                            \
                 : "+v"(xa), "+v"(xb), "+v"(ga), "+v"(gb) : "v"(xo), "s"(xres), "v"(go), "s"(gres), "s"(SEL) : "memory", "scc")
  const int qt = blockIdx.x >> 6;
#pragma unroll 1
  for (int seg = 0; seg < 4; ++seg) {
    char* wsl = p.ws; int wl = w; u32 bl = blockIdx.x;
    if (seg != 0) asm volatile("" : "+s"(wsl), "+s"(wl), "+s"(bl));
    if (seg == 1) {
      if (!kh) {
        PREP_ST()
        const u32 o = ((wl >> 1) & 1) * 69632 + 40960 + (wl & 1) * 14336;
        dma4s(wsl + WS_KV + o, vo, o); dma4s(wsl + WS_KV + o + 4096, vo, o + 4096); dma4s(wsl + WS_KV + o + 8192, vo, o + 8192);
        dma<2, 1>(wsl + WS_KV + o + 12288, vo, o + 12288);
        WAITV(14);
      } else {
        WAITV(0);
      }
      BAR();
      if (w == 0 && l < 8) put_flag(wsl + WS_FP + l * 1024 + bl * 4, tag);
    } else if (seg == 2) {
      if (w == 0) {
        u32x4 f;
#pragma unroll 1
        for (int it = 0; it < SPIN_CAP; ++it) {
          asm volatile("global_load_dwordx4 %0, %1, %2 sc1\n\ts_waitcnt vmcnt(0)" : "=&v"(f) : "v"(vo), "s"(wsl + WS_FP + (bl & 7) * 1024) : "memory");
          if (__all(f[0] == tag && f[1] == tag && f[2] == tag && f[3] == tag)) break;
          __builtin_amdgcn_s_sleep(1);
        }
        if (bl == 0 && l == 0) put_flag(wsl + WS_EPOCH, epoch + 1u);
      }
      WAITV_BAR(0);
    } else if (seg == 3) {
      if (!kh) {
        dma4s((wsl + WS_WGU + (size_t)(((bl & 1) * 8 + ((bl >> 3) & 7)) * 7) * 32768) + wl * 8192, vo, SL0 + wl * 8192); dma4s((wsl + WS_WGU + (size_t)(((bl & 1) * 8 + ((bl >> 3) & 7)) * 7) * 32768) + wl * 8192 + 4096, vo, SL0 + wl * 8192 + 4096);
        dma4s((wsl + WS_WGU + (size_t)(((bl & 1) * 8 + ((bl >> 3) & 7)) * 7) * 32768) + 32768 + wl * 8192, vo, SL1 + wl * 8192); dma4s((wsl + WS_WGU + (size_t)(((bl & 1) * 8 + ((bl >> 3) & 7)) * 7) * 32768) + 32768 + wl * 8192 + 4096, vo, SL1 + wl * 8192 + 4096);
        touch8k((wsl + WS_WGU + (size_t)(((bl & 1) * 8 + ((bl >> 3) & 7)) * 7) * 32768) + 65536 + (bl >> 6) * 40960 + wl * 8192, l * 128);
        if (w < 2) touch8k(w == 0 ? (wsl + WS_WGU + (size_t)(((bl & 1) * 8 + ((bl >> 3) & 7)) * 7) * 32768) + 65536 + (bl >> 6) * 40960 + 32768 : wsl + WS_AUG + (size_t)((bl & 1) * 8 + ((bl >> 3) & 7)) * 7168, l * 128);
      }
    }
    LDWF((seg ^ 2) | (kh ^ 1));
    LDXR((seg ^ 2) | (kh ^ 1));
    TILE(0, kA, kB, vA0, vA1, vB0, vB1) TILE(1, kB, kA, vB0, vB1, vA0, vA1) TILE(2, kA, kB, vA0, vA1, vB0, vB1) TILE(3, kB, kA, vB0, vB1, vA0, vA1)
    TILE(4, kA, kB, vA0, vA1, vB0, vB1) TILE(5, kB, kA, vB0, vB1, vA0, vA1) TILE(6, kA, kB, vA0, vA1, vB0, vB1) TILE(7, kB, kA, vB0, vB1, vA0, vA1)
    ka += 8 * TS; va += 8 * TS; PIN(ka); PIN(va);
  }
  o1 = mfma(vB0, pb0, o1); o1 = mfma(vB1, pb1, o1);
  const u32 oxs = OEX + w * 2304, oxr = OEX + (w ^ 4) * 2304;
  if (kh) { stsf4(oxs + vo, f32x4{o0[0], o0[1], o0[2], o0[3]}); stsf4(oxs + 1024 + vo, f32x4{o0[4], o0[5], o0[6], o0[7]}); *(LAS float*)(uintptr_t)(oxs + 2048 + l * 4) = o0[8]; }
  LDWF(kh);
  LDXR(kh);
  if (!kh) {
    touch8k(p.ws + WS_WD + (size_t)(2 * j) * 114688 + qt * 57344 + w * 8192, l * 128);
    if (w < 3) touch8k(p.ws + WS_WD + (size_t)(2 * j) * 114688 + qt * 57344 + (w + 4) * 8192, l * 128);
  }
#undef HALF
#undef TILE
  {
    if (!kh) { stsf4(oxs + vo, f32x4{o1[0], o1[1], o1[2], o1[3]}); stsf4(oxs + 1024 + vo, f32x4{o1[4], o1[5], o1[6], o1[7]}); *(LAS float*)(uintptr_t)(oxs + 2048 + l * 4) = o1[8]; }
    char* odst = p.ws + WS_O + fo + kh * 1024;
    asm volatile("" : "+s"(odst));
    BAR();
    const f32x4 r0 = ldsf4(oxr + vo), r1 = ldsf4(oxr + 1024 + vo);
    const float r2 = *(const LAS float*)(uintptr_t)(oxr + 2048 + l * 4);
    float o[9];
#pragma unroll
    for (int e = 0; e < 9; ++e) { float a = o0[e], b = o1[e]; PIN(a); PIN(b); o[e] = kh ? b : a; }
#pragma unroll
    for (int e = 0; e < 4; ++e) { o[e] += r0[e]; o[4 + e] += r1[e]; }
    float ls = lowhalf(o[8] + r2);
    bool bad = !(ls < 1.2676506e30f);
#pragma unroll
    for (int e = 0; e < 8; ++e) bad |= !(fabsf(o[e]) < 3.0e38f);
    if (__builtin_expect(__any(bad), 0)) {
      const u32x4 qf = kh ? q1 : q0;
      float m = -INFINITY;
      f32x16 oo = {};
#pragma unroll 1
      for (int t = 0; t < 64; ++t) {
        u32x4 kf, v0, v1;
        asm volatile("global_load_dwordx4 %0, %3, %5 sc1\n\tglobal_load_dwordx4 %1, %4, %5 sc1\n\tglobal_load_dwordx4 %2, %4, %5 offset:576 sc1\n\ts_waitcnt vmcnt(0)"
                     : "=&v"(kf), "=&v"(v0), "=&v"(v1) : "v"(vo), "v"(vrow), "s"(p.ws + WS_KV + t * TS) : "memory");
        const f32x16 s = mfma(kf, qf, f32x16{});
        const float mn = fmaxf(m, rowmax16(s)), al = EXP2(m - mn);
        m = mn;
        u32x4 pa, pb;
#pragma unroll
        for (int r = 0; r < 4; ++r) { pa[r] = pk(EXP2(s[2 * r] - mn), EXP2(s[2 * r + 1] - mn)); pb[r] = pk(EXP2(s[8 + 2 * r] - mn), EXP2(s[9 + 2 * r] - mn)); }
#pragma unroll
        for (int r = 0; r < 16; ++r) oo[r] *= al;
        oo = mfma(v0, pa, oo); oo = mfma(v1, pb, oo);
      }
#pragma unroll
      for (int e = 0; e < 8; ++e) o[e] = oo[e];
      ls = lowhalf(oo[8]);
    }
    const float rl = __builtin_amdgcn_rcpf(ls);
    gst16_sc1(odst, vo, u32x4{pk(o[0] * rl, o[1] * rl), pk(o[2] * rl, o[3] * rl), pk(o[4] * rl, o[5] * rl), pk(o[6] * rl, o[7] * rl)});
  }
  char* fo_own = p.ws + WS_FO + g * 512 + fofs<4>(j);
  const char* fo_poll = p.ws + WS_FO + g * 512 + fofs<4>(l < 8 ? l : 0);
  const char* osrc = p.ws + WS_O + ((size_t)(2 * g + tt) * 32 + 8 * kq) * 1024;
  char* x1dst = p.ws + WS_X1 + fo + kh * 1024;
  char* fx_own = p.ws + WS_FX + (g >> 1) * 512 + fofs<8>((g & 1) * 8 + j);
  asm volatile("" : "+s"(fo_own), "+s"(osrc), "+s"(x1dst), "+s"(fx_own));
  PIN(fo_poll);
  asm volatile("s_waitcnt vmcnt(0)\n\ts_barrier"
               : "+v"(wf[0]), "+v"(wf[1]), "+v"(wf[2]), "+v"(wf[3]), "+v"(wf[4]), "+v"(wf[5]), "+v"(wf[6]), "+v"(wf[7]), "+v"(wf[8]), "+v"(wf[9]),
                 "+v"(wf[10]), "+v"(wf[11]), "+v"(wf[12]), "+v"(wf[13]), "+v"(wf[14]), "+v"(wf[15]), "+v"(xa), "+v"(xb), "+v"(ga), "+v"(gb)::"memory");
  if (w == 1 && l == 0) put_flag(fo_own, tag);
  if (w == 0) poll_at<0>(fo_poll, tag);
  BAR();
  u32x4 x1;
  {
    u32x4 xf[8];
    xf[0] = gld_s<1, 0>(osrc, vo); xf[1] = gld_s<1, 1024>(osrc, vo); xf[2] = gld_s<1, 2048>(osrc, vo); xf[3] = gld_s<1, 3072>(osrc, vo);
    xf[4] = gld_s<1, 0>(osrc + 4096, vo); xf[5] = gld_s<1, 1024>(osrc + 4096, vo); xf[6] = gld_s<1, 2048>(osrc + 4096, vo); xf[7] = gld_s<1, 3072>(osrc + 4096, vo);
    float gf[8] = {bflo(ga[0]), bfhi(ga[0]), bflo(ga[2]), bfhi(ga[2]), bflo(gb[0]), bfhi(gb[0]), bflo(gb[2]), bfhi(gb[2])};
    float xr[8] = {bflo(xa[0]), bfhi(xa[0]), bflo(xa[1]), bfhi(xa[1]), bflo(xb[0]), bfhi(xb[0]), bflo(xb[1]), bfhi(xb[1])};
#pragma unroll
    for (int e = 0; e < 8; ++e) { PIN(gf[e]); PIN(xr[e]); }
    f32x16 acc0, acc1;
#define P3_STEP(S) { waitv_pin<7 - (S)>(xf[S]); if ((S) == 0) { mf0(acc0, wf[0], xf[0]); mf0(acc1, wf[8], xf[0]); } else { mfa(acc0, wf[S], xf[S]); mfa(acc1, wf[8 + (S)], xf[S]); } }
    P3_STEP(0) P3_STEP(1) P3_STEP(2) P3_STEP(3) P3_STEP(4) P3_STEP(5) P3_STEP(6) P3_STEP(7)
#undef P3_STEP
    MFMA_SETTLE();
    PIN(acc0); PIN(acc1);
    float r[8];
    {
      const u32 d = SL3 + tt * 4096 + kq * 1024 + vo;
      sts128(d, pack8(acc0, 0)); sts128(d + 8192, pack8(acc1, 0)); sts128(d + 16384, pack8(acc0, 8)); sts128(d + 24576, pack8(acc1, 8));
      BAR();
      sum_pieces<4>(SL3 + w * 4096 + vo, r);
    }
    x1 = u32x4{pk(fmaf(gf[0], r[0], xr[0]), fmaf(gf[1], r[1], xr[1])), pk(fmaf(gf[2], r[2], xr[2]), fmaf(gf[3], r[3], xr[3])),
               pk(fmaf(gf[4], r[4], xr[4]), fmaf(gf[5], r[5], xr[5])), pk(fmaf(gf[6], r[6], xr[6]), fmaf(gf[7], r[7], xr[7]))};
    gst16_sc1(x1dst, vo, x1);
    dma4s(wgu + 2 * 32768 + w * 4096, vo, SL2 + w * 4096);
    float s1 = 0.f, s2 = 0.f;
#pragma unroll
    for (int e = 0; e < 4; ++e) { const u32 v = x1[e]; s1 = dot2(v, 0x3f803f80u, s1); s2 = dot2(v, v, s2); }
    s1 = swap32_sum(s1); s2 = swap32_sum(s2);
    if (l < 32) *(LAS f32x2*)(uintptr_t)(GAPA + kq * 512 + tt * 256 + l * 8) = f32x2{s1, s2};
    BAR();
    if (w == 7) {
      f32x2 s = {0.f, 0.f};
#pragma unroll
      for (int i = 0; i < 4; ++i) s += ldsf2(GAPA + i * 512 + l * 8);
      gst8_sc1(p.ws + WS_STAT + ((size_t)64 * g * 8 + j) * 8, l * 64, u32x2{__float_as_uint(s[0]), __float_as_uint(s[1])});
      WAITV(5);
    } else {
      WAITV(4);
    }
    asm volatile("s_barrier" ::: "memory");
    if (w == 0 && l == 0) put_flag(fx_own, tag);
    if (w == 7) {
      WAITV(0);
      if (l == 0) put_flag(fx_own + (WS_FS - WS_FX), tag);
    }
  }
  u32x4 wd[28];
  if (kh == 0) p4_compute(p, id, l, w, tag, wd); else p4_helper(p, id, l, w, tag, wd);
  {
    int ly = l; PIN(ly);
    const char* xsrc = p.ws + WS_ACT + ((size_t)(2 * g) * 112 + 14 * w) * 1024;
    u32x4 xf[16];
#define XL(N) (xf[(N) % 16] = gld_s<1, 0>(xsrc + dks((N) >> 1) * 1024 + ((N) & 1) * 114688, vo))
    if (!kh) {
      WAITV(0);
      if (ly == 0 && __hip_atomic_fetch_add((LAS u32*)(uintptr_t)GAPA, 1u, __ATOMIC_RELAXED, __HIP_MEMORY_SCOPE_WORKGROUP) == 3u)
        put_flag(p.ws + WS_FA + id.G * 512 + fofs<8>(id.c), tag);
      BAR();
    }
    XL(0); XL(1); XL(2); XL(3); XL(4); XL(5); XL(6); XL(7); XL(8); XL(9); XL(10); XL(11);
    u32x4 g0 = gld_s<1, 0>(modr + 10240 + (64 * j + 32 * nt + 16 * kh) * 4, (ly >> 5) * 16), g1 = gld_s<1, 32>(modr + 10240 + (64 * j + 32 * nt + 16 * kh) * 4, (ly >> 5) * 16);
    if (kh) asm volatile("s_barrier" ::: "memory");
    f32x16 acc[4];
#define K5_X(N, CNT)                                                                                 \
  {                                                                                                  \
    waitv_pin<CNT>(xf[(N) % 16]);                                                                    \
    if ((N) < 2) { mf0(acc[(N) & 1], wd[0], xf[(N) & 1]); mf0(acc[2 + ((N) & 1)], wd[14], xf[(N) & 1]); } \
    else { mfa(acc[(N) & 1], wd[dks((N) >> 1)], xf[(N) % 16]); mfa(acc[2 + ((N) & 1)], wd[14 + dks((N) >> 1)], xf[(N) % 16]); } \
  }
    K5_X(0, 13) K5_X(1, 12) K5_X(2, 11) K5_X(3, 10) K5_X(4, 9) K5_X(5, 8) K5_X(6, 7) K5_X(7, 6) K5_X(8, 5) K5_X(9, 4) K5_X(10, 3) K5_X(11, 2)
    if (w == 4) poll_flags<16, 8>(p.ws + WS_FA + (g >> 1) * 512, tag, ly);
    asm volatile("s_barrier" ::: "memory");
    XL(12); XL(13); XL(14); XL(15); XL(16); XL(17); XL(18); XL(19); XL(20); XL(21); XL(22); XL(23); XL(24); XL(25); XL(26); XL(27);
    K5_X(12, 15) K5_X(13, 14) K5_X(14, 13) K5_X(15, 12) K5_X(16, 11) K5_X(17, 10) K5_X(18, 9) K5_X(19, 8) K5_X(20, 7) K5_X(21, 6) K5_X(22, 5) K5_X(23, 4)
    K5_X(24, 3) K5_X(25, 2) K5_X(26, 1) K5_X(27, 0)
#undef K5_X
    MFMA_SETTLE();
    PIN(g0); PIN(g1); PIN(acc[0]); PIN(acc[1]); PIN(acc[2]); PIN(acc[3]);
    float r[8], gf[8], x1f[8];
    u16* optr;
    {
      const u32 d = w * 1024 + vo;
#pragma unroll
      for (int pc = 0; pc < 8; ++pc) sts128(d + pc * 8192, pack8(acc[pc & 3], (pc >> 2) * 8));
#pragma unroll
      for (int e = 0; e < 4; ++e) { x1f[2 * e] = bflo(x1[e]); x1f[2 * e + 1] = bfhi(x1[e]); }
      gf[0] = bflo(g0[0]); gf[1] = bfhi(g0[0]); gf[2] = bflo(g0[2]); gf[3] = bfhi(g0[2]); gf[4] = bflo(g1[0]); gf[5] = bfhi(g1[0]); gf[6] = bflo(g1[2]); gf[7] = bfhi(g1[2]);
#pragma unroll
      for (int e = 0; e < 8; ++e) { PIN(gf[e]); PIN(x1f[e]); }
      int lz = l; PIN(lz);
      optr = p.out + (size_t)(64 * g + 32 * tt + (lz & 31)) * DM + 64 * j + 32 * nt + 16 * kh + 8 * (lz >> 5);
      PIN(optr);
      BAR();
      sum_pieces<8>(w * 8192 + vo, r);
    }
    const u32 a0 = pk(fmaf(gf[0], r[0], x1f[0]), fmaf(gf[1], r[1], x1f[1])), a1 = pk(fmaf(gf[2], r[2], x1f[2]), fmaf(gf[3], r[3], x1f[3]));
    const u32 b0 = pk(fmaf(gf[4], r[4], x1f[4]), fmaf(gf[5], r[5], x1f[5])), b1 = pk(fmaf(gf[6], r[6], x1f[6]), fmaf(gf[7], r[7], x1f[7]));
    const auto s0 = __builtin_amdgcn_permlane32_swap(a0, b0, false, false);
    const auto s1 = __builtin_amdgcn_permlane32_swap(a1, b1, false, false);
    *(GAS u32x4*)optr = u32x4{s0[0], s1[0], s0[1], s1[1]};
  }
  asm volatile(".Lcode1:");
}

extern "C" void kernel_launch(void* const* d_in, const int* in_sizes, int n_in, void* d_out, int out_size, void* d_ws, size_t ws_size, hipStream_t stream) {
  Params p;
  p.x = (const u16*)d_in[0]; p.wq = (const u16*)d_in[1]; p.wk = (const u16*)d_in[2]; p.wv = (const u16*)d_in[3]; p.wo = (const u16*)d_in[4];
  p.wg = (const u16*)d_in[6]; p.wu = (const u16*)d_in[7]; p.wd = (const u16*)d_in[8]; p.c = (const u16*)d_in[9]; p.wm = (const u16*)d_in[10];
  p.out = (u16*)d_out; p.ws = (char*)d_ws;
  (void)hipFuncSetAttribute((const void*)k_main, hipFuncAttributeMaxDynamicSharedMemorySize, M_LDS);
  k_main<<<256, 512, M_LDS, stream>>>(p);
}
```

```cpp
#include <hip/hip_runtime.h>
#include <stdint.h>

typedef unsigned short u16;
typedef unsigned int u32;
typedef __attribute__((ext_vector_type(8))) __bf16 bf16x8;
typedef __attribute__((ext_vector_type(2))) __bf16 bf16x2;
typedef __attribute__((ext_vector_type(2))) float f32x2;
typedef __attribute__((ext_vector_type(4))) float f32x4;
typedef __attribute__((ext_vector_type(16))) float f32x16;
typedef __attribute__((ext_vector_type(4))) u32 u32x4;
typedef __attribute__((ext_vector_type(2))) u32 u32x2;
typedef __attribute__((ext_vector_type(4))) short s16x4;
#define LAS __attribute__((address_space(3)))
#define GAS __attribute__((address_space(1)))
#define DI __device__ __forceinline__

constexpr int DM = 512, FF = 1792;
constexpr float EPS = 1e-5f;
constexpr float C2 = 0.25f * 1.4426950408889634f;

constexpr size_t WS_EPOCH = 0;
constexpr size_t WS_FO = 4096;
constexpr size_t WS_FX = WS_FO + 16384;
constexpr size_t WS_FS = WS_FX + 8192;
constexpr size_t WS_FE = WS_FS + 8192;
constexpr size_t WS_FA = WS_FE + 8192;
constexpr size_t WS_FKV = WS_FA + 8192;
constexpr size_t WS_FP = WS_FKV + 8192;
constexpr size_t WS_MOD = 81920;
constexpr size_t WS_KV = WS_MOD + 8 * 12288;
constexpr size_t WS_STAT = WS_KV + 139264;
constexpr size_t WS_AUG = WS_STAT + 131072;
constexpr size_t WS_WO = 1u << 20;
constexpr size_t WS_WD = WS_WO + (512u << 10);
constexpr size_t WS_WGU = WS_WD + (1792u << 10);
constexpr size_t WS_O = WS_WGU + (3584u << 10);
constexpr size_t WS_X1 = WS_O + (2u << 20);
constexpr size_t WS_ACT = WS_X1 + (2u << 20);
static_assert(WS_FP + 8192 <= WS_MOD && WS_AUG + 114688 + 8192 <= WS_WO, "workspace map");
constexpr int TS = 2176;

struct Params {
  const u16 *x, *wq, *wk, *wv, *wo, *wg, *wu, *wd, *c, *wm;
  u16* out;
  char* ws;
};

DI f32x16 mfma(u32x4 a, u32x4 b, f32x16 c) {
  return __builtin_amdgcn_mfma_f32_32x32x16_bf16(__builtin_bit_cast(bf16x8, a), __builtin_bit_cast(bf16x8, b), c, 0, 0, 0);
}
DI u32 pk(float lo, float hi) {
  f32x2 v = {lo, hi};
  return __builtin_bit_cast(u32, __builtin_convertvector(v, bf16x2));
}
DI float bflo(u32 u) { return __uint_as_float(u << 16); }
DI float bfhi(u32 u) { return __uint_as_float(u & 0xffff0000u); }
DI float rbf(float f) { return bflo(pk(f, 0.f)); }
DI float dot2(u32 a, u32 b, float c) { return __builtin_amdgcn_fdot2_f32_bf16(__builtin_bit_cast(bf16x2, a), __builtin_bit_cast(bf16x2, b), c, false); }
template <int N, int SC> DI void dma(const void* base, u32 vo, u32 lds) {
#define DMA_HEAD "s_mov_b32 m0, %2\n\ts_nop 0\n\t"
#define DMA_OPS ::"v"(vo), "s"(base), "s"(lds) : "memory"
  if (N == 1 && !SC) asm volatile(DMA_HEAD "global_load_lds_dwordx4 %0, %1" DMA_OPS);
  if (N == 2 && !SC) asm volatile(DMA_HEAD "global_load_lds_dwordx4 %0, %1\n\tglobal_load_lds_dwordx4 %0, %1 offset:1024" DMA_OPS);
  if (N == 2 && SC) asm volatile(DMA_HEAD "global_load_lds_dwordx4 %0, %1 sc1\n\tglobal_load_lds_dwordx4 %0, %1 offset:1024 sc1" DMA_OPS);
  if (N == 4 && SC) asm volatile(DMA_HEAD "global_load_lds_dwordx4 %0, %1 sc1\n\tglobal_load_lds_dwordx4 %0, %1 offset:1024 sc1\n\t"
                                 "global_load_lds_dwordx4 %0, %1 offset:2048 sc1\n\tglobal_load_lds_dwordx4 %0, %1 offset:3072 sc1" DMA_OPS);
}
DI void dma4s(const void* base, u32 vo, u32 lds) { dma<4, 1>(base, vo, lds); }
template <int SC, int OFF> DI u32x4 gld_s(const void* base, u32 vo) {
  u32x4 r;
  if (SC) asm volatile("global_load_dwordx4 %0, %1, %2 offset:%3 sc1" : "=v"(r) : "v"(vo), "s"(base), "n"(OFF) : "memory");
  else asm volatile("global_load_dwordx4 %0, %1, %2 offset:%3" : "=v"(r) : "v"(vo), "s"(base), "n"(OFF) : "memory");
  return r;
}
template <int OFF> DI u32 gld2_s(const void* base, u32 vo) { u32 r; asm volatile("global_load_ushort %0, %1, %2 offset:%3" : "=v"(r) : "v"(vo), "s"(base), "n"(OFF) : "memory"); return r; }
DI void touch8k(const char* base, u32 vo) { const u32 lds = 163840 - 256; asm volatile(DMA_HEAD "global_load_lds_dword %0, %1 sc1" DMA_OPS); }
DI void gst16_sc1(char* base, u32 vo, u32x4 v) { asm volatile("global_store_dwordx4 %0, %1, %2 sc1\n\ts_nop 1" ::"v"(vo), "v"(v), "s"(base) : "memory"); }
DI void gst8_sc1(char* base, u32 vo, u32x2 v) { asm volatile("global_store_dwordx2 %0, %1, %2 sc1\n\ts_nop 1" ::"v"(vo), "v"(v), "s"(base) : "memory"); }
#define RLX_AGENT __ATOMIC_RELAXED, __HIP_MEMORY_SCOPE_AGENT
DI void put_flag(char* f, u32 tag) { __hip_atomic_store((GAS u32*)f, tag, RLX_AGENT); }
constexpr int SPIN_CAP = 1 << 20;
template <int PER> DI u32 fofs(int i) { return (u32)((i / PER) * 256 + (i % PER) * 4); }
template <int NAP> DI void poll_at(const char* a, u32 tag) {
#pragma unroll 1
  for (int it = 0; it < SPIN_CAP; ++it) {
    const u32 v = __hip_atomic_load((const GAS u32*)a, RLX_AGENT);
    if (__all(v == tag)) break;
    if (NAP) __builtin_amdgcn_s_sleep(NAP);
  }
  __builtin_amdgcn_fence(__ATOMIC_ACQUIRE, "wavefront");
}
template <int N, int PER, int NAP = 0> DI void poll_flags(const char* f, u32 tag, int l) { poll_at<NAP>(f + fofs<PER>(l < N ? l : 0), tag); }
#define SB() __builtin_amdgcn_sched_barrier(0)
#define PIN(x) asm volatile("" : "+v"(x))
#define WAITV(N) asm volatile("s_waitcnt vmcnt(" #N ")" ::: "memory")
#define WAITV_BAR(N) asm volatile("s_waitcnt vmcnt(" #N ") lgkmcnt(0)\n\ts_barrier" ::: "memory")
#define BAR() asm volatile("s_waitcnt lgkmcnt(0)\n\ts_barrier" ::: "memory")
template <int N> DI void waitv_pin(u32x4& v) { asm volatile("s_waitcnt vmcnt(%1)" : "+v"(v) : "n"(N) : "memory"); }
DI u32x4 lds128(u32 a) { return *(const LAS u32x4*)(uintptr_t)a; }
DI f32x4 ldsf4(u32 a) { return *(const LAS f32x4*)(uintptr_t)a; }
DI f32x2 ldsf2(u32 a) { return *(const LAS f32x2*)(uintptr_t)a; }
DI void sts128(u32 a, u32x4 v) { *(LAS u32x4*)(uintptr_t)a = v; }
DI void stsf4(u32 a, f32x4 v) { *(LAS f32x4*)(uintptr_t)a = v; }
DI u32x2 ldstr(u32 a) { return __builtin_bit_cast(u32x2, __builtin_amdgcn_ds_read_tr16_b64_v4i16((LAS s16x4*)(uintptr_t)a)); }
DI u32x4 ldstr2(u32 a0, u32 a1) { u32x2 p = ldstr(a0), q = ldstr(a1); return u32x4{p[0], p[1], q[0], q[1]}; }
template <int CTRL> DI float dpp(float v) { return __int_as_float(__builtin_amdgcn_mov_dpp(__float_as_int(v), CTRL, 0xf, 0xf, true)); }
DI float swap32_sum(float v) { auto r = __builtin_amdgcn_permlane32_swap(__float_as_uint(v), __float_as_uint(v), false, false); return __uint_as_float(r[0]) + __uint_as_float(r[1]); }
DI float swap32_max(float v) { auto r = __builtin_amdgcn_permlane32_swap(__float_as_uint(v), __float_as_uint(v), false, false); return fmaxf(__uint_as_float(r[0]), __uint_as_float(r[1])); }
DI float swap16_sum(float v) { auto r = __builtin_amdgcn_permlane16_swap(__float_as_uint(v), __float_as_uint(v), false, false); return __uint_as_float(r[0]) + __uint_as_float(r[1]); }
DI float lowhalf(float v) { auto r = __builtin_amdgcn_permlane32_swap(__float_as_uint(v), __float_as_uint(v), false, false); return __uint_as_float(r[0]); }
template <int CTRL> DI float halve(bool bit, float a, float b) { return (bit ? b : a) + dpp<CTRL>(bit ? a : b); }
DI int rfl(int v) { return __builtin_amdgcn_readfirstlane(v); }
#define EXP2(v) __builtin_amdgcn_exp2f(v)

struct Ids { int G, c, g, j; };
DI Ids ids() { const int b = blockIdx.x, x = b & 7, i = b >> 3; Ids r; r.G = (x >> 1) * 4 + (i >> 3); r.c = (x & 1) * 8 + (i & 7); r.g = 4 * x + (i >> 3); r.j = i & 7; return r; }

constexpr u32 M_LDS = 163840;
constexpr u32 F_A = 0;
constexpr u32 F_H = 65536;
constexpr u32 F_B = 131072;
constexpr u32 F_D = F_B + 16640;
constexpr u32 F_M = F_D + 10240;
static_assert(F_M + 4608 <= M_LDS, "front-end LDS map");
constexpr u32 SL0 = 0, SL1 = 69632, SL2 = 32768, SL3 = 102400, TOPA = 135168, GAPA = 65536, OEX = 143616;

DI void mfa(f32x16& c, u32x4 a, u32x4 b) { asm volatile("v_mfma_f32_32x32x16_bf16 %0, %1, %2, %0" : "+v"(c) : "v"(a), "v"(b) : "memory"); }
DI void mf0(f32x16& c, u32x4 a, u32x4 b) { asm volatile("v_mfma_f32_32x32x16_bf16 %0, %1, %2, 0" : "=&v"(c) : "v"(a), "v"(b) : "memory"); }
DI float vexp(float a) { float r; asm volatile("v_exp_f32 %0, %1" : "=v"(r) : "v"(a)); return r; }
DI float vrcp(float a) { float r; asm volatile("v_rcp_f32 %0, %1" : "=v"(r) : "v"(a)); return r; }
DI u32 vcvt(float a, float b) { u32 r; asm volatile("v_cvt_pk_bf16_f32 %0, %1, %2" : "=v"(r) : "v"(a), "v"(b)); return r; }
DI float vmul(float a, float b) { float r; asm volatile("v_mul_f32 %0, %1, %2" : "=v"(r) : "v"(a), "v"(b)); return r; }
DI float vfma_k(float a, float k, float c) { float r; asm volatile("v_fma_f32 %0, %1, %2, %3" : "=v"(r) : "v"(a), "s"(k), "v"(c)); return r; }
DI float vadd1(float a) { float r; asm volatile("v_add_f32 %0, 1.0, %1" : "=v"(r) : "v"(a)); return r; }
#define MFMA_SETTLE() asm volatile("s_nop 15\n\ts_nop 3" ::: "memory")
DI float rowmax16(const f32x16& s) {
  float m = fmaxf(fmaxf(s[0], s[1]), s[2]);
#pragma unroll
  for (int r = 3; r < 15; r += 2) m = fmaxf(fmaxf(m, s[r]), s[r + 1]);
  return swap32_max(fmaxf(m, s[15]));
}
constexpr float GC0 = -2.f * 0.7978845608028654f * 1.4426950408889634f, GC1 = GC0 * 0.044715f;

DI u32x4 pack8(const f32x16& a, int o8) { return u32x4{pk(a[o8], a[o8 + 1]), pk(a[o8 + 2], a[o8 + 3]), pk(a[o8 + 4], a[o8 + 5]), pk(a[o8 + 6], a[o8 + 7])}; }
template <int N> DI void sum_pieces(u32 a, float (&r)[8]) {
  u32 klo = 0x00003f80u, khi = 0x3f800000u;
  asm volatile("" : "+s"(klo), "+s"(khi));
  u32x4 u[N];
#pragma unroll
  for (int i = 0; i < N; ++i) u[i] = lds128(a + i * 1024);
#pragma unroll
  for (int e = 0; e < 4; ++e) { r[2 * e] = bflo(u[0][e]); r[2 * e + 1] = bfhi(u[0][e]); }
#pragma unroll
  for (int i = 1; i < N; ++i)
#pragma unroll
    for (int e = 0; e < 4; ++e) { const u32 v = u[i][e]; r[2 * e] = dot2(v, klo, r[2 * e]); r[2 * e + 1] = dot2(v, khi, r[2 * e + 1]); }
}

constexpr u32 SL4 = 0xffffffffu;
template <u32 SL, int S> DI u32x4 wrd(u32 lo, u32 mid, u32 hi) {
  constexpr u32 A = SL == SL4 ? (S < 28 ? TOPA + S * 1024 : GAPA + (S - 28) * 1024) : SL + S * 1024;
  return A < 65536 ? lds128(lo + A) : (A < 131072 ? lds128(mid + (A - 65536)) : lds128(hi + (A - 131072)));
}
DI constexpr int dks(int i) { return i < 6 ? 7 * (i / 3) + i % 3 : 7 * ((i - 6) >> 2) + 3 + ((i - 6) & 3); }

DI void p4_compute(const Params& p, const Ids& id, int l, int w, u32 tag, u32x4 (&wd)[28]) {
  const u32 vo = l * 16;
  char* adst = p.ws + WS_ACT + ((size_t)(4 * id.G + w) * 112 + 7 * id.c) * 1024;
  const char* asrc = p.ws + WS_AUG + (size_t)id.c * 7168;
  u32x4 aug[7];
  aug[0] = gld_s<1, 0>(asrc, vo); aug[1] = gld_s<1, 1024>(asrc, vo); aug[2] = gld_s<1, 2048>(asrc, vo);
  if (w == 0) poll_flags<16, 8>(p.ws + WS_FX + id.G * 512, tag, l);
  asm volatile("s_waitcnt vmcnt(0)\n\ts_barrier" : "+v"(aug[0]), "+v"(aug[1]), "+v"(aug[2])::"memory");
  const char* xsrc = p.ws + WS_X1 + (size_t)(4 * id.G + w) * 32768;
  u32x4 xf[32];
#define XLD(S) (xf[S] = gld_s<1, ((S) & 3) * 1024>(xsrc + ((S) >> 2) * 4096, vo))
  XLD(0); XLD(1); XLD(2); XLD(3); XLD(4); XLD(5); XLD(6); XLD(7); XLD(8); XLD(9); XLD(10); XLD(11); XLD(12); XLD(13); XLD(14); XLD(15);
  float rs, gc0 = GC0; u32x4 xaug;
  u32 lo = vo, mid = vo + 65536, hi = vo + 131072;
  PIN(lo); PIN(mid); PIN(hi); PIN(gc0);
  f32x16 acc[3]; u32x4 ra[32], rb[32];
  ra[0] = wrd<SL0, 0>(lo, mid, hi); rb[0] = wrd<SL1, 0>(lo, mid, hi); ra[1] = wrd<SL0, 1>(lo, mid, hi); rb[1] = wrd<SL1, 1>(lo, mid, hi);
  ra[2] = wrd<SL0, 2>(lo, mid, hi); rb[2] = wrd<SL1, 2>(lo, mid, hi);
#define PH0(S) { constexpr int s = (S);                                                                            \
      waitv_pin<(s < 16 ? 15 : 31 - s)>(xf[s]);                                                                    \
      if (s == 0) mf0(acc[0], ra[0], xf[0]); else mfa(acc[0], ra[s], xf[s]);                                       \
      if (s + 16 < 32) XLD((s + 16) & 31);                                                                         \
      if (s + 3 < 32) { ra[(s + 3) & 31] = wrd<SL0, (s + 3) & 31>(lo, mid, hi); rb[(s + 3) & 31] = wrd<SL1, (s + 3) & 31>(lo, mid, hi); } \
      if (s == 0) mf0(acc[1], rb[0], xf[0]); else mfa(acc[1], rb[s], xf[s]); }
 PH0(0) PH0(1) PH0(2) PH0(3) PH0(4) PH0(5) PH0(6) PH0(7) \
 PH0(8) PH0(9) PH0(10) PH0(11) PH0(12) PH0(13) PH0(14) PH0(15) \
 PH0(16) PH0(17) PH0(18) PH0(19) PH0(20) PH0(21) PH0(22) PH0(23) \
 PH0(24) PH0(25) PH0(26) PH0(27) PH0(28) PH0(29) PH0(30) PH0(31)
#undef PH0
  ra[0] = wrd<SL2, 0>(lo, mid, hi); ra[1] = wrd<SL2, 1>(lo, mid, hi); ra[2] = wrd<SL2, 2>(lo, mid, hi); ra[3] = wrd<SL2, 3>(lo, mid, hi);
  asm volatile("s_barrier" ::: "memory");
  xaug = lds128(GAPA + w * 512 + (l & 31) * 16);
  rs = *(const LAS float*)(uintptr_t)(GAPA + 2048 + w * 128 + (l & 31) * 4);
  f32x4 gv, uv, zz; u32x4 q;
#define E4(F) { _Pragma("unroll") for (int e = 0; e < 4; ++e) { F; } }
#define E2(B, F) { _Pragma("unroll") for (int e = B; e < B + 2; ++e) { F; } }
#define UNIT(T, U) { constexpr int u = (U) % 13, GO = ((U) / 13) * 8; const f32x16& k_ = acc[(T) % 3];              \
      if (u == 0) E4(gv[e] = vmul(k_[GO + e], rs))                                                                  \
      if (u == 1) E4(zz[e] = vmul(gv[e], gv[e]))                                                                    \
      if (u == 2) E4(zz[e] = vfma_k(zz[e], GC1, gc0))                                                               \
      if (u == 3) E4(zz[e] = vmul(zz[e], gv[e]))                                                                    \
      if (u == 4) E2(0, zz[e] = vexp(zz[e]))                                                                        \
      if (u == 5) E2(2, zz[e] = vexp(zz[e]))                                                                        \
      if (u == 6) E4(uv[e] = vmul(k_[GO + 4 + e], rs))                                                              \
      if (u == 7) E4(zz[e] = vadd1(zz[e]))                                                                          \
      if (u == 8) E2(0, zz[e] = vrcp(zz[e]))                                                                        \
      if (u == 9) E2(2, zz[e] = vrcp(zz[e]))                                                                        \
      if (u == 10) E4(uv[e] = vmul(uv[e], gv[e]))                                                                   \
      if (u == 11) E4(uv[e] = vmul(uv[e], zz[e]))                                                                   \
      if (u == 12) { q[GO / 4] = vcvt(uv[0], uv[1]); q[GO / 4 + 1] = vcvt(uv[2], uv[3]); if (GO) gst16_sc1(adst + (T) * 1024, vo, q); } }
#define E0(T) ((T) == 0 ? 9 : (T) == 1 ? 35 : (T) == 2 ? 61 : (T) == 3 ? 87 : (T) == 4 ? 113 : 139)
#define GAP1(T, G) { if ((G) >= E0(T) && (G) < E0(T) + 26) UNIT(T, ((G) - E0(T) + 26) % 26) }
#define GAP(G) { GAP1(0, G) GAP1(1, G) GAP1(2, G) GAP1(3, G) GAP1(4, G) GAP1(5, G) }
#define PH(S) { constexpr int s = (S);                                                                             \
      if (s == 0) mf0(acc[TT % 3], ra[0], xf[0]); else if (s < 32) mfa(acc[TT % 3], ra[s & 31], xf[s & 31]); else mfa(acc[TT % 3], aug[TT], xaug); \
      if (s < 32) { if (s + 4 < 32) ra[(s + 4) & 31] = wrd<SA, (s + 4) & 31>(lo, mid, hi); else if (TT < 6) ra[(s + 4) & 31] = wrd<SN, (s + 4) & 3>(lo, mid, hi); } \
      if (TT == 2 && s == 3) { mfa(acc[0], aug[0], xaug); mfa(acc[1], aug[1], xaug); }                             \
      if (s == 1 && TT < 6) aug[TT < 6 ? TT + 1 : 0] = gld_s<1, 0>(asrc + (TT + 1) * 1024, vo);                    \
      GAP((TT - 2) * 33 + s) }
#define PHASE  PH(0) PH(1) PH(2) PH(3) PH(4) PH(5) PH(6) PH(7) \
 PH(8) PH(9) PH(10) PH(11) PH(12) PH(13) PH(14) PH(15) \
 PH(16) PH(17) PH(18) PH(19) PH(20) PH(21) PH(22) PH(23) \
 PH(24) PH(25) PH(26) PH(27) PH(28) PH(29) PH(30) PH(31) \
 PH(32)
  { constexpr int TT = 2; constexpr u32 SA = SL2, SN = SL3; PHASE }
  asm volatile("s_waitcnt vmcnt(0)\n\ts_barrier" : "+v"(aug[3])::"memory");
  { constexpr int TT = 3; constexpr u32 SA = SL3, SN = SL4; PHASE }
  asm volatile("s_waitcnt vmcnt(1)\n\ts_barrier" : "+v"(aug[4])::"memory");
  { constexpr int TT = 4; constexpr u32 SA = SL4, SN = SL0; PHASE }
  asm volatile("s_waitcnt vmcnt(1)\n\ts_barrier" : "+v"(aug[5])::"memory");
  { constexpr int TT = 5; constexpr u32 SA = SL0, SN = SL1; PHASE }
  asm volatile("s_waitcnt vmcnt(1)\n\ts_barrier" : "+v"(aug[6])::"memory");
  { constexpr int TT = 6; constexpr u32 SA = SL1, SN = SL1; PHASE }
  MFMA_SETTLE();
 UNIT(6, 0) UNIT(6, 1) UNIT(6, 2) UNIT(6, 3) UNIT(6, 4) UNIT(6, 5) UNIT(6, 6) UNIT(6, 7) UNIT(6, 8)
 UNIT(6, 9) UNIT(6, 10) UNIT(6, 11) UNIT(6, 12) UNIT(6, 13) UNIT(6, 14) UNIT(6, 15) UNIT(6, 16) UNIT(6, 17)
 UNIT(6, 18) UNIT(6, 19) UNIT(6, 20) UNIT(6, 21) UNIT(6, 22) UNIT(6, 23) UNIT(6, 24) UNIT(6, 25)
  asm volatile("s_barrier" ::: "memory");
  {
    const u32 b = w * 8192;
#define WDR(F) (wd[F] = (F) < 8 ? lds128(lo + SL2 + b + ((F) & 7) * 1024) : (F) < 16 ? lds128(mid + (SL3 - 65536) + b + ((F) & 7) * 1024) : (F) < 22 ? lds128(hi + (TOPA - 131072) + w * 6144 + (((F) - 16) & 7) * 1024) : lds128(lo + SL0 + w * 6144 + (((F) - 22) & 7) * 1024))
 WDR(0); WDR(1); WDR(2); WDR(3); WDR(4); WDR(5); WDR(6); WDR(7); WDR(8); WDR(9);
 WDR(10); WDR(11); WDR(12); WDR(13); WDR(14); WDR(15); WDR(16); WDR(17); WDR(18); WDR(19);
 WDR(20); WDR(21); WDR(22); WDR(23); WDR(24); WDR(25); WDR(26); WDR(27);
#undef WDR
  }
#undef PHASE
#undef PH
#undef GAP
#undef GAP1
#undef UNIT
#undef XLD
}

DI void p4_helper(const Params& p, const Ids& id, int l, int w, u32 tag, u32x4 (&wd)[28]) {
  const u32 vo = l * 16;
  const int hw = w - 4;
  const char* wsrc = p.ws + WS_WGU + (size_t)id.c * 7 * 32768 + hw * 8192;
  const char* dme = p.ws + WS_WD + ((size_t)(2 * id.j) * 112 + 14 * w) * 1024;
  const char* dcw = p.ws + WS_WD + ((size_t)(2 * id.j) * 112 + 14 * hw) * 1024;
#define D8(SRC, DST) { dma4s(SRC, vo, DST); dma4s((SRC) + 4096, vo, (DST) + 4096); }
#define D6(SRC, DST) { dma4s(SRC, vo, DST); dma<2, 1>((SRC) + 4096, vo, (DST) + 4096); }
#define WDG(F) (wd[F] = gld_s<1, 0>(dme + ((F) < 14 ? (F) : 98 + (F)) * 1024, vo))
  D8(wsrc + 3 * 32768, SL3 + hw * 8192)
  WDG(16); WDG(17); WDG(18); WDG(19); WDG(20); WDG(21); WDG(22); WDG(23); WDG(24); WDG(25); WDG(26); WDG(27);
  WAITV_BAR(20);
  {
    __builtin_amdgcn_s_sleep(8);
    poll_flags<16, 8, 3>(p.ws + WS_FS + id.G * 512, tag, l);
    u32x4 st0, st1;
    asm volatile("global_load_dwordx4 %0, %2, %3 sc1\n\tglobal_load_dwordx4 %1, %2, %3 offset:16 sc1\n\ts_waitcnt vmcnt(0)" : "=&v"(st0), "=&v"(st1)
                 : "v"((l & 31) * 64 + (l >> 5) * 32), "s"(p.ws + WS_STAT + (size_t)(128 * id.G + 32 * hw) * 64) : "memory");
    const float s1 = swap32_sum((__uint_as_float(st0[0]) + __uint_as_float(st0[2])) + (__uint_as_float(st1[0]) + __uint_as_float(st1[2])));
    const float s2 = swap32_sum((__uint_as_float(st0[1]) + __uint_as_float(st0[3])) + (__uint_as_float(st1[1]) + __uint_as_float(st1[3])));
    const float nmu = -s1 * (1.f / DM), var = fmaxf(s2 * (1.f / DM) - nmu * nmu, 0.f) + EPS;
    const float rs = __builtin_amdgcn_rsqf(var), sg = var * rs, mh = rbf(nmu), sh = rbf(sg);
    if (l < 32) { sts128(GAPA + hw * 512 + vo, u32x4{pk(mh, mh), pk(nmu - mh, sh), pk(sh, sg - sh), 0u}); *(LAS float*)(uintptr_t)(GAPA + 2048 + hw * 128 + l * 4) = rs; }
  }
  dma4s(wsrc + 4 * 32768, vo, TOPA + hw * 8192); if (hw < 3) dma4s(wsrc + 4 * 32768 + 4096, vo, TOPA + hw * 8192 + 4096);
  BAR();
  D8(wsrc + 5 * 32768, SL0 + hw * 8192) WDG(0); WDG(1); WDG(2); WDG(3);
  WAITV_BAR(12);
  if (hw == 3) dma4s(wsrc + 4 * 32768 + 4096, vo, GAPA);
  D8(wsrc + 6 * 32768, SL1 + hw * 8192) dma4s(dcw, vo, SL2 + hw * 8192);
  WAITV_BAR(12);
  dma4s(dcw + 4096, vo, SL2 + hw * 8192 + 4096); D6(dcw + 8192, SL3 + hw * 8192)
  dma<2, 1>(dcw + 114688, vo, SL3 + hw * 8192 + 6144);
  WAITV_BAR(16);
  D6(dcw + 114688 + 2048, TOPA + hw * 6144) WDG(4); WDG(5); WDG(6); WDG(7); WDG(8); WDG(9);
  if (w == 4 && l == 0) *(LAS u32*)(uintptr_t)GAPA = 0u;
  BAR();
  if (w == 5 && l == 0) put_flag(p.ws + WS_FE + id.G * 512 + fofs<8>(id.c), tag);
  D6(dcw + 114688 + 8192, SL0 + hw * 6144) WDG(10); WDG(11); WDG(12); WDG(13); WDG(14); WDG(15);
  WAITV_BAR(6);
  poll_flags<16, 8, 4>(p.ws + WS_FE + (id.g >> 1) * 512, tag, l);
  asm volatile("s_waitcnt vmcnt(0)" : "+v"(wd[0]), "+v"(wd[1]), "+v"(wd[2]), "+v"(wd[3]), "+v"(wd[4]), "+v"(wd[5]), "+v"(wd[6]), "+v"(wd[7]), "+v"(wd[8]), "+v"(wd[9]), "+v"(wd[10]),
               "+v"(wd[11]), "+v"(wd[12]), "+v"(wd[13])::"memory");
  asm volatile("" : "+v"(wd[14]), "+v"(wd[15]), "+v"(wd[16]), "+v"(wd[17]), "+v"(wd[18]), "+v"(wd[19]), "+v"(wd[20]), "+v"(wd[21]), "+v"(wd[22]), "+v"(wd[23]), "+v"(wd[24]),
               "+v"(wd[25]), "+v"(wd[26]), "+v"(wd[27])::"memory");
#undef WDG
#undef D8
#undef D6
}

__global__ __launch_bounds__(512, 2) void k_main(Params p) {
  extern __shared__ __attribute__((aligned(16))) char smem[];
  const int tid = threadIdx.x, l = tid & 63, w = rfl(tid >> 6);
  const Ids id = ids();
  const int g = id.g, j = id.j, h = l >> 5;
  const u32 vo = l * 16;
  const u32 epoch = *(const u32*)(p.ws + WS_EPOCH);
  const u32 tag = epoch * 0x9E3779B1u + 0x7F4A7C15u;
  const int tt = w & 1, nt = (w >> 1) & 1, kh = w >> 2, kq = w >> 1;
  const char* modr = p.ws + WS_MOD + (blockIdx.x & 7) * 12288;
  const char* wgu = p.ws + WS_WGU + (size_t)id.c * 7 * 32768;
  const char* wob = p.ws + WS_WO + ((size_t)(2 * j) * 32 + 8 * kq) * 1024;
  const char *wob1 = wob + 4096, *wob2 = wob + 32768, *wob3 = wob + 36864;
  const int cb = rfl(64 * j + 32 * nt + 16 * kh);
  u32 xo = (u32)((l & 31) * DM + 4 * h) * 2, go = h * 16;
  const char* xres = (const char*)(p.x + (size_t)(64 * g + 32 * tt) * DM + cb);
  const char* gres = modr + 4096 + cb * 4;
  const u32 vrow = 1024 + h * 288 + min(l & 31, 17) * 16;
  u32 ka = kh * 32 * TS + vo, va = kh * 32 * TS + vrow;
  u32 kv2 = nt * 69632 + 24576 + tt * 8192;
  const char* kv2p = p.ws + WS_KV + kv2;
  u32x4 q0, q1, fo_, fd_, fg[2];
  char *pdo, *pdd, *pdg;
  f32x16 o0, o1;
  u32x4 vB0, vB1, pb0, pb1;
  {
    const int nh = (l >> 4) & 1, q = (l & 15) >> 2, pp = l & 3;
    const int uid = g * 7 + j - 1, uT = uid >> 1, ukh = uid & 1;
    const int rk = (blockIdx.x >> 6) * 7 + j - 1;
    const bool gemv = j != 0 && rk < 24;
    const int mu = 4 * ((blockIdx.x & 7) + 8 * (rk >> 2)) + (rk & 3);
    const u32 rawo = (l >> 3) * 1024 + (((l & 7) ^ (((l >> 4) & 1) << 2)) * 16);
#define WQ_DMA() { _Pragma("unroll") for (int i = 0; i < 8; ++i) { dma<1, 0>(p.wq + (size_t)(64 * w + 8 * i) * DM + 64 * j, rawo, F_A + (8 * w + i) * 1024); } }
#define LATE_DMA() {                                                                                                                              \
      if (j != 0) {                                                                                                                               \
        const u32 go = (l >> 1) * (FF * 2) + (l & 1) * 16;                                                                                        \
        dma<1, 0>(p.wg + (size_t)(256 * ukh + 32 * w) * FF + 16 * uT, go, F_B + w * 1024);                                                   \
        dma<1, 0>(p.wu + (size_t)(256 * ukh + 32 * w) * FF + 16 * uT, go, F_B + 8192 + 128 + w * 1024);                                      \
        dma<1, 0>(p.wd + (size_t)(64 * (uid >> 3) + 8 * w) * DM + 64 * (uid & 7), rawo, F_D + w * 1024);                                     \
      }                                                                                                                                           \
      if (w >= 6) { dma<1, 0>(p.wo + (size_t)(16 * g + 8 * (w - 6)) * DM + 64 * j, rawo, F_D + 8192 + (w - 6) * 1024); } }
    u32x4 w0, w1; u32 c0, c1;
    if (gemv) {
      const u32 mo = (tid >> 1) * 6144 + (tid & 1) * 16;
      c0 = gld2_s<0>(p.c, (tid >> 1) * 2); c1 = gld2_s<512>(p.c, (tid >> 1) * 2);
      w0 = gld_s<0, 0>(p.wm + 16 * mu, mo); w1 = gld_s<0, 0>(p.wm + 16 * mu + 256 * 3072, mo);
    }
    u32x4 xv[8];
    {
      const char* xs = (const char*)(p.x + (size_t)(64 * g + 8 * w) * DM);
      xv[0] = gld_s<0, 0>(xs, vo); xv[1] = gld_s<0, 1024>(xs, vo); xv[2] = gld_s<0, 2048>(xs, vo); xv[3] = gld_s<0, 3072>(xs, vo);
      xv[4] = gld_s<0, 0>(xs + 4096, vo); xv[5] = gld_s<0, 1024>(xs + 4096, vo); xv[6] = gld_s<0, 2048>(xs + 4096, vo); xv[7] = gld_s<0, 3072>(xs + 4096, vo);
    }
    if (j == 0) {
      dma<2, 0>(p.wv + w * 1024, vo, F_H + w * 2048);
      dma<2, 0>(p.wk + w * 1024, vo, F_H + 16384 + 128 + w * 2048);
      WQ_DMA() LATE_DMA()
    }
    if (gemv) {
      asm volatile("s_waitcnt vmcnt(10)" : "+v"(c0), "+v"(c1)::"memory");
      const float f0 = bflo(c0), f1 = bflo(c1);
      u32 pa = F_M + w * 64 + ((l & 1) * 8 + (l & 2) * 2 + ((l >> 1) & 2) + ((l >> 3) & 1)) * 4, ma = (l >> 3) * 12288 + (l & 7) * 8;
      char* mdst = p.ws + WS_MOD + mu * 64;
      float s0 = rbf(f0 * __builtin_amdgcn_rcpf(1.f + EXP2(-1.4426950408889634f * f0))), s1 = rbf(f1 * __builtin_amdgcn_rcpf(1.f + EXP2(-1.4426950408889634f * f1)));
      PIN(s0); PIN(s1); PIN(pa); PIN(ma);
      asm volatile("" : "+s"(mdst));
      asm volatile("s_waitcnt vmcnt(8)" : "+v"(w0), "+v"(w1)::"memory");
      float a[8];
#pragma unroll
      for (int i = 0; i < 4; ++i) { a[2 * i] = s0 * bflo(w0[i]) + s1 * bflo(w1[i]); a[2 * i + 1] = s0 * bfhi(w0[i]) + s1 * bfhi(w1[i]); }
#pragma unroll
      for (int i = 0; i < 4; ++i) a[i] = halve<0x4E>(l & 2, a[i], a[i + 4]);
#pragma unroll
      for (int i = 0; i < 2; ++i) a[i] = halve<0x124>(l & 4, a[i], a[i + 2]);
      a[0] = halve<0x128>(l & 8, a[0], a[1]);
      a[0] = swap32_sum(swap16_sum(a[0]));
      if (l < 16) *(LAS float*)(uintptr_t)pa = a[0];
      BAR();
      if (w == 7) {
        const f32x2 s = ldsf2(F_M + l * 8);
        const float t0 = swap32_sum(swap16_sum(s[0] + dpp<0x128>(s[0]))), t1 = swap32_sum(swap16_sum(s[1] + dpp<0x128>(s[1])));
        gst8_sc1(mdst, ma, u32x2{pk(t0, t1), tag});
      }
    }
    if (j == 0) { if (w >= 6) WAITV(13); else WAITV(12); } else if (gemv && w == 7) WAITV(1); else WAITV(0);
    asm volatile("" : "+v"(xv[0]), "+v"(xv[1]), "+v"(xv[2]), "+v"(xv[3]), "+v"(xv[4]), "+v"(xv[5]), "+v"(xv[6]), "+v"(xv[7]));
    float xh[8][8];
    {
      float s1[8], s2[8];
#pragma unroll
      for (int i = 0; i < 8; ++i) {
        s1[i] = 0.f; s2[i] = 0.f;
#pragma unroll
        for (int e = 0; e < 4; ++e) { const u32 v = xv[i][e]; s1[i] = dot2(v, 0x3f803f80u, s1[i]); s2[i] = dot2(v, v, s2[i]); }
      }
#pragma unroll
      for (int i = 0; i < 4; ++i) { s1[i] = halve<0xB1>(l & 1, s1[i], s1[i + 4]); s2[i] = halve<0xB1>(l & 1, s2[i], s2[i + 4]); }
#pragma unroll
      for (int i = 0; i < 2; ++i) { s1[i] = halve<0x4E>(l & 2, s1[i], s1[i + 2]); s2[i] = halve<0x4E>(l & 2, s2[i], s2[i + 2]); }
      float t1 = halve<0x124>(l & 4, s1[0], s1[1]), t2 = halve<0x124>(l & 4, s2[0], s2[1]);
      t1 += dpp<0x128>(t1); t2 += dpp<0x128>(t2);
      t1 = swap32_sum(swap16_sum(t1)); t2 = swap32_sum(swap16_sum(t2));
      const float m = t1 * (1.f / DM), r = __builtin_amdgcn_rsqf(fmaxf(t2 * (1.f / DM) - m * m, 0.f) + EPS), nm = -m * r;
#define LNROW(I, LN) { const float ri = dpp<0x150 + (LN)>(r), ni = dpp<0x150 + (LN)>(nm); _Pragma("unroll") for (int e = 0; e < 4; ++e) { xh[I][2 * e] = fmaf(bflo(xv[I][e]), ri, ni); xh[I][2 * e + 1] = fmaf(bfhi(xv[I][e]), ri, ni); } }
      LNROW(0, 0) LNROW(1, 4) LNROW(2, 2) LNROW(3, 6) LNROW(4, 1) LNROW(5, 5) LNROW(6, 3) LNROW(7, 7)
#undef LNROW
#pragma unroll
      for (int i = 0; i < 8; ++i) asm volatile("" : "+v"(xh[i][0]), "+v"(xh[i][1]), "+v"(xh[i][2]), "+v"(xh[i][3]), "+v"(xh[i][4]), "+v"(xh[i][5]), "+v"(xh[i][6]), "+v"(xh[i][7]));
    }
    if (w > 0 && j != 0) WQ_DMA()
    u32x4 wkvf[16];
    if (j == 0) {
      if (w >= 6) WAITV(9); else WAITV(8);
      BAR();
      const u32 a0 = F_H + nh * (16384 + 128) + (256 * kh + 8 * h + q) * 32 + pp * 8;
#pragma unroll
      for (int s = 0; s < 16; ++s) wkvf[s] = ldstr2(a0 + s * 512, a0 + s * 512 + 128);
    }
    u32 ha[8], xa_[4];
#pragma unroll
    for (int i = 0; i < 8; ++i) { const int tk = 8 * w + i; ha[i] = F_H + ((tk >> 5) * 32 + (l >> 1)) * 1024 + (l & 1) * 512 + (((tk & 31) ^ (l & 7)) * 16); PIN(ha[i]); }
#pragma unroll
    for (int s = 0; s < 4; ++s) { xa_[s] = F_H + (tt * 32 + 16 * kh + s) * 1024 + h * 512 + (((l & 31) ^ ((2 * s + h) & 7)) * 16); PIN(xa_[s]); }
    if (w == 0) {
      u32x4 a0, a1, b0, b1;
#pragma unroll 1
      for (int it = 0; it < SPIN_CAP; ++it) {
        asm volatile("global_load_dwordx4 %0, %4, %5 sc1\n\tglobal_load_dwordx4 %1, %4, %5 offset:16 sc1\n\tglobal_load_dwordx4 %2, %4, %5 offset:2048 sc1\n\t"
                     "global_load_dwordx4 %3, %4, %5 offset:2064 sc1\n\ts_waitcnt vmcnt(0)" : "=&v"(b0), "=&v"(b1), "=&v"(a0), "=&v"(a1) : "v"(l * 32), "s"(modr) : "memory");
        if (__all(a0[1] == tag && a0[3] == tag && a1[1] == tag && a1[3] == tag && b0[1] == tag && b0[3] == tag && b1[1] == tag && b1[3] == tag)) break;
      }
      sts128(F_M + 512 + vo, u32x4{pk(1.f + bflo(a0[0]), 1.f + bfhi(a0[0])), pk(1.f + bflo(a0[2]), 1.f + bfhi(a0[2])), pk(1.f + bflo(a1[0]), 1.f + bfhi(a1[0])), pk(1.f + bflo(a1[2]), 1.f + bfhi(a1[2]))});
      sts128(F_M + 1536 + vo, u32x4{b0[0], b0[2], b1[0], b1[2]});
    }
    BAR();
    if (w == 0 && j != 0) WQ_DMA()
    if (j != 0) LATE_DMA()
    {
      const u32x4 ca = lds128(F_M + 512 + vo), sa = lds128(F_M + 1536 + vo);
      float A[8], B[8];
#pragma unroll
      for (int e = 0; e < 4; ++e) { A[2 * e] = bflo(ca[e]); A[2 * e + 1] = bfhi(ca[e]); B[2 * e] = bflo(sa[e]); B[2 * e + 1] = bfhi(sa[e]); }
#pragma unroll
      for (int i = 0; i < 8; ++i) {
        u32x4 o;
#pragma unroll
        for (int e = 0; e < 4; ++e) o[e] = pk(fmaf(xh[i][2 * e], A[2 * e], B[2 * e]), fmaf(xh[i][2 * e + 1], A[2 * e + 1], B[2 * e + 1]));
        sts128(ha[i], o);
      }
    }
    BAR();
    u32x4 xf[16];
#pragma unroll
    for (int s = 0; s < 16; ++s) xf[s] = lds128(xa_[s & 3] + (s & 12) * 1024);
    if (j == 0) {
      const u32 xs = F_B + w * 2048 + vo, xr = F_B + (w ^ 4) * 2048 + vo;
      f32x16 acc = {};
      if (nt == 0) {
#pragma unroll
        for (int s = 0; s < 16; ++s) acc = mfma(wkvf[s], xf[s], acc);
      } else {
#pragma unroll
        for (int s = 0; s < 16; ++s) acc = mfma(xf[s], wkvf[s], acc);
      }
      float keep[8], send[8];
#pragma unroll
      for (int e = 0; e < 8; ++e) {
        float lo = acc[e], hi = acc[8 + e];
        PIN(lo); PIN(hi);
        send[e] = kh ? lo : hi; keep[e] = kh ? hi : lo;
      }
      stsf4(xs, f32x4{send[0], send[1], send[2], send[3]}); stsf4(xs + 1024, f32x4{send[4], send[5], send[6], send[7]});
      BAR();
      const f32x4 r0 = ldsf4(xr), r1 = ldsf4(xr + 1024);
      u32x4 o = {pk(keep[0] + r0[0], keep[1] + r0[1]), pk(keep[2] + r0[2], keep[3] + r0[3]), pk(keep[4] + r1[0], keep[5] + r1[1]), pk(keep[6] + r1[2], keep[7] + r1[3])};
      char* img = p.ws + WS_KV + (size_t)(2 * g + tt) * TS;
      if (nt == 0) { if (kh == 1) gst16_sc1(img, vo, o); }
      else if ((l & 31) <= 17) {
        if ((l & 31) == 16) o = u32x4{0x3f803f80u, 0x3f803f80u, 0x3f803f80u, 0x3f803f80u};
        if ((l & 31) == 17) o = u32x4{0u, 0u, 0u, 0u};
        gst16_sc1(img + 1024 + kh * 576, h * 288 + (l & 31) * 16, o);
      }
      WAITV_BAR(0);
      if (w == 0 && l < 8) put_flag(p.ws + WS_FKV + l * 1024 + fofs<8>(g), tag);
    }
    if (j != 0) { if (w >= 6) WAITV(4); else WAITV(3); }
    BAR();
    const u32 rawr = (((nh * 2 + (pp >> 1)) ^ (((q >> 1) & 1) << 2)) * 16) + (pp & 1) * 8;
    f32x16 acc = {};
    {
      const u32 a0 = F_A + (256 * kh + 8 * h + q) * 128 + (rawr ^ (nt * 64));
#pragma unroll
      for (int s = 0; s < 16; ++s) acc = mfma(ldstr2(a0 + s * 2048, a0 + s * 2048 + 512), xf[s], acc);
    }
    const u32 xs = F_H + w * 4096 + vo, xr = F_H + (w ^ 4) * 4096 + vo;
    stsf4(xs, f32x4{acc[0], acc[1], acc[2], acc[3]}); stsf4(xs + 1024, f32x4{acc[4], acc[5], acc[6], acc[7]});
    stsf4(xs + 2048, f32x4{acc[8], acc[9], acc[10], acc[11]}); stsf4(xs + 3072, f32x4{acc[12], acc[13], acc[14], acc[15]});
    if (w == 1 && j != 0) {
      {
        u32x4 a0, b0;
#pragma unroll 1
        for (int it = 0; it < SPIN_CAP; ++it) {
          asm volatile("global_load_dwordx4 %0, %2, %3 sc1\n\tglobal_load_dwordx4 %1, %2, %3 offset:2048 sc1\n\ts_waitcnt vmcnt(0)" : "=&v"(b0), "=&v"(a0) : "v"(vo), "s"(modr + 6144 + ukh * 1024) : "memory");
          if (__all(a0[1] == tag && a0[3] == tag && b0[1] == tag && b0[3] == tag)) break;
          __builtin_amdgcn_s_sleep(2);
        }
        stsf4(F_M + 2560 + vo, f32x4{rbf(1.f + bflo(a0[0])), rbf(1.f + bfhi(a0[0])), rbf(1.f + bflo(a0[2])), rbf(1.f + bfhi(a0[2]))});
        stsf4(F_M + 3584 + vo, f32x4{bflo(b0[0]), bfhi(b0[0]), bflo(b0[2]), bfhi(b0[2])});
      }
    }
    WAITV_BAR(0);
    f32x4 cs[2][4];
    if (w >= 6) { const u32 a0 = F_D + 8192 + (4 * h + q) * 128 + (rawr ^ (tt * 64)); fo_ = ldstr2(a0, a0 + 1024); }
    if (j != 0) {
      const u32 a0 = F_D + (16 * kq + 4 * h + q) * 128 + (rawr ^ (tt * 64));
      fd_ = ldstr2(a0, a0 + 1024);
#pragma unroll
      for (int i = 0; i < 2; ++i) {
        const int s = 2 * w + i;
        const u32 a1 = F_B + (pp >> 1) * (8192 + 128) + (16 * s + 4 * h + q) * 32 + nh * 16 + (pp & 1) * 8;
        fg[i] = ldstr2(a1, a1 + 256);
        cs[i][0] = ldsf4(F_M + 2560 + (16 * s + 4 * h) * 4); cs[i][1] = ldsf4(F_M + 2560 + (16 * s + 8 + 4 * h) * 4);
        cs[i][2] = ldsf4(F_M + 3584 + (16 * s + 4 * h) * 4); cs[i][3] = ldsf4(F_M + 3584 + (16 * s + 8 + 4 * h) * 4);
      }
    }
    const f32x4 r0 = ldsf4(xr), r1 = ldsf4(xr + 1024), r2 = ldsf4(xr + 2048), r3 = ldsf4(xr + 3072);
    q0 = u32x4{pk((acc[0] + r0[0]) * C2, (acc[1] + r0[1]) * C2), pk((acc[2] + r0[2]) * C2, (acc[3] + r0[3]) * C2), pk((acc[4] + r1[0]) * C2, (acc[5] + r1[1]) * C2), pk((acc[6] + r1[2]) * C2, (acc[7] + r1[3]) * C2)};
    q1 = u32x4{pk((acc[8] + r2[0]) * C2, (acc[9] + r2[1]) * C2), pk((acc[10] + r2[2]) * C2, (acc[11] + r2[3]) * C2), pk((acc[12] + r3[0]) * C2, (acc[13] + r3[1]) * C2), pk((acc[14] + r3[2]) * C2, (acc[15] + r3[3]) * C2)};
    PIN(q0); PIN(q1);
    pdo = p.ws + WS_WO + ((size_t)(2 * j + tt) * 32 + g) * 1024;
    pdd = p.ws + WS_WD + ((size_t)(2 * (uid & 7) + tt) * 112 + 4 * (uid >> 3) + kq) * 1024;
    pdg = p.ws + WS_WGU + ((size_t)uT * 32 + 16 * ukh + 2 * w) * 1024;
    asm volatile("" : "+s"(pdo), "+s"(pdd), "+s"(pdg), "+s"(wob), "+s"(wob1), "+s"(wob2), "+s"(wob3), "+s"(xres), "+s"(gres), "+s"(kv2), "+s"(kv2p));
    asm volatile("" : "+v"(xo), "+v"(go), "+v"(ka), "+v"(va));
    if (w == 2) poll_flags<32, 8>(p.ws + WS_FKV + (blockIdx.x & 7) * 1024, tag, l);
    if (w == 3) {
#pragma unroll 1
      for (int it = 0; it < SPIN_CAP; ++it) {
        const u32 v = __hip_atomic_load((const GAS u32*)(modr + 4096 + j * 256 + h * 6144 + (l & 31) * 8 + 4), RLX_AGENT);
        if (__all(v == tag)) break;
        __builtin_amdgcn_s_sleep(2);
      }
    }
    BAR();
    { const u32 o = (w & 1) * 69632 + (w >> 1) * 6144;
      dma4s(p.ws + WS_KV + o, vo, o); dma<2, 1>(p.ws + WS_KV + o + 4096, vo, o + 4096); }
    o0 = f32x16{}; o1 = f32x16{}; vB0 = u32x4{}; vB1 = u32x4{}; pb0 = u32x4{}; pb1 = u32x4{};
    PIN(o0); PIN(o1); PIN(vB0); PIN(vB1); PIN(pb0); PIN(pb1);
    if (j != 0) {
      float us = 0.f, wsum = 0.f;
#pragma unroll
      for (int i = 0; i < 2; ++i) {
        const u32x4 f = fg[i];
        const f32x4 c0 = cs[i][0], c1 = cs[i][1], s0 = cs[i][2], s1 = cs[i][3];
        u32x4 o;
        o[0] = pk(bflo(f[0]) * c0[0], bfhi(f[0]) * c0[1]); o[1] = pk(bflo(f[1]) * c0[2], bfhi(f[1]) * c0[3]);
        o[2] = pk(bflo(f[2]) * c1[0], bfhi(f[2]) * c1[1]); o[3] = pk(bflo(f[3]) * c1[2], bfhi(f[3]) * c1[3]);
#pragma unroll
        for (int e = 0; e < 4; ++e) { const u32 v = o[e]; us = dot2(v, 0x3f803f80u, us); }
        wsum += bflo(f[0]) * s0[0] + bfhi(f[0]) * s0[1] + bflo(f[1]) * s0[2] + bfhi(f[1]) * s0[3] + bflo(f[2]) * s1[0] + bfhi(f[2]) * s1[1] + bflo(f[3]) * s1[2] + bfhi(f[3]) * s1[3];
        fg[i] = o;
      }
      us = swap32_sum(us); wsum = swap32_sum(wsum);
      if (l < 32) *(LAS f32x2*)(uintptr_t)(OEX + w * 256 + l * 8) = f32x2{us, wsum};
      WAITV_BAR(0);
      if (w == 7) {
        f32x2 s = {0.f, 0.f};
#pragma unroll
        for (int i = 0; i < 8; ++i) s += ldsf2(OEX + i * 256 + (l & 31) * 8);
        const float uh = rbf(s[0]), wh = rbf(s[1]);
        if (l < 32) gst16_sc1(p.ws + WS_AUG + (size_t)uT * 1024 + ukh * 512, vo, u32x4{pk(uh, s[0] - uh), pk(uh, wh), pk(s[1] - wh, wh), 0u});
      }
    } else {
      WAITV_BAR(0);
    }
  }
  const size_t fo = ((size_t)(2 * g + tt) * 32 + 4 * j + 2 * nt) * 1024;
  f32x16 nm0, nm1, s0, s1;
  const u32x4 k0 = lds128(vo);
  u32x4 kA = lds128(ka), kB = lds128(ka + TS);
  u32x4 vA0 = lds128(va), vA1 = lds128(va + 576);
  u32x4 pa0, pa1;
  {
    mf0(s0, k0, q0); mf0(s1, k0, q1);
    MFMA_SETTLE();
    PIN(s0); PIN(s1);
#define MX3(a, b, c) fmaxf(fmaxf(a, b), c)
    const float m0 = swap32_max(MX3(MX3(MX3(s0[0], s0[1], s0[2]), MX3(s0[3], s0[4], s0[5]), MX3(s0[6], s0[7], s0[8])), MX3(MX3(s0[9], s0[10], s0[11]), MX3(s0[12], s0[13], s0[14]), s0[15]), -3.0e38f));
    const float m1 = swap32_max(MX3(MX3(MX3(s1[0], s1[1], s1[2]), MX3(s1[3], s1[4], s1[5]), MX3(s1[6], s1[7], s1[8])), MX3(MX3(s1[9], s1[10], s1[11]), MX3(s1[12], s1[13], s1[14]), s1[15]), -3.0e38f));
#undef MX3
#pragma unroll
    for (int r = 0; r < 16; ++r) nm0[r] = -m0;
    PIN(nm0);
    s0 = mfma(kA, q0, nm0);
#pragma unroll
    for (int r = 0; r < 16; ++r) nm1[r] = -m1;
    PIN(nm1);
  }
  if (!kh) {
    dma4s(kv2p, vo, kv2); dma4s(kv2p + 4096, vo, kv2 + 4096);
  }
#define PREP_ST() { if (j != 0) { gst16_sc1(pdd, vo, fd_); gst16_sc1(pdg, vo, fg[0]); gst16_sc1(pdg + 1024, vo, fg[1]); } }
  if (kh) { PREP_ST() if (w >= 6) gst16_sc1(pdo, vo, fo_); }
  u32x4 wf[16];
#define HALF(SC, N0, N1, MF, OP, VP0, P0, VP1, P1, LD1, LD2)                                              \
  {                                                                                                       \
    MF; LD1; SB();                                                                                        \
    float e0 = EXP2(SC[0]), e1 = EXP2(SC[1]), e2 = EXP2(SC[2]), e3 = EXP2(SC[3]), e4 = EXP2(SC[4]), e5 = EXP2(SC[5]), e6 = EXP2(SC[6]), e7 = EXP2(SC[7]); SB(); \
    OP = mfma(VP0, P0, OP); SB();                                                                         \
    float e8 = EXP2(SC[8]), e9 = EXP2(SC[9]), e10 = EXP2(SC[10]), e11 = EXP2(SC[11]), e12 = EXP2(SC[12]), e13 = EXP2(SC[13]), e14 = EXP2(SC[14]), e15 = EXP2(SC[15]); SB(); \
    OP = mfma(VP1, P1, OP); LD2; SB();                                                                    \
    N0[0] = pk(e0, e1); N0[1] = pk(e2, e3); N0[2] = pk(e4, e5); N0[3] = pk(e6, e7); N1[0] = pk(e8, e9);   \
    N1[1] = pk(e10, e11); N1[2] = pk(e12, e13); N1[3] = pk(e14, e15); SB();                               \
  }
#define TILE(T, KC, KN, VC0, VC1, VP0, VP1)                                                               \
    HALF(s0, pa0, pa1, s1 = mfma(KC, q1, nm1), o1, VP0, pb0, VP1, pb1, KC = lds128(ka + ((T) + 2) * TS),  \
         (VP0 = lds128(va + ((T) + 1) * TS), VP1 = lds128(va + ((T) + 1) * TS + 576)))                    \
    HALF(s1, pb0, pb1, s0 = mfma(KN, q0, nm0), o0, VC0, pa0, VC1, pa1, , )
#define LDWF(SEL)                                                                                                     \
  asm volatile("s_cmp_lg_u32 %21, 0\n\ts_cbranch_scc1 .Lwf%=\n\t"                                                     \
               "global_load_dwordx4 %0, %16, %17 sc1\n\tglobal_load_dwordx4 %1, %16, %17 offset:1024 sc1\n\tglobal_load_dwordx4 %2, %16, %17 offset:2048 sc1\n\tglobal_load_dwordx4 %3, %16, %17 offset:3072 sc1\n\t"     \
               "global_load_dwordx4 %4, %16, %18 sc1\n\tglobal_load_dwordx4 %5, %16, %18 offset:1024 sc1\n\tglobal_load_dwordx4 %6, %16, %18 offset:2048 sc1\n\tglobal_load_dwordx4 %7, %16, %18 offset:3072 sc1\n\t"     \
               "global_load_dwordx4 %8, %16, %19 sc1\n\tglobal_load_dwordx4 %9, %16, %19 offset:1024 sc1\n\tglobal_load_dwordx4 %10, %16, %19 offset:2048 sc1\n\tglobal_load_dwordx4 %11, %16, %19 offset:3072 sc1\n\t"   \
               "global_load_dwordx4 %12, %16, %20 sc1\n\tglobal_load_dwordx4 %13, %16, %20 offset:1024 sc1\n\tglobal_load_dwordx4 %14, %16, %20 offset:2048 sc1\n\tglobal_load_dwordx4 %15, %16, %20 offset:3072 sc1\n"   \
               ".Lwf%=:"                                                                                              \
               : "+v"(wf[0]), "+v"(wf[1]), "+v"(wf[2]), "+v"(wf[3]), "+v"(wf[4]), "+v"(wf[5]), "+v"(wf[6]), "+v"(wf[7]), "+v"(wf[8]), "+v"(wf[9]), "+v"(wf[10]), \
                 "+v"(wf[11]), "+v"(wf[12]), "+v"(wf[13]), "+v"(wf[14]), "+v"(wf[15])                                  \
               : "v"(vo), "s"(wob), "s"(wob1), "s"(wob2), "s"(wob3), "s"(SEL) : "memory", "scc")
#pragma unroll
  for (int s4 = 0; s4 < 16; ++s4) asm volatile("" : "=v"(wf[s4]));
  u32x2 xa, xb; u32x4 ga, gb;
  asm volatile("" : "=v"(xa), "=v"(xb), "=v"(ga), "=v"(gb));
#define LDXR(SEL)                                                                                                     \
    asm volatile("s_cmp_lg_u32 %8, 0\n\ts_cbranch_scc1 .Lxr%=\n\t"                                                   \
                 "global_load_dwordx2 %0, %4, %5\n\tglobal_load_dwordx2 %1, %4, %5 offset:16\n\tglobal_load_dwordx4 %2, %6, %7 sc1\n\tglobal_load_dwordx4 %3, %6, %7 offset:32 sc1\n" \
                 ".Lxr%=:"                                                                                            \
                 : "+v"(xa), "+v"(xb), "+v"(ga), "+v"(gb) : "v"(xo), "s"(xres), "v"(go), "s"(gres), "s"(SEL) : "memory", "scc")
  const int qt = blockIdx.x >> 6;
#pragma unroll 1
  for (int seg = 0; seg < 4; ++seg) {
    char* wsl = p.ws; int wl = w; u32 bl = blockIdx.x;
    if (seg != 0) asm volatile("" : "+s"(wsl), "+s"(wl), "+s"(bl));
    if (seg == 1) {
      if (!kh) {
        PREP_ST()
        const u32 o = ((wl >> 1) & 1) * 69632 + 40960 + (wl & 1) * 14336;
        dma4s(wsl + WS_KV + o, vo, o); dma4s(wsl + WS_KV + o + 4096, vo, o + 4096); dma4s(wsl + WS_KV + o + 8192, vo, o + 8192);
        dma<2, 1>(wsl + WS_KV + o + 12288, vo, o + 12288);
        WAITV(14);
      } else {
        WAITV(0);
      }
      BAR();
      if (w == 0 && l < 8) put_flag(wsl + WS_FP + l * 1024 + bl * 4, tag);
    } else if (seg == 2) {
      if (w == 0) {
        u32x4 f;
#pragma unroll 1
        for (int it = 0; it < SPIN_CAP; ++it) {
          asm volatile("global_load_dwordx4 %0, %1, %2 sc1\n\ts_waitcnt vmcnt(0)" : "=&v"(f) : "v"(vo), "s"(wsl + WS_FP + (bl & 7) * 1024) : "memory");
          if (__all(f[0] == tag && f[1] == tag && f[2] == tag && f[3] == tag)) break;
          __builtin_amdgcn_s_sleep(1);
        }
        if (bl == 0 && l == 0) put_flag(wsl + WS_EPOCH, epoch + 1u);
      }
      WAITV_BAR(0);
    } else if (seg == 3) {
      if (!kh) {
        dma4s((wsl + WS_WGU + (size_t)(((bl & 1) * 8 + ((bl >> 3) & 7)) * 7) * 32768) + wl * 8192, vo, SL0 + wl * 8192); dma4s((wsl + WS_WGU + (size_t)(((bl & 1) * 8 + ((bl >> 3) & 7)) * 7) * 32768) + wl * 8192 + 4096, vo, SL0 + wl * 8192 + 4096);
        dma4s((wsl + WS_WGU + (size_t)(((bl & 1) * 8 + ((bl >> 3) & 7)) * 7) * 32768) + 32768 + wl * 8192, vo, SL1 + wl * 8192); dma4s((wsl + WS_WGU + (size_t)(((bl & 1) * 8 + ((bl >> 3) & 7)) * 7) * 32768) + 32768 + wl * 8192 + 4096, vo, SL1 + wl * 8192 + 4096);
        touch8k((wsl + WS_WGU + (size_t)(((bl & 1) * 8 + ((bl >> 3) & 7)) * 7) * 32768) + 65536 + (bl >> 6) * 40960 + wl * 8192, l * 128);
        if (w < 2) touch8k(w == 0 ? (wsl + WS_WGU + (size_t)(((bl & 1) * 8 + ((bl >> 3) & 7)) * 7) * 32768) + 65536 + (bl >> 6) * 40960 + 32768 : wsl + WS_AUG + (size_t)((bl & 1) * 8 + ((bl >> 3) & 7)) * 7168, l * 128);
      }
    }
    LDWF((seg ^ 2) | (kh ^ 1));
    LDXR((seg ^ 2) | (kh ^ 1));
    TILE(0, kA, kB, vA0, vA1, vB0, vB1) TILE(1, kB, kA, vB0, vB1, vA0, vA1) TILE(2, kA, kB, vA0, vA1, vB0, vB1) TILE(3, kB, kA, vB0, vB1, vA0, vA1)
    TILE(4, kA, kB, vA0, vA1, vB0, vB1) TILE(5, kB, kA, vB0, vB1, vA0, vA1) TILE(6, kA, kB, vA0, vA1, vB0, vB1) TILE(7, kB, kA, vB0, vB1, vA0, vA1)
    ka += 8 * TS; va += 8 * TS; PIN(ka); PIN(va);
  }
  o1 = mfma(vB0, pb0, o1); o1 = mfma(vB1, pb1, o1);
  const u32 oxs = OEX + w * 2304, oxr = OEX + (w ^ 4) * 2304;
  if (kh) { stsf4(oxs + vo, f32x4{o0[0], o0[1], o0[2], o0[3]}); stsf4(oxs + 1024 + vo, f32x4{o0[4], o0[5], o0[6], o0[7]}); *(LAS float*)(uintptr_t)(oxs + 2048 + l * 4) = o0[8]; }
  LDWF(kh);
  LDXR(kh);
  if (!kh) {
    touch8k(p.ws + WS_WD + (size_t)(2 * j) * 114688 + qt * 57344 + w * 8192, l * 128);
    if (w < 3) touch8k(p.ws + WS_WD + (size_t)(2 * j) * 114688 + qt * 57344 + (w + 4) * 8192, l * 128);
  }
#undef HALF
#undef TILE
  {
    if (!kh) { stsf4(oxs + vo, f32x4{o1[0], o1[1], o1[2], o1[3]}); stsf4(oxs + 1024 + vo, f32x4{o1[4], o1[5], o1[6], o1[7]}); *(LAS float*)(uintptr_t)(oxs + 2048 + l * 4) = o1[8]; }
    char* odst = p.ws + WS_O + fo + kh * 1024;
    asm volatile("" : "+s"(odst));
    BAR();
    const f32x4 r0 = ldsf4(oxr + vo), r1 = ldsf4(oxr + 1024 + vo);
    const float r2 = *(const LAS float*)(uintptr_t)(oxr + 2048 + l * 4);
    float o[9];
#pragma unroll
    for (int e = 0; e < 9; ++e) { float a = o0[e], b = o1[e]; PIN(a); PIN(b); o[e] = kh ? b : a; }
#pragma unroll
    for (int e = 0; e < 4; ++e) { o[e] += r0[e]; o[4 + e] += r1[e]; }
    float ls = lowhalf(o[8] + r2);
    bool bad = !(ls < 1.2676506e30f);
#pragma unroll
    for (int e = 0; e < 8; ++e) bad |= !(fabsf(o[e]) < 3.0e38f);
    if (__builtin_expect(__any(bad), 0)) {
      const u32x4 qf = kh ? q1 : q0;
      float m = -INFINITY;
      f32x16 oo = {};
#pragma unroll 1
      for (int t = 0; t < 64; ++t) {
        u32x4 kf, v0, v1;
        asm volatile("global_load_dwordx4 %0, %3, %5 sc1\n\tglobal_load_dwordx4 %1, %4, %5 sc1\n\tglobal_load_dwordx4 %2, %4, %5 offset:576 sc1\n\ts_waitcnt vmcnt(0)"
                     : "=&v"(kf), "=&v"(v0), "=&v"(v1) : "v"(vo), "v"(vrow), "s"(p.ws + WS_KV + t * TS) : "memory");
        const f32x16 s = mfma(kf, qf, f32x16{});
        const float mn = fmaxf(m, rowmax16(s)), al = EXP2(m - mn);
        m = mn;
        u32x4 pa, pb;
#pragma unroll
        for (int r = 0; r < 4; ++r) { pa[r] = pk(EXP2(s[2 * r] - mn), EXP2(s[2 * r + 1] - mn)); pb[r] = pk(EXP2(s[8 + 2 * r] - mn), EXP2(s[9 + 2 * r] - mn)); }
#pragma unroll
        for (int r = 0; r < 16; ++r) oo[r] *= al;
        oo = mfma(v0, pa, oo); oo = mfma(v1, pb, oo);
      }
#pragma unroll
      for (int e = 0; e < 8; ++e) o[e] = oo[e];
      ls = lowhalf(oo[8]);
    }
    const float rl = __builtin_amdgcn_rcpf(ls);
    gst16_sc1(odst, vo, u32x4{pk(o[0] * rl, o[1] * rl), pk(o[2] * rl, o[3] * rl), pk(o[4] * rl, o[5] * rl), pk(o[6] * rl, o[7] * rl)});
  }
  char* fo_own = p.ws + WS_FO + g * 512 + fofs<4>(j);
  const char* fo_poll = p.ws + WS_FO + g * 512 + fofs<4>(l < 8 ? l : 0);
  const char* osrc = p.ws + WS_O + ((size_t)(2 * g + tt) * 32 + 8 * kq) * 1024;
  char* x1dst = p.ws + WS_X1 + fo + kh * 1024;
  char* fx_own = p.ws + WS_FX + (g >> 1) * 512 + fofs<8>((g & 1) * 8 + j);
  asm volatile("" : "+s"(fo_own), "+s"(osrc), "+s"(x1dst), "+s"(fx_own));
  PIN(fo_poll);
  asm volatile("s_waitcnt vmcnt(0)\n\ts_barrier"
               : "+v"(wf[0]), "+v"(wf[1]), "+v"(wf[2]), "+v"(wf[3]), "+v"(wf[4]), "+v"(wf[5]), "+v"(wf[6]), "+v"(wf[7]), "+v"(wf[8]), "+v"(wf[9]),
                 "+v"(wf[10]), "+v"(wf[11]), "+v"(wf[12]), "+v"(wf[13]), "+v"(wf[14]), "+v"(wf[15]), "+v"(xa), "+v"(xb), "+v"(ga), "+v"(gb)::"memory");
  if (w == 1 && l == 0) put_flag(fo_own, tag);
  if (w == 0) poll_at<0>(fo_poll, tag);
  BAR();
  u32x4 x1;
  {
    u32x4 xf[8];
    xf[0] = gld_s<1, 0>(osrc, vo); xf[1] = gld_s<1, 1024>(osrc, vo); xf[2] = gld_s<1, 2048>(osrc, vo); xf[3] = gld_s<1, 3072>(osrc, vo);
    xf[4] = gld_s<1, 0>(osrc + 4096, vo); xf[5] = gld_s<1, 1024>(osrc + 4096, vo); xf[6] = gld_s<1, 2048>(osrc + 4096, vo); xf[7] = gld_s<1, 3072>(osrc + 4096, vo);
    float gf[8] = {bflo(ga[0]), bfhi(ga[0]), bflo(ga[2]), bfhi(ga[2]), bflo(gb[0]), bfhi(gb[0]), bflo(gb[2]), bfhi(gb[2])};
    float xr[8] = {bflo(xa[0]), bfhi(xa[0]), bflo(xa[1]), bfhi(xa[1]), bflo(xb[0]), bfhi(xb[0]), bflo(xb[1]), bfhi(xb[1])};
#pragma unroll
    for (int e = 0; e < 8; ++e) { PIN(gf[e]); PIN(xr[e]); }
    f32x16 acc0, acc1;
#define P3_STEP(S) { waitv_pin<7 - (S)>(xf[S]); if ((S) == 0) { mf0(acc0, wf[0], xf[0]); mf0(acc1, wf[8], xf[0]); } else { mfa(acc0, wf[S], xf[S]); mfa(acc1, wf[8 + (S)], xf[S]); } }
    P3_STEP(0) P3_STEP(1) P3_STEP(2) P3_STEP(3) P3_STEP(4) P3_STEP(5) P3_STEP(6) P3_STEP(7)
#undef P3_STEP
    MFMA_SETTLE();
    PIN(acc0); PIN(acc1);
    float r[8];
    {
      const u32 d = SL3 + tt * 4096 + kq * 1024 + vo;
      sts128(d, pack8(acc0, 0)); sts128(d + 8192, pack8(acc1, 0)); sts128(d + 16384, pack8(acc0, 8)); sts128(d + 24576, pack8(acc1, 8));
      BAR();
      sum_pieces<4>(SL3 + w * 4096 + vo, r);
    }
    x1 = u32x4{pk(fmaf(gf[0], r[0], xr[0]), fmaf(gf[1], r[1], xr[1])), pk(fmaf(gf[2], r[2], xr[2]), fmaf(gf[3], r[3], xr[3])),
               pk(fmaf(gf[4], r[4], xr[4]), fmaf(gf[5], r[5], xr[5])), pk(fmaf(gf[6], r[6], xr[6]), fmaf(gf[7], r[7], xr[7]))};
    gst16_sc1(x1dst, vo, x1);
    dma4s(wgu + 2 * 32768 + w * 4096, vo, SL2 + w * 4096);
    float s1 = 0.f, s2 = 0.f;
#pragma unroll
    for (int e = 0; e < 4; ++e) { const u32 v = x1[e]; s1 = dot2(v, 0x3f803f80u, s1); s2 = dot2(v, v, s2); }
    s1 = swap32_sum(s1); s2 = swap32_sum(s2);
    if (l < 32) *(LAS f32x2*)(uintptr_t)(GAPA + kq * 512 + tt * 256 + l * 8) = f32x2{s1, s2};
    BAR();
    if (w == 7) {
      f32x2 s = {0.f, 0.f};
#pragma unroll
      for (int i = 0; i < 4; ++i) s += ldsf2(GAPA + i * 512 + l * 8);
      gst8_sc1(p.ws + WS_STAT + ((size_t)64 * g * 8 + j) * 8, l * 64, u32x2{__float_as_uint(s[0]), __float_as_uint(s[1])});
      WAITV(5);
    } else {
      WAITV(4);
    }
    asm volatile("s_barrier" ::: "memory");
    if (w == 0 && l == 0) put_flag(fx_own, tag);
    if (w == 7) {
      WAITV(0);
      if (l == 0) put_flag(fx_own + (WS_FS - WS_FX), tag);
    }
  }
  u32x4 wd[28];
  if (kh == 0) p4_compute(p, id, l, w, tag, wd); else p4_helper(p, id, l, w, tag, wd);
  {
    int ly = l; PIN(ly);
    const char* xsrc = p.ws + WS_ACT + ((size_t)(2 * g) * 112 + 14 * w) * 1024;
    u32x4 xf[16];
#define XL(N) (xf[(N) % 16] = gld_s<1, 0>(xsrc + dks((N) >> 1) * 1024 + ((N) & 1) * 114688, vo))
    if (!kh) {
      WAITV(0);
      if (ly == 0 && __hip_atomic_fetch_add((LAS u32*)(uintptr_t)GAPA, 1u, __ATOMIC_RELAXED, __HIP_MEMORY_SCOPE_WORKGROUP) == 3u)
        put_flag(p.ws + WS_FA + id.G * 512 + fofs<8>(id.c), tag);
      BAR();
    }
    XL(0); XL(1); XL(2); XL(3); XL(4); XL(5); XL(6); XL(7); XL(8); XL(9); XL(10); XL(11);
    u32x4 g0 = gld_s<1, 0>(modr + 10240 + (64 * j + 32 * nt + 16 * kh) * 4, (ly >> 5) * 16), g1 = gld_s<1, 32>(modr + 10240 + (64 * j + 32 * nt + 16 * kh) * 4, (ly >> 5) * 16);
    if (kh) asm volatile("s_barrier" ::: "memory");
    f32x16 acc[4];
#define K5_X(N, CNT)                                                                                 \
  {                                                                                                  \
    waitv_pin<CNT>(xf[(N) % 16]);                                                                    \
    if ((N) < 2) { mf0(acc[(N) & 1], wd[0], xf[(N) & 1]); mf0(acc[2 + ((N) & 1)], wd[14], xf[(N) & 1]); } \
    else { mfa(acc[(N) & 1], wd[dks((N) >> 1)], xf[(N) % 16]); mfa(acc[2 + ((N) & 1)], wd[14 + dks((N) >> 1)], xf[(N) % 16]); } \
  }
    K5_X(0, 13) K5_X(1, 12) K5_X(2, 11) K5_X(3, 10) K5_X(4, 9) K5_X(5, 8) K5_X(6, 7) K5_X(7, 6) K5_X(8, 5) K5_X(9, 4) K5_X(10, 3) K5_X(11, 2)
    if (w == 4) poll_flags<16, 8>(p.ws + WS_FA + (g >> 1) * 512, tag, ly);
    asm volatile("s_barrier" ::: "memory");
    XL(12); XL(13); XL(14); XL(15); XL(16); XL(17); XL(18); XL(19); XL(20); XL(21); XL(22); XL(23); XL(24); XL(25); XL(26); XL(27);
    K5_X(12, 15) K5_X(13, 14) K5_X(14, 13) K5_X(15, 12) K5_X(16, 11) K5_X(17, 10) K5_X(18, 9) K5_X(19, 8) K5_X(20, 7) K5_X(21, 6) K5_X(22, 5) K5_X(23, 4)
    K5_X(24, 3) K5_X(25, 2) K5_X(26, 1) K5_X(27, 0)
#undef K5_X
    MFMA_SETTLE();
    PIN(g0); PIN(g1); PIN(acc[0]); PIN(acc[1]); PIN(acc[2]); PIN(acc[3]);
    float r[8], gf[8], x1f[8];
    u16* optr;
    {
      const u32 d = w * 1024 + vo;
#pragma unroll
      for (int pc = 0; pc < 8; ++pc) sts128(d + pc * 8192, pack8(acc[pc & 3], (pc >> 2) * 8));
#pragma unroll
      for (int e = 0; e < 4; ++e) { x1f[2 * e] = bflo(x1[e]); x1f[2 * e + 1] = bfhi(x1[e]); }
      gf[0] = bflo(g0[0]); gf[1] = bfhi(g0[0]); gf[2] = bflo(g0[2]); gf[3] = bfhi(g0[2]); gf[4] = bflo(g1[0]); gf[5] = bfhi(g1[0]); gf[6] = bflo(g1[2]); gf[7] = bfhi(g1[2]);
#pragma unroll
      for (int e = 0; e < 8; ++e) { PIN(gf[e]); PIN(x1f[e]); }
      int lz = l; PIN(lz);
      optr = p.out + (size_t)(64 * g + 32 * tt + (lz & 31)) * DM + 64 * j + 32 * nt + 16 * kh + 8 * (lz >> 5);
      PIN(optr);
      BAR();
      sum_pieces<8>(w * 8192 + vo, r);
    }
    const u32 a0 = pk(fmaf(gf[0], r[0], x1f[0]), fmaf(gf[1], r[1], x1f[1])), a1 = pk(fmaf(gf[2], r[2], x1f[2]), fmaf(gf[3], r[3], x1f[3]));
    const u32 b0 = pk(fmaf(gf[4], r[4], x1f[4]), fmaf(gf[5], r[5], x1f[5])), b1 = pk(fmaf(gf[6], r[6], x1f[6]), fmaf(gf[7], r[7], x1f[7]));
    const auto s0 = __builtin_amdgcn_permlane32_swap(a0, b0, false, false);
    const auto s1 = __builtin_amdgcn_permlane32_swap(a1, b1, false, false);
    *(GAS u32x4*)optr = u32x4{s0[0], s1[0], s0[1], s1[1]};
  }
}

extern "C" void kernel_launch(void* const* d_in, const int* in_sizes, int n_in, void* d_out, int out_size, void* d_ws, size_t ws_size, hipStream_t stream) {
  Params p;
  p.x = (const u16*)d_in[0]; p.wq = (const u16*)d_in[1]; p.wk = (const u16*)d_in[2]; p.wv = (const u16*)d_in[3]; p.wo = (const u16*)d_in[4];
  p.wg = (const u16*)d_in[6]; p.wu = (const u16*)d_in[7]; p.wd = (const u16*)d_in[8]; p.c = (const u16*)d_in[9]; p.wm = (const u16*)d_in[10];
  p.out = (u16*)d_out; p.ws = (char*)d_ws;
  (void)hipFuncSetAttribute((const void*)k_main, hipFuncAttributeMaxDynamicSharedMemorySize, M_LDS);
  k_main<<<256, 512, M_LDS, stream>>>(p);
}
```
